```python
import torch

def _softmax(x, axis=-1):
    return torch.softmax(x, dim=axis)
def _gelu(x):
    return torch.nn.functional.gelu(x, approximate="tanh")
def _silu(x):
    return torch.nn.functional.silu(x)
def _sigmoid(x):
    return torch.sigmoid(x)
def _layernorm(x, axis=-1, eps=1e-5):
    m = x.mean(dim=axis, keepdim=True)
    v = x.var(dim=axis, keepdim=True, unbiased=False)
    return (x - m) / torch.sqrt(v + eps)
def _scatter_add(tgt, idx, src, axis=0):
    return tgt.clone().index_add_(axis, idx, src)
def _segment_sum(src, seg, n):
    out = torch.zeros((n,) + src.shape[1:], dtype=src.dtype)
    return out.index_add_(0, seg, src)
def _pad(x, pw):
    flat = tuple(v for pair in reversed(pw) for v in pair)
    return torch.nn.functional.pad(x, flat)

kernel_name = 'sequential_preLN__ssm_conv_s4__glu_gated_bilinear__prompt'

def setup_inputs():

    _w = (lambda a: (lambda t: t.to(torch.bfloat16) if t.is_floating_point() else t)(torch.as_tensor(a)))
    def _A_setup():
        B, L, D, N = 1, 2048, 640, 128
        A_re = -(lambda _x: torch.exp(torch.as_tensor(_x)))((lambda *s: torch.randn(*s, dtype=torch.bfloat16) * 6.4)(D, N))
        A_im = torch.pi * (lambda *s: torch.randn(*s, dtype=torch.bfloat16) * 0.05)(D, N)
        B_re = (lambda *s: torch.randn(*s, dtype=torch.bfloat16) * 0.05)(D, N)
        B_im = (lambda *s: torch.randn(*s, dtype=torch.bfloat16) * 0.05)(D, N)
        C_re = (lambda *s: torch.randn(*s, dtype=torch.bfloat16) * 0.05)(D, N)
        C_im = (lambda *s: torch.randn(*s, dtype=torch.bfloat16) * 0.05)(D, N)
        log_dt = (lambda *s: torch.randn(*s, dtype=torch.bfloat16) * 0.05)(D) * 0.5 - 4.0
        Dskip = (lambda *s: torch.randn(*s, dtype=torch.bfloat16) * 0.05)(D)
        return {
            "u": _w((lambda *s: torch.randn(*s, dtype=torch.bfloat16) * 0.05)(B, L, D)),
            "A_re": _w(A_re),
            "A_im": _w(A_im),
            "B_re": _w(B_re),
            "B_im": _w(B_im),
            "C_re": _w(C_re),
            "C_im": _w(C_im),
            "log_dt": _w(log_dt),
            "Dskip": _w(Dskip),
        }
    _A_in = _A_setup()

    def _B_setup():
        B, S, D, F = 1, 2048, 640, 1728
        return {
            "x": _w((lambda *s: torch.randn(*s, dtype=torch.bfloat16) * 0.05)(B, S, D)),
            "w_gate": _w((lambda *s: torch.randn(*s, dtype=torch.bfloat16) * 0.4)(D, F) * (D ** -0.5)),
            "w_up": _w((lambda *s: torch.randn(*s, dtype=torch.bfloat16) * 0.4)(D, F) * (D ** -0.5)),
            "w_down": _w((lambda *s: torch.randn(*s, dtype=torch.bfloat16) * 0.4)(F, D) * (F ** -0.5)),
        }
    _B_in = _B_setup()

    return {
        "A_u": _A_in["u"],
        "A_A_re": _A_in["A_re"],
        "A_A_im": _A_in["A_im"],
        "A_B_re": _A_in["B_re"],
        "A_B_im": _A_in["B_im"],
        "A_C_re": _A_in["C_re"],
        "A_C_im": _A_in["C_im"],
        "A_log_dt": _A_in["log_dt"],
        "A_Dskip": _A_in["Dskip"],
        "B_x": _B_in["x"],
        "B_w_gate": _B_in["w_gate"],
        "B_w_up": _B_in["w_up"],
        "B_w_down": _B_in["w_down"],
    }

def reference(A_u, A_A_re, A_A_im, A_B_re, A_B_im, A_C_re, A_C_im, A_log_dt, A_Dskip, B_x, B_w_gate, B_w_up, B_w_down):
    def _A_ref(u, A_re, A_im, B_re, B_im, C_re, C_im, log_dt, Dskip):
        Bsz, L, D = u.shape
        dt = torch.exp(log_dt)[:, None]
        A = A_re + 1j * A_im
        Bc = B_re + 1j * B_im
        Cc = C_re + 1j * C_im
        logdA = dt * A
        dB = dt * Bc
        l = (lambda x, dt: x.to(dt))(torch.arange(L), A_re.dtype)
        P = torch.exp(logdA[:, :, None] * l[None, None, :])
        K = torch.einsum("dn,dnl,dn->dl", Cc, P, dB).real
        L2 = 2 * L
        Kp = _pad(K, ((0, 0), (0, L)))
        up = (lambda x, *d: x.permute(*d))(u, 0, 2, 1)
        up = _pad(up, ((0, 0), (0, 0), (0, L)))
        Kf = (lambda x, axes=(-2,-1): torch.fft.rfft2(x.float(), dim=axes))(Kp[:, None, :], axes=(-2, -1))
        Uf = (lambda x, axes=(-2,-1): torch.fft.rfft2(x.float(), dim=axes))(up[:, :, None, :], axes=(-2, -1))
        Yf = Uf * Kf[None, :, :, :]
        y = (lambda x, s, axes=(-2,-1): torch.fft.irfft2(x, s=s, dim=axes).to(torch.bfloat16))(Yf, (1, L2), axes=(-2, -1))[:, :, 0, :L]
        y = (lambda x, *d: x.permute(*d))(y, 0, 2, 1) + u * Dskip[None, None, :]
        return y

    def _B_ref(x, w_gate, w_up, w_down):
        g = torch.einsum("bsd,df->bsf", x, w_gate)
        u = torch.einsum("bsd,df->bsf", x, w_up)
        h = g * u
        return torch.einsum("bsf,fd->bsd", h, w_down)

    x = A_u
    h = x + _A_ref(_layernorm(x, axis=-1), A_A_re, A_A_im, A_B_re, A_B_im, A_C_re, A_C_im, A_log_dt, A_Dskip)
    h2 = h + _B_ref(_layernorm(h, axis=-1), B_w_gate, B_w_up, B_w_down)
    return h2
```

```cpp
#include <hip/hip_runtime.h>
#include <hip/hip_bf16.h>
#include <stdint.h>

typedef unsigned short u16;
typedef _Float16 h2_t __attribute__((ext_vector_type(2)));
typedef _Float16 h8_t __attribute__((ext_vector_type(8)));
typedef __bf16 b2_t __attribute__((ext_vector_type(2)));
typedef __bf16 b8_t __attribute__((ext_vector_type(8)));
typedef float f2_t __attribute__((ext_vector_type(2)));
typedef float f4_t __attribute__((ext_vector_type(4)));
typedef unsigned u2_t __attribute__((ext_vector_type(2)));
typedef unsigned u4_t __attribute__((ext_vector_type(4)));
typedef short s4_t __attribute__((ext_vector_type(4)));
#define LAS __attribute__((address_space(3)))

constexpr int SL = 2048;
constexpr int DM = 640;
constexpr int FF = 1728;

constexpr size_t OFF_X4 = 0;
constexpr size_t OFF_S1 = 0x280000;
constexpr size_t OFF_KD = 0x290000;
constexpr size_t OFF_INVS = 0x590000;
constexpr size_t OFF_H8 = 0x5A0000;
constexpr size_t OFF_HH = 0x820000;
constexpr size_t OFF_WP2 = 0xF00000;
constexpr int KDS = 4864;
constexpr int KD_O = 2240;
constexpr int KD_DR = 4384;
constexpr int P0 = 1056;

__device__ __forceinline__ float bf_lo(unsigned d) { return __uint_as_float(d << 16); }
__device__ __forceinline__ float bf_hi(unsigned d) { return __uint_as_float(d & 0xffff0000u); }
__device__ __forceinline__ float bf2f(u16 v) { return __uint_as_float((unsigned)v << 16); }
__device__ __forceinline__ unsigned pk_bf16(float a, float b) {
  f2_t v = {a, b};
  return __builtin_bit_cast(unsigned, __builtin_convertvector(v, b2_t));
}
__device__ __forceinline__ unsigned pk_f16(float a, float b) {
  f2_t v = {a, b};
  return __builtin_bit_cast(unsigned, __builtin_convertvector(v, h2_t));
}
__device__ __forceinline__ float rbf(float a) { return bf_lo(pk_bf16(a, 0.f)); }
template <int N> __device__ __forceinline__ float dpp_ror_add(float v) {
  int r = __builtin_amdgcn_update_dpp(0, __float_as_int(v), 0x120 + N, 0xf, 0xf, false);
  return v + __int_as_float(r);
}
__device__ __forceinline__ float row16_sum(float v) {
  v = dpp_ror_add<8>(v); v = dpp_ror_add<4>(v); v = dpp_ror_add<2>(v); v = dpp_ror_add<1>(v);
  return v;
}
__device__ __forceinline__ void wave_fence() {
  __builtin_amdgcn_fence(__ATOMIC_RELEASE, "wavefront");
  __builtin_amdgcn_wave_barrier();
  __builtin_amdgcn_fence(__ATOMIC_ACQUIRE, "wavefront");
}
__device__ __forceinline__ s4_t tr_read(const char* p) {
  return __builtin_amdgcn_ds_read_tr16_b64_v4i16((LAS s4_t*)p);
}

struct Cw { f2_t w, ws; };
__device__ __forceinline__ Cw mkw(f2_t w) { Cw r; r.w = w; r.ws = f2_t{-w.y, w.x}; return r; }
__device__ __forceinline__ f2_t cmulw(f2_t a, const Cw& W) {
  return __builtin_elementwise_fma(f2_t{a.y, a.y}, W.ws, f2_t{a.x, a.x} * W.w);
}
__device__ __forceinline__ f2_t cexp_l(float er, float wr, float l) {
  float mag = __builtin_amdgcn_exp2f(er * l);
  float rev = __builtin_amdgcn_fractf(wr * l);
  return f2_t{mag * __builtin_amdgcn_cosf(rev), mag * __builtin_amdgcn_sinf(rev)};
}
__device__ __forceinline__ float pow2_scale(float bound, int target) {
  unsigned e = (__float_as_uint(bound) >> 23) & 0xff;
  int k = target - ((int)e - 126);
  k = k > 126 ? 126 : (k < -126 ? -126 : k);
  return (bound > 0.f) ? __uint_as_float((unsigned)(k + 127) << 23) : 1.0f;
}
__device__ __forceinline__ int lrow(int rho) {
  if (rho < 16) return 128 * (rho >> 3) + (rho & 7);
  if (rho < 24) return 128 * (2 + ((rho - 16) >> 2)) + 2 * ((rho - 16) & 3);
  if (rho < 32) return 128 * (4 + ((rho - 24) >> 1)) + 4 * ((rho - 24) & 1);
  return 128 * (8 + (rho - 32));
}
__device__ __forceinline__ int rnd_lag(int l) {
  unsigned u = __float_as_uint((float)l);
  u = (u + 0x7fffu + ((u >> 16) & 1u)) & 0xffff0000u;
  return (int)__uint_as_float(u);
}

constexpr int A_XBLK = 128, A_KBLK = 320;
constexpr int KG_WBUF = 2 * 16 * 272;
constexpr int KG_CH = 2 * KG_WBUF + 3072 + 16 + 4352;
constexpr int A_LDS = 2 * KG_CH;

__global__ __launch_bounds__(256) void kA(const u16* __restrict__ x, const u16* __restrict__ Are, const u16* __restrict__ Aim,
                                          const u16* __restrict__ Bre, const u16* __restrict__ Bim, const u16* __restrict__ Cre,
                                          const u16* __restrict__ Cim, const u16* __restrict__ logdt, char* __restrict__ ws) {
  extern __shared__ __attribute__((aligned(16))) char smem[];
  const int tid = threadIdx.x, lane = tid & 63, w = tid >> 6;
  if (blockIdx.x < A_XBLK) {
    const int t0 = blockIdx.x * 16;
    const int r = lane >> 4, c = lane & 15, rl = w * 4 + r;
    const u4_t* p = (const u4_t*)(x + (size_t)(t0 + rl) * DM);
    u4_t v[5];
#pragma unroll
    for (int i = 0; i < 5; ++i) v[i] = p[c + 16 * i];
    float s = 0.f;
#pragma unroll
    for (int i = 0; i < 5; ++i)
#pragma unroll
      for (int j = 0; j < 4; ++j) s += bf_lo(v[i][j]) + bf_hi(v[i][j]);
    const float mean = row16_sum(s) * (1.0f / DM);
    float q = 0.f;
#pragma unroll
    for (int i = 0; i < 5; ++i)
#pragma unroll
      for (int j = 0; j < 4; ++j) {
        float a = bf_lo(v[i][j]) - mean, b = bf_hi(v[i][j]) - mean;
        q += a * a; q += b * b;
      }
    const float var = row16_sum(q) * (1.0f / DM);
    if (c == 0) {
      float mb = rbf(mean), sd = rbf(sqrtf(rbf(rbf(var) + 1e-5f)));
      ((unsigned*)(ws + OFF_S1))[t0 + rl] = (__float_as_uint(mb) >> 16) | (__float_as_uint(sd) & 0xffff0000u);
    }
#pragma unroll
    for (int i = 0; i < 5; ++i) *(u4_t*)(smem + rl * 1296 + (c + 16 * i) * 16) = v[i];
    __syncthreads();
#pragma unroll
    for (int it = 0; it < 5; ++it) {
      const int item = tid + 256 * it, q4 = item >> 3, tp = item & 7;
      u2_t a = *(const u2_t*)(smem + (2 * tp) * 1296 + 8 * q4);
      u2_t b = *(const u2_t*)(smem + (2 * tp + 1) * 1296 + 8 * q4);
      *(u4_t*)(ws + OFF_X4 + ((size_t)q4 * SL + t0 + 2 * tp) * 8) = u4_t{a.x, a.y, b.x, b.y};
    }
    return;
  }
  const int cl = w >> 1, hw = w & 1;
  const int ch = (blockIdx.x - A_XBLK) * 2 + cl;
  char* chb = smem + cl * KG_CH;
  char* lbuf = chb + hw * KG_WBUF;
  char* rbuf = lbuf + 16 * 272;
  float* xch = (float*)(chb + 2 * KG_WBUF);
  u16* tab = (u16*)(chb + 2 * KG_WBUF + 3072 + 16);
  const int n = ch * 128 + hw * 64 + lane;
  const float dt = rbf(expf(bf2f(logdt[ch])));
  const float are = dt * bf2f(Are[n]), aim = dt * bf2f(Aim[n]);
  const float bre = dt * bf2f(Bre[n]), bim = dt * bf2f(Bim[n]);
  const float cre = bf2f(Cre[n]), cim = bf2f(Cim[n]);
  f2_t c = {cre * bre - cim * bim, cre * bim + cim * bre};
  const float er = are * 1.44269504088896341f, wr = aim * 0.15915494309189535f;
  float bnd = (fabsf(c.x) + fabsf(c.y)) * fmaxf(1.0f, __builtin_amdgcn_exp2f(er * 2048.f));
  float bmax = bnd, bsum = bnd;
#pragma unroll
  for (int o = 1; o < 64; o <<= 1) { bmax = fmaxf(bmax, __shfl_xor(bmax, o)); bsum += __shfl_xor(bsum, o); }
  const float Sw = pow2_scale(bmax, 14);
  c *= Sw;
  const Cw z1 = mkw(cexp_l(er, wr, 1.f)), w8 = mkw(cexp_l(er, wr, 8.f)), w128 = mkw(cexp_l(er, wr, 128.f));
  const Cw z2 = mkw(cmulw(z1.w, z1));
  const Cw z4 = mkw(cmulw(z2.w, z2));
  {
    f2_t R = {1.f, 0.f};
#pragma unroll
    for (int i = 0; i < 16; ++i) {
      *(unsigned*)(rbuf + i * 272 + 4 * lane) = pk_f16(R.x, R.y);
      if (i < 15) R = (i == 0) ? w8.w : cmulw(R, w8);
    }
  }
  f2_t T1[17];
  T1[0] = c;
#pragma unroll
  for (int i = 1; i < 17; ++i) T1[i] = cmulw(T1[i - 1], w128);
  const int fr = lane & 15, kg = lane >> 4;
  h8_t Bf[4];
  f4_t acc[3];
  auto put = [&](int row, f2_t v) { *(unsigned*)(lbuf + row * 272 + 4 * lane) = pk_f16(v.x, -v.y); };
  auto mm = [&](int tile) {
    wave_fence();
    if (tile == 0) {
#pragma unroll
      for (int ks = 0; ks < 4; ++ks) Bf[ks] = *(const h8_t*)(rbuf + fr * 272 + 64 * ks + 16 * kg);
    }
    f4_t a = {0.f, 0.f, 0.f, 0.f};
#pragma unroll
    for (int ks = 0; ks < 4; ++ks) {
      h8_t Af = *(const h8_t*)(lbuf + fr * 272 + 64 * ks + 16 * kg);
      a = __builtin_amdgcn_mfma_f32_16x16x32_f16(Af, Bf[ks], a, 0, 0, 0);
    }
    acc[tile] = a;
    wave_fence();
  };
#pragma unroll
  for (int i1 = 0; i1 < 2; ++i1) {
    f2_t v = T1[i1];
#pragma unroll
    for (int i0 = 0; i0 < 8; ++i0) { put(8 * i1 + i0, v); if (i0 < 7) v = cmulw(v, z1); }
  }
  mm(0);
#pragma unroll
  for (int i1 = 2; i1 < 4; ++i1) {
    f2_t v = T1[i1];
#pragma unroll
    for (int i0 = 0; i0 < 4; ++i0) { put(4 * (i1 - 2) + i0, v); if (i0 < 3) v = cmulw(v, z2); }
  }
#pragma unroll
  for (int i1 = 4; i1 < 8; ++i1) { put(8 + 2 * (i1 - 4), T1[i1]); put(8 + 2 * (i1 - 4) + 1, cmulw(T1[i1], z4)); }
  mm(1);
#pragma unroll
  for (int i1 = 8; i1 < 17; ++i1) put(i1 - 8, T1[i1]);
  mm(2);
  const float invSw = 1.0f / Sw;
#pragma unroll
  for (int t = 0; t < 3; ++t) acc[t] *= invSw;
  if (hw == 1) {
#pragma unroll
    for (int t = 0; t < 3; ++t) *(f4_t*)(xch + (t * 64 + lane) * 4) = acc[t];
    if (lane == 0) xch[768] = bsum;
  }
  __syncthreads();
  if (hw == 0) {
    const float Sc = pow2_scale(bsum + xch[768], 14);
#pragma unroll
    for (int t = 0; t < 3; ++t) {
      f4_t o = *(const f4_t*)(xch + (t * 64 + lane) * 4);
#pragma unroll
      for (int i = 0; i < 4; ++i) {
        const int rho = 16 * t + 4 * kg + i;
        const int l = lrow(rho) + 8 * fr;
        if (rho <= 40 && l <= 2048) tab[l] = (u16)(pk_f16((acc[t][i] + o[i]) * Sc, 0.f) & 0xffff);
      }
    }
    if (lane == 0) ((float*)(ws + OFF_INVS))[ch] = 1.0f / Sc;
  }
  __syncthreads();
  char* kd = ws + OFF_KD + (size_t)ch * KDS;
  const int j = hw * 64 + lane;
#pragma unroll
  for (int it = 0; it < 5; ++it) {
    const int m = j + 128 * it;
    if (m < 536) {
      const int lA = P0 - 2 * m;
      unsigned k3[3];
#pragma unroll
      for (int e = 0; e < 3; ++e) {
        const int l = lA - e;
        k3[e] = (l >= 0 && l <= 1028) ? (unsigned)tab[rnd_lag(l < 0 ? 0 : l)] : 0u;
      }
      ((unsigned*)kd)[m] = k3[0] | (k3[1] << 16);
      ((unsigned*)(kd + KD_O))[m] = k3[1] | (k3[2] << 16);
    }
  }
  if (j < 80) {
    const int dl = 127 - j;
    unsigned v = 0;
    if (dl >= 64) v = (unsigned)tab[8 * (2 * dl + 1)] | ((unsigned)tab[8 * (2 * dl + 2)] << 16);
    ((unsigned*)(kd + KD_DR))[j] = v;
  }
}

constexpr int B_CONV = 160;
constexpr int B_KOFF = 0;
constexpr int B_UOFF = 4 * KDS;
constexpr int B_UCH = 4608;
constexpr int B_YCH = 10240;
constexpr int B_LDS = 40960;

__global__ __launch_bounds__(512) void kB(const u16* __restrict__ dskip, const u16* __restrict__ wd, char* __restrict__ ws) {
  extern __shared__ __attribute__((aligned(16))) char smem[];
  const int tid = threadIdx.x, lane = tid & 63;
  const int w = __builtin_amdgcn_readfirstlane(tid >> 6);
  if (blockIdx.x >= B_CONV) {
    const int u = (blockIdx.x - B_CONV) * 8 + w;
    if (u >= 432) return;
    const int ks = u >> 3, dg = u & 7;
    char* buf = smem + w * 5120;
#pragma unroll
    for (int i = 0; i < 5; ++i) {
      const int idx = i * 64 + lane, row = idx / 10, c = idx % 10;
      __builtin_amdgcn_global_load_lds((const unsigned*)((const char*)wd + (size_t)(32 * ks + row) * 1280 + 160 * dg + 16 * c),
                                       (LAS unsigned*)(buf + i * 1024), 16, 0, 0);
    }
    asm volatile("s_waitcnt vmcnt(0)" ::: "memory");
    wave_fence();
    const int g = lane >> 4, q = (lane & 15) >> 2, p = lane & 3;
    char* dst = ws + OFF_WP2 + ((size_t)(dg * 54 + ks) * 5) * 1024 + lane * 16;
#pragma unroll
    for (int dt = 0; dt < 5; ++dt) {
      s4_t a = tr_read(buf + (4 * g + q) * 160 + 32 * dt + 8 * p);
      s4_t b = tr_read(buf + (16 + 4 * g + q) * 160 + 32 * dt + 8 * p);
      u2_t ua = __builtin_bit_cast(u2_t, a), ub = __builtin_bit_cast(u2_t, b);
      *(u4_t*)(dst + dt * 1024) = u4_t{ua.x, ua.y, ub.x, ub.y};
    }
    return;
  }
  const int a_ = blockIdx.x >> 3, x_ = blockIdx.x & 7;
  const int q = 16 * (a_ >> 1) + 2 * x_ + (a_ & 1);
  for (int i = w; i < 19; i += 8)
    __builtin_amdgcn_global_load_lds((const unsigned*)(ws + OFF_KD + (size_t)q * (4 * KDS) + i * 1024 + lane * 16),
                                     (LAS unsigned*)(smem + B_KOFF + i * 1024), 16, 0, 0);
  u4_t xv[2]; u2_t sv[2];
#pragma unroll
  for (int it = 0; it < 2; ++it) {
    const int j = tid + 512 * it;
    xv[it] = *(const u4_t*)(ws + OFF_X4 + ((size_t)q * SL + 2 * j) * 8);
    sv[it] = *(const u2_t*)(ws + OFF_S1 + 8 * j);
  }
  float dk[4], is[4];
#pragma unroll
  for (int c = 0; c < 4; ++c) { dk[c] = bf2f(dskip[4 * q + c]); is[c] = ((const float*)(ws + OFF_INVS))[4 * q + c]; }
  *(unsigned*)(smem + B_UOFF + (tid >> 7) * B_UCH + 4 * (tid & 127)) = 0u;
  float z[2][8];
#pragma unroll
  for (int it = 0; it < 2; ++it) {
    const int j = tid + 512 * it;
    const float m0 = bf_lo(sv[it].x), r0 = 1.0f / bf_hi(sv[it].x), m1 = bf_lo(sv[it].y), r1 = 1.0f / bf_hi(sv[it].y);
    float x0[4] = {bf_lo(xv[it].x), bf_hi(xv[it].x), bf_lo(xv[it].y), bf_hi(xv[it].y)};
    float x1[4] = {bf_lo(xv[it].z), bf_hi(xv[it].z), bf_lo(xv[it].w), bf_hi(xv[it].w)};
#pragma unroll
    for (int c = 0; c < 4; ++c) {
      const float u0 = (x0[c] - m0) * r0, u1 = (x1[c] - m1) * r1;
      *(unsigned*)(smem + B_UOFF + c * B_UCH + 512 + 4 * j) = pk_f16(u0, u1);
      z[it][c] = x0[c] + u0 * dk[c];
      z[it][4 + c] = x1[c] + u1 * dk[c];
    }
  }
  __syncthreads();
  f4_t acc[8];
  if (w < 4) {
    const int c = lane & 15, kg = lane >> 4;
#pragma unroll
    for (int t = 0; t < 8; ++t) acc[t] = f4_t{0.f, 0.f, 0.f, 0.f};
    const char* ub = smem + B_UOFF + w * B_UCH + 2 * (256 + 16 * c - 16 + 8 * kg) - 64 * 7;
    const char* kb = smem + B_KOFF + w * KDS + (c & 1) * KD_O + 4 * (520 - ((c + 1) >> 1) + 4 * kg) - 64 * 7;
#pragma unroll
    for (int i0 = 0; i0 < 8; ++i0) {
      h8_t Bf[8];
#pragma unroll
      for (int p = 0; p < 8; ++p) Bf[p] = *(const h8_t*)(ub + 512 * p + 64 * (7 - i0));
#pragma unroll
      for (int i1 = 0; i1 < 5; ++i1) {
        if (i1 == 4 && i0 != 0) continue;
        const unsigned* ap = (const unsigned*)(kb - 512 * i1 + 64 * (7 - i0));
        u4_t au = {ap[0], ap[1], ap[2], ap[3]};
        h8_t Af = __builtin_bit_cast(h8_t, au);
#pragma unroll
        for (int t = i1; t < 8; ++t) acc[t] = __builtin_amdgcn_mfma_f32_16x16x32_f16(Af, Bf[t - i1], acc[t], 0, 0, 0);
      }
    }
    h8_t band9, band7;
#pragma unroll
    for (int j = 0; j < 8; ++j) {
      const int k = 8 * kg + j;
      band9[j] = (k >= c + 4 && k <= c + 12) ? (_Float16)1.0f : (_Float16)0.0f;
      band7[j] = (k >= c + 5 && k <= c + 11) ? (_Float16)1.0f : (_Float16)0.0f;
    }
    h8_t Tf[4];
    const unsigned* dr = (const unsigned*)(smem + B_KOFF + w * KDS + KD_DR) + (127 - c + 4 * kg);
#pragma unroll
    for (int dl = 0; dl < 4; ++dl) {
      const unsigned* tp = dr - 16 * (dl + 4);
      Tf[dl] = __builtin_bit_cast(h8_t, (u4_t{tp[0], tp[1], tp[2], tp[3]}));
    }
    const char* uw = smem + B_UOFF + w * B_UCH + 2 * (256 + 16 * c + 8 * kg);
#pragma unroll
    for (int bt = 0; bt < 4; ++bt) {
      h8_t U9 = *(const h8_t*)(uw + 512 * bt - 48);
      h8_t U7 = *(const h8_t*)(uw + 512 * bt - 32);
      f4_t zz = {0.f, 0.f, 0.f, 0.f};
      f4_t X9 = __builtin_amdgcn_mfma_f32_16x16x32_f16(U9, band9, zz, 0, 0, 0);
      f4_t X7 = __builtin_amdgcn_mfma_f32_16x16x32_f16(U7, band7, zz, 0, 0, 0);
      u4_t vu = {pk_f16(X7[0], X9[0]), pk_f16(X7[1], X9[1]), pk_f16(X7[2], X9[2]), pk_f16(X7[3], X9[3])};
      h8_t Vf = __builtin_bit_cast(h8_t, vu);
#pragma unroll
      for (int at = 4 + bt; at < 8; ++at) acc[at] = __builtin_amdgcn_mfma_f32_16x16x32_f16(Vf, Tf[at - bt - 4], acc[at], 0, 0, 0);
    }
  }
  __syncthreads();
  if (w < 4) {
    const float s = (w == 0) ? is[0] : (w == 1) ? is[1] : (w == 2) ? is[2] : is[3];
    const int c = lane & 15, g = lane >> 4;
#pragma unroll
    for (int t = 0; t < 8; ++t) *(f4_t*)(smem + w * B_YCH + t * 1280 + g * 320 + c * 16) = acc[t] * s;
  }
  __syncthreads();
#pragma unroll
  for (int it = 0; it < 2; ++it) {
    const int j = tid + 512 * it, t = 2 * j;
    const int yo = (t >> 8) * 1280 + ((t >> 2) & 3) * 320 + ((t >> 4) & 15) * 16 + (t & 3) * 4;
    float h0[4], h1[4];
#pragma unroll
    for (int c = 0; c < 4; ++c) {
      f2_t y = *(const f2_t*)(smem + c * B_YCH + yo);
      h0[c] = z[it][c] + y.x;
      h1[c] = z[it][4 + c] + y.y;
    }
    char* dst = ws + OFF_H8 + ((size_t)(q >> 1) * SL + t) * 16 + (q & 1) * 8;
    *(u2_t*)dst = u2_t{pk_bf16(h0[0], h0[1]), pk_bf16(h0[2], h0[3])};
    *(u2_t*)(dst + 16) = u2_t{pk_bf16(h1[0], h1[1]), pk_bf16(h1[2], h1[3])};
  }
}

__device__ __forceinline__ void glds16(const void* sbase, unsigned voff, unsigned lds_addr) {
  asm volatile("s_mov_b32 m0, %2\n\ts_nop 0\n\tglobal_load_lds_dwordx4 %0, %1" ::"v"(voff), "s"(sbase), "s"(lds_addr) : "memory");
}
template <int N> __device__ __forceinline__ void wait_vm() { asm volatile("s_waitcnt vmcnt(%0)" ::"i"(N) : "memory"); }
__device__ __forceinline__ void bar() { asm volatile("s_barrier" ::: "memory"); }
__device__ __forceinline__ float dot2bf(unsigned a, unsigned b, float c) {
  return __builtin_amdgcn_fdot2_f32_bf16(__builtin_bit_cast(b2_t, a), __builtin_bit_cast(b2_t, b), c, false);
}

constexpr int C_T = 240;
constexpr int C_SLOT = 23552, C_NSLOT = 6, C_GOFF = 15360;
constexpr int C_STAT = C_NSLOT * C_SLOT;
constexpr int C_CS = C_STAT + 1920;
constexpr int C_LDS = C_CS + 512;
constexpr int C_STEPS = 20;

template <int NDMA>
__device__ __forceinline__ void c_loader(char* smem, const char* h8, const char* wbase, int w4, int lane, int t0, int jline) {
  const unsigned lds0 = (unsigned)(size_t)(LAS char*)smem;
  unsigned voffH[4], voffW[2];
#pragma unroll
  for (int k = 0; k < 4; ++k) {
    const int idx = 64 * (w4 + 4 * k) + lane, pl = idx / C_T, tt = idx % C_T;
    const int t = min(t0 + tt, SL - 1);
    voffH[k] = pl * 32768 + t * 16;
  }
#pragma unroll
  for (int k = 0; k < 2; ++k) {
    const int r = 8 * (2 * (w4 & 1) + k) + (lane >> 3), c = lane & 7;
    const int f = ((r >> 1) & 1) | ((r >> 2) & 2);
    voffW[k] = r * (FF * 2) + 128 * jline + (((c >> 1) ^ f) * 32) + (c & 1) * 16;
  }
  const unsigned ldsW = C_GOFF + (w4 >> 1) * 4096 + (w4 & 1) * 2048;
  auto issue = [&](int step, unsigned so) {
    const char* hb = h8 + (size_t)step * (4 * 32768);
    const char* wb = wbase + (size_t)step * (32 * FF * 2);
#pragma unroll
    for (int k = 0; k < 4; ++k)
      if (k < 3 || NDMA == 6) glds16(hb, voffH[k], lds0 + so + (w4 + 4 * k) * 1024);
#pragma unroll
    for (int k = 0; k < 2; ++k) glds16(wb, voffW[k], lds0 + so + ldsW + k * 1024);
  };
  const int tl = min(60 * w4 + lane, C_T - 1);
  const int c8 = lane & 7, mat = (lane >> 3) & 1, rg = lane >> 4;
  float ssum = 0.f, ssq = 0.f, clo = 0.f, chi = 0.f;
  unsigned k_lo = 0x00003f80u, k_hi = 0x3f800000u, k_both = 0x3f803f80u;
  asm volatile("" : "+s"(k_lo), "+s"(k_hi), "+s"(k_both));
  unsigned csoff[2];
#pragma unroll
  for (int k = 0; k < 2; ++k)
    csoff[k] = C_GOFF + mat * 4096 + (8 * rg) * 128 + ((w4 ^ (k | (2 * (rg & 1)))) * 32) + 4 * c8;
  auto stats = [&](unsigned so) {
    const char* sp = smem + so;
#pragma unroll
    for (int pl = 0; pl < 4; ++pl) {
      u4_t v = *(const u4_t*)(sp + pl * 3840 + tl * 16);
#pragma unroll
      for (int j = 0; j < 4; ++j) { ssum = dot2bf(v[j], k_both, ssum); ssq = dot2bf(v[j], v[j], ssq); }
    }
#pragma unroll
    for (int i = 0; i < 8; ++i) {
      unsigned v = *(const unsigned*)(sp + csoff[(i >> 1) & 1] + i * 128);
      clo = dot2bf(v, k_lo, clo); chi = dot2bf(v, k_hi, chi);
    }
  };
#pragma unroll
  for (int s = 0; s < C_NSLOT; ++s) issue(s, s * C_SLOT);
  wait_vm<5 * NDMA>();
  bar();
  stats(0);
  wait_vm<4 * NDMA>();
  bar();
  stats(C_SLOT);
  unsigned sr = 2 * C_SLOT;
  unsigned sw = 0;
  for (int b = 1; b < 15; ++b) {
    wait_vm<3 * NDMA>();
    bar();
    issue(b + 5, sw);
    stats(sr);
    sr += C_SLOT; if (sr == C_NSLOT * C_SLOT) sr = 0;
    sw += C_SLOT; if (sw == C_NSLOT * C_SLOT) sw = 0;
  }
  wait_vm<3 * NDMA>(); bar(); stats(sr); sr += C_SLOT; if (sr == C_NSLOT * C_SLOT) sr = 0;
  wait_vm<2 * NDMA>(); bar(); stats(sr); sr += C_SLOT; if (sr == C_NSLOT * C_SLOT) sr = 0;
  wait_vm<1 * NDMA>(); bar(); stats(sr); sr += C_SLOT; if (sr == C_NSLOT * C_SLOT) sr = 0;
  wait_vm<0>();        bar(); stats(sr);
  bar();
  {
    const float mean = ssum * (1.0f / DM);
    const float var = fmaxf(ssq * (1.0f / DM) - mean * mean, 0.f);
    const float mb = rbf(mean), sd = rbf(sqrtf(rbf(rbf(var) + 1e-5f)));
    if (lane < 60) *(f2_t*)(smem + C_STAT + (60 * w4 + lane) * 8) = f2_t{mb, 1.0f / sd};
    clo += __shfl_xor(clo, 16); chi += __shfl_xor(chi, 16);
    clo += __shfl_xor(clo, 32); chi += __shfl_xor(chi, 32);
    if (lane < 16) *(f2_t*)(smem + C_CS + mat * 256 + (16 * w4 + 2 * c8) * 4) = f2_t{clo, chi};
  }
  __syncthreads();
}

struct CFrag { b8_t A[2][2]; b8_t B[8]; };

__global__ __launch_bounds__(512, 2) void kC(const u16* __restrict__ wg, const u16* __restrict__ wu, char* __restrict__ ws) {
  extern __shared__ __attribute__((aligned(16))) char smem[];
  const int tid = threadIdx.x, lane = tid & 63;
  const int w = __builtin_amdgcn_readfirstlane(tid >> 6);
  int panel, jline;
  {
    const int xcd = blockIdx.x & 7, rank = blockIdx.x >> 3;
    const int g4 = xcd >> 1, a = xcd & 1, nl = (g4 < 3) ? 7 : 6, total = 9 * nl, n0 = (total + 1) >> 1;
    if (a == 0 ? rank >= n0 : rank >= total - n0) return;
    const int idx = rank + a * n0;
    panel = idx / nl; jline = 7 * g4 + idx % nl;
  }
  const int t0 = panel * C_T;
  if (w >= 4) {
    const int w4 = w - 4;
    const char* wbase = (const char*)((w4 >> 1) ? wu : wg);
    if (w4 < 3) c_loader<6>(smem, ws + OFF_H8, wbase, w4, lane, t0, jline);
    else c_loader<5>(smem, ws + OFF_H8, wbase, w4, lane, t0, jline);
    return;
  }
  const int s = w >> 1, th = w & 1;
  const int fr = lane & 15, kg = lane >> 4, q = (lane & 15) >> 2, p = lane & 3;
  const unsigned hb0 = kg * 3840 + fr * 16 + 256 * 7;
  const unsigned hb1 = kg * 3840 + fr * 16 + 256 * (th ? 8 : 0);
  unsigned wa[2];
#pragma unroll
  for (int v = 0; v < 2; ++v) {
    const int sub = 2 * s + (v ? 1 - th : th);
    const int f = (q >> 1) | (2 * (kg & 1));
    wa[v] = C_GOFF + (8 * kg + q) * 128 + ((sub ^ f) * 32) + 8 * p;
  }
  f4_t ag[8][2], au[8][2];
#pragma unroll
  for (int u = 0; u < 8; ++u)
#pragma unroll
    for (int v = 0; v < 2; ++v) { ag[u][v] = f4_t{0.f, 0.f, 0.f, 0.f}; au[u][v] = f4_t{0.f, 0.f, 0.f, 0.f}; }
  auto load = [&](CFrag& F, unsigned so) {
    const char* sp = smem + so;
#pragma unroll
    for (int v = 0; v < 2; ++v)
#pragma unroll
      for (int m = 0; m < 2; ++m) {
        s4_t lo = tr_read(sp + wa[v] + m * 4096), hi = tr_read(sp + wa[v] + m * 4096 + 512);
        u2_t a = __builtin_bit_cast(u2_t, lo), b = __builtin_bit_cast(u2_t, hi);
        F.A[v][m] = __builtin_bit_cast(b8_t, (u4_t{a.x, a.y, b.x, b.y}));
      }
    F.B[0] = *(const b8_t*)(sp + hb0);
#pragma unroll
    for (int u = 1; u < 8; ++u) F.B[u] = *(const b8_t*)(sp + hb1 + 256 * (u - 1));
  };
  auto mma = [&](const CFrag& F) {
    ag[0][0] = __builtin_amdgcn_mfma_f32_16x16x32_bf16(F.A[0][0], F.B[0], ag[0][0], 0, 0, 0);
    au[0][0] = __builtin_amdgcn_mfma_f32_16x16x32_bf16(F.A[0][1], F.B[0], au[0][0], 0, 0, 0);
#pragma unroll
    for (int u = 1; u < 8; ++u)
#pragma unroll
      for (int v = 0; v < 2; ++v) {
        ag[u][v] = __builtin_amdgcn_mfma_f32_16x16x32_bf16(F.A[v][0], F.B[u], ag[u][v], 0, 0, 0);
        au[u][v] = __builtin_amdgcn_mfma_f32_16x16x32_bf16(F.A[v][1], F.B[u], au[u][v], 0, 0, 0);
      }
  };
  CFrag F0, F1;
  bar();
  load(F0, 0);
  unsigned sn = C_SLOT;
  auto interleave = [&]() {
#pragma unroll
    for (int i = 0; i < 16; ++i) {
      __builtin_amdgcn_sched_group_barrier(0x008, 1, 0);
      __builtin_amdgcn_sched_group_barrier(0x100, 1, 0);
    }
    __builtin_amdgcn_sched_group_barrier(0x008, 14, 0);
  };
#define SB __builtin_amdgcn_sched_barrier(0)
  SB;
#pragma unroll 1
  for (int b = 0; b < C_STEPS - 2; b += 2) {
    bar(); SB;
    load(F1, sn);
    mma(F0);
    interleave();
    SB;
    sn += C_SLOT; if (sn == C_NSLOT * C_SLOT) sn = 0;
    bar(); SB;
    load(F0, sn);
    mma(F1);
    interleave();
    SB;
    sn += C_SLOT; if (sn == C_NSLOT * C_SLOT) sn = 0;
  }
  bar(); SB;
  load(F1, sn);
  mma(F0);
  interleave();
  SB;
  bar(); SB;
  mma(F1);
  SB;
  __syncthreads();
  f4_t csg[2], csu[2];
#pragma unroll
  for (int v = 0; v < 2; ++v) {
    const int sub = 2 * s + (v ? 1 - th : th);
    csg[v] = *(const f4_t*)(smem + C_CS + (16 * sub + 4 * kg) * 4);
    csu[v] = *(const f4_t*)(smem + C_CS + 256 + (16 * sub + 4 * kg) * 4);
  }
  char* hh = ws + OFF_HH + (size_t)(2 * jline + s) * 1024 + lane * 16;
#pragma unroll
  for (int u = 0; u < 8; ++u) {
    const int tile = (u == 0) ? 7 : (th ? 7 + u : u - 1);
    const f2_t st = *(const f2_t*)(smem + C_STAT + (16 * tile + fr) * 8);
    const float m = st.x, r2 = st.y * st.y;
    const int t16 = 15 * panel + tile;
    u2_t o[2];
#pragma unroll
    for (int v = 0; v < 2; ++v) {
      if (u == 0 && v == 1) continue;
      f4_t g = ag[u][v] - m * csg[v], uu = au[u][v] - m * csu[v];
      f4_t r = g * uu * r2;
      o[v] = u2_t{pk_bf16(r[0], r[1]), pk_bf16(r[2], r[3])};
    }
    if (t16 < 128) {
      char* dst = hh + (size_t)t16 * (54 * 1024);
      if (u == 0) *(u2_t*)(dst + 8 * th) = o[0];
      else *(u4_t*)dst = th ? u4_t{o[1].x, o[1].y, o[0].x, o[0].y} : u4_t{o[0].x, o[0].y, o[1].x, o[1].y};
    }
  }
}

constexpr int D_SLOT = 27648, D_NSLOT = 5, D_HOFF = 15360, D_STEPS = 18;
constexpr int D_LDS = D_NSLOT * D_SLOT;

template <int NDMA>
__device__ __forceinline__ void d_loader(char* smem, const char* wp, const char* hh, int w4, int lane) {
  const unsigned lds0 = (unsigned)(size_t)(LAS char*)smem;
  const unsigned voff = lane * 16;
  auto issue = [&](int step, unsigned so) {
#pragma unroll
    for (int k = 0; k < NDMA; ++k) {
      const int i = w4 + 4 * k;
      const char* src = (i < 15) ? wp + (size_t)step * 15360 + i * 1024
                                 : hh + (size_t)((i - 15) / 3) * (54 * 1024) + (size_t)(3 * step + (i - 15) % 3) * 1024;
      glds16(src, voff, lds0 + so + i * 1024);
    }
  };
#pragma unroll
  for (int s = 0; s < D_NSLOT; ++s) issue(s, s * D_SLOT);
  wait_vm<4 * NDMA>();
  bar();
  unsigned sw = 0;
  for (int b = 1; b < 14; ++b) {
    wait_vm<3 * NDMA>();
    bar();
    issue(b + 4, sw);
    sw += D_SLOT; if (sw == D_NSLOT * D_SLOT) sw = 0;
  }
  wait_vm<3 * NDMA>(); bar();
  wait_vm<2 * NDMA>(); bar();
  wait_vm<1 * NDMA>(); bar();
  wait_vm<0>(); bar();
}

__global__ __launch_bounds__(512, 2) void kD(char* __restrict__ ws, u16* __restrict__ out) {
  extern __shared__ __attribute__((aligned(16))) char smem[];
  const int tid = threadIdx.x, lane = tid & 63;
  const int w = __builtin_amdgcn_readfirstlane(tid >> 6);
  const int xcd = blockIdx.x & 7, rank = blockIdx.x >> 3;
  const int it = 8 * (xcd >> 1) + (rank & 7), dg = 4 * (xcd & 1) + (rank >> 3);
  if (w >= 4) {
    const int w4 = w - 4;
    const char* wp = ws + OFF_WP2 + (size_t)dg * (54 * 5120);
    const char* hh = ws + OFF_HH + (size_t)(4 * it) * (54 * 1024);
    if (w4 < 3) d_loader<7>(smem, wp, hh, w4, lane);
    else d_loader<6>(smem, wp, hh, w4, lane);
    return;
  }
  const int fr = lane & 15, g = lane >> 4;
  const int t = 64 * it + 16 * w + fr;
  u2_t res[5];
#pragma unroll
  for (int dt = 0; dt < 5; ++dt) {
    const int d = 80 * dg + 16 * dt + 4 * g;
    res[dt] = *(const u2_t*)(ws + OFF_H8 + ((size_t)(d >> 3) * SL + t) * 16 + (d & 4) * 2);
  }
  f4_t acc[5];
#pragma unroll
  for (int dt = 0; dt < 5; ++dt) acc[dt] = f4_t{0.f, 0.f, 0.f, 0.f};
  unsigned so = 0;
#pragma unroll 1
  for (int b = 0; b < D_STEPS; ++b) {
    bar();
    const char* sp = smem + so + lane * 16;
#pragma unroll
    for (int kk = 0; kk < 3; ++kk) {
      b8_t B = *(const b8_t*)(sp + D_HOFF + (w * 3 + kk) * 1024);
#pragma unroll
      for (int dt = 0; dt < 5; ++dt) {
        b8_t A = *(const b8_t*)(sp + (kk * 5 + dt) * 1024);
        acc[dt] = __builtin_amdgcn_mfma_f32_16x16x32_bf16(A, B, acc[dt], 0, 0, 0);
      }
    }
    asm volatile("s_waitcnt lgkmcnt(0)" ::: "memory");
    so += D_SLOT; if (so == D_NSLOT * D_SLOT) so = 0;
  }
#pragma unroll
  for (int dt = 0; dt < 5; ++dt) {
    const int d = 80 * dg + 16 * dt + 4 * g;
    const float h0 = bf_lo(res[dt].x), h1 = bf_hi(res[dt].x), h2 = bf_lo(res[dt].y), h3 = bf_hi(res[dt].y);
    *(u2_t*)(out + (size_t)t * DM + d) = u2_t{pk_bf16(h0 + acc[dt][0], h1 + acc[dt][1]), pk_bf16(h2 + acc[dt][2], h3 + acc[dt][3])};
  }
}

extern "C" void kernel_launch(void* const* d_in, const int* in_sizes, int n_in, void* d_out, int out_size, void* d_ws,
                              size_t ws_size, hipStream_t stream) {
  const u16* x = (const u16*)d_in[0];
  char* ws = (char*)d_ws;
  kA<<<A_XBLK + A_KBLK, 256, A_LDS, stream>>>(x, (const u16*)d_in[1], (const u16*)d_in[2], (const u16*)d_in[3], (const u16*)d_in[4],
                                              (const u16*)d_in[5], (const u16*)d_in[6], (const u16*)d_in[7], ws);
  kB<<<256, 512, B_LDS, stream>>>((const u16*)d_in[8], (const u16*)d_in[12], ws);
  (void)hipFuncSetAttribute((const void*)kC, hipFuncAttributeMaxDynamicSharedMemorySize, C_LDS);
  kC<<<256, 512, C_LDS, stream>>>((const u16*)d_in[10], (const u16*)d_in[11], ws);
  (void)hipFuncSetAttribute((const void*)kD, hipFuncAttributeMaxDynamicSharedMemorySize, D_LDS);
  kD<<<256, 512, D_LDS, stream>>>(ws, (u16*)d_out);
}
```

```cpp
#include <hip/hip_runtime.h>
#include <hip/hip_bf16.h>
#include <stdint.h>

typedef unsigned short u16;
typedef _Float16 h2_t __attribute__((ext_vector_type(2)));
typedef _Float16 h8_t __attribute__((ext_vector_type(8)));
typedef __bf16 b2_t __attribute__((ext_vector_type(2)));
typedef __bf16 b8_t __attribute__((ext_vector_type(8)));
typedef float f2_t __attribute__((ext_vector_type(2)));
typedef float f4_t __attribute__((ext_vector_type(4)));
typedef unsigned u2_t __attribute__((ext_vector_type(2)));
typedef unsigned u4_t __attribute__((ext_vector_type(4)));
typedef short s4_t __attribute__((ext_vector_type(4)));
#define LAS __attribute__((address_space(3)))

constexpr int SL = 2048;
constexpr int DM = 640;
constexpr int FF = 1728;

constexpr size_t OFF_X4 = 0;
constexpr size_t OFF_S1 = 0x280000;
constexpr size_t OFF_KD = 0x290000;
constexpr size_t OFF_INVS = 0x590000;
constexpr size_t OFF_H8 = 0x5A0000;
constexpr size_t OFF_HH = 0x820000;
constexpr size_t OFF_WP2 = 0xF00000;
constexpr int KDS = 4864;
constexpr int KD_O = 2240;
constexpr int KD_DR = 4384;
constexpr int P0 = 1056;

#define STAMP(k, i)

__device__ __forceinline__ float bf_lo(unsigned d) { return __uint_as_float(d << 16); }
__device__ __forceinline__ float bf_hi(unsigned d) { return __uint_as_float(d & 0xffff0000u); }
__device__ __forceinline__ float bf2f(u16 v) { return __uint_as_float((unsigned)v << 16); }
__device__ __forceinline__ unsigned pk_bf16(float a, float b) {
  f2_t v = {a, b};
  return __builtin_bit_cast(unsigned, __builtin_convertvector(v, b2_t));
}
__device__ __forceinline__ unsigned pk_f16(float a, float b) {
  f2_t v = {a, b};
  return __builtin_bit_cast(unsigned, __builtin_convertvector(v, h2_t));
}
__device__ __forceinline__ float rbf(float a) { return bf_lo(pk_bf16(a, 0.f)); }
template <int N> __device__ __forceinline__ float dpp_ror_add(float v) {
  int r = __builtin_amdgcn_update_dpp(0, __float_as_int(v), 0x120 + N, 0xf, 0xf, false);
  return v + __int_as_float(r);
}
__device__ __forceinline__ float row16_sum(float v) {
  v = dpp_ror_add<8>(v); v = dpp_ror_add<4>(v); v = dpp_ror_add<2>(v); v = dpp_ror_add<1>(v);
  return v;
}
__device__ __forceinline__ void wave_fence() {
  __builtin_amdgcn_fence(__ATOMIC_RELEASE, "wavefront");
  __builtin_amdgcn_wave_barrier();
  __builtin_amdgcn_fence(__ATOMIC_ACQUIRE, "wavefront");
}
__device__ __forceinline__ s4_t tr_read(const char* p) {
  return __builtin_amdgcn_ds_read_tr16_b64_v4i16((LAS s4_t*)p);
}

struct Cw { f2_t w, ws; };
__device__ __forceinline__ Cw mkw(f2_t w) { Cw r; r.w = w; r.ws = f2_t{-w.y, w.x}; return r; }
__device__ __forceinline__ f2_t cmulw(f2_t a, const Cw& W) {
  return __builtin_elementwise_fma(f2_t{a.y, a.y}, W.ws, f2_t{a.x, a.x} * W.w);
}
__device__ __forceinline__ f2_t cexp_l(float er, float wr, float l) {
  float mag = __builtin_amdgcn_exp2f(er * l);
  float rev = __builtin_amdgcn_fractf(wr * l);
  return f2_t{mag * __builtin_amdgcn_cosf(rev), mag * __builtin_amdgcn_sinf(rev)};
}
__device__ __forceinline__ float pow2_scale(float bound, int target) {
  unsigned e = (__float_as_uint(bound) >> 23) & 0xff;
  int k = target - ((int)e - 126);
  k = k > 126 ? 126 : (k < -126 ? -126 : k);
  return (bound > 0.f) ? __uint_as_float((unsigned)(k + 127) << 23) : 1.0f;
}
__device__ __forceinline__ int lrow(int rho) {
  if (rho < 16) return 128 * (rho >> 3) + (rho & 7);
  if (rho < 24) return 128 * (2 + ((rho - 16) >> 2)) + 2 * ((rho - 16) & 3);
  if (rho < 32) return 128 * (4 + ((rho - 24) >> 1)) + 4 * ((rho - 24) & 1);
  return 128 * (8 + (rho - 32));
}
__device__ __forceinline__ int rnd_lag(int l) {
  unsigned u = __float_as_uint((float)l);
  u = (u + 0x7fffu + ((u >> 16) & 1u)) & 0xffff0000u;
  return (int)__uint_as_float(u);
}

constexpr int A_XBLK = 128, A_KBLK = 320;
constexpr int KG_WBUF = 2 * 16 * 272;
constexpr int KG_CH = 2 * KG_WBUF + 3072 + 16 + 4352;
constexpr int A_LDS = 2 * KG_CH;

__global__ __launch_bounds__(256) void kA(const u16* __restrict__ x, const u16* __restrict__ Are, const u16* __restrict__ Aim,
                                          const u16* __restrict__ Bre, const u16* __restrict__ Bim, const u16* __restrict__ Cre,
                                          const u16* __restrict__ Cim, const u16* __restrict__ logdt, char* __restrict__ ws) {
  extern __shared__ __attribute__((aligned(16))) char smem[];
  const int tid = threadIdx.x, lane = tid & 63, w = tid >> 6;
  if (w == 0) STAMP(0, 0);
  if (blockIdx.x < A_XBLK) {
    const int t0 = blockIdx.x * 16;
    const int r = lane >> 4, c = lane & 15, rl = w * 4 + r;
    const u4_t* p = (const u4_t*)(x + (size_t)(t0 + rl) * DM);
    u4_t v[5];
#pragma unroll
    for (int i = 0; i < 5; ++i) v[i] = p[c + 16 * i];
    float s = 0.f;
#pragma unroll
    for (int i = 0; i < 5; ++i)
#pragma unroll
      for (int j = 0; j < 4; ++j) s += bf_lo(v[i][j]) + bf_hi(v[i][j]);
    const float mean = row16_sum(s) * (1.0f / DM);
    if (w == 0) STAMP(0, 1);
    float q = 0.f;
#pragma unroll
    for (int i = 0; i < 5; ++i)
#pragma unroll
      for (int j = 0; j < 4; ++j) {
        float a = bf_lo(v[i][j]) - mean, b = bf_hi(v[i][j]) - mean;
        q += a * a; q += b * b;
      }
    const float var = row16_sum(q) * (1.0f / DM);
    if (c == 0) {
      float mb = rbf(mean), sd = rbf(sqrtf(rbf(rbf(var) + 1e-5f)));
      ((unsigned*)(ws + OFF_S1))[t0 + rl] = (__float_as_uint(mb) >> 16) | (__float_as_uint(sd) & 0xffff0000u);
    }
#pragma unroll
    for (int i = 0; i < 5; ++i) *(u4_t*)(smem + rl * 1296 + (c + 16 * i) * 16) = v[i];
    __syncthreads();
#pragma unroll
    for (int it = 0; it < 5; ++it) {
      const int item = tid + 256 * it, q4 = item >> 3, tp = item & 7;
      u2_t a = *(const u2_t*)(smem + (2 * tp) * 1296 + 8 * q4);
      u2_t b = *(const u2_t*)(smem + (2 * tp + 1) * 1296 + 8 * q4);
      *(u4_t*)(ws + OFF_X4 + ((size_t)q4 * SL + t0 + 2 * tp) * 8) = u4_t{a.x, a.y, b.x, b.y};
    }
    if (w == 0) STAMP(0, 7);
    return;
  }
  const int cl = w >> 1, hw = w & 1;
  const int ch = (blockIdx.x - A_XBLK) * 2 + cl;
  char* chb = smem + cl * KG_CH;
  char* lbuf = chb + hw * KG_WBUF;
  char* rbuf = lbuf + 16 * 272;
  float* xch = (float*)(chb + 2 * KG_WBUF);
  u16* tab = (u16*)(chb + 2 * KG_WBUF + 3072 + 16);
  const int n = ch * 128 + hw * 64 + lane;
  const float dt = rbf(expf(bf2f(logdt[ch])));
  const float are = dt * bf2f(Are[n]), aim = dt * bf2f(Aim[n]);
  const float bre = dt * bf2f(Bre[n]), bim = dt * bf2f(Bim[n]);
  const float cre = bf2f(Cre[n]), cim = bf2f(Cim[n]);
  f2_t c = {cre * bre - cim * bim, cre * bim + cim * bre};
  const float er = are * 1.44269504088896341f, wr = aim * 0.15915494309189535f;
  float bnd = (fabsf(c.x) + fabsf(c.y)) * fmaxf(1.0f, __builtin_amdgcn_exp2f(er * 2048.f));
  if (w == 0) STAMP(0, 1);
  float bmax = bnd, bsum = bnd;
#pragma unroll
  for (int o = 1; o < 64; o <<= 1) { bmax = fmaxf(bmax, __shfl_xor(bmax, o)); bsum += __shfl_xor(bsum, o); }
  const float Sw = pow2_scale(bmax, 14);
  c *= Sw;
  const Cw z1 = mkw(cexp_l(er, wr, 1.f)), w8 = mkw(cexp_l(er, wr, 8.f)), w128 = mkw(cexp_l(er, wr, 128.f));
  const Cw z2 = mkw(cmulw(z1.w, z1));
  const Cw z4 = mkw(cmulw(z2.w, z2));
  {
    f2_t R = {1.f, 0.f};
#pragma unroll
    for (int i = 0; i < 16; ++i) {
      *(unsigned*)(rbuf + i * 272 + 4 * lane) = pk_f16(R.x, R.y);
      if (i < 15) R = (i == 0) ? w8.w : cmulw(R, w8);
    }
  }
  f2_t T1[17];
  T1[0] = c;
#pragma unroll
  for (int i = 1; i < 17; ++i) T1[i] = cmulw(T1[i - 1], w128);
  const int fr = lane & 15, kg = lane >> 4;
  h8_t Bf[4];
  f4_t acc[3];
  auto put = [&](int row, f2_t v) { *(unsigned*)(lbuf + row * 272 + 4 * lane) = pk_f16(v.x, -v.y); };
  auto mm = [&](int tile) {
    wave_fence();
    if (tile == 0) {
#pragma unroll
      for (int ks = 0; ks < 4; ++ks) Bf[ks] = *(const h8_t*)(rbuf + fr * 272 + 64 * ks + 16 * kg);
    }
    f4_t a = {0.f, 0.f, 0.f, 0.f};
#pragma unroll
    for (int ks = 0; ks < 4; ++ks) {
      h8_t Af = *(const h8_t*)(lbuf + fr * 272 + 64 * ks + 16 * kg);
      a = __builtin_amdgcn_mfma_f32_16x16x32_f16(Af, Bf[ks], a, 0, 0, 0);
    }
    acc[tile] = a;
    wave_fence();
  };
#pragma unroll
  for (int i1 = 0; i1 < 2; ++i1) {
    f2_t v = T1[i1];
#pragma unroll
    for (int i0 = 0; i0 < 8; ++i0) { put(8 * i1 + i0, v); if (i0 < 7) v = cmulw(v, z1); }
  }
  mm(0);
#pragma unroll
  for (int i1 = 2; i1 < 4; ++i1) {
    f2_t v = T1[i1];
#pragma unroll
    for (int i0 = 0; i0 < 4; ++i0) { put(4 * (i1 - 2) + i0, v); if (i0 < 3) v = cmulw(v, z2); }
  }
#pragma unroll
  for (int i1 = 4; i1 < 8; ++i1) { put(8 + 2 * (i1 - 4), T1[i1]); put(8 + 2 * (i1 - 4) + 1, cmulw(T1[i1], z4)); }
  mm(1);
#pragma unroll
  for (int i1 = 8; i1 < 17; ++i1) put(i1 - 8, T1[i1]);
  mm(2);
  if (w == 0) STAMP(0, 2);
  const float invSw = 1.0f / Sw;
#pragma unroll
  for (int t = 0; t < 3; ++t) acc[t] *= invSw;
  if (hw == 1) {
#pragma unroll
    for (int t = 0; t < 3; ++t) *(f4_t*)(xch + (t * 64 + lane) * 4) = acc[t];
    if (lane == 0) xch[768] = bsum;
  }
  __syncthreads();
  if (hw == 0) {
    const float Sc = pow2_scale(bsum + xch[768], 14);
#pragma unroll
    for (int t = 0; t < 3; ++t) {
      f4_t o = *(const f4_t*)(xch + (t * 64 + lane) * 4);
#pragma unroll
      for (int i = 0; i < 4; ++i) {
        const int rho = 16 * t + 4 * kg + i;
        const int l = lrow(rho) + 8 * fr;
        if (rho <= 40 && l <= 2048) tab[l] = (u16)(pk_f16((acc[t][i] + o[i]) * Sc, 0.f) & 0xffff);
      }
    }
    if (lane == 0) ((float*)(ws + OFF_INVS))[ch] = 1.0f / Sc;
  }
  __syncthreads();
  if (w == 0) STAMP(0, 3);
  char* kd = ws + OFF_KD + (size_t)ch * KDS;
  const int j = hw * 64 + lane;
#pragma unroll
  for (int it = 0; it < 5; ++it) {
    const int m = j + 128 * it;
    if (m < 536) {
      const int lA = P0 - 2 * m;
      unsigned k3[3];
#pragma unroll
      for (int e = 0; e < 3; ++e) {
        const int l = lA - e;
        k3[e] = (l >= 0 && l <= 1028) ? (unsigned)tab[rnd_lag(l < 0 ? 0 : l)] : 0u;
      }
      ((unsigned*)kd)[m] = k3[0] | (k3[1] << 16);
      ((unsigned*)(kd + KD_O))[m] = k3[1] | (k3[2] << 16);
    }
  }
  if (j < 80) {
    const int dl = 127 - j;
    unsigned v = 0;
    if (dl >= 64) v = (unsigned)tab[8 * (2 * dl + 1)] | ((unsigned)tab[8 * (2 * dl + 2)] << 16);
    ((unsigned*)(kd + KD_DR))[j] = v;
  }
  if (w == 0) STAMP(0, 7);
}

constexpr int B_CONV = 160;
constexpr int B_KOFF = 0;
constexpr int B_UOFF = 4 * KDS;
constexpr int B_UCH = 4608;
constexpr int B_YCH = 10240;
constexpr int B_LDS = 40960;

__global__ __launch_bounds__(512) void kB(const u16* __restrict__ dskip, const u16* __restrict__ wd, char* __restrict__ ws) {
  extern __shared__ __attribute__((aligned(16))) char smem[];
  const int tid = threadIdx.x, lane = tid & 63;
  const int w = __builtin_amdgcn_readfirstlane(tid >> 6);
  if (w == 0) STAMP(1, 0);
  if (blockIdx.x >= B_CONV) {
    const int u = (blockIdx.x - B_CONV) * 8 + w;
    if (u >= 432) return;
    const int ks = u >> 3, dg = u & 7;
    char* buf = smem + w * 5120;
#pragma unroll
    for (int i = 0; i < 5; ++i) {
      const int idx = i * 64 + lane, row = idx / 10, c = idx % 10;
      __builtin_amdgcn_global_load_lds((const unsigned*)((const char*)wd + (size_t)(32 * ks + row) * 1280 + 160 * dg + 16 * c),
                                       (LAS unsigned*)(buf + i * 1024), 16, 0, 0);
    }
    asm volatile("s_waitcnt vmcnt(0)" ::: "memory");
    wave_fence();
    const int g = lane >> 4, q = (lane & 15) >> 2, p = lane & 3;
    char* dst = ws + OFF_WP2 + ((size_t)(dg * 54 + ks) * 5) * 1024 + lane * 16;
#pragma unroll
    for (int dt = 0; dt < 5; ++dt) {
      s4_t a = tr_read(buf + (4 * g + q) * 160 + 32 * dt + 8 * p);
      s4_t b = tr_read(buf + (16 + 4 * g + q) * 160 + 32 * dt + 8 * p);
      u2_t ua = __builtin_bit_cast(u2_t, a), ub = __builtin_bit_cast(u2_t, b);
      *(u4_t*)(dst + dt * 1024) = u4_t{ua.x, ua.y, ub.x, ub.y};
    }
    if (w == 0) STAMP(1, 7);
    return;
  }
  const int a_ = blockIdx.x >> 3, x_ = blockIdx.x & 7;
  const int q = 16 * (a_ >> 1) + 2 * x_ + (a_ & 1);
  for (int i = w; i < 19; i += 8)
    __builtin_amdgcn_global_load_lds((const unsigned*)(ws + OFF_KD + (size_t)q * (4 * KDS) + i * 1024 + lane * 16),
                                     (LAS unsigned*)(smem + B_KOFF + i * 1024), 16, 0, 0);
  u4_t xv[2]; u2_t sv[2];
#pragma unroll
  for (int it = 0; it < 2; ++it) {
    const int j = tid + 512 * it;
    xv[it] = *(const u4_t*)(ws + OFF_X4 + ((size_t)q * SL + 2 * j) * 8);
    sv[it] = *(const u2_t*)(ws + OFF_S1 + 8 * j);
  }
  float dk[4], is[4];
#pragma unroll
  for (int c = 0; c < 4; ++c) { dk[c] = bf2f(dskip[4 * q + c]); is[c] = ((const float*)(ws + OFF_INVS))[4 * q + c]; }
  *(unsigned*)(smem + B_UOFF + (tid >> 7) * B_UCH + 4 * (tid & 127)) = 0u;
  float z[2][8];
  if (w == 0) { asm volatile("s_waitcnt vmcnt(0)" ::: "memory"); STAMP(1, 1); }
#pragma unroll
  for (int it = 0; it < 2; ++it) {
    const int j = tid + 512 * it;
    const float m0 = bf_lo(sv[it].x), r0 = 1.0f / bf_hi(sv[it].x), m1 = bf_lo(sv[it].y), r1 = 1.0f / bf_hi(sv[it].y);
    float x0[4] = {bf_lo(xv[it].x), bf_hi(xv[it].x), bf_lo(xv[it].y), bf_hi(xv[it].y)};
    float x1[4] = {bf_lo(xv[it].z), bf_hi(xv[it].z), bf_lo(xv[it].w), bf_hi(xv[it].w)};
#pragma unroll
    for (int c = 0; c < 4; ++c) {
      const float u0 = (x0[c] - m0) * r0, u1 = (x1[c] - m1) * r1;
      *(unsigned*)(smem + B_UOFF + c * B_UCH + 512 + 4 * j) = pk_f16(u0, u1);
      z[it][c] = x0[c] + u0 * dk[c];
      z[it][4 + c] = x1[c] + u1 * dk[c];
    }
  }
  __syncthreads();
  if (w == 0) STAMP(1, 2);
  f4_t acc[8];
  if (w < 4) {
    const int c = lane & 15, kg = lane >> 4;
#pragma unroll
    for (int t = 0; t < 8; ++t) acc[t] = f4_t{0.f, 0.f, 0.f, 0.f};
    const char* ub = smem + B_UOFF + w * B_UCH + 2 * (256 + 16 * c - 16 + 8 * kg) - 64 * 7;
    const char* kb = smem + B_KOFF + w * KDS + (c & 1) * KD_O + 4 * (520 - ((c + 1) >> 1) + 4 * kg) - 64 * 7;
#pragma unroll
    for (int i0 = 0; i0 < 8; ++i0) {
      h8_t Bf[8];
#pragma unroll
      for (int p = 0; p < 8; ++p) Bf[p] = *(const h8_t*)(ub + 512 * p + 64 * (7 - i0));
#pragma unroll
      for (int i1 = 0; i1 < 5; ++i1) {
        if (i1 == 4 && i0 != 0) continue;
        const unsigned* ap = (const unsigned*)(kb - 512 * i1 + 64 * (7 - i0));
        u4_t au = {ap[0], ap[1], ap[2], ap[3]};
        h8_t Af = __builtin_bit_cast(h8_t, au);
#pragma unroll
        for (int t = i1; t < 8; ++t) acc[t] = __builtin_amdgcn_mfma_f32_16x16x32_f16(Af, Bf[t - i1], acc[t], 0, 0, 0);
      }
    }
    h8_t band9, band7;
#pragma unroll
    for (int j = 0; j < 8; ++j) {
      const int k = 8 * kg + j;
      band9[j] = (k >= c + 4 && k <= c + 12) ? (_Float16)1.0f : (_Float16)0.0f;
      band7[j] = (k >= c + 5 && k <= c + 11) ? (_Float16)1.0f : (_Float16)0.0f;
    }
    h8_t Tf[4];
    const unsigned* dr = (const unsigned*)(smem + B_KOFF + w * KDS + KD_DR) + (127 - c + 4 * kg);
#pragma unroll
    for (int dl = 0; dl < 4; ++dl) {
      const unsigned* tp = dr - 16 * (dl + 4);
      Tf[dl] = __builtin_bit_cast(h8_t, (u4_t{tp[0], tp[1], tp[2], tp[3]}));
    }
    const char* uw = smem + B_UOFF + w * B_UCH + 2 * (256 + 16 * c + 8 * kg);
#pragma unroll
    for (int bt = 0; bt < 4; ++bt) {
      h8_t U9 = *(const h8_t*)(uw + 512 * bt - 48);
      h8_t U7 = *(const h8_t*)(uw + 512 * bt - 32);
      f4_t zz = {0.f, 0.f, 0.f, 0.f};
      f4_t X9 = __builtin_amdgcn_mfma_f32_16x16x32_f16(U9, band9, zz, 0, 0, 0);
      f4_t X7 = __builtin_amdgcn_mfma_f32_16x16x32_f16(U7, band7, zz, 0, 0, 0);
      u4_t vu = {pk_f16(X7[0], X9[0]), pk_f16(X7[1], X9[1]), pk_f16(X7[2], X9[2]), pk_f16(X7[3], X9[3])};
      h8_t Vf = __builtin_bit_cast(h8_t, vu);
#pragma unroll
      for (int at = 4 + bt; at < 8; ++at) acc[at] = __builtin_amdgcn_mfma_f32_16x16x32_f16(Vf, Tf[at - bt - 4], acc[at], 0, 0, 0);
    }
  }
  if (w == 0) STAMP(1, 3);
  __syncthreads();
  if (w < 4) {
    const float s = (w == 0) ? is[0] : (w == 1) ? is[1] : (w == 2) ? is[2] : is[3];
    const int c = lane & 15, g = lane >> 4;
#pragma unroll
    for (int t = 0; t < 8; ++t) *(f4_t*)(smem + w * B_YCH + t * 1280 + g * 320 + c * 16) = acc[t] * s;
  }
  __syncthreads();
#pragma unroll
  for (int it = 0; it < 2; ++it) {
    const int j = tid + 512 * it, t = 2 * j;
    const int yo = (t >> 8) * 1280 + ((t >> 2) & 3) * 320 + ((t >> 4) & 15) * 16 + (t & 3) * 4;
    float h0[4], h1[4];
#pragma unroll
    for (int c = 0; c < 4; ++c) {
      f2_t y = *(const f2_t*)(smem + c * B_YCH + yo);
      h0[c] = z[it][c] + y.x;
      h1[c] = z[it][4 + c] + y.y;
    }
    char* dst = ws + OFF_H8 + ((size_t)(q >> 1) * SL + t) * 16 + (q & 1) * 8;
    *(u2_t*)dst = u2_t{pk_bf16(h0[0], h0[1]), pk_bf16(h0[2], h0[3])};
    *(u2_t*)(dst + 16) = u2_t{pk_bf16(h1[0], h1[1]), pk_bf16(h1[2], h1[3])};
  }
  if (w == 0) STAMP(1, 7);
}

__device__ __forceinline__ void glds16(const void* sbase, unsigned voff, unsigned lds_addr) {
  asm volatile("s_mov_b32 m0, %2\n\ts_nop 0\n\tglobal_load_lds_dwordx4 %0, %1" ::"v"(voff), "s"(sbase), "s"(lds_addr) : "memory");
}
template <int N> __device__ __forceinline__ void wait_vm() { asm volatile("s_waitcnt vmcnt(%0)" ::"i"(N) : "memory"); }
__device__ __forceinline__ void bar() { asm volatile("s_barrier" ::: "memory"); }
__device__ __forceinline__ float dot2bf(unsigned a, unsigned b, float c) {
  return __builtin_amdgcn_fdot2_f32_bf16(__builtin_bit_cast(b2_t, a), __builtin_bit_cast(b2_t, b), c, false);
}

constexpr int C_T = 240;
constexpr int C_SLOT = 23552, C_NSLOT = 6, C_GOFF = 15360;
constexpr int C_STAT = C_NSLOT * C_SLOT;
constexpr int C_CS = C_STAT + 1920;
constexpr int C_LDS = C_CS + 512;
constexpr int C_STEPS = 20;

template <int NDMA>
__device__ __forceinline__ void c_loader(char* smem, const char* h8, const char* wbase, int w4, int lane, int t0, int jline) {
  const unsigned lds0 = (unsigned)(size_t)(LAS char*)smem;
  unsigned voffH[4], voffW[2];
#pragma unroll
  for (int k = 0; k < 4; ++k) {
    const int idx = 64 * (w4 + 4 * k) + lane, pl = idx / C_T, tt = idx % C_T;
    const int t = min(t0 + tt, SL - 1);
    voffH[k] = pl * 32768 + t * 16;
  }
#pragma unroll
  for (int k = 0; k < 2; ++k) {
    const int r = 8 * (2 * (w4 & 1) + k) + (lane >> 3), c = lane & 7;
    const int f = ((r >> 1) & 1) | ((r >> 2) & 2);
    voffW[k] = r * (FF * 2) + 128 * jline + (((c >> 1) ^ f) * 32) + (c & 1) * 16;
  }
  const unsigned ldsW = C_GOFF + (w4 >> 1) * 4096 + (w4 & 1) * 2048;
  auto issue = [&](int step, unsigned so) {
    const char* hb = h8 + (size_t)step * (4 * 32768);
    const char* wb = wbase + (size_t)step * (32 * FF * 2);
#pragma unroll
    for (int k = 0; k < 4; ++k)
      if (k < 3 || NDMA == 6) glds16(hb, voffH[k], lds0 + so + (w4 + 4 * k) * 1024);
#pragma unroll
    for (int k = 0; k < 2; ++k) glds16(wb, voffW[k], lds0 + so + ldsW + k * 1024);
  };
  const int tl = min(60 * w4 + lane, C_T - 1);
  const int c8 = lane & 7, mat = (lane >> 3) & 1, rg = lane >> 4;
  float ssum = 0.f, ssq = 0.f, clo = 0.f, chi = 0.f;
  unsigned k_lo = 0x00003f80u, k_hi = 0x3f800000u, k_both = 0x3f803f80u;
  asm volatile("" : "+s"(k_lo), "+s"(k_hi), "+s"(k_both));
  unsigned csoff[2];
#pragma unroll
  for (int k = 0; k < 2; ++k)
    csoff[k] = C_GOFF + mat * 4096 + (8 * rg) * 128 + ((w4 ^ (k | (2 * (rg & 1)))) * 32) + 4 * c8;
  auto stats = [&](unsigned so) {
    const char* sp = smem + so;
#pragma unroll
    for (int pl = 0; pl < 4; ++pl) {
      u4_t v = *(const u4_t*)(sp + pl * 3840 + tl * 16);
#pragma unroll
      for (int j = 0; j < 4; ++j) { ssum = dot2bf(v[j], k_both, ssum); ssq = dot2bf(v[j], v[j], ssq); }
    }
#pragma unroll
    for (int i = 0; i < 8; ++i) {
      unsigned v = *(const unsigned*)(sp + csoff[(i >> 1) & 1] + i * 128);
      clo = dot2bf(v, k_lo, clo); chi = dot2bf(v, k_hi, chi);
    }
  };
#pragma unroll
  for (int s = 0; s < C_NSLOT; ++s) issue(s, s * C_SLOT);
  wait_vm<5 * NDMA>();
  bar();
  stats(0);
  wait_vm<4 * NDMA>();
  bar();
  stats(C_SLOT);
  unsigned sr = 2 * C_SLOT;
  unsigned sw = 0;
  for (int b = 1; b < 15; ++b) {
    wait_vm<3 * NDMA>();
    bar();
    issue(b + 5, sw);
    stats(sr);
    sr += C_SLOT; if (sr == C_NSLOT * C_SLOT) sr = 0;
    sw += C_SLOT; if (sw == C_NSLOT * C_SLOT) sw = 0;
  }
  wait_vm<3 * NDMA>(); bar(); stats(sr); sr += C_SLOT; if (sr == C_NSLOT * C_SLOT) sr = 0;
  wait_vm<2 * NDMA>(); bar(); stats(sr); sr += C_SLOT; if (sr == C_NSLOT * C_SLOT) sr = 0;
  wait_vm<1 * NDMA>(); bar(); stats(sr); sr += C_SLOT; if (sr == C_NSLOT * C_SLOT) sr = 0;
  wait_vm<0>();        bar(); stats(sr);
  bar();
  {
    const float mean = ssum * (1.0f / DM);
    const float var = fmaxf(ssq * (1.0f / DM) - mean * mean, 0.f);
    const float mb = rbf(mean), sd = rbf(sqrtf(rbf(rbf(var) + 1e-5f)));
    if (lane < 60) *(f2_t*)(smem + C_STAT + (60 * w4 + lane) * 8) = f2_t{mb, 1.0f / sd};
    clo += __shfl_xor(clo, 16); chi += __shfl_xor(chi, 16);
    clo += __shfl_xor(clo, 32); chi += __shfl_xor(chi, 32);
    if (lane < 16) *(f2_t*)(smem + C_CS + mat * 256 + (16 * w4 + 2 * c8) * 4) = f2_t{clo, chi};
  }
  __syncthreads();
}

struct CFrag { b8_t A[2][2]; b8_t B[8]; };

__global__ __launch_bounds__(512, 2) void kC(const u16* __restrict__ wg, const u16* __restrict__ wu, char* __restrict__ ws) {
  extern __shared__ __attribute__((aligned(16))) char smem[];
  const int tid = threadIdx.x, lane = tid & 63;
  const int w = __builtin_amdgcn_readfirstlane(tid >> 6);
  int panel, jline;
  {
    const int xcd = blockIdx.x & 7, rank = blockIdx.x >> 3;
    const int g4 = xcd >> 1, a = xcd & 1, nl = (g4 < 3) ? 7 : 6, total = 9 * nl, n0 = (total + 1) >> 1;
    if (a == 0 ? rank >= n0 : rank >= total - n0) return;
    const int idx = rank + a * n0;
    panel = idx / nl; jline = 7 * g4 + idx % nl;
  }
  const int t0 = panel * C_T;
  if (w == 0) STAMP(2, 0);
  if (w >= 4) {
    const int w4 = w - 4;
    const char* wbase = (const char*)((w4 >> 1) ? wu : wg);
    if (w4 < 3) c_loader<6>(smem, ws + OFF_H8, wbase, w4, lane, t0, jline);
    else c_loader<5>(smem, ws + OFF_H8, wbase, w4, lane, t0, jline);
    return;
  }
  const int s = w >> 1, th = w & 1;
  const int fr = lane & 15, kg = lane >> 4, q = (lane & 15) >> 2, p = lane & 3;
  const unsigned hb0 = kg * 3840 + fr * 16 + 256 * 7;
  const unsigned hb1 = kg * 3840 + fr * 16 + 256 * (th ? 8 : 0);
  unsigned wa[2];
#pragma unroll
  for (int v = 0; v < 2; ++v) {
    const int sub = 2 * s + (v ? 1 - th : th);
    const int f = (q >> 1) | (2 * (kg & 1));
    wa[v] = C_GOFF + (8 * kg + q) * 128 + ((sub ^ f) * 32) + 8 * p;
  }
  f4_t ag[8][2], au[8][2];
#pragma unroll
  for (int u = 0; u < 8; ++u)
#pragma unroll
    for (int v = 0; v < 2; ++v) { ag[u][v] = f4_t{0.f, 0.f, 0.f, 0.f}; au[u][v] = f4_t{0.f, 0.f, 0.f, 0.f}; }
  auto load = [&](CFrag& F, unsigned so) {
    const char* sp = smem + so;
#pragma unroll
    for (int v = 0; v < 2; ++v)
#pragma unroll
      for (int m = 0; m < 2; ++m) {
        s4_t lo = tr_read(sp + wa[v] + m * 4096), hi = tr_read(sp + wa[v] + m * 4096 + 512);
        u2_t a = __builtin_bit_cast(u2_t, lo), b = __builtin_bit_cast(u2_t, hi);
        F.A[v][m] = __builtin_bit_cast(b8_t, (u4_t{a.x, a.y, b.x, b.y}));
      }
    F.B[0] = *(const b8_t*)(sp + hb0);
#pragma unroll
    for (int u = 1; u < 8; ++u) F.B[u] = *(const b8_t*)(sp + hb1 + 256 * (u - 1));
  };
  auto mma = [&](const CFrag& F) {
    ag[0][0] = __builtin_amdgcn_mfma_f32_16x16x32_bf16(F.A[0][0], F.B[0], ag[0][0], 0, 0, 0);
    au[0][0] = __builtin_amdgcn_mfma_f32_16x16x32_bf16(F.A[0][1], F.B[0], au[0][0], 0, 0, 0);
#pragma unroll
    for (int u = 1; u < 8; ++u)
#pragma unroll
      for (int v = 0; v < 2; ++v) {
        ag[u][v] = __builtin_amdgcn_mfma_f32_16x16x32_bf16(F.A[v][0], F.B[u], ag[u][v], 0, 0, 0);
        au[u][v] = __builtin_amdgcn_mfma_f32_16x16x32_bf16(F.A[v][1], F.B[u], au[u][v], 0, 0, 0);
      }
  };
  CFrag F0, F1;
  bar();
  if (w == 0) STAMP(2, 1);
  load(F0, 0);
  unsigned sn = C_SLOT;
  auto interleave = [&]() {
#pragma unroll
    for (int i = 0; i < 16; ++i) {
      __builtin_amdgcn_sched_group_barrier(0x008, 1, 0);
      __builtin_amdgcn_sched_group_barrier(0x100, 1, 0);
    }
    __builtin_amdgcn_sched_group_barrier(0x008, 14, 0);
  };
#define SB __builtin_amdgcn_sched_barrier(0)
  SB;
#pragma unroll 1
  for (int b = 0; b < C_STEPS - 2; b += 2) {
    bar(); SB;
    load(F1, sn);
    mma(F0);
    interleave();
    SB;
    sn += C_SLOT; if (sn == C_NSLOT * C_SLOT) sn = 0;
    bar(); SB;
    load(F0, sn);
    mma(F1);
    interleave();
    SB;
    sn += C_SLOT; if (sn == C_NSLOT * C_SLOT) sn = 0;
  }
  bar(); SB;
  load(F1, sn);
  mma(F0);
  interleave();
  SB;
  bar(); SB;
  mma(F1);
  SB;
  if (w == 0) STAMP(2, 2);
  __syncthreads();
  if (w == 0) STAMP(2, 3);
  f4_t csg[2], csu[2];
#pragma unroll
  for (int v = 0; v < 2; ++v) {
    const int sub = 2 * s + (v ? 1 - th : th);
    csg[v] = *(const f4_t*)(smem + C_CS + (16 * sub + 4 * kg) * 4);
    csu[v] = *(const f4_t*)(smem + C_CS + 256 + (16 * sub + 4 * kg) * 4);
  }
  char* hh = ws + OFF_HH + (size_t)(2 * jline + s) * 1024 + lane * 16;
#pragma unroll
  for (int u = 0; u < 8; ++u) {
    const int tile = (u == 0) ? 7 : (th ? 7 + u : u - 1);
    const f2_t st = *(const f2_t*)(smem + C_STAT + (16 * tile + fr) * 8);
    const float m = st.x, r2 = st.y * st.y;
    const int t16 = 15 * panel + tile;
    u2_t o[2];
#pragma unroll
    for (int v = 0; v < 2; ++v) {
      if (u == 0 && v == 1) continue;
      f4_t g = ag[u][v] - m * csg[v], uu = au[u][v] - m * csu[v];
      f4_t r = g * uu * r2;
      o[v] = u2_t{pk_bf16(r[0], r[1]), pk_bf16(r[2], r[3])};
    }
    if (t16 < 128) {
      char* dst = hh + (size_t)t16 * (54 * 1024);
      if (u == 0) *(u2_t*)(dst + 8 * th) = o[0];
      else *(u4_t*)dst = th ? u4_t{o[1].x, o[1].y, o[0].x, o[0].y} : u4_t{o[0].x, o[0].y, o[1].x, o[1].y};
    }
  }
  if (w == 0) STAMP(2, 7);
}

constexpr int D_SLOT = 27648, D_NSLOT = 5, D_HOFF = 15360, D_STEPS = 18;
constexpr int D_LDS = D_NSLOT * D_SLOT;

template <int NDMA>
__device__ __forceinline__ void d_loader(char* smem, const char* wp, const char* hh, int w4, int lane) {
  const unsigned lds0 = (unsigned)(size_t)(LAS char*)smem;
  const unsigned voff = lane * 16;
  auto issue = [&](int step, unsigned so) {
#pragma unroll
    for (int k = 0; k < NDMA; ++k) {
      const int i = w4 + 4 * k;
      const char* src = (i < 15) ? wp + (size_t)step * 15360 + i * 1024
                                 : hh + (size_t)((i - 15) / 3) * (54 * 1024) + (size_t)(3 * step + (i - 15) % 3) * 1024;
      glds16(src, voff, lds0 + so + i * 1024);
    }
  };
#pragma unroll
  for (int s = 0; s < D_NSLOT; ++s) issue(s, s * D_SLOT);
  wait_vm<4 * NDMA>();
  bar();
  unsigned sw = 0;
  for (int b = 1; b < 14; ++b) {
    wait_vm<3 * NDMA>();
    bar();
    issue(b + 4, sw);
    sw += D_SLOT; if (sw == D_NSLOT * D_SLOT) sw = 0;
  }
  wait_vm<3 * NDMA>(); bar();
  wait_vm<2 * NDMA>(); bar();
  wait_vm<1 * NDMA>(); bar();
  wait_vm<0>(); bar();
}

__global__ __launch_bounds__(512, 2) void kD(char* __restrict__ ws, u16* __restrict__ out) {
  extern __shared__ __attribute__((aligned(16))) char smem[];
  const int tid = threadIdx.x, lane = tid & 63;
  const int w = __builtin_amdgcn_readfirstlane(tid >> 6);
  const int xcd = blockIdx.x & 7, rank = blockIdx.x >> 3;
  const int it = 8 * (xcd >> 1) + (rank & 7), dg = 4 * (xcd & 1) + (rank >> 3);
  if (w == 0) STAMP(3, 0);
  if (w >= 4) {
    const int w4 = w - 4;
    const char* wp = ws + OFF_WP2 + (size_t)dg * (54 * 5120);
    const char* hh = ws + OFF_HH + (size_t)(4 * it) * (54 * 1024);
    if (w4 < 3) d_loader<7>(smem, wp, hh, w4, lane);
    else d_loader<6>(smem, wp, hh, w4, lane);
    return;
  }
  const int fr = lane & 15, g = lane >> 4;
  const int t = 64 * it + 16 * w + fr;
  u2_t res[5];
#pragma unroll
  for (int dt = 0; dt < 5; ++dt) {
    const int d = 80 * dg + 16 * dt + 4 * g;
    res[dt] = *(const u2_t*)(ws + OFF_H8 + ((size_t)(d >> 3) * SL + t) * 16 + (d & 4) * 2);
  }
  f4_t acc[5];
#pragma unroll
  for (int dt = 0; dt < 5; ++dt) acc[dt] = f4_t{0.f, 0.f, 0.f, 0.f};
  unsigned so = 0;
#pragma unroll 1
  for (int b = 0; b < D_STEPS; ++b) {
    bar();
    if (w == 0 && b == 0) STAMP(3, 1);
    const char* sp = smem + so + lane * 16;
#pragma unroll
    for (int kk = 0; kk < 3; ++kk) {
      b8_t B = *(const b8_t*)(sp + D_HOFF + (w * 3 + kk) * 1024);
#pragma unroll
      for (int dt = 0; dt < 5; ++dt) {
        b8_t A = *(const b8_t*)(sp + (kk * 5 + dt) * 1024);
        acc[dt] = __builtin_amdgcn_mfma_f32_16x16x32_bf16(A, B, acc[dt], 0, 0, 0);
      }
    }
    asm volatile("s_waitcnt lgkmcnt(0)" ::: "memory");
    so += D_SLOT; if (so == D_NSLOT * D_SLOT) so = 0;
  }
  if (w == 0) STAMP(3, 2);
#pragma unroll
  for (int dt = 0; dt < 5; ++dt) {
    const int d = 80 * dg + 16 * dt + 4 * g;
    const float h0 = bf_lo(res[dt].x), h1 = bf_hi(res[dt].x), h2 = bf_lo(res[dt].y), h3 = bf_hi(res[dt].y);
    *(u2_t*)(out + (size_t)t * DM + d) = u2_t{pk_bf16(h0 + acc[dt][0], h1 + acc[dt][1]), pk_bf16(h2 + acc[dt][2], h3 + acc[dt][3])};
  }
  if (w == 0) STAMP(3, 7);
}

extern "C" void kernel_launch(void* const* d_in, const int* in_sizes, int n_in, void* d_out, int out_size, void* d_ws,
                              size_t ws_size, hipStream_t stream) {
  const u16* x = (const u16*)d_in[0];
  char* ws = (char*)d_ws;
  kA<<<A_XBLK + A_KBLK, 256, A_LDS, stream>>>(x, (const u16*)d_in[1], (const u16*)d_in[2], (const u16*)d_in[3], (const u16*)d_in[4],
                                              (const u16*)d_in[5], (const u16*)d_in[6], (const u16*)d_in[7], ws);
  kB<<<256, 512, B_LDS, stream>>>((const u16*)d_in[8], (const u16*)d_in[12], ws);
  (void)hipFuncSetAttribute((const void*)kC, hipFuncAttributeMaxDynamicSharedMemorySize, C_LDS);
  kC<<<256, 512, C_LDS, stream>>>((const u16*)d_in[10], (const u16*)d_in[11], ws);
  (void)hipFuncSetAttribute((const void*)kD, hipFuncAttributeMaxDynamicSharedMemorySize, D_LDS);
  kD<<<256, 512, D_LDS, stream>>>(ws, (u16*)d_out);
}
```

```cpp
#include <hip/hip_runtime.h>
#include <hip/hip_bf16.h>
#include <stdint.h>
#include <type_traits>

typedef unsigned short u16;
typedef _Float16 h2_t __attribute__((ext_vector_type(2)));
typedef _Float16 h8_t __attribute__((ext_vector_type(8)));
typedef __bf16 b2_t __attribute__((ext_vector_type(2)));
typedef __bf16 b8_t __attribute__((ext_vector_type(8)));
typedef float f2_t __attribute__((ext_vector_type(2)));
typedef float f4_t __attribute__((ext_vector_type(4)));
typedef unsigned u2_t __attribute__((ext_vector_type(2)));
typedef unsigned u4_t __attribute__((ext_vector_type(4)));
typedef short s4_t __attribute__((ext_vector_type(4)));
#define LAS __attribute__((address_space(3)))
#define SB __builtin_amdgcn_sched_barrier(0)

constexpr int SL = 2048;
constexpr int DM = 640;
constexpr int FF = 1728;

constexpr size_t OFF_X4 = 0;
constexpr size_t OFF_S1 = 0x280000;
constexpr size_t OFF_KD = 0x290000;
constexpr size_t OFF_INVS = 0x590000;
constexpr size_t OFF_H8 = 0x5A0000;
constexpr size_t OFF_HH = 0x820000;
constexpr size_t OFF_WP2 = 0xF00000;
constexpr int KDS = 4864;
constexpr int KD_O = 2240;
constexpr int KD_DR = 4384;
constexpr size_t OFF_CS = 0x1120000;
constexpr size_t OFF_G2 = 0x1130000;

#define STAMP(k, i)

__device__ __forceinline__ float bf_lo(unsigned d) { return __uint_as_float(d << 16); }
__device__ __forceinline__ float bf_hi(unsigned d) { return __uint_as_float(d & 0xffff0000u); }
__device__ __forceinline__ float bf2f(u16 v) { return __uint_as_float((unsigned)v << 16); }
__device__ __forceinline__ unsigned pk_bf16(float a, float b) {
  f2_t v = {a, b};
  return __builtin_bit_cast(unsigned, __builtin_convertvector(v, b2_t));
}
__device__ __forceinline__ unsigned pk_f16(float a, float b) {
  f2_t v = {a, b};
  return __builtin_bit_cast(unsigned, __builtin_convertvector(v, h2_t));
}
__device__ __forceinline__ float rbf(float a) { return bf_lo(pk_bf16(a, 0.f)); }
template <int N> __device__ __forceinline__ float dpp_ror_add(float v) {
  int r = __builtin_amdgcn_update_dpp(0, __float_as_int(v), 0x120 + N, 0xf, 0xf, false);
  return v + __int_as_float(r);
}
__device__ __forceinline__ float row16_sum(float v) {
  v = dpp_ror_add<8>(v); v = dpp_ror_add<4>(v); v = dpp_ror_add<2>(v); v = dpp_ror_add<1>(v);
  return v;
}
__device__ __forceinline__ void wave_fence() {
  __builtin_amdgcn_fence(__ATOMIC_RELEASE, "wavefront");
  __builtin_amdgcn_wave_barrier();
  __builtin_amdgcn_fence(__ATOMIC_ACQUIRE, "wavefront");
}
__device__ __forceinline__ s4_t tr_read(const char* p) {
  return __builtin_amdgcn_ds_read_tr16_b64_v4i16((LAS s4_t*)p);
}

struct Cw { f2_t w, ws; };
__device__ __forceinline__ Cw mkw(f2_t w) { Cw r; r.w = w; r.ws = f2_t{-w.y, w.x}; return r; }
__device__ __forceinline__ f2_t cmulw(f2_t a, const Cw& W) {
  return __builtin_elementwise_fma(f2_t{a.y, a.y}, W.ws, f2_t{a.x, a.x} * W.w);
}
__device__ __forceinline__ f2_t cexp_l(float er, float wr, float l) {
  float mag = __builtin_amdgcn_exp2f(er * l);
  float rev = __builtin_amdgcn_fractf(wr * l);
  return f2_t{mag * __builtin_amdgcn_cosf(rev), mag * __builtin_amdgcn_sinf(rev)};
}
__device__ __forceinline__ float pow2_scale(float bound, int target) {
  unsigned e = (__float_as_uint(bound) >> 23) & 0xff;
  int k = target - ((int)e - 126);
  k = k > 126 ? 126 : (k < -126 ? -126 : k);
  return (bound > 0.f) ? __uint_as_float((unsigned)(k + 127) << 23) : 1.0f;
}
__device__ __forceinline__ int lrow(int rho) {
  if (rho < 16) return 128 * (rho >> 3) + (rho & 7);
  if (rho < 24) return 128 * (2 + ((rho - 16) >> 2)) + 2 * ((rho - 16) & 3);
  if (rho < 32) return 128 * (4 + ((rho - 24) >> 1)) + 4 * ((rho - 24) & 1);
  return 128 * (8 + (rho - 32));
}
__device__ __forceinline__ float wave_sum(float v) {
  v = row16_sum(v);
  const int b = __float_as_int(v);
  return (__int_as_float(__builtin_amdgcn_readlane(b, 0)) + __int_as_float(__builtin_amdgcn_readlane(b, 16))) +
         (__int_as_float(__builtin_amdgcn_readlane(b, 32)) + __int_as_float(__builtin_amdgcn_readlane(b, 48)));
}
template <typename T> __device__ __forceinline__ T ldnt(const T* p) { return __builtin_nontemporal_load(p); }
__device__ __forceinline__ int rnd_lag(int l) {
  unsigned u = __float_as_uint((float)l);
  u = (u + 0x7fffu + ((u >> 16) & 1u)) & 0xffff0000u;
  return (int)__uint_as_float(u);
}

constexpr int A_XBLK = 128, A_KBLK = 320;
constexpr int KG_WBUF = 2 * 16 * 272;
constexpr int KG_TAB = 4160;
constexpr int KG_CH = 2 * KG_WBUF + KG_TAB;
constexpr int A_LDS = 2 * KG_CH;

__global__ __launch_bounds__(256) void kA(const u16* __restrict__ x, const u16* __restrict__ Are, const u16* __restrict__ Aim,
                                          const u16* __restrict__ Bre, const u16* __restrict__ Bim, const u16* __restrict__ Cre,
                                          const u16* __restrict__ Cim, const u16* __restrict__ logdt, char* __restrict__ ws) {
  extern __shared__ __attribute__((aligned(16))) char smem[];
  const int tid = threadIdx.x, lane = tid & 63, w = tid >> 6;
  if (w == 0) STAMP(0, 0);
  if (blockIdx.x >= A_KBLK) {
    const int t0 = (blockIdx.x - A_KBLK) * 16;
    const int r = lane >> 4, c = lane & 15, rl = w * 4 + r;
    const u4_t* p = (const u4_t*)(x + (size_t)(t0 + rl) * DM);
    u4_t v[5];
#pragma unroll
    for (int i = 0; i < 5; ++i) v[i] = ldnt(p + c + 16 * i);
    if (tid < 16) ((unsigned long long*)(ws + OFF_G2))[t0 + tid] = 0ull;
    float s = 0.f;
#pragma unroll
    for (int i = 0; i < 5; ++i)
#pragma unroll
      for (int j = 0; j < 4; ++j) s += bf_lo(v[i][j]) + bf_hi(v[i][j]);
#pragma unroll
    for (int i = 0; i < 5; ++i) *(u4_t*)(smem + rl * 1296 + (c + 16 * i) * 16) = v[i];
    const float mean = row16_sum(s) * (1.0f / DM);
    if (w == 0) STAMP(0, 1);
    float q = 0.f;
#pragma unroll
    for (int i = 0; i < 5; ++i)
#pragma unroll
      for (int j = 0; j < 4; ++j) {
        float a = bf_lo(v[i][j]) - mean, b = bf_hi(v[i][j]) - mean;
        q += a * a; q += b * b;
      }
    const float var = row16_sum(q) * (1.0f / DM);
    if (c == 0) {
      float mb = rbf(mean), sd = rbf(sqrtf(rbf(rbf(var) + 1e-5f)));
      ((unsigned*)(ws + OFF_S1))[t0 + rl] = (__float_as_uint(mb) >> 16) | (__float_as_uint(sd) & 0xffff0000u);
    }
    __syncthreads();
#pragma unroll
    for (int it = 0; it < 5; ++it) {
      const int item = tid + 256 * it, q4 = item >> 3, tp = item & 7;
      u2_t a = *(const u2_t*)(smem + (2 * tp) * 1296 + 8 * q4);
      u2_t b = *(const u2_t*)(smem + (2 * tp + 1) * 1296 + 8 * q4);
      *(u4_t*)(ws + OFF_X4 + ((size_t)q4 * SL + t0 + 2 * tp) * 8) = u4_t{a.x, a.y, b.x, b.y};
    }
    if (w == 0) STAMP(0, 7);
    return;
  }
  const int cl = w >> 1, hw = w & 1;
  const int ch = blockIdx.x * 2 + cl;
  char* chb = smem + cl * KG_CH;
  char* lbuf = chb + hw * KG_WBUF;
  char* rbuf = lbuf + 16 * 272;
  float* xch = (float*)(chb + KG_WBUF);
  u16* tab = (u16*)(chb + 2 * KG_WBUF);
  const int n = ch * 128 + hw * 64 + lane;
  const u16 i_are = ldnt(Are + n), i_aim = ldnt(Aim + n), i_bre = ldnt(Bre + n), i_bim = ldnt(Bim + n), i_cre = ldnt(Cre + n), i_cim = ldnt(Cim + n);
  const float dt = rbf(expf(bf2f(logdt[ch])));
  const float are = dt * bf2f(i_are), aim = dt * bf2f(i_aim);
  const float bre = dt * bf2f(i_bre), bim = dt * bf2f(i_bim);
  const float cre = bf2f(i_cre), cim = bf2f(i_cim);
  f2_t c = {cre * bre - cim * bim, cre * bim + cim * bre};
  const float er = are * 1.44269504088896341f, wr = aim * 0.15915494309189535f;
  const float bnd = (fabsf(c.x) + fabsf(c.y)) * fmaxf(1.0f, __builtin_amdgcn_exp2f(er * 2048.f));
  const float bsum = wave_sum(bnd);
  if (w == 0) STAMP(0, 1);
  const float Sw = pow2_scale(bsum, 15);
  c *= Sw;
  const Cw z1 = mkw(cexp_l(er, wr, 1.f)), w8 = mkw(cexp_l(er, wr, 8.f)), w128 = mkw(cexp_l(er, wr, 128.f));
  const Cw z2 = mkw(cmulw(z1.w, z1));
  const Cw z4 = mkw(cmulw(z2.w, z2));
  {
    f2_t R = {1.f, 0.f};
#pragma unroll
    for (int i = 0; i < 16; ++i) {
      *(unsigned*)(rbuf + i * 272 + 4 * lane) = pk_f16(R.x, R.y);
      if (i < 15) R = (i == 0) ? w8.w : cmulw(R, w8);
    }
  }
  f2_t T1[17];
  T1[0] = c;
#pragma unroll
  for (int i = 1; i < 17; ++i) T1[i] = cmulw(T1[i - 1], w128);
  const int fr = lane & 15, kg = lane >> 4;
  h8_t Bf[4];
  f4_t acc[3];
  auto put = [&](int row, f2_t v) { *(unsigned*)(lbuf + row * 272 + 4 * lane) = pk_f16(v.x, -v.y); };
  auto mm = [&](int tile) {
    wave_fence();
    if (tile == 0) {
#pragma unroll
      for (int ks = 0; ks < 4; ++ks) Bf[ks] = *(const h8_t*)(rbuf + fr * 272 + 64 * ks + 16 * kg);
    }
    f4_t a = {0.f, 0.f, 0.f, 0.f};
#pragma unroll
    for (int ks = 0; ks < 4; ++ks) {
      h8_t Af = *(const h8_t*)(lbuf + fr * 272 + 64 * ks + 16 * kg);
      a = __builtin_amdgcn_mfma_f32_16x16x32_f16(Af, Bf[ks], a, 0, 0, 0);
    }
    acc[tile] = a;
    wave_fence();
  };
#pragma unroll
  for (int i1 = 0; i1 < 2; ++i1) {
    f2_t v = T1[i1];
#pragma unroll
    for (int i0 = 0; i0 < 8; ++i0) { put(8 * i1 + i0, v); if (i0 < 7) v = cmulw(v, z1); }
  }
  mm(0);
#pragma unroll
  for (int i1 = 2; i1 < 4; ++i1) {
    f2_t v = T1[i1];
#pragma unroll
    for (int i0 = 0; i0 < 4; ++i0) { put(4 * (i1 - 2) + i0, v); if (i0 < 3) v = cmulw(v, z2); }
  }
#pragma unroll
  for (int i1 = 4; i1 < 8; ++i1) { put(8 + 2 * (i1 - 4), T1[i1]); put(8 + 2 * (i1 - 4) + 1, cmulw(T1[i1], z4)); }
  mm(1);
#pragma unroll
  for (int i1 = 8; i1 < 17; ++i1) put(i1 - 8, T1[i1]);
  mm(2);
  if (w == 0) STAMP(0, 2);
  const float invSw = __uint_as_float(0x7f000000u - __float_as_uint(Sw));
  if (hw == 1) {
#pragma unroll
    for (int t = 0; t < 3; ++t) *(f4_t*)(xch + (t * 64 + lane) * 4) = acc[t] * invSw;
    if (lane == 0) xch[768] = bsum;
  }
  __syncthreads();
  if (hw == 0) {
    const float Sc = pow2_scale(bsum + xch[768], 15);
    const int l0 = 128 * (kg >> 1) + 4 * (kg & 1) + 8 * fr;
    const int l1 = (kg < 2) ? 128 * (2 + kg) + 8 * fr : 128 * (4 + 2 * (kg - 2)) + 8 * fr;
    const int l2 = 128 * (8 + 4 * kg) + 8 * fr;
#pragma unroll
    for (int t = 0; t < 3; ++t) {
      const f4_t o = *(const f4_t*)(xch + (t * 64 + lane) * 4);
#pragma unroll
      for (int i = 0; i < 4; ++i) {
        const u16 v = (u16)(pk_f16((acc[t][i] * invSw + o[i]) * Sc, 0.f) & 0xffff);
        int l = (t == 0) ? l0 + i : (t == 1) ? ((kg < 2) ? l1 + 2 * i : l1 + 128 * (i >> 1) + 4 * (i & 1)) : l2 + 128 * i;
        if (t == 2) l = min(l, 2056);
        tab[l] = v;
      }
    }
    if (lane == 0) ((float*)(ws + OFF_INVS))[ch] = __uint_as_float(0x7f000000u - __float_as_uint(Sc));
  }
  __syncthreads();
  if (w == 0) STAMP(0, 3);
  char* kd = ws + OFF_KD + (size_t)ch * KDS;
  const int j = hw * 64 + lane;
#pragma unroll
  for (int it = 0; it < 2; ++it) {
    const int cp = j + 128 * it;
    if (it == 0 || j < 6) {
      const int cc = cp - 2;
      const int ca = min(max(cc, 0), 128);
      const u4_t d = *(const u4_t*)(tab + 8 * ca);
      const unsigned d4 = *(const unsigned*)(tab + 8 * (ca + 1));
      const unsigned k0 = d[0] & 0xffffu, k2 = d[1] & 0xffffu, k4 = d[2] & 0xffffu, k6 = d[3] & 0xffffu, k8 = d4 & 0xffffu;
      unsigned o0, o1, o2, o3, nx;
      if (cc < 32) { o0 = d[0]; o1 = d[1]; o2 = d[2]; o3 = d[3]; nx = k8; }
      else if (cc < 64) { o0 = k0 * 0x10001u; o1 = k2 | (k4 << 16); o2 = k4 * 0x10001u; o3 = k6 | (k8 << 16); nx = k8; }
      else if (cc < 128) { o0 = k0 * 0x10001u; o1 = k0 | (k4 << 16); o2 = k4 * 0x10001u; o3 = k8 * 0x10001u; nx = k8; }
      else { o0 = k0 * 0x10001u; o1 = o0; o2 = k0; o3 = 0u; nx = 0u; }
      if (cc < 0 || cc > 128) { o0 = o1 = o2 = o3 = 0u; nx = (cc == -1) ? k0 : 0u; }
      *(u4_t*)(kd + 16 * cp) = u4_t{o0, o1, o2, o3};
      *(u4_t*)(kd + KD_O + 16 * cp) = u4_t{__builtin_amdgcn_alignbit(o1, o0, 16), __builtin_amdgcn_alignbit(o2, o1, 16),
                                           __builtin_amdgcn_alignbit(o3, o2, 16), __builtin_amdgcn_alignbit(nx, o3, 16)};
    }
  }
  if (j < 80) {
    const int dl = 127 - j;
    unsigned v = 0;
    if (dl >= 64) v = (unsigned)tab[8 * (2 * dl + 1)] | ((unsigned)tab[8 * (2 * dl + 2)] << 16);
    ((unsigned*)(kd + KD_DR))[j] = v;
  }
  if (w == 0) STAMP(0, 7);
}

constexpr int B_CONV = 160;
constexpr int B_KOFF = 0;
constexpr int B_UOFF = 4 * KDS;
constexpr int B_UCH = 4608;
constexpr int B_YCH = 10240;
constexpr int B_LDS = 43520;

struct BFrag { h8_t B[8]; h8_t A[5]; };

__global__ __launch_bounds__(512) void kB(const u16* __restrict__ dskip, const u16* __restrict__ wg, const u16* __restrict__ wu,
                                          const u16* __restrict__ wd, char* __restrict__ ws) {
  extern __shared__ __attribute__((aligned(16))) char smem[];
  const int tid = threadIdx.x, lane = tid & 63;
  const int w = __builtin_amdgcn_readfirstlane(tid >> 6);
  if (w == 0) STAMP(1, 0);
  if (blockIdx.x >= B_CONV) {
    const int xcd = blockIdx.x & 7, rk = (blockIdx.x - B_CONV) >> 3;
    const int g4 = xcd >> 1, a = xcd & 1, nl = (g4 < 3) ? 7 : 6;
    const bool strip = rk < nl;
    if (strip && w < 5) {
      const int jl = 7 * g4 + rk;
      const char* pm = (const char*)(a ? wu : wg) + (size_t)(128 * w + (lane >> 3)) * (FF * 2) + 128 * jl + (lane & 7) * 16;
      u4_t v[16];
#pragma unroll
      for (int i = 0; i < 16; ++i) v[i] = ldnt((const u4_t*)(pm + (size_t)i * 8 * (FF * 2)));
      const char* po = (const char*)(a ? wg : wu) + (size_t)(128 * w + lane) * (FF * 2) + 128 * jl;
      unsigned t0v, t1v;
      asm volatile("global_load_dword %0, %1, off nt" : "=v"(t0v) : "v"(po) : "memory");
      asm volatile("global_load_dword %0, %1, off nt" : "=v"(t1v) : "v"(po + (size_t)64 * (FF * 2)) : "memory");
      float s[8];
#pragma unroll
      for (int e = 0; e < 8; ++e) s[e] = 0.f;
#pragma unroll
      for (int i = 0; i < 16; ++i)
#pragma unroll
        for (int jj = 0; jj < 4; ++jj) { s[2 * jj] += bf_lo(v[i][jj]); s[2 * jj + 1] += bf_hi(v[i][jj]); }
#pragma unroll
      for (int e = 0; e < 8; ++e) {
        float t = s[e];
        t += __int_as_float(__builtin_amdgcn_update_dpp(0, __float_as_int(t), 0x128, 0xf, 0xf, false));
        t += __shfl_xor(t, 16);
        t += __shfl_xor(t, 32);
        s[e] = t;
      }
      if (lane < 8) {
        float* pp = (float*)(smem + 8 * 5120) + w * 64 + lane * 8;
        *(f4_t*)pp = f4_t{s[0], s[1], s[2], s[3]};
        *(f4_t*)(pp + 4) = f4_t{s[4], s[5], s[6], s[7]};
      }
      asm volatile("s_waitcnt vmcnt(0)" :: "v"(t0v), "v"(t1v) : "memory");
    } else {
      const int pu = strip ? 3 * rk + (w - 5) : 3 * nl + 8 * (rk - nl) + w;
      if (pu < 54) {
        const int u = xcd * 54 + pu, ks = u >> 3, dg = u & 7;
        char* buf = smem + w * 5120;
#pragma unroll
        for (int i = 0; i < 5; ++i) {
          const int idx = i * 64 + lane, row = idx / 10, c = idx % 10;
          __builtin_amdgcn_global_load_lds((const unsigned*)((const char*)wd + (size_t)(32 * ks + row) * 1280 + 160 * dg + 16 * c),
                                           (LAS unsigned*)(buf + i * 1024), 16, 0, 2);
        }
        asm volatile("s_waitcnt vmcnt(0)" ::: "memory");
        wave_fence();
        const int g = lane >> 4, q = (lane & 15) >> 2, p = lane & 3;
        char* dst = ws + OFF_WP2 + ((size_t)(dg * 54 + ks) * 5) * 1024 + lane * 16;
#pragma unroll
        for (int dt = 0; dt < 5; ++dt) {
          s4_t aa = tr_read(buf + (4 * g + q) * 160 + 32 * dt + 8 * p);
          s4_t bb = tr_read(buf + (16 + 4 * g + q) * 160 + 32 * dt + 8 * p);
          u2_t ua = __builtin_bit_cast(u2_t, aa), ub = __builtin_bit_cast(u2_t, bb);
          *(u4_t*)(dst + dt * 1024) = u4_t{ua.x, ua.y, ub.x, ub.y};
        }
      }
    }
    if (strip) {
      __syncthreads();
      if (tid < 64) {
        const float* pp = (const float*)(smem + 8 * 5120) + tid;
        ((float*)(ws + OFF_CS))[a * FF + 64 * (7 * g4 + rk) + tid] = (pp[0] + pp[64]) + (pp[128] + pp[192]) + pp[256];
      }
    }
    if (w == 0) STAMP(1, 7);
    return;
  }
  const int a_ = blockIdx.x >> 3, x_ = blockIdx.x & 7;
  const int q = 16 * (a_ >> 1) + 2 * x_ + (a_ & 1);
  u4_t xv[2]; u2_t sv[2];
#pragma unroll
  for (int it = 0; it < 2; ++it) {
    const int j = tid + 512 * it;
    xv[it] = *(const u4_t*)(ws + OFF_X4 + ((size_t)q * SL + 2 * j) * 8);
    sv[it] = *(const u2_t*)(ws + OFF_S1 + 8 * j);
  }
  for (int i = w; i < 19; i += 8)
    __builtin_amdgcn_global_load_lds((const unsigned*)(ws + OFF_KD + (size_t)q * (4 * KDS) + i * 1024 + lane * 16),
                                     (LAS unsigned*)(smem + B_KOFF + i * 1024), 16, 0, 0);
  float dk[4], is[4];
#pragma unroll
  for (int c = 0; c < 4; ++c) { dk[c] = bf2f(dskip[4 * q + c]); is[c] = ((const float*)(ws + OFF_INVS))[4 * q + c]; }
  *(unsigned*)(smem + B_UOFF + (tid >> 7) * B_UCH + 4096 + 4 * (tid & 127)) = 0u;
  float z[2][8];
  if (w == 0) { STAMP(1, 1); }
#pragma unroll
  for (int it = 0; it < 2; ++it) {
    const int j = tid + 512 * it;
    const float m0 = bf_lo(sv[it].x), r0 = __builtin_amdgcn_rcpf(bf_hi(sv[it].x)), m1 = bf_lo(sv[it].y), r1 = __builtin_amdgcn_rcpf(bf_hi(sv[it].y));
    float x0[4] = {bf_lo(xv[it].x), bf_hi(xv[it].x), bf_lo(xv[it].y), bf_hi(xv[it].y)};
    float x1[4] = {bf_lo(xv[it].z), bf_hi(xv[it].z), bf_lo(xv[it].w), bf_hi(xv[it].w)};
#pragma unroll
    for (int c = 0; c < 4; ++c) {
      const float u0 = (x0[c] - m0) * r0, u1 = (x1[c] - m1) * r1;
      *(unsigned*)(smem + B_UOFF + c * B_UCH + 4 * (1023 - j)) = pk_f16(u1, u0);
      z[it][c] = x0[c] + u0 * dk[c];
      z[it][4 + c] = x1[c] + u1 * dk[c];
    }
  }
  __syncthreads();
  if (w == 0) STAMP(1, 2);
  f4_t acc[8];
  if (w < 4) {
    const int c = lane & 15, kg = lane >> 4;
#pragma unroll
    for (int t = 0; t < 8; ++t) acc[t] = f4_t{0.f, 0.f, 0.f, 0.f};
    const char* ub = smem + B_UOFF + w * B_UCH + 4112 - 32 * c - 16 * kg - 512 * 7;
    const char* kb = smem + B_KOFF + w * KDS + ((c & 1) ? 0 : KD_O) + 4 * (12 + ((c + 1) >> 1) - 4 * kg);
    auto load = [&](BFrag& F, int i0) {
#pragma unroll
      for (int i1 = 0; i1 < 5; ++i1) {
        if (i1 == 4 && i0 != 0) continue;
        const unsigned* ap = (const unsigned*)(kb + 64 * i0 + 512 * i1);
        F.A[i1] = __builtin_bit_cast(h8_t, (u4_t{ap[0], ap[1], ap[2], ap[3]}));
      }
#pragma unroll
      for (int p = 0; p < 8; ++p) F.B[p] = *(const h8_t*)(ub + 512 * (7 - p) + 64 * i0);
    };
    auto mma = [&](const BFrag& F, int i0) {
#pragma unroll
      for (int i1 = 0; i1 < 5; ++i1) {
        if (i1 == 4 && i0 != 0) continue;
#pragma unroll
        for (int t = i1; t < 8; ++t) acc[t] = __builtin_amdgcn_mfma_f32_16x16x32_f16(F.A[i1], F.B[t - i1], acc[t], 0, 0, 0);
      }
    };
    auto interleave = [&]() {
#pragma unroll
      for (int i = 0; i < 16; ++i) {
        __builtin_amdgcn_sched_group_barrier(0x008, 1, 0);
        __builtin_amdgcn_sched_group_barrier(0x100, 1, 0);
      }
      __builtin_amdgcn_sched_group_barrier(0x008, 14, 0);
    };
    BFrag F0, F1;
    load(F0, 0);
    h8_t band9, band7;
#pragma unroll
    for (int j = 0; j < 8; ++j) {
      const int k = 8 * kg + 7 - j;
      band9[j] = (k >= c + 4 && k <= c + 12) ? (_Float16)1.0f : (_Float16)0.0f;
      band7[j] = (k >= c + 5 && k <= c + 11) ? (_Float16)1.0f : (_Float16)0.0f;
    }
    SB;
#pragma unroll
    for (int i0 = 0; i0 < 8; i0 += 2) {
      load(F1, i0 + 1);
      mma(F0, i0);
      interleave();
      SB;
      if (i0 + 2 < 8) load(F0, i0 + 2);
      mma(F1, i0 + 1);
      if (i0 + 2 < 8) interleave();
      SB;
    }
    h8_t Tf[4];
    const unsigned* dr = (const unsigned*)(smem + B_KOFF + w * KDS + KD_DR) + (127 - c + 4 * kg);
#pragma unroll
    for (int dl = 0; dl < 4; ++dl) {
      const unsigned* tp = dr - 16 * (dl + 4);
      Tf[dl] = __builtin_bit_cast(h8_t, (u4_t{tp[0], tp[1], tp[2], tp[3]}));
    }
    const char* uw = smem + B_UOFF + w * B_UCH + 4112 - 32 * c - 16 * kg;
    h8_t U9[4], U7[4];
#pragma unroll
    for (int bt = 0; bt < 4; ++bt) { U9[bt] = *(const h8_t*)(uw - 512 * bt + 16); U7[bt] = *(const h8_t*)(uw - 512 * bt); }
    f4_t X9[4], X7[4];
#pragma unroll
    for (int bt = 0; bt < 4; ++bt) {
      const f4_t zz = {0.f, 0.f, 0.f, 0.f};
      X9[bt] = __builtin_amdgcn_mfma_f32_16x16x32_f16(U9[bt], band9, zz, 0, 0, 0);
      X7[bt] = __builtin_amdgcn_mfma_f32_16x16x32_f16(U7[bt], band7, zz, 0, 0, 0);
    }
#pragma unroll
    for (int bt = 0; bt < 4; ++bt) {
      u4_t vu = {pk_f16(X7[bt][0], X9[bt][0]), pk_f16(X7[bt][1], X9[bt][1]), pk_f16(X7[bt][2], X9[bt][2]), pk_f16(X7[bt][3], X9[bt][3])};
      h8_t Vf = __builtin_bit_cast(h8_t, vu);
#pragma unroll
      for (int at = 4 + bt; at < 8; ++at) acc[at] = __builtin_amdgcn_mfma_f32_16x16x32_f16(Vf, Tf[at - bt - 4], acc[at], 0, 0, 0);
    }
  }
  if (w == 0) STAMP(1, 3);
  __syncthreads();
  if (w < 4) {
    const float s = (w == 0) ? is[0] : (w == 1) ? is[1] : (w == 2) ? is[2] : is[3];
    const int c = lane & 15, g = lane >> 4;
#pragma unroll
    for (int t = 0; t < 8; ++t) *(f4_t*)(smem + w * B_YCH + t * 1280 + g * 320 + c * 16) = acc[t] * s;
  }
  __syncthreads();
#pragma unroll
  for (int it = 0; it < 2; ++it) {
    const int j = tid + 512 * it, t = 2 * j;
    const int yo = (t >> 8) * 1280 + ((t >> 2) & 3) * 320 + ((t >> 4) & 15) * 16 + (t & 3) * 4;
    float h0[4], h1[4];
#pragma unroll
    for (int c = 0; c < 4; ++c) {
      f2_t y = *(const f2_t*)(smem + c * B_YCH + yo);
      h0[c] = z[it][c] + y.x;
      h1[c] = z[it][4 + c] + y.y;
    }
    char* dst = ws + OFF_H8 + ((size_t)(q >> 1) * SL + t) * 16 + (q & 1) * 8;
    *(u2_t*)dst = u2_t{pk_bf16(h0[0], h0[1]), pk_bf16(h0[2], h0[3])};
    *(u2_t*)(dst + 16) = u2_t{pk_bf16(h1[0], h1[1]), pk_bf16(h1[2], h1[3])};
  }
  if (w == 0) STAMP(1, 7);
}

__device__ __forceinline__ void glds16(const void* sbase, unsigned voff, unsigned lds_addr) {
  asm volatile("s_mov_b32 m0, %2\n\ts_nop 0\n\tglobal_load_lds_dwordx4 %0, %1" ::"v"(voff), "s"(sbase), "s"(lds_addr) : "memory");
}
template <int N> __device__ __forceinline__ void wait_vm() { asm volatile("s_waitcnt vmcnt(%0)" ::"i"(N) : "memory"); }
#define TLREC(slot)
__device__ __forceinline__ void bar() { asm volatile("s_barrier" ::: "memory"); }
__device__ __forceinline__ float dot2bf(unsigned a, unsigned b, float c) {
  return __builtin_amdgcn_fdot2_f32_bf16(__builtin_bit_cast(b2_t, a), __builtin_bit_cast(b2_t, b), c, false);
}

constexpr int C_T = 240;
constexpr int C_SLOT = 23552, C_NSLOT = 6, C_GOFF = 15360;
constexpr int C_STAT = C_NSLOT * C_SLOT;
constexpr int C_CS = C_STAT + 1920;
constexpr int C_LDS = C_CS + 512 + 1024;
constexpr int C_STEPS = 20;
typedef __attribute__((address_space(1))) unsigned long long gu64;

__device__ __forceinline__ unsigned ln_pack(float sum, float sq) {
  const float mean = sum * (1.0f / DM);
  const float var = fmaxf(sq * (1.0f / DM) - mean * mean, 0.f);
  const float mb = rbf(mean), sd = rbf(sqrtf(rbf(rbf(var) + 1e-5f)));
  return (__float_as_uint(mb) >> 16) | (__float_as_uint(sd) & 0xffff0000u);
}

template <int NDMA>
__device__ __forceinline__ void c_loader(char* smem, char* ws, const char* wbase, int w4, int lane, int t0, int jline, int xcd) {
  const unsigned lds0 = (unsigned)(size_t)(LAS char*)smem;
  const char* h8 = ws + OFF_H8;
  unsigned voffH[4], voffW[2];
#pragma unroll
  for (int k = 0; k < 2; ++k) {
    const int r = 8 * (2 * (w4 & 1) + k) + (lane >> 3), c = lane & 7;
    const int f = ((r >> 1) & 1) | ((r >> 2) & 2);
    voffW[k] = r * (FF * 2) + 128 * jline + (((c >> 1) ^ f) * 32) + (c & 1) * 16;
  }
#pragma unroll
  for (int k = 0; k < 4; ++k) {
    const int idx = 64 * (w4 + 4 * k) + lane, pl = idx / C_T, tt = idx % C_T;
    const int t = min(t0 + tt, SL - 1);
    voffH[k] = pl * 32768 + t * 16;
  }
  const unsigned ldsW = C_GOFF + (w4 >> 1) * 4096 + (w4 & 1) * 2048;
  auto issue = [&](int step, unsigned so) {
#pragma unroll
    for (int k = 0; k < 2; ++k) glds16(wbase + (size_t)step * (32 * FF * 2), voffW[k], lds0 + so + ldsW + k * 1024);
#pragma unroll
    for (int k = 0; k < NDMA - 2; ++k) glds16(h8 + (size_t)step * (4 * 32768), voffH[k], lds0 + so + (w4 + 4 * k) * 1024);
  };
  constexpr unsigned S = C_SLOT;
  issue(0, 0); issue(1, S);
  TLREC(0); wait_vm<NDMA>(); bar(); TLREC(1);
  issue(2, 2 * S); issue(3, 3 * S);
  wait_vm<2 * NDMA>(); bar(); TLREC(2);
  issue(4, 4 * S); issue(5, 5 * S);
  wait_vm<3 * NDMA>(); bar(); TLREC(3);
  issue(6, 0); issue(7, S);
  unsigned sw = 2 * S;
#pragma unroll 1
  for (int b = 2; b < 14; ++b) {
    wait_vm<4 * NDMA>();
    bar();
    TLREC(2 + b);
    issue(b + 6, sw);
    sw += S; if (sw == C_NSLOT * S) sw = 0;
  }
  wait_vm<4 * NDMA>(); bar(); TLREC(16);
  const int tl = min(60 * w4 + lane, C_T - 1), tg = min(t0 + tl, SL - 1);
  const gu64* gp = (const gu64*)(ws + OFF_G2) + tg;
  const float* csp = (const float*)(ws + OFF_CS) + ((lane >> 4) & 1) * FF + 64 * jline + 16 * w4 + (lane & 15);
  const char* tp = ws + OFF_WP2 + (size_t)(4 * (xcd & 1)) * (54 * 5120) + (size_t)min(((int)(blockIdx.x >> 3) * 4 + w4) * 68 + lane, 8639) * 128;
  const char* tq = ws + OFF_WP2 + (size_t)(4 * (xcd & 1)) * (54 * 5120) + (size_t)min(((int)(blockIdx.x >> 3) * 4 + w4) * 68 + 64 + (lane & 3), 8639) * 128;
  u2_t gv; float csv; unsigned tv0, tv1;
  asm volatile("global_load_dwordx2 %0, %1, off sc1" : "=v"(gv) : "v"(gp) : "memory");
  asm volatile("global_load_dword %0, %1, off" : "=v"(csv) : "v"(csp) : "memory");
  asm volatile("global_load_dword %0, %1, off" : "=v"(tv0) : "v"(tp) : "memory");
  asm volatile("global_load_dword %0, %1, off" : "=v"(tv1) : "v"(tq) : "memory");
  wait_vm<3 * NDMA + 4>(); bar(); TLREC(17);
  wait_vm<2 * NDMA + 4>(); bar(); TLREC(18);
  wait_vm<1 * NDMA + 4>(); bar(); TLREC(19);
  asm volatile("s_waitcnt vmcnt(0)" : "+v"(gv), "+v"(csv), "+v"(tv0), "+v"(tv1) :: "memory");
  bar(); TLREC(20);
  {
    bool ok = gv.y == 1u;
    for (int spin = 0; spin < 4096 && !__all(ok); ++spin) {
      const unsigned long long g = __hip_atomic_load(gp, __ATOMIC_RELAXED, __HIP_MEMORY_SCOPE_AGENT);
      gv.x = (unsigned)g; ok = (unsigned)(g >> 32) == 1u;
      __builtin_amdgcn_s_sleep(2);
    }
    if (!__all(ok)) {
      float s = 0.f, q = 0.f;
      for (int pl = 0; pl < 80; ++pl) {
        const u4_t v = *(const u4_t*)(h8 + (size_t)pl * 32768 + tg * 16);
        for (int j = 0; j < 4; ++j) { const float a = bf_lo(v[j]), b = bf_hi(v[j]); s += a + b; q += a * a + b * b; }
      }
      gv.x = ln_pack(s, q);
    }
    if (lane < 60) *(f2_t*)(smem + C_STAT + (60 * w4 + lane) * 8) = f2_t{bf_lo(gv.x), 1.0f / bf_hi(gv.x)};
    if (lane < 32) *(float*)(smem + C_CS + (lane >> 4) * 256 + (16 * w4 + (lane & 15)) * 4) = csv;
  }
  asm volatile("s_waitcnt lgkmcnt(0)" ::: "memory");
  bar(); TLREC(21);
}

struct CFrag { b8_t A[2][2]; b8_t B[8]; };

__global__ __launch_bounds__(512, 2) void kC(const u16* __restrict__ wg, const u16* __restrict__ wu, char* __restrict__ ws) {
  extern __shared__ __attribute__((aligned(16))) char smem[];
  const int tid = threadIdx.x, lane = tid & 63;
  const int w = __builtin_amdgcn_readfirstlane(tid >> 6);
  int panel, jline;
  const int xcd = blockIdx.x & 7, rank = blockIdx.x >> 3;
  {
    const int g4 = xcd >> 1, a = xcd & 1, nl = (g4 < 3) ? 7 : 6, total = 9 * nl, n0 = (total + 1) >> 1;
    if (a == 0 ? rank >= n0 : rank >= total - n0) {
      const int sp = (xcd < 6) ? (xcd >> 1) : 3 + 5 * (xcd - 6) + (rank - 27);
      const int wv = sp * 8 + w;
      if (w == 0) STAMP(2, 0);
      for (int tile = wv; tile < 128; tile += 104) {
        const int t = 16 * tile + (lane & 15);
        const char* hp = ws + OFF_H8 + (size_t)(lane >> 4) * 32768 + t * 16;
        u4_t v[20];
#pragma unroll
        for (int i = 0; i < 20; ++i) v[i] = *(const u4_t*)(hp + (size_t)i * (4 * 32768));
        float s0 = 0.f, s1 = 0.f, q0 = 0.f, q1 = 0.f;
        unsigned k_both = 0x3f803f80u;
        asm volatile("" : "+s"(k_both));
#pragma unroll
        for (int i = 0; i < 20; ++i) {
          s0 = dot2bf(v[i][0], k_both, s0); q0 = dot2bf(v[i][0], v[i][0], q0);
          s1 = dot2bf(v[i][1], k_both, s1); q1 = dot2bf(v[i][1], v[i][1], q1);
          s0 = dot2bf(v[i][2], k_both, s0); q0 = dot2bf(v[i][2], v[i][2], q0);
          s1 = dot2bf(v[i][3], k_both, s1); q1 = dot2bf(v[i][3], v[i][3], q1);
        }
        float s = s0 + s1, q = q0 + q1;
        s += __shfl_xor(s, 16); q += __shfl_xor(q, 16);
        s += __shfl_xor(s, 32); q += __shfl_xor(q, 32);
        if (lane < 16)
          __hip_atomic_store((gu64*)(ws + OFF_G2) + t, (1ull << 32) | ln_pack(s, q), __ATOMIC_RELAXED, __HIP_MEMORY_SCOPE_AGENT);
      }
      if (w == 0) STAMP(2, 7);
      return;
    }
    const int idx = rank + a * n0;
    panel = idx / nl; jline = 7 * g4 + idx % nl;
  }
  const int t0 = panel * C_T;
  if (w == 0) STAMP(2, 0);
  if (w >= 4) {
    const int w4 = w - 4;
    const char* wbase = (const char*)((w4 >> 1) ? wu : wg);
    if (w4 < 3) c_loader<6>(smem, ws, wbase, w4, lane, t0, jline, xcd);
    else c_loader<5>(smem, ws, wbase, w4, lane, t0, jline, xcd);
    return;
  }
  const int s = w >> 1, th = w & 1;
  const int fr = lane & 15, kg = lane >> 4, q = (lane & 15) >> 2, p = lane & 3;
  const unsigned hb0 = kg * 3840 + fr * 16 + 256 * 7;
  const unsigned hb1 = kg * 3840 + fr * 16 + 256 * (th ? 8 : 0);
  unsigned wa[2];
#pragma unroll
  for (int v = 0; v < 2; ++v) {
    const int sub = 2 * s + (v ? 1 - th : th);
    const int f = (q >> 1) | (2 * (kg & 1));
    wa[v] = C_GOFF + (8 * kg + q) * 128 + ((sub ^ f) * 32) + 8 * p;
  }
  f4_t ag[8][2], au[8][2];
#pragma unroll
  for (int u = 0; u < 8; ++u)
#pragma unroll
    for (int v = 0; v < 2; ++v) { ag[u][v] = f4_t{0.f, 0.f, 0.f, 0.f}; au[u][v] = f4_t{0.f, 0.f, 0.f, 0.f}; }
  auto load = [&](CFrag& F, unsigned so) {
    const char* sp = smem + (so == 0x7fffffff ? 0 : so);
#pragma unroll
    for (int v = 0; v < 2; ++v)
#pragma unroll
      for (int m = 0; m < 2; ++m) {
        s4_t lo = tr_read(sp + wa[v] + m * 4096), hi = tr_read(sp + wa[v] + m * 4096 + 512);
        u2_t a = __builtin_bit_cast(u2_t, lo), b = __builtin_bit_cast(u2_t, hi);
        F.A[v][m] = __builtin_bit_cast(b8_t, (u4_t{a.x, a.y, b.x, b.y}));
      }
    F.B[0] = *(const b8_t*)(sp + hb0);
#pragma unroll
    for (int u = 1; u < 8; ++u) F.B[u] = *(const b8_t*)(sp + hb1 + 256 * (u - 1));
  };
  auto mma = [&](const CFrag& F) {
    ag[0][0] = __builtin_amdgcn_mfma_f32_16x16x32_bf16(F.A[0][0], F.B[0], ag[0][0], 0, 0, 0);
    au[0][0] = __builtin_amdgcn_mfma_f32_16x16x32_bf16(F.A[0][1], F.B[0], au[0][0], 0, 0, 0);
#pragma unroll
    for (int u = 1; u < 8; ++u)
#pragma unroll
      for (int v = 0; v < 2; ++v) {
        ag[u][v] = __builtin_amdgcn_mfma_f32_16x16x32_bf16(F.A[v][0], F.B[u], ag[u][v], 0, 0, 0);
        au[u][v] = __builtin_amdgcn_mfma_f32_16x16x32_bf16(F.A[v][1], F.B[u], au[u][v], 0, 0, 0);
      }
  };
  auto interleave = [&]() {
#pragma unroll
    for (int i = 0; i < 16; ++i) {
      __builtin_amdgcn_sched_group_barrier(0x008, 1, 0);
      __builtin_amdgcn_sched_group_barrier(0x100, 1, 0);
    }
    __builtin_amdgcn_sched_group_barrier(0x008, 14, 0);
  };
  CFrag F0, F1;
  bar();
  if (w == 0) STAMP(2, 1);
  load(F0, 0);
  unsigned sn = C_SLOT;
  SB;
#pragma unroll 1
  for (int b = 0; b < C_STEPS - 2; b += 2) {
    asm volatile("s_waitcnt lgkmcnt(0)" ::: "memory");
    bar(); SB;
    load(F1, sn);
    mma(F0);
    interleave();
    SB;
    sn += C_SLOT; if (sn == C_NSLOT * C_SLOT) sn = 0;
    asm volatile("s_waitcnt lgkmcnt(0)" ::: "memory");
    bar(); SB;
    load(F0, sn);
    mma(F1);
    interleave();
    SB;
    sn += C_SLOT; if (sn == C_NSLOT * C_SLOT) sn = 0;
  }
  asm volatile("s_waitcnt lgkmcnt(0)" ::: "memory");
  bar(); SB;
  load(F1, sn);
  mma(F0);
  interleave();
  SB;
  bar(); SB;
  f4_t csg[2], csu[2];
  f2_t st[8];
#pragma unroll
  for (int v = 0; v < 2; ++v) {
    const int sub = 2 * s + (v ? 1 - th : th);
    csg[v] = *(const f4_t*)(smem + C_CS + (16 * sub + 4 * kg) * 4);
    csu[v] = *(const f4_t*)(smem + C_CS + 256 + (16 * sub + 4 * kg) * 4);
  }
#pragma unroll
  for (int u = 0; u < 8; ++u) {
    const int tile = (u == 0) ? 7 : (th ? 7 + u : u - 1);
    st[u] = *(const f2_t*)(smem + C_STAT + (16 * tile + fr) * 8);
  }
  mma(F1);
  SB;
  if (w == 0) STAMP(2, 2);
  char* hh = ws + OFF_HH + (size_t)(2 * jline + s) * 1024 + lane * 16;
#pragma unroll
  for (int u = 0; u < 8; ++u) {
    const int tile = (u == 0) ? 7 : (th ? 7 + u : u - 1);
    const float m = st[u].x, r2 = st[u].y * st[u].y;
    const int t16 = 15 * panel + tile;
    u2_t o[2];
#pragma unroll
    for (int v = 0; v < 2; ++v) {
      if (u == 0 && v == 1) continue;
      f4_t g = ag[u][v] - m * csg[v], uu = au[u][v] - m * csu[v];
      f4_t r = g * uu * r2;
      o[v] = u2_t{pk_bf16(r[0], r[1]), pk_bf16(r[2], r[3])};
    }
    if (t16 < 128) {
      char* dst = hh + (size_t)t16 * (54 * 1024);
      if (u == 0) *(u2_t*)(dst + 8 * th) = o[0];
      else *(u4_t*)dst = th ? u4_t{o[1].x, o[1].y, o[0].x, o[0].y} : u4_t{o[0].x, o[0].y, o[1].x, o[1].y};
    }
  }
  if (w == 0) STAMP(2, 7);
}

constexpr int D_SLOT = 15360, D_NSLOT = 9, D_STEPS = 18;
constexpr int D_LDS = D_NSLOT * D_SLOT;

template <int NDMA>
__device__ __forceinline__ void d_loader(char* smem, const char* wp, int w4, int lane) {
  const unsigned lds0 = (unsigned)(size_t)(LAS char*)smem;
  const unsigned voff = lane * 16;
  auto issue = [&](int step, unsigned so) {
#pragma unroll
    for (int k = 0; k < NDMA; ++k) {
      const int i = w4 + 4 * k;
      glds16(wp + (size_t)step * D_SLOT + i * 1024, voff, lds0 + so + i * 1024);
    }
  };
  constexpr unsigned S = D_SLOT;
  issue(0, 0); issue(1, S);
  wait_vm<NDMA>(); bar();
  issue(2, 2 * S); issue(3, 3 * S); issue(4, 4 * S);
  wait_vm<3 * NDMA>(); bar();
  issue(5, 5 * S); issue(6, 6 * S); issue(7, 7 * S); issue(8, 8 * S);
  wait_vm<6 * NDMA>(); bar();
  issue(9, 0);
  unsigned sw = S;
#pragma unroll 1
  for (int b = 2; b < 10; ++b) {
    wait_vm<6 * NDMA>();
    bar();
    issue(b + 8, sw);
    sw += S; if (sw == D_NSLOT * S) sw = 0;
  }
  wait_vm<6 * NDMA>(); bar();
  wait_vm<5 * NDMA>(); bar();
  wait_vm<4 * NDMA>(); bar();
  wait_vm<3 * NDMA>(); bar();
  wait_vm<2 * NDMA>(); bar();
  wait_vm<1 * NDMA>(); bar();
  wait_vm<0>(); bar();
  bar();
}

__global__ __launch_bounds__(512, 2) void kD(char* __restrict__ ws, u16* __restrict__ out) {
  extern __shared__ __attribute__((aligned(16))) char smem[];
  const int tid = threadIdx.x, lane = tid & 63;
  const int w = __builtin_amdgcn_readfirstlane(tid >> 6);
  const int xcd = blockIdx.x & 7, rank = blockIdx.x >> 3;
  const int it = 8 * (xcd >> 1) + (rank & 7), dg = 4 * (xcd & 1) + (rank >> 3);
  if (w == 0) STAMP(3, 0);
  if (w >= 4) {
    const int w4 = w - 4;
    const char* wp = ws + OFF_WP2 + (size_t)dg * (54 * 5120);
    if (w4 < 3) d_loader<4>(smem, wp, w4, lane);
    else d_loader<3>(smem, wp, w4, lane);
    return;
  }
  const int fr = lane & 15, g = lane >> 4;
  const int t = 64 * it + 16 * w + fr;
  const char* hp = ws + OFF_HH + (size_t)(4 * it + w) * (54 * 1024);
  const unsigned voff = lane * 16;
  u4_t R[9];
#pragma unroll
  for (int i = 0; i < 9; ++i)
    asm volatile("global_load_dwordx4 %0, %1, %2" : "=v"(R[i]) : "v"(voff), "s"(hp + (size_t)i * 1024) : "memory");
  u2_t res[5];
#pragma unroll
  for (int dt = 0; dt < 5; ++dt) {
    const int d = 80 * dg + 16 * dt + 4 * g;
    const char* rp = ws + OFF_H8 + ((size_t)(d >> 3) * SL + t) * 16 + (d & 4) * 2;
    asm volatile("global_load_dwordx2 %0, %1, off" : "=v"(res[dt]) : "v"(rp) : "memory");
  }
  f4_t acc[5];
#pragma unroll
  for (int dt = 0; dt < 5; ++dt) acc[dt] = f4_t{0.f, 0.f, 0.f, 0.f};
  b8_t A0[5], A1[5];
  auto rdA = [&](b8_t (&A)[5], unsigned off) {
    const char* sp = smem + off + lane * 16;
#pragma unroll
    for (int dt = 0; dt < 5; ++dt) A[dt] = *(const b8_t*)(sp + dt * 1024);
  };
  bar();
  if (w == 0) STAMP(3, 1);
  rdA(A0, 0);
  unsigned so = 0;
  auto kstep = [&](auto mode, auto idx, b8_t (&Ac)[5], b8_t (&An)[5], const char* hnext) {
    constexpr int MODE = decltype(mode)::value, i = decltype(idx)::value, kk = i % 3;
    if (kk == 0) bar();
    unsigned nx = so + (kk + 1) * 5120;
    if (kk == 2) { so += D_SLOT; if (so == D_NSLOT * D_SLOT) so = 0; nx = so; }
    SB;
    if (!(MODE == 2 && i == 8)) rdA(An, nx);
    asm volatile("s_waitcnt vmcnt(%1)" : "+v"(R[i]) : "i"(MODE == 0 ? 13 : MODE == 1 ? 8 : 8 - i) : "memory");
    const b8_t B = __builtin_bit_cast(b8_t, R[i]);
#pragma unroll
    for (int dt = 0; dt < 5; ++dt) acc[dt] = __builtin_amdgcn_mfma_f32_16x16x32_bf16(Ac[dt], B, acc[dt], 0, 0, 0);
#pragma unroll
    for (int dt = 0; dt < 5; ++dt) {
      __builtin_amdgcn_sched_group_barrier(0x100, 1, 0);
      __builtin_amdgcn_sched_group_barrier(0x008, 1, 0);
    }
    SB;
    if (MODE != 2) asm volatile("global_load_dwordx4 %0, %1, %2" : "+v"(R[i]) : "v"(voff), "s"(hnext + (size_t)i * 1024) : "memory");
    SB;
  };
#define IC(n) std::integral_constant<int, n>{}
  auto round = [&](auto mode, b8_t (&P)[5], b8_t (&Q)[5], const char* hnext) {
    kstep(mode, IC(0), P, Q, hnext); kstep(mode, IC(1), Q, P, hnext); kstep(mode, IC(2), P, Q, hnext);
    kstep(mode, IC(3), Q, P, hnext); kstep(mode, IC(4), P, Q, hnext); kstep(mode, IC(5), Q, P, hnext);
    kstep(mode, IC(6), P, Q, hnext); kstep(mode, IC(7), Q, P, hnext); kstep(mode, IC(8), P, Q, hnext);
  };
  round(IC(0), A0, A1, hp + 9 * 1024);
#pragma unroll 1
  for (int r = 1; r < 5; r += 2) {
    round(IC(1), A1, A0, hp + (size_t)(9 * (r + 1)) * 1024);
    round(IC(1), A0, A1, hp + (size_t)(9 * (r + 2)) * 1024);
  }
  round(IC(2), A1, A0, hp);
  if (w == 0) STAMP(3, 2);
  asm volatile("s_waitcnt vmcnt(0)" : "+v"(res[0]), "+v"(res[1]), "+v"(res[2]), "+v"(res[3]), "+v"(res[4]) :: "memory");
#pragma unroll
  for (int dt = 0; dt < 5; ++dt) {
    const int d = 80 * dg + 16 * dt + 4 * g;
    const float h0 = bf_lo(res[dt].x), h1 = bf_hi(res[dt].x), h2 = bf_lo(res[dt].y), h3 = bf_hi(res[dt].y);
    *(u2_t*)(out + (size_t)t * DM + d) = u2_t{pk_bf16(h0 + acc[dt][0], h1 + acc[dt][1]), pk_bf16(h2 + acc[dt][2], h3 + acc[dt][3])};
  }
  if (w == 0) STAMP(3, 7);
}

extern "C" void kernel_launch(void* const* d_in, const int* in_sizes, int n_in, void* d_out, int out_size, void* d_ws,
                              size_t ws_size, hipStream_t stream) {
  const u16* x = (const u16*)d_in[0];
  char* ws = (char*)d_ws;
  kA<<<A_KBLK + A_XBLK, 256, A_LDS, stream>>>(x, (const u16*)d_in[1], (const u16*)d_in[2], (const u16*)d_in[3], (const u16*)d_in[4],
                                              (const u16*)d_in[5], (const u16*)d_in[6], (const u16*)d_in[7], ws);
  kB<<<256, 512, B_LDS, stream>>>((const u16*)d_in[8], (const u16*)d_in[10], (const u16*)d_in[11], (const u16*)d_in[12], ws);
  (void)hipFuncSetAttribute((const void*)kC, hipFuncAttributeMaxDynamicSharedMemorySize, C_LDS);
  kC<<<256, 512, C_LDS, stream>>>((const u16*)d_in[10], (const u16*)d_in[11], ws);
  (void)hipFuncSetAttribute((const void*)kD, hipFuncAttributeMaxDynamicSharedMemorySize, D_LDS);
  kD<<<256, 512, D_LDS, stream>>>(ws, (u16*)d_out);
}
```

```cpp
#include <hip/hip_runtime.h>
#include <hip/hip_bf16.h>
#include <stdint.h>
#include <type_traits>

typedef unsigned short u16;
typedef _Float16 h2_t __attribute__((ext_vector_type(2)));
typedef _Float16 h8_t __attribute__((ext_vector_type(8)));
typedef __bf16 b2_t __attribute__((ext_vector_type(2)));
typedef __bf16 b8_t __attribute__((ext_vector_type(8)));
typedef float f2_t __attribute__((ext_vector_type(2)));
typedef float f4_t __attribute__((ext_vector_type(4)));
typedef unsigned u2_t __attribute__((ext_vector_type(2)));
typedef unsigned u4_t __attribute__((ext_vector_type(4)));
typedef short s4_t __attribute__((ext_vector_type(4)));
typedef __attribute__((address_space(1))) unsigned long long gu64;
#define LAS __attribute__((address_space(3)))
#define SB __builtin_amdgcn_sched_barrier(0)
#define IC(n) std::integral_constant<int, n>{}

constexpr int SL = 2048;
constexpr int DM = 640;
constexpr int FF = 1728;

constexpr size_t OFF_X4 = 0;
constexpr size_t OFF_S1 = 0x280000;
constexpr size_t OFF_KD = 0x290000;
constexpr size_t OFF_INVS = 0x590000;
constexpr size_t OFF_H8 = 0x5A0000;
constexpr size_t OFF_HH = 0x820000;
constexpr size_t OFF_WP2 = 0xF00000;
constexpr size_t OFF_CS = 0x1120000;
constexpr size_t OFF_G2 = 0x1130000;
constexpr int KRS = 1344;
constexpr int KDS = 4864;
constexpr int KD_O = 2240;
constexpr int KD_DR = 4384;

#define STAMP(k, i)

__device__ __forceinline__ float bf_lo(unsigned d) { return __uint_as_float(d << 16); }
__device__ __forceinline__ float bf_hi(unsigned d) { return __uint_as_float(d & 0xffff0000u); }
__device__ __forceinline__ float bf2f(u16 v) { return __uint_as_float((unsigned)v << 16); }
__device__ __forceinline__ unsigned pk_bf16(float a, float b) {
  f2_t v = {a, b};
  return __builtin_bit_cast(unsigned, __builtin_convertvector(v, b2_t));
}
__device__ __forceinline__ unsigned pk_f16(float a, float b) {
  f2_t v = {a, b};
  return __builtin_bit_cast(unsigned, __builtin_convertvector(v, h2_t));
}
__device__ __forceinline__ float rbf(float a) { return bf_lo(pk_bf16(a, 0.f)); }
template <int N> __device__ __forceinline__ float dpp_ror_add(float v) {
  int r = __builtin_amdgcn_update_dpp(0, __float_as_int(v), 0x120 + N, 0xf, 0xf, false);
  return v + __int_as_float(r);
}
__device__ __forceinline__ float row16_sum(float v) {
  v = dpp_ror_add<8>(v); v = dpp_ror_add<4>(v); v = dpp_ror_add<2>(v); v = dpp_ror_add<1>(v);
  return v;
}
__device__ __forceinline__ float wave_sum(float v) {
  v = row16_sum(v);
  const int b = __float_as_int(v);
  return (__int_as_float(__builtin_amdgcn_readlane(b, 0)) + __int_as_float(__builtin_amdgcn_readlane(b, 16))) +
         (__int_as_float(__builtin_amdgcn_readlane(b, 32)) + __int_as_float(__builtin_amdgcn_readlane(b, 48)));
}
template <typename T> __device__ __forceinline__ T ldnt(const T* p) { return __builtin_nontemporal_load(p); }
__device__ __forceinline__ void wave_fence() {
  __builtin_amdgcn_fence(__ATOMIC_RELEASE, "wavefront");
  __builtin_amdgcn_wave_barrier();
  __builtin_amdgcn_fence(__ATOMIC_ACQUIRE, "wavefront");
}
__device__ __forceinline__ s4_t tr_read(const char* p) {
  return __builtin_amdgcn_ds_read_tr16_b64_v4i16((LAS s4_t*)p);
}
__device__ __forceinline__ void glds16(const void* sbase, unsigned voff, unsigned lds_addr) {
  asm volatile("s_mov_b32 m0, %2\n\ts_nop 0\n\tglobal_load_lds_dwordx4 %0, %1" ::"v"(voff), "s"(sbase), "s"(lds_addr) : "memory");
}
template <int N> __device__ __forceinline__ void wait_vm() { asm volatile("s_waitcnt vmcnt(%0)" ::"i"(N) : "memory"); }
__device__ __forceinline__ void bar() { asm volatile("s_barrier" ::: "memory"); }
__device__ __forceinline__ float dot2bf(unsigned a, unsigned b, float c) {
  return __builtin_amdgcn_fdot2_f32_bf16(__builtin_bit_cast(b2_t, a), __builtin_bit_cast(b2_t, b), c, false);
}

struct Cw { f2_t w, ws; };
__device__ __forceinline__ Cw mkw(f2_t w) { Cw r; r.w = w; r.ws = f2_t{-w.y, w.x}; return r; }
__device__ __forceinline__ f2_t cmulw(f2_t a, const Cw& W) {
  return __builtin_elementwise_fma(f2_t{a.y, a.y}, W.ws, f2_t{a.x, a.x} * W.w);
}
__device__ __forceinline__ f2_t cexp_l(float er, float wr, float l) {
  float mag = __builtin_amdgcn_exp2f(er * l);
  float rev = __builtin_amdgcn_fractf(wr * l);
  return f2_t{mag * __builtin_amdgcn_cosf(rev), mag * __builtin_amdgcn_sinf(rev)};
}
__device__ __forceinline__ float pow2_scale(float bound, int target) {
  unsigned e = (__float_as_uint(bound) >> 23) & 0xff;
  int k = target - ((int)e - 126);
  k = k > 126 ? 126 : (k < -126 ? -126 : k);
  return (bound > 0.f) ? __uint_as_float((unsigned)(k + 127) << 23) : 1.0f;
}
__device__ __forceinline__ float pow2_inv(float p) { return __uint_as_float(0x7f000000u - __float_as_uint(p)); }

constexpr int A_KBLK = 214;
constexpr int KG_R = 0, KG_L0 = 4352, KG_L1 = 8704, KG_HW = 8704 + 25 * 272;
constexpr int KG_CH = 2 * KG_HW + 16;
constexpr int A_SIDE = 3 * KG_CH;
constexpr int A_LDS = A_SIDE + 20736;

__device__ __forceinline__ void pack_wd(const u16* __restrict__ wd, char* __restrict__ ws, char* buf, int u, int lane) {
  const int ks = u >> 3, dg = u & 7;
#pragma unroll
  for (int i = 0; i < 5; ++i) {
    const int idx = i * 64 + lane, row = idx / 10, c = idx % 10;
    __builtin_amdgcn_global_load_lds((const unsigned*)((const char*)wd + (size_t)(32 * ks + row) * 1280 + 160 * dg + 16 * c),
                                     (LAS unsigned*)(buf + i * 1024), 16, 0, 2);
  }
  asm volatile("s_waitcnt vmcnt(0)" ::: "memory");
  wave_fence();
  const int g = lane >> 4, q = (lane & 15) >> 2, p = lane & 3;
  char* dst = ws + OFF_WP2 + ((size_t)(dg * 54 + ks) * 5) * 1024 + lane * 16;
#pragma unroll
  for (int dt = 0; dt < 5; ++dt) {
    s4_t aa = tr_read(buf + (4 * g + q) * 160 + 32 * dt + 8 * p);
    s4_t bb = tr_read(buf + (16 + 4 * g + q) * 160 + 32 * dt + 8 * p);
    u2_t ua = __builtin_bit_cast(u2_t, aa), ub = __builtin_bit_cast(u2_t, bb);
    *(u4_t*)(dst + dt * 1024) = u4_t{ua.x, ua.y, ub.x, ub.y};
  }
}

__global__ __launch_bounds__(1024) void kA(const u16* __restrict__ x, const u16* __restrict__ Are, const u16* __restrict__ Aim,
                                           const u16* __restrict__ Bre, const u16* __restrict__ Bim, const u16* __restrict__ Cre,
                                           const u16* __restrict__ Cim, const u16* __restrict__ logdt, const u16* __restrict__ wd,
                                           char* __restrict__ ws) {
  extern __shared__ __attribute__((aligned(16))) char smem[];
  const int tid = threadIdx.x, lane = tid & 63;
  const int w = __builtin_amdgcn_readfirstlane(tid >> 6);
  if (w == 0) STAMP(0, 0);
  if (blockIdx.x >= A_KBLK) {
    const int u = 344 + (blockIdx.x - A_KBLK) * 16 + w;
    if (u < 432) pack_wd(wd, ws, smem + w * 5120, u, lane);
    if (w == 0) STAMP(0, 7);
    return;
  }
  if (w >= 12) {
    const int w4 = w - 12;
    if (blockIdx.x >= 128) {
      pack_wd(wd, ws, smem + A_SIDE + w4 * 5120, (blockIdx.x - 128) * 4 + w4, lane);
      bar();
      return;
    }
    const int t0 = blockIdx.x * 16;
    const int r = lane >> 4, c = lane & 15, rl = w4 * 4 + r;
    char* xs = smem + A_SIDE;
    const u4_t* p = (const u4_t*)(x + (size_t)(t0 + rl) * DM);
    u4_t v[5];
#pragma unroll
    for (int i = 0; i < 5; ++i) v[i] = ldnt(p + c + 16 * i);
    if (w4 == 0 && lane < 16) ((unsigned long long*)(ws + OFF_G2))[t0 + lane] = 0ull;
    float s = 0.f;
#pragma unroll
    for (int i = 0; i < 5; ++i)
#pragma unroll
      for (int j = 0; j < 4; ++j) s += bf_lo(v[i][j]) + bf_hi(v[i][j]);
#pragma unroll
    for (int i = 0; i < 5; ++i) *(u4_t*)(xs + rl * 1296 + (c + 16 * i) * 16) = v[i];
    const float mean = row16_sum(s) * (1.0f / DM);
    float q = 0.f;
#pragma unroll
    for (int i = 0; i < 5; ++i)
#pragma unroll
      for (int j = 0; j < 4; ++j) {
        float a = bf_lo(v[i][j]) - mean, b = bf_hi(v[i][j]) - mean;
        q += a * a; q += b * b;
      }
    const float var = row16_sum(q) * (1.0f / DM);
    if (c == 0) {
      float mb = rbf(mean), sd = rbf(sqrtf(rbf(rbf(var) + 1e-5f)));
      ((unsigned*)(ws + OFF_S1))[t0 + rl] = (__float_as_uint(mb) >> 16) | (__float_as_uint(sd) & 0xffff0000u);
    }
    __syncthreads();
    const int t4 = w4 * 64 + lane;
#pragma unroll
    for (int it = 0; it < 5; ++it) {
      const int item = t4 + 256 * it, q4 = item >> 3, tp = item & 7;
      u2_t a = *(const u2_t*)(xs + (2 * tp) * 1296 + 8 * q4);
      u2_t b = *(const u2_t*)(xs + (2 * tp + 1) * 1296 + 8 * q4);
      *(u4_t*)(ws + OFF_X4 + ((size_t)q4 * SL + t0 + 2 * tp) * 8) = u4_t{a.x, a.y, b.x, b.y};
    }
    if (w4 == 0) STAMP(0, 6);
    return;
  }
  const int cl = w >> 2, part = (w >> 1) & 1, hw = w & 1;
  const int chr = blockIdx.x * 3 + cl, ch = min(chr, DM - 1);
  char* chb = smem + cl * KG_CH;
  char* hb = chb + hw * KG_HW;
  const int n = ch * 128 + hw * 64 + lane;
  const u16 i_are = ldnt(Are + n), i_aim = ldnt(Aim + n), i_bre = ldnt(Bre + n), i_bim = ldnt(Bim + n), i_cre = ldnt(Cre + n), i_cim = ldnt(Cim + n);
  const float dt = rbf(expf(bf2f(logdt[ch])));
  const float are = dt * bf2f(i_are), aim = dt * bf2f(i_aim);
  const float bre = dt * bf2f(i_bre), bim = dt * bf2f(i_bim);
  const float cre = bf2f(i_cre), cim = bf2f(i_cim);
  f2_t c = {cre * bre - cim * bim, cre * bim + cim * bre};
  const float er = are * 1.44269504088896341f, wr = aim * 0.15915494309189535f;
  const float bnd = (fabsf(c.x) + fabsf(c.y)) * fmaxf(1.0f, __builtin_amdgcn_exp2f(er * 2048.f));
  const float bsum = wave_sum(bnd);
  if (w == 0) STAMP(0, 1);
  const float Sw = pow2_scale(bsum, 15);
  c *= Sw;
  const Cw z1 = mkw(cexp_l(er, wr, 1.f)), w128 = mkw(cexp_l(er, wr, 128.f));
  auto put = [&](char* buf, int row, f2_t v) { *(unsigned*)(buf + row * 272 + 4 * lane) = pk_f16(v.x, -v.y); };
  if (part == 0) {
    const Cw w8 = mkw(cexp_l(er, wr, 8.f));
    f2_t R = w8.w, v0 = c, v1 = cmulw(c, w128);
    *(unsigned*)(hb + KG_R + 4 * lane) = pk_f16(1.f, 0.f);
#pragma unroll
    for (int i = 0; i < 8; ++i) {
      *(unsigned*)(hb + KG_R + (2 * i + 1) * 272 + 4 * lane) = pk_f16(R.x, R.y);
      if (i < 7) {
        R = cmulw(R, w8);
        *(unsigned*)(hb + KG_R + (2 * i + 2) * 272 + 4 * lane) = pk_f16(R.x, R.y);
        R = cmulw(R, w8);
      }
      put(hb + KG_L0, i, v0); put(hb + KG_L0, 8 + i, v1);
      if (i < 7) { v0 = cmulw(v0, z1); v1 = cmulw(v1, z1); }
    }
  } else {
    const Cw z2 = mkw(cmulw(z1.w, z1));
    const Cw z4 = mkw(cmulw(z2.w, z2));
    f2_t T = cmulw(c, w128);
#pragma unroll
    for (int i1 = 2; i1 < 17; ++i1) {
      T = cmulw(T, w128);
      if (i1 < 4) {
        f2_t v = T;
#pragma unroll
        for (int i0 = 0; i0 < 4; ++i0) { put(hb + KG_L1, 4 * (i1 - 2) + i0, v); if (i0 < 3) v = cmulw(v, z2); }
      } else if (i1 < 8) {
        put(hb + KG_L1, 8 + 2 * (i1 - 4), T); put(hb + KG_L1, 8 + 2 * (i1 - 4) + 1, cmulw(T, z4));
      } else {
        put(hb + KG_L1, 16 + (i1 - 8), T);
      }
    }
  }
  if (part == 0 && lane == 0) ((float*)(chb + 2 * KG_HW))[hw] = bsum;
  __syncthreads();
  if (w == 0) STAMP(0, 2);
  if (part == 1 && hw == 1) return;
  const int tile = part ? 1 : 2 * hw;
  const int fr = lane & 15, kg = lane >> 4;
  const int row = (tile == 2) ? 16 + min(fr, 8) : fr;
  const int lo = (tile == 0) ? KG_L0 : KG_L1;
  f4_t acc[2];
#pragma unroll
  for (int h = 0; h < 2; ++h) {
    const char* hbb = chb + h * KG_HW;
    f4_t a = {0.f, 0.f, 0.f, 0.f};
#pragma unroll
    for (int ks = 0; ks < 4; ++ks) {
      const h8_t Bf = *(const h8_t*)(hbb + KG_R + fr * 272 + 64 * ks + 16 * kg);
      const h8_t Af = *(const h8_t*)(hbb + lo + row * 272 + 64 * ks + 16 * kg);
      a = __builtin_amdgcn_mfma_f32_16x16x32_f16(Af, Bf, a, 0, 0, 0);
    }
    acc[h] = a;
  }
  const float b0 = ((const float*)(chb + 2 * KG_HW))[0], b1 = ((const float*)(chb + 2 * KG_HW))[1];
  const float Sc = pow2_scale(b0 + b1, 15);
  const f4_t kv = acc[0] * (pow2_inv(pow2_scale(b0, 15)) * Sc) + acc[1] * (pow2_inv(pow2_scale(b1, 15)) * Sc);
  const unsigned p01 = pk_f16(kv[0], kv[1]), p23 = pk_f16(kv[2], kv[3]);
  char* kr = ws + OFF_KD + (size_t)ch * KRS;
  if (tile == 0) {
    *(u2_t*)(kr + 2 * (128 * (kg >> 1) + 4 * (kg & 1) + 8 * fr)) = u2_t{p01, p23};
    if (lane == 0) ((float*)(ws + OFF_INVS))[ch] = pow2_inv(Sc);
  } else if (tile == 1) {
    if (kg < 2) {
      *(u2_t*)(kr + 2 * (256 + 64 * kg + 4 * fr)) = u2_t{p01, p23};
    } else {
      *(unsigned*)(kr + 2 * (384 + 64 * (kg - 2) + 2 * fr)) = p01;
      *(unsigned*)(kr + 2 * (384 + 64 * (kg - 2) + 32 + 2 * fr)) = p23;
    }
  } else {
    u16* k3 = (u16*)kr + 512 + 64 * kg + fr;
    if (kg < 2) { k3[0] = (u16)p01; k3[16] = (u16)(p01 >> 16); k3[32] = (u16)p23; k3[48] = (u16)(p23 >> 16); }
    else if (lane == 32) k3[0] = (u16)p01;
  }
  if (w == 0) STAMP(0, 7);
}

constexpr int B_CONV = 160;
constexpr int B_KOFF = 0;
constexpr int B_UOFF = 4 * KDS;
constexpr int B_UCH = 4608;
constexpr int B_YCH = 10240;
constexpr int B_LDS = 40960;
#define B_DELAY 0

struct BFrag { h8_t B[8]; h8_t A[5]; };

__global__ __launch_bounds__(512) void kB(const u16* __restrict__ dskip, const u16* __restrict__ wg, const u16* __restrict__ wu,
                                          char* __restrict__ ws) {
  extern __shared__ __attribute__((aligned(16))) char smem[];
  const int tid = threadIdx.x, lane = tid & 63;
  const int w = __builtin_amdgcn_readfirstlane(tid >> 6);
  if (w == 0) STAMP(1, 0);
  if (blockIdx.x >= B_CONV) {
    const int xcd = blockIdx.x & 7, rk = (blockIdx.x - B_CONV) >> 3;
    const int g4 = xcd >> 1, a = xcd & 1, nl = (g4 < 3) ? 7 : 6;
    if (rk >= nl) return;
#pragma unroll
    for (int i = 0; i < B_DELAY; ++i) __builtin_amdgcn_s_sleep(16);
    const int jl = 7 * g4 + rk;
    const char* pm = (const char*)(a ? wu : wg) + (size_t)(80 * w + (lane >> 3)) * (FF * 2) + 128 * jl + (lane & 7) * 16;
    u4_t v[10];
#pragma unroll
    for (int i = 0; i < 10; ++i) v[i] = ldnt((const u4_t*)(pm + (size_t)i * 8 * (FF * 2)));
    const char* po = (const char*)(a ? wg : wu) + (size_t)(80 * w) * (FF * 2) + 128 * jl;
    unsigned t0v, t1v;
    asm volatile("global_load_dword %0, %1, off nt" : "=v"(t0v) : "v"(po + (size_t)lane * (FF * 2)) : "memory");
    asm volatile("global_load_dword %0, %1, off nt" : "=v"(t1v) : "v"(po + (size_t)(64 + (lane & 15)) * (FF * 2)) : "memory");
    f2_t s[4];
#pragma unroll
    for (int e = 0; e < 4; ++e) s[e] = f2_t{0.f, 0.f};
#pragma unroll
    for (int i = 0; i < 10; ++i)
#pragma unroll
      for (int jj = 0; jj < 4; ++jj) s[jj] += f2_t{bf_lo(v[i][jj]), bf_hi(v[i][jj])};
    float* pp = (float*)smem;
    *(f4_t*)(pp + (w * 8 + (lane >> 3)) * 68 + (lane & 7) * 8) = f4_t{s[0].x, s[0].y, s[1].x, s[1].y};
    *(f4_t*)(pp + (w * 8 + (lane >> 3)) * 68 + (lane & 7) * 8 + 4) = f4_t{s[2].x, s[2].y, s[3].x, s[3].y};
    __syncthreads();
    float acc = 0.f;
#pragma unroll
    for (int i = 0; i < 8; ++i) acc += pp[(w * 8 + i) * 68 + lane];
    __syncthreads();
    pp[w * 64 + lane] = acc;
    __syncthreads();
    if (w == 0) {
      float t = 0.f;
#pragma unroll
      for (int i = 0; i < 8; ++i) t += pp[i * 64 + lane];
      ((float*)(ws + OFF_CS))[a * FF + 64 * jl + lane] = t;
    }
    asm volatile("s_waitcnt vmcnt(0)" ::"v"(t0v), "v"(t1v) : "memory");
    if (w == 0) STAMP(1, 7);
    return;
  }
  const int a_ = blockIdx.x >> 3, x_ = blockIdx.x & 7;
  const int q = 16 * (a_ >> 1) + 2 * x_ + (a_ & 1);
  u4_t xv[2]; u2_t sv[2];
#pragma unroll
  for (int it = 0; it < 2; ++it) {
    const int j = tid + 512 * it;
    xv[it] = *(const u4_t*)(ws + OFF_X4 + ((size_t)q * SL + 2 * j) * 8);
    sv[it] = *(const u2_t*)(ws + OFF_S1 + 8 * j);
  }
  const int kch = tid >> 7, kc = tid & 127;
  const char* kr = ws + OFF_KD + (size_t)(4 * q + kch) * KRS;
  const int e0 = (kc < 32) ? 8 * kc : (kc < 64) ? 256 + 4 * (kc - 32) : 384 + 2 * (kc - 64);
  const u4_t kd4 = *(const u4_t*)(kr + 2 * e0);
  const unsigned kd5 = *(const unsigned*)(kr + 2 * e0 + 16);
  const unsigned kfirst = *(const u16*)kr, k1024 = *(const u16*)(kr + 2 * 512);
  const int dl = 127 - kc;
  const unsigned kfa = *(const u16*)(kr + 2 * (512 + max(2 * dl - 127, 0))), kfb = *(const u16*)(kr + 2 * (512 + max(2 * dl - 126, 0)));
  float dk[4];
#pragma unroll
  for (int c = 0; c < 4; ++c) dk[c] = bf2f(dskip[4 * q + c]);
  const float invs = ((const float*)(ws + OFF_INVS))[4 * q + (w & 3)];
  *(unsigned*)(smem + B_UOFF + (tid >> 7) * B_UCH + 4096 + 4 * (tid & 127)) = 0u;
  const int c = lane & 15, kg = lane >> 4;
  h8_t band9, band7;
#pragma unroll
  for (int j = 0; j < 8; ++j) {
    const int k = 8 * kg + 7 - j;
    band9[j] = (k >= c + 4 && k <= c + 12) ? (_Float16)1.0f : (_Float16)0.0f;
    band7[j] = (k >= c + 5 && k <= c + 11) ? (_Float16)1.0f : (_Float16)0.0f;
  }
  {
    char* ki = smem + B_KOFF + kch * KDS;
    const unsigned k0 = kd4[0] & 0xffffu;
    unsigned o0, o1, o2, o3, nx;
    if (kc < 32) { o0 = kd4[0]; o1 = kd4[1]; o2 = kd4[2]; o3 = kd4[3]; nx = kd5; }
    else if (kc < 64) {
      const unsigned k2 = kd4[0] >> 16, k4 = kd4[1] & 0xffffu, k6 = kd4[1] >> 16, k8 = kd4[2] & 0xffffu;
      o0 = k0 * 0x10001u; o1 = k2 | (k4 << 16); o2 = k4 * 0x10001u; o3 = k6 | (k8 << 16); nx = k8;
    } else {
      const unsigned k4 = kd4[0] >> 16, k8 = kd4[1] & 0xffffu;
      o0 = k0 * 0x10001u; o1 = k0 | (k4 << 16); o2 = k4 * 0x10001u; o3 = k8 * 0x10001u; nx = k8;
    }
    *(u4_t*)(ki + 16 * (kc + 2)) = u4_t{o0, o1, o2, o3};
    *(u4_t*)(ki + KD_O + 16 * (kc + 2)) = u4_t{__builtin_amdgcn_alignbit(o1, o0, 16), __builtin_amdgcn_alignbit(o2, o1, 16),
                                              __builtin_amdgcn_alignbit(o3, o2, 16), __builtin_amdgcn_alignbit(nx, o3, 16)};
    if (kc < 6) {
      const int cp = (kc < 2) ? kc : 128 + kc;
      const unsigned kk = k1024 * 0x10001u;
      const u4_t e = (kc == 2) ? u4_t{kk, kk, k1024, 0u} : u4_t{0u, 0u, 0u, 0u};
      const u4_t o = (kc == 2) ? u4_t{kk, kk, 0u, 0u} : u4_t{0u, 0u, 0u, (kc == 1) ? (kfirst << 16) : 0u};
      *(u4_t*)(ki + 16 * cp) = e;
      *(u4_t*)(ki + KD_O + 16 * cp) = o;
    }
    if (kc < 80) *(unsigned*)(ki + KD_DR + 4 * kc) = (dl >= 64) ? (kfa | (kfb << 16)) : 0u;
  }
  float z[2][8];
  if (w == 0) { STAMP(1, 1); }
#pragma unroll
  for (int it = 0; it < 2; ++it) {
    const int j = tid + 512 * it;
    const float m0 = bf_lo(sv[it].x), r0 = __builtin_amdgcn_rcpf(bf_hi(sv[it].x)), m1 = bf_lo(sv[it].y), r1 = __builtin_amdgcn_rcpf(bf_hi(sv[it].y));
    float x0[4] = {bf_lo(xv[it].x), bf_hi(xv[it].x), bf_lo(xv[it].y), bf_hi(xv[it].y)};
    float x1[4] = {bf_lo(xv[it].z), bf_hi(xv[it].z), bf_lo(xv[it].w), bf_hi(xv[it].w)};
#pragma unroll
    for (int cc = 0; cc < 4; ++cc) {
      const float u0 = (x0[cc] - m0) * r0, u1 = (x1[cc] - m1) * r1;
      *(unsigned*)(smem + B_UOFF + cc * B_UCH + 4 * (1023 - j)) = pk_f16(u1, u0);
      z[it][cc] = x0[cc] + u0 * dk[cc];
      z[it][4 + cc] = x1[cc] + u1 * dk[cc];
    }
  }
  __syncthreads();
  if (w == 0) STAMP(1, 2);
  f4_t acc[8];
  if (w < 4) {
#pragma unroll
    for (int t = 0; t < 8; ++t) acc[t] = f4_t{0.f, 0.f, 0.f, 0.f};
    const char* ub = smem + B_UOFF + w * B_UCH + 4112 - 32 * c - 16 * kg - 512 * 7;
    const char* kb = smem + B_KOFF + w * KDS + ((c & 1) ? 0 : KD_O) + 4 * (12 + ((c + 1) >> 1) - 4 * kg);
    auto load = [&](BFrag& F, int i0) {
#pragma unroll
      for (int i1 = 0; i1 < 5; ++i1) {
        if (i1 == 4 && i0 != 0) continue;
        const unsigned* ap = (const unsigned*)(kb + 64 * i0 + 512 * i1);
        F.A[i1] = __builtin_bit_cast(h8_t, (u4_t{ap[0], ap[1], ap[2], ap[3]}));
      }
#pragma unroll
      for (int p = 0; p < 8; ++p) F.B[p] = *(const h8_t*)(ub + 512 * (7 - p) + 64 * i0);
    };
    auto mma = [&](const BFrag& F, int i0) {
#pragma unroll
      for (int i1 = 0; i1 < 5; ++i1) {
        if (i1 == 4 && i0 != 0) continue;
#pragma unroll
        for (int t = i1; t < 8; ++t) acc[t] = __builtin_amdgcn_mfma_f32_16x16x32_f16(F.A[i1], F.B[t - i1], acc[t], 0, 0, 0);
      }
    };
    auto interleave = [&]() {
#pragma unroll
      for (int i = 0; i < 16; ++i) {
        __builtin_amdgcn_sched_group_barrier(0x008, 1, 0);
        __builtin_amdgcn_sched_group_barrier(0x100, 1, 0);
      }
      __builtin_amdgcn_sched_group_barrier(0x008, 14, 0);
    };
    BFrag F0, F1;
    load(F0, 0);
    SB;
#pragma unroll
    for (int i0 = 0; i0 < 6; i0 += 2) {
      load(F1, i0 + 1);
      mma(F0, i0);
      interleave();
      SB;
      load(F0, i0 + 2);
      mma(F1, i0 + 1);
      interleave();
      SB;
    }
    load(F1, 7);
    mma(F0, 6);
    interleave();
    SB;
    h8_t Tf[4], U9[4], U7[4];
    const unsigned* dr = (const unsigned*)(smem + B_KOFF + w * KDS + KD_DR) + (127 - c + 4 * kg);
    const char* uw = smem + B_UOFF + w * B_UCH + 4112 - 32 * c - 16 * kg;
#pragma unroll
    for (int bt = 0; bt < 4; ++bt) { U9[bt] = *(const h8_t*)(uw - 512 * bt + 16); U7[bt] = *(const h8_t*)(uw - 512 * bt); }
#pragma unroll
    for (int dl = 0; dl < 4; ++dl) {
      const unsigned* tp = dr - 16 * (dl + 4);
      Tf[dl] = __builtin_bit_cast(h8_t, (u4_t{tp[0], tp[1], tp[2], tp[3]}));
    }
    mma(F1, 7);
    interleave();
    SB;
    f4_t X9[4], X7[4];
#pragma unroll
    for (int bt = 0; bt < 4; ++bt) {
      const f4_t zz = {0.f, 0.f, 0.f, 0.f};
      X9[bt] = __builtin_amdgcn_mfma_f32_16x16x32_f16(U9[bt], band9, zz, 0, 0, 0);
      X7[bt] = __builtin_amdgcn_mfma_f32_16x16x32_f16(U7[bt], band7, zz, 0, 0, 0);
    }
#pragma unroll
    for (int bt = 0; bt < 4; ++bt) {
      u4_t vu = {pk_f16(X7[bt][0], X9[bt][0]), pk_f16(X7[bt][1], X9[bt][1]), pk_f16(X7[bt][2], X9[bt][2]), pk_f16(X7[bt][3], X9[bt][3])};
      h8_t Vf = __builtin_bit_cast(h8_t, vu);
#pragma unroll
      for (int at = 4 + bt; at < 8; ++at) acc[at] = __builtin_amdgcn_mfma_f32_16x16x32_f16(Vf, Tf[at - bt - 4], acc[at], 0, 0, 0);
    }
  }
  if (w == 0) STAMP(1, 3);
  __syncthreads();
  if (w < 4) {
#pragma unroll
    for (int t = 0; t < 8; ++t) *(f4_t*)(smem + w * B_YCH + t * 1280 + kg * 320 + c * 16) = acc[t] * invs;
  }
  __syncthreads();
#pragma unroll
  for (int it = 0; it < 2; ++it) {
    const int j = tid + 512 * it, t = 2 * j;
    const int yo = (t >> 8) * 1280 + ((t >> 2) & 3) * 320 + ((t >> 4) & 15) * 16 + (t & 3) * 4;
    float h0[4], h1[4];
#pragma unroll
    for (int cc = 0; cc < 4; ++cc) {
      f2_t y = *(const f2_t*)(smem + cc * B_YCH + yo);
      h0[cc] = z[it][cc] + y.x;
      h1[cc] = z[it][4 + cc] + y.y;
    }
    char* dst = ws + OFF_H8 + ((size_t)(q >> 1) * SL + t) * 16 + (q & 1) * 8;
    *(u2_t*)dst = u2_t{pk_bf16(h0[0], h0[1]), pk_bf16(h0[2], h0[3])};
    *(u2_t*)(dst + 16) = u2_t{pk_bf16(h1[0], h1[1]), pk_bf16(h1[2], h1[3])};
  }
  if (w == 0) STAMP(1, 7);
}

constexpr int C_T = 240;
constexpr int C_SLOT = 23552, C_NSLOT = 6, C_GOFF = 15360;
constexpr int C_STAT = C_NSLOT * C_SLOT;
constexpr int C_CS = C_STAT + 960;
constexpr int C_LDS = C_CS + 512;
constexpr int C_STEPS = 20;
constexpr size_t OFF_DUMMY = 0x1140000;

__device__ __forceinline__ unsigned ln_pack(float sum, float sq) {
  const float mean = sum * (1.0f / DM);
  const float var = fmaxf(sq * (1.0f / DM) - mean * mean, 0.f);
  const float mb = rbf(mean), sd = rbf(sqrtf(rbf(rbf(var) + 1e-5f)));
  return (__float_as_uint(mb) >> 16) | (__float_as_uint(sd) & 0xffff0000u);
}
__device__ __forceinline__ unsigned ln_row(const char* h8, int t) {
  float s = 0.f, q = 0.f;
  for (int pl = 0; pl < 80; ++pl) {
    const u4_t v = *(const u4_t*)(h8 + (size_t)pl * 32768 + t * 16);
    for (int j = 0; j < 4; ++j) { const float a = bf_lo(v[j]), b = bf_hi(v[j]); s += a + b; q += a * a + b * b; }
  }
  return ln_pack(s, q);
}

template <int NDMA>
__device__ __forceinline__ void c_loader(char* smem, char* ws, const char* wbase, int w4, int lane, int t0, int jline, int xcd) {
  const unsigned lds0 = (unsigned)(size_t)(LAS char*)smem;
  const char* h8 = ws + OFF_H8;
  unsigned voffH[4], voffW[2];
#pragma unroll
  for (int k = 0; k < 2; ++k) {
    const int r = 8 * (2 * (w4 & 1) + k) + (lane >> 3), c = lane & 7;
    const int f = ((r >> 1) & 1) | ((r >> 2) & 2);
    voffW[k] = r * (FF * 2) + 128 * jline + (((c >> 1) ^ f) * 32) + (c & 1) * 16;
  }
#pragma unroll
  for (int k = 0; k < 4; ++k) {
    const int idx = 64 * (w4 + 4 * k) + lane, pl = idx / C_T, tt = idx % C_T;
    const int t = min(t0 + tt, SL - 1);
    voffH[k] = pl * 32768 + t * 16;
  }
  const unsigned ldsW = C_GOFF + (w4 >> 1) * 4096 + (w4 & 1) * 2048;
  auto issue = [&](int step, unsigned so) {
#pragma unroll
    for (int k = 0; k < 2; ++k) glds16(wbase + (size_t)step * (32 * FF * 2), voffW[k], lds0 + so + ldsW + k * 1024);
#pragma unroll
    for (int k = 0; k < NDMA - 2; ++k) glds16(h8 + (size_t)step * (4 * 32768), voffH[k], lds0 + so + (w4 + 4 * k) * 1024);
  };
  constexpr unsigned S = C_SLOT;
  issue(0, 0); issue(1, S); issue(2, 2 * S); issue(3, 3 * S);
  wait_vm<3 * NDMA>(); bar();
  issue(4, 4 * S); issue(5, 5 * S);
  wait_vm<4 * NDMA>(); bar();
  unsigned sw = 0;
#pragma unroll 1
  for (int b = 0; b < 13; ++b) {
    issue(b + 6, sw);
    sw += S; if (sw == C_NSLOT * S) sw = 0;
    wait_vm<4 * NDMA>();
    bar();
  }
  issue(19, sw);
  wait_vm<4 * NDMA>(); bar();
  const int tl = min(60 * w4 + lane, C_T - 1), tg = min(t0 + tl, SL - 1);
  const gu64* gp = (const gu64*)(ws + OFF_G2) + tg;
  const float* csp = (const float*)(ws + OFF_CS) + ((lane >> 4) & 1) * FF + 64 * jline + 16 * w4 + (lane & 15);
  const char* tb = ws + OFF_WP2 + (size_t)(4 * (xcd & 1)) * (54 * 5120);
  const int tl0 = ((int)(blockIdx.x >> 3) * 4 + w4) * 68;
  u2_t gv; float csv; unsigned tv0, tv1;
  asm volatile("global_load_dwordx2 %0, %1, off sc1" : "=v"(gv) : "v"(gp) : "memory");
  asm volatile("global_load_dword %0, %1, off" : "=v"(csv) : "v"(csp) : "memory");
  asm volatile("global_load_dword %0, %1, off" : "=v"(tv0) : "v"(tb + (size_t)min(tl0 + lane, 8639) * 128) : "memory");
  asm volatile("global_load_dword %0, %1, off" : "=v"(tv1) : "v"(tb + (size_t)min(tl0 + 64 + (lane & 3), 8639) * 128) : "memory");
  wait_vm<3 * NDMA + 4>(); bar();
  wait_vm<2 * NDMA + 4>(); bar();
  wait_vm<1 * NDMA + 4>(); bar();
  asm volatile("s_waitcnt vmcnt(0)" : "+v"(gv), "+v"(csv), "+v"(tv0), "+v"(tv1)::"memory");
  bar();
  {
    bool ok = gv.y == 1u;
    for (int spin = 0; spin < 4096 && !__all(ok); ++spin) {
      const unsigned long long g = __hip_atomic_load(gp, __ATOMIC_RELAXED, __HIP_MEMORY_SCOPE_AGENT);
      gv.x = (unsigned)g; ok = (unsigned)(g >> 32) == 1u;
      __builtin_amdgcn_s_sleep(2);
    }
    if (!__all(ok)) gv.x = ln_row(h8, tg);
    if (lane < 60) *(float*)(smem + C_STAT + (60 * w4 + lane) * 4) = bf_lo(gv.x);
    if (lane < 32) *(float*)(smem + C_CS + (lane >> 4) * 256 + (16 * w4 + (lane & 15)) * 4) = csv;
  }
  asm volatile("s_waitcnt lgkmcnt(0)" ::: "memory");
  bar();
}

struct CFrag { b8_t A[2][2]; b8_t B[8]; };

__global__ __launch_bounds__(512, 2) void kC(const u16* __restrict__ wg, const u16* __restrict__ wu, char* __restrict__ ws) {
  extern __shared__ __attribute__((aligned(16))) char smem[];
  const int tid = threadIdx.x, lane = tid & 63;
  const int w = __builtin_amdgcn_readfirstlane(tid >> 6);
  int panel, jline;
  const int xcd = blockIdx.x & 7, rank = blockIdx.x >> 3;
  {
    const int g4 = xcd >> 1, a = xcd & 1, nl = (g4 < 3) ? 7 : 6, total = 9 * nl, n0 = (total + 1) >> 1;
    if (a == 0 ? rank >= n0 : rank >= total - n0) {
      const int sp = (xcd < 6) ? (xcd >> 1) : 3 + 5 * (xcd - 6) + (rank - 27);
      const int wv = sp * 8 + w;
      if (w == 0) STAMP(2, 0);
      for (int tile = wv; tile < 128; tile += 104) {
        const int t = 16 * tile + (lane & 15);
        const char* hp = ws + OFF_H8 + (size_t)(lane >> 4) * 32768 + t * 16;
        u4_t v[20];
#pragma unroll
        for (int i = 0; i < 20; ++i) v[i] = *(const u4_t*)(hp + (size_t)i * (4 * 32768));
        float s0 = 0.f, s1 = 0.f, q0 = 0.f, q1 = 0.f;
        unsigned k_both = 0x3f803f80u;
        asm volatile("" : "+s"(k_both));
#pragma unroll
        for (int i = 0; i < 20; ++i) {
          s0 = dot2bf(v[i][0], k_both, s0); q0 = dot2bf(v[i][0], v[i][0], q0);
          s1 = dot2bf(v[i][1], k_both, s1); q1 = dot2bf(v[i][1], v[i][1], q1);
          s0 = dot2bf(v[i][2], k_both, s0); q0 = dot2bf(v[i][2], v[i][2], q0);
          s1 = dot2bf(v[i][3], k_both, s1); q1 = dot2bf(v[i][3], v[i][3], q1);
        }
        float s = s0 + s1, q = q0 + q1;
        s += __shfl_xor(s, 16); q += __shfl_xor(q, 16);
        s += __shfl_xor(s, 32); q += __shfl_xor(q, 32);
        if (lane < 16)
          __hip_atomic_store((gu64*)(ws + OFF_G2) + t, (1ull << 32) | ln_pack(s, q), __ATOMIC_RELAXED, __HIP_MEMORY_SCOPE_AGENT);
      }
      if (w == 0) STAMP(2, 7);
      return;
    }
    const int idx = rank + a * n0;
    panel = idx / nl; jline = 7 * g4 + idx % nl;
  }
  const int t0 = panel * C_T;
  if (w == 0) STAMP(2, 0);
  if (w >= 4) {
    const int w4 = w - 4;
    const char* wbase = (const char*)((w4 >> 1) ? wu : wg);
    if (w4 < 3) c_loader<6>(smem, ws, wbase, w4, lane, t0, jline, xcd);
    else c_loader<5>(smem, ws, wbase, w4, lane, t0, jline, xcd);
    return;
  }
  const int s = w >> 1, th = w & 1;
  const int fr = lane & 15, kg = lane >> 4, q = (lane & 15) >> 2, p = lane & 3;
  const unsigned hb0 = kg * 3840 + fr * 16 + 256 * 7;
  const unsigned hb1 = kg * 3840 + fr * 16 + 256 * (th ? 8 : 0);
  unsigned wa[2];
#pragma unroll
  for (int v = 0; v < 2; ++v) {
    const int sub = 2 * s + (v ? 1 - th : th);
    const int f = (q >> 1) | (2 * (kg & 1));
    wa[v] = C_GOFF + (8 * kg + q) * 128 + ((sub ^ f) * 32) + 8 * p;
  }
  f4_t ag[8][2], au[8][2];
#pragma unroll
  for (int u = 0; u < 8; ++u)
#pragma unroll
    for (int v = 0; v < 2; ++v) { ag[u][v] = f4_t{0.f, 0.f, 0.f, 0.f}; au[u][v] = f4_t{0.f, 0.f, 0.f, 0.f}; }
  auto load = [&](CFrag& F, unsigned so) {
    const char* sp = smem + so;
#pragma unroll
    for (int v = 0; v < 2; ++v)
#pragma unroll
      for (int m = 0; m < 2; ++m) {
        s4_t lo = tr_read(sp + wa[v] + m * 4096), hi = tr_read(sp + wa[v] + m * 4096 + 512);
        u2_t a = __builtin_bit_cast(u2_t, lo), b = __builtin_bit_cast(u2_t, hi);
        F.A[v][m] = __builtin_bit_cast(b8_t, (u4_t{a.x, a.y, b.x, b.y}));
      }
    F.B[0] = *(const b8_t*)(sp + hb0);
#pragma unroll
    for (int u = 1; u < 8; ++u) F.B[u] = *(const b8_t*)(sp + hb1 + 256 * (u - 1));
  };
  auto mma = [&](const CFrag& F) {
    ag[0][0] = __builtin_amdgcn_mfma_f32_16x16x32_bf16(F.A[0][0], F.B[0], ag[0][0], 0, 0, 0);
    au[0][0] = __builtin_amdgcn_mfma_f32_16x16x32_bf16(F.A[0][1], F.B[0], au[0][0], 0, 0, 0);
#pragma unroll
    for (int u = 1; u < 8; ++u)
#pragma unroll
      for (int v = 0; v < 2; ++v) {
        ag[u][v] = __builtin_amdgcn_mfma_f32_16x16x32_bf16(F.A[v][0], F.B[u], ag[u][v], 0, 0, 0);
        au[u][v] = __builtin_amdgcn_mfma_f32_16x16x32_bf16(F.A[v][1], F.B[u], au[u][v], 0, 0, 0);
      }
  };
  auto interleave = [&]() {
#pragma unroll
    for (int i = 0; i < 16; ++i) {
      __builtin_amdgcn_sched_group_barrier(0x008, 1, 0);
      __builtin_amdgcn_sched_group_barrier(0x100, 1, 0);
    }
    __builtin_amdgcn_sched_group_barrier(0x008, 14, 0);
  };
  CFrag F0, F1;
  bar();
  if (w == 0) STAMP(2, 1);
  load(F0, 0);
  unsigned sn = C_SLOT;
  SB;
#pragma unroll 1
  for (int b = 0; b < C_STEPS - 2; b += 2) {
    asm volatile("s_waitcnt lgkmcnt(0)" ::: "memory");
    bar(); SB;
    load(F1, sn);
    mma(F0);
    interleave();
    SB;
    sn += C_SLOT; if (sn == C_NSLOT * C_SLOT) sn = 0;
    asm volatile("s_waitcnt lgkmcnt(0)" ::: "memory");
    bar(); SB;
    load(F0, sn);
    mma(F1);
    interleave();
    SB;
    sn += C_SLOT; if (sn == C_NSLOT * C_SLOT) sn = 0;
  }
  asm volatile("s_waitcnt lgkmcnt(0)" ::: "memory");
  bar(); SB;
  load(F1, sn);
  mma(F0);
  interleave();
  SB;
  bar(); SB;
  f4_t csg[2], csu[2];
  float mu[8];
#pragma unroll
  for (int v = 0; v < 2; ++v) {
    const int sub = 2 * s + (v ? 1 - th : th);
    csg[v] = *(const f4_t*)(smem + C_CS + (16 * sub + 4 * kg) * 4);
    csu[v] = *(const f4_t*)(smem + C_CS + 256 + (16 * sub + 4 * kg) * 4);
  }
#pragma unroll
  for (int u = 0; u < 8; ++u) {
    const int tile = (u == 0) ? 7 : (th ? 7 + u : u - 1);
    mu[u] = *(const float*)(smem + C_STAT + (16 * tile + fr) * 4);
  }
  mma(F1);
  SB;
  if (w == 0) STAMP(2, 2);
  char* hh = ws + OFF_HH + (size_t)(2 * jline + s) * 1024 + lane * 16;
  char* sink = ws + OFF_DUMMY + lane * 16;
  auto epi = [&](auto thc) {
    constexpr int TH = decltype(thc)::value;
#pragma unroll
    for (int u = 0; u < 8; ++u) {
      const int tile = (u == 0) ? 7 : (TH ? 7 + u : u - 1);
      const int t16 = 15 * panel + tile;
      char* dst = (t16 < 128) ? hh + (size_t)t16 * (54 * 1024) : sink;
      u2_t o[2];
#pragma unroll
      for (int v = 0; v < 2; ++v) {
        if (u == 0 && v == 1) continue;
        const f4_t r = (ag[u][v] - mu[u] * csg[v]) * (au[u][v] - mu[u] * csu[v]);
        o[v] = u2_t{pk_bf16(r[0], r[1]), pk_bf16(r[2], r[3])};
      }
      if (u == 0) *(u2_t*)(dst + 8 * TH) = o[0];
      else *(u4_t*)dst = u4_t{o[TH].x, o[TH].y, o[1 - TH].x, o[1 - TH].y};
    }
  };
  if (th) epi(IC(1)); else epi(IC(0));
  if (w == 0) STAMP(2, 7);
}

constexpr int D_SLOT = 15360, D_NSLOT = 9, D_STEPS = 18;
constexpr int D_LDS = D_NSLOT * D_SLOT;

template <int NDMA>
__device__ __forceinline__ void d_loader(char* smem, const char* wp, int w4, int lane) {
  const unsigned lds0 = (unsigned)(size_t)(LAS char*)smem;
  const unsigned voff = lane * 16;
  auto issue = [&](int step, unsigned so) {
#pragma unroll
    for (int k = 0; k < NDMA; ++k) {
      const int i = w4 + 4 * k;
      glds16(wp + (size_t)step * D_SLOT + i * 1024, voff, lds0 + so + i * 1024);
    }
  };
  constexpr unsigned S = D_SLOT;
  issue(0, 0); issue(1, S); issue(2, 2 * S); issue(3, 3 * S);
  wait_vm<3 * NDMA>(); bar();
  issue(4, 4 * S); issue(5, 5 * S);
  wait_vm<4 * NDMA>(); bar();
  issue(6, 6 * S); issue(7, 7 * S);
  wait_vm<5 * NDMA>(); bar();
  issue(8, 8 * S); issue(9, 0);
  unsigned sw = S;
#pragma unroll 1
  for (int b = 2; b < 10; ++b) {
    wait_vm<6 * NDMA>();
    bar();
    issue(b + 8, sw);
    sw += S; if (sw == D_NSLOT * S) sw = 0;
  }
  wait_vm<6 * NDMA>(); bar();
  wait_vm<5 * NDMA>(); bar();
  wait_vm<4 * NDMA>(); bar();
  wait_vm<3 * NDMA>(); bar();
  wait_vm<2 * NDMA>(); bar();
  wait_vm<1 * NDMA>(); bar();
  wait_vm<0>(); bar();
  bar();
}

__global__ __launch_bounds__(512, 2) void kD(char* __restrict__ ws, u16* __restrict__ out) {
  extern __shared__ __attribute__((aligned(16))) char smem[];
  const int tid = threadIdx.x, lane = tid & 63;
  const int w = __builtin_amdgcn_readfirstlane(tid >> 6);
  const int xcd = blockIdx.x & 7, rank = blockIdx.x >> 3;
  const int it = 8 * (xcd >> 1) + (rank & 7), dg = 4 * (xcd & 1) + (rank >> 3);
  if (w == 0) STAMP(3, 0);
  if (w >= 4) {
    const int w4 = w - 4;
    const char* wp = ws + OFF_WP2 + (size_t)dg * (54 * 5120);
    if (w4 < 3) d_loader<4>(smem, wp, w4, lane);
    else d_loader<3>(smem, wp, w4, lane);
    return;
  }
  const int fr = lane & 15, g = lane >> 4;
  const int t = 64 * it + 16 * w + fr;
  const char* hp = ws + OFF_HH + (size_t)(4 * it + w) * (54 * 1024);
  const unsigned voff = lane * 16;
  u4_t R[9];
#pragma unroll
  for (int i = 0; i < 9; ++i)
    asm volatile("global_load_dwordx4 %0, %1, %2" : "=v"(R[i]) : "v"(voff), "s"(hp + (size_t)i * 1024) : "memory");
  u2_t res[5]; unsigned st;
#pragma unroll
  for (int dt = 0; dt < 5; ++dt) {
    const int d = 80 * dg + 16 * dt + 4 * g;
    const char* rp = ws + OFF_H8 + ((size_t)(d >> 3) * SL + t) * 16 + (d & 4) * 2;
    asm volatile("global_load_dwordx2 %0, %1, off" : "=v"(res[dt]) : "v"(rp) : "memory");
  }
  asm volatile("global_load_dword %0, %1, off" : "=v"(st) : "v"(ws + OFF_G2 + (size_t)t * 8) : "memory");
  f4_t acc[5];
#pragma unroll
  for (int dt = 0; dt < 5; ++dt) acc[dt] = f4_t{0.f, 0.f, 0.f, 0.f};
  b8_t A0[5], A1[5];
  auto rdA = [&](b8_t (&A)[5], unsigned off) {
    const char* sp = smem + off + lane * 16;
#pragma unroll
    for (int dt = 0; dt < 5; ++dt) A[dt] = *(const b8_t*)(sp + dt * 1024);
  };
  bar();
  if (w == 0) STAMP(3, 1);
  rdA(A0, 0);
  unsigned so = 0;
  auto kstep = [&](auto mode, auto idx, b8_t (&Ac)[5], b8_t (&An)[5], const char* hnext) {
    constexpr int MODE = decltype(mode)::value, i = decltype(idx)::value, kk = i % 3;
    if (kk == 0) bar();
    unsigned nx = so + (kk + 1) * 5120;
    if (kk == 2) { so += D_SLOT; if (so == D_NSLOT * D_SLOT) so = 0; nx = so; }
    SB;
    if (!(MODE == 2 && i == 8)) rdA(An, nx);
    asm volatile("s_waitcnt vmcnt(%1)" : "+v"(R[i]) : "i"(MODE == 0 ? 14 : MODE == 1 ? 8 : 8 - i) : "memory");
    const b8_t B = __builtin_bit_cast(b8_t, R[i]);
#pragma unroll
    for (int dt = 0; dt < 5; ++dt) acc[dt] = __builtin_amdgcn_mfma_f32_16x16x32_bf16(Ac[dt], B, acc[dt], 0, 0, 0);
    SB;
    if (MODE != 2) asm volatile("global_load_dwordx4 %0, %1, %2" : "+v"(R[i]) : "v"(voff), "s"(hnext + (size_t)i * 1024) : "memory");
    SB;
  };
  auto round = [&](auto mode, b8_t (&P)[5], b8_t (&Q)[5], const char* hnext) {
    kstep(mode, IC(0), P, Q, hnext); kstep(mode, IC(1), Q, P, hnext); kstep(mode, IC(2), P, Q, hnext);
    kstep(mode, IC(3), Q, P, hnext); kstep(mode, IC(4), P, Q, hnext); kstep(mode, IC(5), Q, P, hnext);
    kstep(mode, IC(6), P, Q, hnext); kstep(mode, IC(7), Q, P, hnext); kstep(mode, IC(8), P, Q, hnext);
  };
  round(IC(0), A0, A1, hp + 9 * 1024);
#pragma unroll 1
  for (int r = 1; r < 5; r += 2) {
    round(IC(1), A1, A0, hp + (size_t)(9 * (r + 1)) * 1024);
    round(IC(1), A0, A1, hp + (size_t)(9 * (r + 2)) * 1024);
  }
  round(IC(2), A1, A0, hp);
  if (w == 0) STAMP(3, 2);
  asm volatile("s_waitcnt vmcnt(0)" : "+v"(res[0]), "+v"(res[1]), "+v"(res[2]), "+v"(res[3]), "+v"(res[4]), "+v"(st)::"memory");
  const float sd = bf_hi(st), r2 = 1.0f / (sd * sd);
#pragma unroll
  for (int dt = 0; dt < 5; ++dt) {
    const int d = 80 * dg + 16 * dt + 4 * g;
    const float h0 = bf_lo(res[dt].x), h1 = bf_hi(res[dt].x), h2 = bf_lo(res[dt].y), h3 = bf_hi(res[dt].y);
    *(u2_t*)(out + (size_t)t * DM + d) = u2_t{pk_bf16(h0 + r2 * acc[dt][0], h1 + r2 * acc[dt][1]), pk_bf16(h2 + r2 * acc[dt][2], h3 + r2 * acc[dt][3])};
  }
  if (w == 0) STAMP(3, 7);
}

extern "C" void kernel_launch(void* const* d_in, const int* in_sizes, int n_in, void* d_out, int out_size, void* d_ws,
                              size_t ws_size, hipStream_t stream) {
  char* ws = (char*)d_ws;
  (void)hipFuncSetAttribute((const void*)kA, hipFuncAttributeMaxDynamicSharedMemorySize, A_LDS);
  (void)hipFuncSetAttribute((const void*)kC, hipFuncAttributeMaxDynamicSharedMemorySize, C_LDS);
  (void)hipFuncSetAttribute((const void*)kD, hipFuncAttributeMaxDynamicSharedMemorySize, D_LDS);
  kA<<<256, 1024, A_LDS, stream>>>((const u16*)d_in[0], (const u16*)d_in[1], (const u16*)d_in[2], (const u16*)d_in[3], (const u16*)d_in[4],
                                   (const u16*)d_in[5], (const u16*)d_in[6], (const u16*)d_in[7], (const u16*)d_in[12], ws);
  kB<<<256, 512, B_LDS, stream>>>((const u16*)d_in[8], (const u16*)d_in[10], (const u16*)d_in[11], ws);
  kC<<<256, 512, C_LDS, stream>>>((const u16*)d_in[10], (const u16*)d_in[11], ws);
  kD<<<256, 512, D_LDS, stream>>>(ws, (u16*)d_out);
}
```

```cpp
#include <hip/hip_runtime.h>
#include <hip/hip_bf16.h>
#include <stdint.h>
#include <type_traits>

typedef unsigned short u16;
typedef _Float16 h2_t __attribute__((ext_vector_type(2)));
typedef _Float16 h8_t __attribute__((ext_vector_type(8)));
typedef __bf16 b2_t __attribute__((ext_vector_type(2)));
typedef __bf16 b8_t __attribute__((ext_vector_type(8)));
typedef float f2_t __attribute__((ext_vector_type(2)));
typedef float f4_t __attribute__((ext_vector_type(4)));
typedef unsigned u2_t __attribute__((ext_vector_type(2)));
typedef unsigned u4_t __attribute__((ext_vector_type(4)));
typedef short s4_t __attribute__((ext_vector_type(4)));
typedef __attribute__((address_space(1))) unsigned long long gu64;
#define LAS __attribute__((address_space(3)))
#define SB __builtin_amdgcn_sched_barrier(0)
#define IC(n) std::integral_constant<int, n>{}

constexpr int SL = 2048;
constexpr int DM = 640;
constexpr int FF = 1728;

constexpr size_t OFF_X4 = 0;
constexpr size_t OFF_S1 = 0x280000;
constexpr size_t OFF_KD = 0x290000;
constexpr size_t OFF_INVS = 0x590000;
constexpr size_t OFF_H8 = 0x5A0000;
constexpr size_t OFF_HH = 0x820000;
constexpr size_t OFF_WP2 = 0xF00000;
constexpr size_t OFF_CS = 0x1120000;
constexpr size_t OFF_G2 = 0x1130000;
constexpr int KRS = 1344;
constexpr int KDS = 4864;
constexpr int KD_O = 2240;
constexpr int KD_DR = 4384;

#define STAMP(k, i)

__device__ __forceinline__ float bf_lo(unsigned d) { return __uint_as_float(d << 16); }
__device__ __forceinline__ float bf_hi(unsigned d) { return __uint_as_float(d & 0xffff0000u); }
__device__ __forceinline__ float bf2f(u16 v) { return __uint_as_float((unsigned)v << 16); }
__device__ __forceinline__ unsigned pk_bf16(float a, float b) {
  f2_t v = {a, b};
  return __builtin_bit_cast(unsigned, __builtin_convertvector(v, b2_t));
}
__device__ __forceinline__ unsigned pk_f16(float a, float b) {
  f2_t v = {a, b};
  return __builtin_bit_cast(unsigned, __builtin_convertvector(v, h2_t));
}
__device__ __forceinline__ float rbf(float a) { return bf_lo(pk_bf16(a, 0.f)); }
template <int N> __device__ __forceinline__ float dpp_ror_add(float v) {
  int r = __builtin_amdgcn_update_dpp(0, __float_as_int(v), 0x120 + N, 0xf, 0xf, false);
  return v + __int_as_float(r);
}
__device__ __forceinline__ float row16_sum(float v) {
  v = dpp_ror_add<8>(v); v = dpp_ror_add<4>(v); v = dpp_ror_add<2>(v); v = dpp_ror_add<1>(v);
  return v;
}
__device__ __forceinline__ float wave_sum(float v) {
  v = row16_sum(v);
  const int b = __float_as_int(v);
  return (__int_as_float(__builtin_amdgcn_readlane(b, 0)) + __int_as_float(__builtin_amdgcn_readlane(b, 16))) +
         (__int_as_float(__builtin_amdgcn_readlane(b, 32)) + __int_as_float(__builtin_amdgcn_readlane(b, 48)));
}
template <typename T> __device__ __forceinline__ T ldnt(const T* p) { return __builtin_nontemporal_load(p); }
__device__ __forceinline__ void wave_fence() {
  __builtin_amdgcn_fence(__ATOMIC_RELEASE, "wavefront");
  __builtin_amdgcn_wave_barrier();
  __builtin_amdgcn_fence(__ATOMIC_ACQUIRE, "wavefront");
}
__device__ __forceinline__ s4_t tr_read(const char* p) {
  return __builtin_amdgcn_ds_read_tr16_b64_v4i16((LAS s4_t*)p);
}
__device__ __forceinline__ void glds16(const void* sbase, unsigned voff, unsigned lds_addr) {
  asm volatile("s_mov_b32 m0, %2\n\ts_nop 0\n\tglobal_load_lds_dwordx4 %0, %1" ::"v"(voff), "s"(sbase), "s"(lds_addr) : "memory");
}
template <int N> __device__ __forceinline__ void wait_vm() { asm volatile("s_waitcnt vmcnt(%0)" ::"i"(N) : "memory"); }
__device__ __forceinline__ void bar() { asm volatile("s_barrier" ::: "memory"); }
__device__ __forceinline__ float dot2bf(unsigned a, unsigned b, float c) {
  return __builtin_amdgcn_fdot2_f32_bf16(__builtin_bit_cast(b2_t, a), __builtin_bit_cast(b2_t, b), c, false);
}

#define ST_MODE 0
__device__ __forceinline__ void st16(void* p, u4_t v) {
  *(u4_t*)p = v;
}
__device__ __forceinline__ void st8(void* p, u2_t v) {
  *(u2_t*)p = v;
}

struct Cw { f2_t w, ws; };
__device__ __forceinline__ Cw mkw(f2_t w) { Cw r; r.w = w; r.ws = f2_t{-w.y, w.x}; return r; }
__device__ __forceinline__ f2_t cmulw(f2_t a, const Cw& W) {
  return __builtin_elementwise_fma(f2_t{a.y, a.y}, W.ws, f2_t{a.x, a.x} * W.w);
}
__device__ __forceinline__ f2_t cexp_l(float er, float wr, float l) {
  float mag = __builtin_amdgcn_exp2f(er * l);
  float rev = __builtin_amdgcn_fractf(wr * l);
  return f2_t{mag * __builtin_amdgcn_cosf(rev), mag * __builtin_amdgcn_sinf(rev)};
}
__device__ __forceinline__ float pow2_scale(float bound, int target) {
  unsigned e = (__float_as_uint(bound) >> 23) & 0xff;
  int k = target - ((int)e - 126);
  k = k > 126 ? 126 : (k < -126 ? -126 : k);
  return (bound > 0.f) ? __uint_as_float((unsigned)(k + 127) << 23) : 1.0f;
}
__device__ __forceinline__ float pow2_inv(float p) { return __uint_as_float(0x7f000000u - __float_as_uint(p)); }

constexpr int A_KBLK = 214;
constexpr int KG_R = 0, KG_L0 = 4352, KG_L1 = 8704, KG_HW = 8704 + 25 * 272;
constexpr int KG_CH = 2 * KG_HW + 16;
constexpr int A_SIDE = 3 * KG_CH;
constexpr int A_LDS = A_SIDE + 20736;

__device__ __forceinline__ void pack_wd(const u16* __restrict__ wd, char* __restrict__ ws, char* buf, int u, int lane) {
  const int ks = u >> 3, dg = u & 7;
#pragma unroll
  for (int i = 0; i < 5; ++i) {
    const int idx = i * 64 + lane, row = idx / 10, c = idx % 10;
    __builtin_amdgcn_global_load_lds((const unsigned*)((const char*)wd + (size_t)(32 * ks + row) * 1280 + 160 * dg + 16 * c),
                                     (LAS unsigned*)(buf + i * 1024), 16, 0, 2);
  }
  asm volatile("s_waitcnt vmcnt(0)" ::: "memory");
  wave_fence();
  const int g = lane >> 4, q = (lane & 15) >> 2, p = lane & 3;
  char* dst = ws + OFF_WP2 + ((size_t)(dg * 54 + ks) * 5) * 1024 + lane * 16;
#pragma unroll
  for (int dt = 0; dt < 5; ++dt) {
    s4_t aa = tr_read(buf + (4 * g + q) * 160 + 32 * dt + 8 * p);
    s4_t bb = tr_read(buf + (16 + 4 * g + q) * 160 + 32 * dt + 8 * p);
    u2_t ua = __builtin_bit_cast(u2_t, aa), ub = __builtin_bit_cast(u2_t, bb);
    st16(dst + dt * 1024, u4_t{ua.x, ua.y, ub.x, ub.y});
  }
}

__global__ __launch_bounds__(1024) void kA(const u16* __restrict__ x, const u16* __restrict__ Are, const u16* __restrict__ Aim,
                                           const u16* __restrict__ Bre, const u16* __restrict__ Bim, const u16* __restrict__ Cre,
                                           const u16* __restrict__ Cim, const u16* __restrict__ logdt, const u16* __restrict__ wd,
                                           char* __restrict__ ws) {
  extern __shared__ __attribute__((aligned(16))) char smem[];
  const int tid = threadIdx.x, lane = tid & 63;
  const int w = __builtin_amdgcn_readfirstlane(tid >> 6);
  if (w == 0) STAMP(0, 0);
  if (blockIdx.x >= A_KBLK) {
    const int u = 344 + (blockIdx.x - A_KBLK) * 16 + w;
    if (u < 432) pack_wd(wd, ws, smem + w * 5120, u, lane);
    if (w == 0) STAMP(0, 7);
    return;
  }
  if (w >= 12) {
    const int w4 = w - 12;
    if (blockIdx.x >= 128) {
      pack_wd(wd, ws, smem + A_SIDE + w4 * 5120, (blockIdx.x - 128) * 4 + w4, lane);
      bar();
      return;
    }
    const int t0 = blockIdx.x * 16;
    const int r = lane >> 4, c = lane & 15, rl = w4 * 4 + r;
    char* xs = smem + A_SIDE;
    const u4_t* p = (const u4_t*)(x + (size_t)(t0 + rl) * DM);
    u4_t v[5];
#pragma unroll
    for (int i = 0; i < 5; ++i) v[i] = ldnt(p + c + 16 * i);
    if (w4 == 0 && lane < 16) ((unsigned long long*)(ws + OFF_G2))[t0 + lane] = 0ull;
    float s = 0.f;
#pragma unroll
    for (int i = 0; i < 5; ++i)
#pragma unroll
      for (int j = 0; j < 4; ++j) s += bf_lo(v[i][j]) + bf_hi(v[i][j]);
#pragma unroll
    for (int i = 0; i < 5; ++i) *(u4_t*)(xs + rl * 1296 + (c + 16 * i) * 16) = v[i];
    const float mean = row16_sum(s) * (1.0f / DM);
    float q = 0.f;
#pragma unroll
    for (int i = 0; i < 5; ++i)
#pragma unroll
      for (int j = 0; j < 4; ++j) {
        float a = bf_lo(v[i][j]) - mean, b = bf_hi(v[i][j]) - mean;
        q += a * a; q += b * b;
      }
    const float var = row16_sum(q) * (1.0f / DM);
    if (c == 0) {
      float mb = rbf(mean), sd = rbf(sqrtf(rbf(rbf(var) + 1e-5f)));
      ((unsigned*)(ws + OFF_S1))[t0 + rl] = (__float_as_uint(mb) >> 16) | (__float_as_uint(sd) & 0xffff0000u);
    }
    __syncthreads();
    const int t4 = w4 * 64 + lane;
#pragma unroll
    for (int it = 0; it < 5; ++it) {
      const int item = t4 + 256 * it, q4 = item >> 3, tp = item & 7;
      u2_t a = *(const u2_t*)(xs + (2 * tp) * 1296 + 8 * q4);
      u2_t b = *(const u2_t*)(xs + (2 * tp + 1) * 1296 + 8 * q4);
      st16(ws + OFF_X4 + ((size_t)q4 * SL + t0 + 2 * tp) * 8, u4_t{a.x, a.y, b.x, b.y});
    }
    if (w4 == 0) STAMP(0, 6);
    return;
  }
  const int cl = w >> 2, part = (w >> 1) & 1, hw = w & 1;
  const int chr = blockIdx.x * 3 + cl, ch = min(chr, DM - 1);
  char* chb = smem + cl * KG_CH;
  char* hb = chb + hw * KG_HW;
  const int n = ch * 128 + hw * 64 + lane;
  const u16 i_are = ldnt(Are + n), i_aim = ldnt(Aim + n), i_bre = ldnt(Bre + n), i_bim = ldnt(Bim + n), i_cre = ldnt(Cre + n), i_cim = ldnt(Cim + n);
  const float dt = rbf(expf(bf2f(logdt[ch])));
  const float are = dt * bf2f(i_are), aim = dt * bf2f(i_aim);
  const float bre = dt * bf2f(i_bre), bim = dt * bf2f(i_bim);
  const float cre = bf2f(i_cre), cim = bf2f(i_cim);
  f2_t c = {cre * bre - cim * bim, cre * bim + cim * bre};
  const float er = are * 1.44269504088896341f, wr = aim * 0.15915494309189535f;
  const float bnd = (fabsf(c.x) + fabsf(c.y)) * fmaxf(1.0f, __builtin_amdgcn_exp2f(er * 2048.f));
  const float bsum = wave_sum(bnd);
  if (w == 0) STAMP(0, 1);
  const float Sw = pow2_scale(bsum, 15);
  c *= Sw;
  const Cw z1 = mkw(cexp_l(er, wr, 1.f)), w128 = mkw(cexp_l(er, wr, 128.f));
  auto put = [&](char* buf, int row, f2_t v) { *(unsigned*)(buf + row * 272 + 4 * lane) = pk_f16(v.x, -v.y); };
  if (part == 0) {
    const Cw w8 = mkw(cexp_l(er, wr, 8.f));
    f2_t R = w8.w, v0 = c, v1 = cmulw(c, w128);
    *(unsigned*)(hb + KG_R + 4 * lane) = pk_f16(1.f, 0.f);
#pragma unroll
    for (int i = 0; i < 8; ++i) {
      *(unsigned*)(hb + KG_R + (2 * i + 1) * 272 + 4 * lane) = pk_f16(R.x, R.y);
      if (i < 7) {
        R = cmulw(R, w8);
        *(unsigned*)(hb + KG_R + (2 * i + 2) * 272 + 4 * lane) = pk_f16(R.x, R.y);
        R = cmulw(R, w8);
      }
      put(hb + KG_L0, i, v0); put(hb + KG_L0, 8 + i, v1);
      if (i < 7) { v0 = cmulw(v0, z1); v1 = cmulw(v1, z1); }
    }
  } else {
    const Cw z2 = mkw(cmulw(z1.w, z1));
    const Cw z4 = mkw(cmulw(z2.w, z2));
    f2_t T = cmulw(c, w128);
#pragma unroll
    for (int i1 = 2; i1 < 17; ++i1) {
      T = cmulw(T, w128);
      if (i1 < 4) {
        f2_t v = T;
#pragma unroll
        for (int i0 = 0; i0 < 4; ++i0) { put(hb + KG_L1, 4 * (i1 - 2) + i0, v); if (i0 < 3) v = cmulw(v, z2); }
      } else if (i1 < 8) {
        put(hb + KG_L1, 8 + 2 * (i1 - 4), T); put(hb + KG_L1, 8 + 2 * (i1 - 4) + 1, cmulw(T, z4));
      } else {
        put(hb + KG_L1, 16 + (i1 - 8), T);
      }
    }
  }
  if (part == 0 && lane == 0) ((float*)(chb + 2 * KG_HW))[hw] = bsum;
  __syncthreads();
  if (w == 0) STAMP(0, 2);
  if (part == 1 && hw == 1) return;
  const int tile = part ? 1 : 2 * hw;
  const int fr = lane & 15, kg = lane >> 4;
  const int row = (tile == 2) ? 16 + min(fr, 8) : fr;
  const int lo = (tile == 0) ? KG_L0 : KG_L1;
  f4_t acc[2];
#pragma unroll
  for (int h = 0; h < 2; ++h) {
    const char* hbb = chb + h * KG_HW;
    f4_t a = {0.f, 0.f, 0.f, 0.f};
#pragma unroll
    for (int ks = 0; ks < 4; ++ks) {
      const h8_t Bf = *(const h8_t*)(hbb + KG_R + fr * 272 + 64 * ks + 16 * kg);
      const h8_t Af = *(const h8_t*)(hbb + lo + row * 272 + 64 * ks + 16 * kg);
      a = __builtin_amdgcn_mfma_f32_16x16x32_f16(Af, Bf, a, 0, 0, 0);
    }
    acc[h] = a;
  }
  const float b0 = ((const float*)(chb + 2 * KG_HW))[0], b1 = ((const float*)(chb + 2 * KG_HW))[1];
  const float Sc = pow2_scale(b0 + b1, 15);
  const f4_t kv = acc[0] * (pow2_inv(pow2_scale(b0, 15)) * Sc) + acc[1] * (pow2_inv(pow2_scale(b1, 15)) * Sc);
  const unsigned p01 = pk_f16(kv[0], kv[1]), p23 = pk_f16(kv[2], kv[3]);
  char* kr = ws + OFF_KD + (size_t)ch * KRS;
  if (tile == 0) {
    *(u2_t*)(kr + 2 * (128 * (kg >> 1) + 4 * (kg & 1) + 8 * fr)) = u2_t{p01, p23};
    if (lane == 0) ((float*)(ws + OFF_INVS))[ch] = pow2_inv(Sc);
  } else if (tile == 1) {
    if (kg < 2) {
      *(u2_t*)(kr + 2 * (256 + 64 * kg + 4 * fr)) = u2_t{p01, p23};
    } else {
      *(unsigned*)(kr + 2 * (384 + 64 * (kg - 2) + 2 * fr)) = p01;
      *(unsigned*)(kr + 2 * (384 + 64 * (kg - 2) + 32 + 2 * fr)) = p23;
    }
  } else {
    u16* k3 = (u16*)kr + 512 + 64 * kg + fr;
    if (kg < 2) { k3[0] = (u16)p01; k3[16] = (u16)(p01 >> 16); k3[32] = (u16)p23; k3[48] = (u16)(p23 >> 16); }
    else if (lane == 32) k3[0] = (u16)p01;
  }
  if (w == 0) STAMP(0, 7);
}

constexpr int B_CONV = 160;
constexpr int B_KOFF = 0;
constexpr int B_UOFF = 4 * KDS;
constexpr int B_UCH = 4608;
constexpr int B_YCH = 10240;
constexpr int B_LDS = 40960;
#define B_DELAY 0

struct BFrag { h8_t B[8]; h8_t A[5]; };

__global__ __launch_bounds__(512) void kB(const u16* __restrict__ dskip, const u16* __restrict__ wg, const u16* __restrict__ wu,
                                          char* __restrict__ ws) {
  extern __shared__ __attribute__((aligned(16))) char smem[];
  const int tid = threadIdx.x, lane = tid & 63;
  const int w = __builtin_amdgcn_readfirstlane(tid >> 6);
  if (w == 0) STAMP(1, 0);
  if (blockIdx.x >= B_CONV) {
    const int xcd = blockIdx.x & 7, rk = (blockIdx.x - B_CONV) >> 3;
    const int g4 = xcd >> 1, a = xcd & 1, nl = (g4 < 3) ? 7 : 6;
    if (rk >= nl) return;
#pragma unroll
    for (int i = 0; i < B_DELAY; ++i) __builtin_amdgcn_s_sleep(16);
    const int jl = 7 * g4 + rk;
    const char* pm = (const char*)(a ? wu : wg) + (size_t)(80 * w + (lane >> 3)) * (FF * 2) + 128 * jl + (lane & 7) * 16;
    u4_t v[10];
#pragma unroll
    for (int i = 0; i < 10; ++i) v[i] = ldnt((const u4_t*)(pm + (size_t)i * 8 * (FF * 2)));
    const char* po = (const char*)(a ? wg : wu) + (size_t)(80 * w) * (FF * 2) + 128 * jl;
    unsigned t0v, t1v;
    asm volatile("global_load_dword %0, %1, off nt" : "=v"(t0v) : "v"(po + (size_t)lane * (FF * 2)) : "memory");
    asm volatile("global_load_dword %0, %1, off nt" : "=v"(t1v) : "v"(po + (size_t)(64 + (lane & 15)) * (FF * 2)) : "memory");
    f2_t s[4];
#pragma unroll
    for (int e = 0; e < 4; ++e) s[e] = f2_t{0.f, 0.f};
#pragma unroll
    for (int i = 0; i < 10; ++i)
#pragma unroll
      for (int jj = 0; jj < 4; ++jj) s[jj] += f2_t{bf_lo(v[i][jj]), bf_hi(v[i][jj])};
    float* pp = (float*)smem;
    *(f4_t*)(pp + (w * 8 + (lane >> 3)) * 68 + (lane & 7) * 8) = f4_t{s[0].x, s[0].y, s[1].x, s[1].y};
    *(f4_t*)(pp + (w * 8 + (lane >> 3)) * 68 + (lane & 7) * 8 + 4) = f4_t{s[2].x, s[2].y, s[3].x, s[3].y};
    __syncthreads();
    float acc = 0.f;
#pragma unroll
    for (int i = 0; i < 8; ++i) acc += pp[(w * 8 + i) * 68 + lane];
    __syncthreads();
    pp[w * 64 + lane] = acc;
    __syncthreads();
    if (w == 0) {
      float t = 0.f;
#pragma unroll
      for (int i = 0; i < 8; ++i) t += pp[i * 64 + lane];
      ((float*)(ws + OFF_CS))[a * FF + 64 * jl + lane] = t;
    }
    asm volatile("s_waitcnt vmcnt(0)" ::"v"(t0v), "v"(t1v) : "memory");
    if (w == 0) STAMP(1, 7);
    return;
  }
  const int a_ = blockIdx.x >> 3, x_ = blockIdx.x & 7;
  const int q = 16 * (a_ >> 1) + 2 * x_ + (a_ & 1);
  u4_t xv[2]; u2_t sv[2];
#pragma unroll
  for (int it = 0; it < 2; ++it) {
    const int j = tid + 512 * it;
    xv[it] = *(const u4_t*)(ws + OFF_X4 + ((size_t)q * SL + 2 * j) * 8);
    sv[it] = *(const u2_t*)(ws + OFF_S1 + 8 * j);
  }
  const int kch = tid >> 7, kc = tid & 127;
  const char* kr = ws + OFF_KD + (size_t)(4 * q + kch) * KRS;
  const int e0 = (kc < 32) ? 8 * kc : (kc < 64) ? 256 + 4 * (kc - 32) : 384 + 2 * (kc - 64);
  const u4_t kd4 = *(const u4_t*)(kr + 2 * e0);
  const unsigned kd5 = *(const unsigned*)(kr + 2 * e0 + 16);
  const unsigned kfirst = *(const u16*)kr, k1024 = *(const u16*)(kr + 2 * 512);
  const int dl = 127 - kc;
  const unsigned kfa = *(const u16*)(kr + 2 * (512 + max(2 * dl - 127, 0))), kfb = *(const u16*)(kr + 2 * (512 + max(2 * dl - 126, 0)));
  float dk[4];
#pragma unroll
  for (int c = 0; c < 4; ++c) dk[c] = bf2f(dskip[4 * q + c]);
  const float invs = ((const float*)(ws + OFF_INVS))[4 * q + (w & 3)];
  *(unsigned*)(smem + B_UOFF + (tid >> 7) * B_UCH + 4096 + 4 * (tid & 127)) = 0u;
  const int c = lane & 15, kg = lane >> 4;
  h8_t band9, band7;
#pragma unroll
  for (int j = 0; j < 8; ++j) {
    const int k = 8 * kg + 7 - j;
    band9[j] = (k >= c + 4 && k <= c + 12) ? (_Float16)1.0f : (_Float16)0.0f;
    band7[j] = (k >= c + 5 && k <= c + 11) ? (_Float16)1.0f : (_Float16)0.0f;
  }
  {
    char* ki = smem + B_KOFF + kch * KDS;
    const unsigned k0 = kd4[0] & 0xffffu;
    unsigned o0, o1, o2, o3, nx;
    if (kc < 32) { o0 = kd4[0]; o1 = kd4[1]; o2 = kd4[2]; o3 = kd4[3]; nx = kd5; }
    else if (kc < 64) {
      const unsigned k2 = kd4[0] >> 16, k4 = kd4[1] & 0xffffu, k6 = kd4[1] >> 16, k8 = kd4[2] & 0xffffu;
      o0 = k0 * 0x10001u; o1 = k2 | (k4 << 16); o2 = k4 * 0x10001u; o3 = k6 | (k8 << 16); nx = k8;
    } else {
      const unsigned k4 = kd4[0] >> 16, k8 = kd4[1] & 0xffffu;
      o0 = k0 * 0x10001u; o1 = k0 | (k4 << 16); o2 = k4 * 0x10001u; o3 = k8 * 0x10001u; nx = k8;
    }
    *(u4_t*)(ki + 16 * (kc + 2)) = u4_t{o0, o1, o2, o3};
    *(u4_t*)(ki + KD_O + 16 * (kc + 2)) = u4_t{__builtin_amdgcn_alignbit(o1, o0, 16), __builtin_amdgcn_alignbit(o2, o1, 16),
                                              __builtin_amdgcn_alignbit(o3, o2, 16), __builtin_amdgcn_alignbit(nx, o3, 16)};
    if (kc < 6) {
      const int cp = (kc < 2) ? kc : 128 + kc;
      const unsigned kk = k1024 * 0x10001u;
      const u4_t e = (kc == 2) ? u4_t{kk, kk, k1024, 0u} : u4_t{0u, 0u, 0u, 0u};
      const u4_t o = (kc == 2) ? u4_t{kk, kk, 0u, 0u} : u4_t{0u, 0u, 0u, (kc == 1) ? (kfirst << 16) : 0u};
      *(u4_t*)(ki + 16 * cp) = e;
      *(u4_t*)(ki + KD_O + 16 * cp) = o;
    }
    if (kc < 80) *(unsigned*)(ki + KD_DR + 4 * kc) = (dl >= 64) ? (kfa | (kfb << 16)) : 0u;
  }
  float z[2][8];
  if (w == 0) { STAMP(1, 1); }
#pragma unroll
  for (int it = 0; it < 2; ++it) {
    const int j = tid + 512 * it;
    const float m0 = bf_lo(sv[it].x), r0 = __builtin_amdgcn_rcpf(bf_hi(sv[it].x)), m1 = bf_lo(sv[it].y), r1 = __builtin_amdgcn_rcpf(bf_hi(sv[it].y));
    float x0[4] = {bf_lo(xv[it].x), bf_hi(xv[it].x), bf_lo(xv[it].y), bf_hi(xv[it].y)};
    float x1[4] = {bf_lo(xv[it].z), bf_hi(xv[it].z), bf_lo(xv[it].w), bf_hi(xv[it].w)};
#pragma unroll
    for (int cc = 0; cc < 4; ++cc) {
      const float u0 = (x0[cc] - m0) * r0, u1 = (x1[cc] - m1) * r1;
      *(unsigned*)(smem + B_UOFF + cc * B_UCH + 4 * (1023 - j)) = pk_f16(u1, u0);
      z[it][cc] = x0[cc] + u0 * dk[cc];
      z[it][4 + cc] = x1[cc] + u1 * dk[cc];
    }
  }
  __syncthreads();
  if (w == 0) STAMP(1, 2);
  f4_t acc[8];
  if (w < 4) {
#pragma unroll
    for (int t = 0; t < 8; ++t) acc[t] = f4_t{0.f, 0.f, 0.f, 0.f};
    const char* ub = smem + B_UOFF + w * B_UCH + 4112 - 32 * c - 16 * kg - 512 * 7;
    const char* kb = smem + B_KOFF + w * KDS + ((c & 1) ? 0 : KD_O) + 4 * (12 + ((c + 1) >> 1) - 4 * kg);
    auto load = [&](BFrag& F, int i0) {
#pragma unroll
      for (int i1 = 0; i1 < 5; ++i1) {
        if (i1 == 4 && i0 != 0) continue;
        const unsigned* ap = (const unsigned*)(kb + 64 * i0 + 512 * i1);
        F.A[i1] = __builtin_bit_cast(h8_t, (u4_t{ap[0], ap[1], ap[2], ap[3]}));
      }
#pragma unroll
      for (int p = 0; p < 8; ++p) F.B[p] = *(const h8_t*)(ub + 512 * (7 - p) + 64 * i0);
    };
    auto mma = [&](const BFrag& F, int i0) {
#pragma unroll
      for (int i1 = 0; i1 < 5; ++i1) {
        if (i1 == 4 && i0 != 0) continue;
#pragma unroll
        for (int t = i1; t < 8; ++t) acc[t] = __builtin_amdgcn_mfma_f32_16x16x32_f16(F.A[i1], F.B[t - i1], acc[t], 0, 0, 0);
      }
    };
    auto interleave = [&]() {
#pragma unroll
      for (int i = 0; i < 16; ++i) {
        __builtin_amdgcn_sched_group_barrier(0x008, 1, 0);
        __builtin_amdgcn_sched_group_barrier(0x100, 1, 0);
      }
      __builtin_amdgcn_sched_group_barrier(0x008, 14, 0);
    };
    BFrag F0, F1;
    load(F0, 0);
    SB;
#pragma unroll
    for (int i0 = 0; i0 < 6; i0 += 2) {
      load(F1, i0 + 1);
      mma(F0, i0);
      interleave();
      SB;
      load(F0, i0 + 2);
      mma(F1, i0 + 1);
      interleave();
      SB;
    }
    load(F1, 7);
    mma(F0, 6);
    interleave();
    SB;
    h8_t Tf[4], U9[4], U7[4];
    const unsigned* dr = (const unsigned*)(smem + B_KOFF + w * KDS + KD_DR) + (127 - c + 4 * kg);
    const char* uw = smem + B_UOFF + w * B_UCH + 4112 - 32 * c - 16 * kg;
#pragma unroll
    for (int bt = 0; bt < 4; ++bt) { U9[bt] = *(const h8_t*)(uw - 512 * bt + 16); U7[bt] = *(const h8_t*)(uw - 512 * bt); }
#pragma unroll
    for (int dl = 0; dl < 4; ++dl) {
      const unsigned* tp = dr - 16 * (dl + 4);
      Tf[dl] = __builtin_bit_cast(h8_t, (u4_t{tp[0], tp[1], tp[2], tp[3]}));
    }
    mma(F1, 7);
    interleave();
    SB;
    f4_t X9[4], X7[4];
#pragma unroll
    for (int bt = 0; bt < 4; ++bt) {
      const f4_t zz = {0.f, 0.f, 0.f, 0.f};
      X9[bt] = __builtin_amdgcn_mfma_f32_16x16x32_f16(U9[bt], band9, zz, 0, 0, 0);
      X7[bt] = __builtin_amdgcn_mfma_f32_16x16x32_f16(U7[bt], band7, zz, 0, 0, 0);
    }
#pragma unroll
    for (int bt = 0; bt < 4; ++bt) {
      u4_t vu = {pk_f16(X7[bt][0], X9[bt][0]), pk_f16(X7[bt][1], X9[bt][1]), pk_f16(X7[bt][2], X9[bt][2]), pk_f16(X7[bt][3], X9[bt][3])};
      h8_t Vf = __builtin_bit_cast(h8_t, vu);
#pragma unroll
      for (int at = 4 + bt; at < 8; ++at) acc[at] = __builtin_amdgcn_mfma_f32_16x16x32_f16(Vf, Tf[at - bt - 4], acc[at], 0, 0, 0);
    }
  }
  if (w == 0) STAMP(1, 3);
  __syncthreads();
  if (w < 4) {
#pragma unroll
    for (int t = 0; t < 8; ++t) *(f4_t*)(smem + w * B_YCH + t * 1280 + kg * 320 + c * 16) = acc[t] * invs;
  }
  __syncthreads();
#pragma unroll
  for (int it = 0; it < 2; ++it) {
    const int j = tid + 512 * it, t = 2 * j;
    const int yo = (t >> 8) * 1280 + ((t >> 2) & 3) * 320 + ((t >> 4) & 15) * 16 + (t & 3) * 4;
    float h0[4], h1[4];
#pragma unroll
    for (int cc = 0; cc < 4; ++cc) {
      f2_t y = *(const f2_t*)(smem + cc * B_YCH + yo);
      h0[cc] = z[it][cc] + y.x;
      h1[cc] = z[it][4 + cc] + y.y;
    }
    char* dst = ws + OFF_H8 + ((size_t)(q >> 1) * SL + t) * 16 + (q & 1) * 8;
    st8(dst, u2_t{pk_bf16(h0[0], h0[1]), pk_bf16(h0[2], h0[3])});
    st8(dst + 16, u2_t{pk_bf16(h1[0], h1[1]), pk_bf16(h1[2], h1[3])});
  }
  if (w == 0) STAMP(1, 7);
}

constexpr int C_T = 240;
constexpr int C_SLOT = 23552, C_NSLOT = 6, C_GOFF = 15360;
constexpr int C_STAT = C_NSLOT * C_SLOT;
constexpr int C_CS = C_STAT + 960;
constexpr int C_LDS = C_CS + 512;
constexpr int C_STEPS = 20;
constexpr size_t OFF_DUMMY = 0x1140000;

__device__ __forceinline__ unsigned ln_pack(float sum, float sq) {
  const float mean = sum * (1.0f / DM);
  const float var = fmaxf(sq * (1.0f / DM) - mean * mean, 0.f);
  const float mb = rbf(mean), sd = rbf(sqrtf(rbf(rbf(var) + 1e-5f)));
  return (__float_as_uint(mb) >> 16) | (__float_as_uint(sd) & 0xffff0000u);
}
__device__ __forceinline__ unsigned ln_row(const char* h8, int t) {
  float s = 0.f, q = 0.f;
  for (int pl = 0; pl < 80; ++pl) {
    const u4_t v = *(const u4_t*)(h8 + (size_t)pl * 32768 + t * 16);
    for (int j = 0; j < 4; ++j) { const float a = bf_lo(v[j]), b = bf_hi(v[j]); s += a + b; q += a * a + b * b; }
  }
  return ln_pack(s, q);
}

template <int NDMA>
__device__ __forceinline__ void c_loader(char* smem, char* ws, const char* wbase, int w4, int lane, int t0, int jline, int xcd) {
  const unsigned lds0 = (unsigned)(size_t)(LAS char*)smem;
  const char* h8 = ws + OFF_H8;
  unsigned voffH[4], voffW[2];
#pragma unroll
  for (int k = 0; k < 2; ++k) {
    const int r = 8 * (2 * (w4 & 1) + k) + (lane >> 3), c = lane & 7;
    const int f = ((r >> 1) & 1) | ((r >> 2) & 2);
    voffW[k] = r * (FF * 2) + 128 * jline + (((c >> 1) ^ f) * 32) + (c & 1) * 16;
  }
#pragma unroll
  for (int k = 0; k < 4; ++k) {
    const int idx = 64 * (w4 + 4 * k) + lane, pl = idx / C_T, tt = idx % C_T;
    const int t = min(t0 + tt, SL - 1);
    voffH[k] = pl * 32768 + t * 16;
  }
  const unsigned ldsW = C_GOFF + (w4 >> 1) * 4096 + (w4 & 1) * 2048;
  auto issue = [&](int step, unsigned so) {
#pragma unroll
    for (int k = 0; k < 2; ++k) glds16(wbase + (size_t)step * (32 * FF * 2), voffW[k], lds0 + so + ldsW + k * 1024);
#pragma unroll
    for (int k = 0; k < NDMA - 2; ++k) glds16(h8 + (size_t)step * (4 * 32768), voffH[k], lds0 + so + (w4 + 4 * k) * 1024);
  };
  constexpr unsigned S = C_SLOT;
  issue(0, 0); issue(1, S); issue(2, 2 * S); issue(3, 3 * S);
  wait_vm<3 * NDMA>(); bar();
  issue(4, 4 * S); issue(5, 5 * S);
  wait_vm<4 * NDMA>(); bar();
  unsigned sw = 0;
#pragma unroll 1
  for (int b = 0; b < 13; ++b) {
    issue(b + 6, sw);
    sw += S; if (sw == C_NSLOT * S) sw = 0;
    wait_vm<4 * NDMA>();
    bar();
  }
  issue(19, sw);
  wait_vm<4 * NDMA>(); bar();
  const int tl = min(60 * w4 + lane, C_T - 1), tg = min(t0 + tl, SL - 1);
  const gu64* gp = (const gu64*)(ws + OFF_G2) + tg;
  const float* csp = (const float*)(ws + OFF_CS) + ((lane >> 4) & 1) * FF + 64 * jline + 16 * w4 + (lane & 15);
  const char* tb = ws + OFF_WP2 + (size_t)(4 * (xcd & 1)) * (54 * 5120);
  const int tl0 = ((int)(blockIdx.x >> 3) * 4 + w4) * 68;
  u2_t gv; float csv; unsigned tv0, tv1;
  asm volatile("global_load_dwordx2 %0, %1, off sc1" : "=v"(gv) : "v"(gp) : "memory");
  asm volatile("global_load_dword %0, %1, off" : "=v"(csv) : "v"(csp) : "memory");
  asm volatile("global_load_dword %0, %1, off" : "=v"(tv0) : "v"(tb + (size_t)min(tl0 + lane, 8639) * 128) : "memory");
  asm volatile("global_load_dword %0, %1, off" : "=v"(tv1) : "v"(tb + (size_t)min(tl0 + 64 + (lane & 3), 8639) * 128) : "memory");
  wait_vm<3 * NDMA + 4>(); bar();
  wait_vm<2 * NDMA + 4>(); bar();
  wait_vm<1 * NDMA + 4>(); bar();
  asm volatile("s_waitcnt vmcnt(0)" : "+v"(gv), "+v"(csv), "+v"(tv0), "+v"(tv1)::"memory");
  bar();
  {
    bool ok = gv.y == 1u;
    for (int spin = 0; spin < 4096 && !__all(ok); ++spin) {
      const unsigned long long g = __hip_atomic_load(gp, __ATOMIC_RELAXED, __HIP_MEMORY_SCOPE_AGENT);
      gv.x = (unsigned)g; ok = (unsigned)(g >> 32) == 1u;
      __builtin_amdgcn_s_sleep(2);
    }
    if (!__all(ok)) gv.x = ln_row(h8, tg);
    if (lane < 60) *(float*)(smem + C_STAT + (60 * w4 + lane) * 4) = bf_lo(gv.x);
    if (lane < 32) *(float*)(smem + C_CS + (lane >> 4) * 256 + (16 * w4 + (lane & 15)) * 4) = csv;
  }
  asm volatile("s_waitcnt lgkmcnt(0)" ::: "memory");
  bar();
}

struct CFrag { b8_t A[2][2]; b8_t B[8]; };

__global__ __launch_bounds__(512, 2) void kC(const u16* __restrict__ wg, const u16* __restrict__ wu, char* __restrict__ ws) {
  extern __shared__ __attribute__((aligned(16))) char smem[];
  const int tid = threadIdx.x, lane = tid & 63;
  const int w = __builtin_amdgcn_readfirstlane(tid >> 6);
  int panel, jline;
  const int xcd = blockIdx.x & 7, rank = blockIdx.x >> 3;
  {
    const int g4 = xcd >> 1, a = xcd & 1, nl = (g4 < 3) ? 7 : 6, total = 9 * nl, n0 = (total + 1) >> 1;
    if (a == 0 ? rank >= n0 : rank >= total - n0) {
      const int sp = (xcd < 6) ? (xcd >> 1) : 3 + 5 * (xcd - 6) + (rank - 27);
      const int wv = sp * 8 + w;
      if (w == 0) STAMP(2, 0);
      for (int tile = wv; tile < 128; tile += 104) {
        const int t = 16 * tile + (lane & 15);
        const char* hp = ws + OFF_H8 + (size_t)(lane >> 4) * 32768 + t * 16;
        u4_t v[20];
#pragma unroll
        for (int i = 0; i < 20; ++i) v[i] = *(const u4_t*)(hp + (size_t)i * (4 * 32768));
        float s0 = 0.f, s1 = 0.f, q0 = 0.f, q1 = 0.f;
        unsigned k_both = 0x3f803f80u;
        asm volatile("" : "+s"(k_both));
#pragma unroll
        for (int i = 0; i < 20; ++i) {
          s0 = dot2bf(v[i][0], k_both, s0); q0 = dot2bf(v[i][0], v[i][0], q0);
          s1 = dot2bf(v[i][1], k_both, s1); q1 = dot2bf(v[i][1], v[i][1], q1);
          s0 = dot2bf(v[i][2], k_both, s0); q0 = dot2bf(v[i][2], v[i][2], q0);
          s1 = dot2bf(v[i][3], k_both, s1); q1 = dot2bf(v[i][3], v[i][3], q1);
        }
        float s = s0 + s1, q = q0 + q1;
        s += __shfl_xor(s, 16); q += __shfl_xor(q, 16);
        s += __shfl_xor(s, 32); q += __shfl_xor(q, 32);
        if (lane < 16)
          __hip_atomic_store((gu64*)(ws + OFF_G2) + t, (1ull << 32) | ln_pack(s, q), __ATOMIC_RELAXED, __HIP_MEMORY_SCOPE_AGENT);
      }
      if (w == 0) STAMP(2, 7);
      return;
    }
    const int idx = rank + a * n0;
    panel = idx / nl; jline = 7 * g4 + idx % nl;
  }
  const int t0 = panel * C_T;
  if (w == 0) STAMP(2, 0);
  if (w >= 4) {
    const int w4 = w - 4;
    const char* wbase = (const char*)((w4 >> 1) ? wu : wg);
    if (w4 < 3) c_loader<6>(smem, ws, wbase, w4, lane, t0, jline, xcd);
    else c_loader<5>(smem, ws, wbase, w4, lane, t0, jline, xcd);
    return;
  }
  const int s = w >> 1, th = w & 1;
  const int fr = lane & 15, kg = lane >> 4, q = (lane & 15) >> 2, p = lane & 3;
  const unsigned hb0 = kg * 3840 + fr * 16 + 256 * 7;
  const unsigned hb1 = kg * 3840 + fr * 16 + 256 * (th ? 8 : 0);
  unsigned wa[2];
#pragma unroll
  for (int v = 0; v < 2; ++v) {
    const int sub = 2 * s + (v ? 1 - th : th);
    const int f = (q >> 1) | (2 * (kg & 1));
    wa[v] = C_GOFF + (8 * kg + q) * 128 + ((sub ^ f) * 32) + 8 * p;
  }
  f4_t ag[8][2], au[8][2];
#pragma unroll
  for (int u = 0; u < 8; ++u)
#pragma unroll
    for (int v = 0; v < 2; ++v) { ag[u][v] = f4_t{0.f, 0.f, 0.f, 0.f}; au[u][v] = f4_t{0.f, 0.f, 0.f, 0.f}; }
  auto load = [&](CFrag& F, unsigned so) {
    const char* sp = smem + so;
#pragma unroll
    for (int v = 0; v < 2; ++v)
#pragma unroll
      for (int m = 0; m < 2; ++m) {
        s4_t lo = tr_read(sp + wa[v] + m * 4096), hi = tr_read(sp + wa[v] + m * 4096 + 512);
        u2_t a = __builtin_bit_cast(u2_t, lo), b = __builtin_bit_cast(u2_t, hi);
        F.A[v][m] = __builtin_bit_cast(b8_t, (u4_t{a.x, a.y, b.x, b.y}));
      }
    F.B[0] = *(const b8_t*)(sp + hb0);
#pragma unroll
    for (int u = 1; u < 8; ++u) F.B[u] = *(const b8_t*)(sp + hb1 + 256 * (u - 1));
  };
  auto mma = [&](const CFrag& F) {
    ag[0][0] = __builtin_amdgcn_mfma_f32_16x16x32_bf16(F.A[0][0], F.B[0], ag[0][0], 0, 0, 0);
    au[0][0] = __builtin_amdgcn_mfma_f32_16x16x32_bf16(F.A[0][1], F.B[0], au[0][0], 0, 0, 0);
#pragma unroll
    for (int u = 1; u < 8; ++u)
#pragma unroll
      for (int v = 0; v < 2; ++v) {
        ag[u][v] = __builtin_amdgcn_mfma_f32_16x16x32_bf16(F.A[v][0], F.B[u], ag[u][v], 0, 0, 0);
        au[u][v] = __builtin_amdgcn_mfma_f32_16x16x32_bf16(F.A[v][1], F.B[u], au[u][v], 0, 0, 0);
      }
  };
  auto interleave = [&]() {
#pragma unroll
    for (int i = 0; i < 16; ++i) {
      __builtin_amdgcn_sched_group_barrier(0x008, 1, 0);
      __builtin_amdgcn_sched_group_barrier(0x100, 1, 0);
    }
    __builtin_amdgcn_sched_group_barrier(0x008, 14, 0);
  };
  CFrag F0, F1;
  bar();
  if (w == 0) STAMP(2, 1);
  load(F0, 0);
  unsigned sn = C_SLOT;
  SB;
#pragma unroll 1
  for (int b = 0; b < C_STEPS - 2; b += 2) {
    asm volatile("s_waitcnt lgkmcnt(0)" ::: "memory");
    bar(); SB;
    load(F1, sn);
    mma(F0);
    interleave();
    SB;
    sn += C_SLOT; if (sn == C_NSLOT * C_SLOT) sn = 0;
    asm volatile("s_waitcnt lgkmcnt(0)" ::: "memory");
    bar(); SB;
    load(F0, sn);
    mma(F1);
    interleave();
    SB;
    sn += C_SLOT; if (sn == C_NSLOT * C_SLOT) sn = 0;
  }
  asm volatile("s_waitcnt lgkmcnt(0)" ::: "memory");
  bar(); SB;
  load(F1, sn);
  mma(F0);
  interleave();
  SB;
  bar(); SB;
  f4_t csg[2], csu[2];
  float mu[8];
#pragma unroll
  for (int v = 0; v < 2; ++v) {
    const int sub = 2 * s + (v ? 1 - th : th);
    csg[v] = *(const f4_t*)(smem + C_CS + (16 * sub + 4 * kg) * 4);
    csu[v] = *(const f4_t*)(smem + C_CS + 256 + (16 * sub + 4 * kg) * 4);
  }
#pragma unroll
  for (int u = 0; u < 8; ++u) {
    const int tile = (u == 0) ? 7 : (th ? 7 + u : u - 1);
    mu[u] = *(const float*)(smem + C_STAT + (16 * tile + fr) * 4);
  }
  mma(F1);
  SB;
  if (w == 0) STAMP(2, 2);
  char* hh = ws + OFF_HH + (size_t)(2 * jline + s) * 1024 + lane * 16;
  char* sink = ws + OFF_DUMMY + lane * 16;
  auto epi = [&](auto thc) {
    constexpr int TH = decltype(thc)::value;
#pragma unroll
    for (int u = 0; u < 8; ++u) {
      const int tile = (u == 0) ? 7 : (TH ? 7 + u : u - 1);
      const int t16 = 15 * panel + tile;
      char* dst = (t16 < 128) ? hh + (size_t)t16 * (54 * 1024) : sink;
      u2_t o[2];
#pragma unroll
      for (int v = 0; v < 2; ++v) {
        if (u == 0 && v == 1) continue;
        const f4_t r = (ag[u][v] - mu[u] * csg[v]) * (au[u][v] - mu[u] * csu[v]);
        o[v] = u2_t{pk_bf16(r[0], r[1]), pk_bf16(r[2], r[3])};
      }
      if (u == 0) st8(dst + 8 * TH, o[0]);
      else st16(dst, u4_t{o[TH].x, o[TH].y, o[1 - TH].x, o[1 - TH].y});
    }
  };
  if (th) epi(IC(1)); else epi(IC(0));
  if (w == 0) STAMP(2, 7);
}

constexpr int D_SLOT = 15360, D_NSLOT = 9, D_STEPS = 18;
constexpr int D_LDS = D_NSLOT * D_SLOT;

template <int NDMA>
__device__ __forceinline__ void d_loader(char* smem, const char* wp, int w4, int lane) {
  const unsigned lds0 = (unsigned)(size_t)(LAS char*)smem;
  const unsigned voff = lane * 16;
  auto issue = [&](int step, unsigned so) {
#pragma unroll
    for (int k = 0; k < NDMA; ++k) {
      const int i = w4 + 4 * k;
      glds16(wp + (size_t)step * D_SLOT + i * 1024, voff, lds0 + so + i * 1024);
    }
  };
  constexpr unsigned S = D_SLOT;
  issue(0, 0); issue(1, S); issue(2, 2 * S); issue(3, 3 * S);
  wait_vm<3 * NDMA>(); bar();
  issue(4, 4 * S); issue(5, 5 * S);
  wait_vm<4 * NDMA>(); bar();
  issue(6, 6 * S); issue(7, 7 * S);
  wait_vm<5 * NDMA>(); bar();
  issue(8, 8 * S); issue(9, 0);
  unsigned sw = S;
#pragma unroll 1
  for (int b = 2; b < 10; ++b) {
    wait_vm<6 * NDMA>();
    bar();
    issue(b + 8, sw);
    sw += S; if (sw == D_NSLOT * S) sw = 0;
  }
  wait_vm<6 * NDMA>(); bar();
  wait_vm<5 * NDMA>(); bar();
  wait_vm<4 * NDMA>(); bar();
  wait_vm<3 * NDMA>(); bar();
  wait_vm<2 * NDMA>(); bar();
  wait_vm<1 * NDMA>(); bar();
  wait_vm<0>(); bar();
  bar();
}

__global__ __launch_bounds__(512, 2) void kD(char* __restrict__ ws, u16* __restrict__ out) {
  extern __shared__ __attribute__((aligned(16))) char smem[];
  const int tid = threadIdx.x, lane = tid & 63;
  const int w = __builtin_amdgcn_readfirstlane(tid >> 6);
  const int xcd = blockIdx.x & 7, rank = blockIdx.x >> 3;
  const int it = 8 * (xcd >> 1) + (rank & 7), dg = 4 * (xcd & 1) + (rank >> 3);
  if (w == 0) STAMP(3, 0);
  if (w >= 4) {
    const int w4 = w - 4;
    const char* wp = ws + OFF_WP2 + (size_t)dg * (54 * 5120);
    if (w4 < 3) d_loader<4>(smem, wp, w4, lane);
    else d_loader<3>(smem, wp, w4, lane);
    return;
  }
  const int fr = lane & 15, g = lane >> 4;
  const int t = 64 * it + 16 * w + fr;
  const char* hp = ws + OFF_HH + (size_t)(4 * it + w) * (54 * 1024);
  const unsigned voff = lane * 16;
  u4_t R[9];
#pragma unroll
  for (int i = 0; i < 9; ++i)
    asm volatile("global_load_dwordx4 %0, %1, %2" : "=v"(R[i]) : "v"(voff), "s"(hp + (size_t)i * 1024) : "memory");
  u2_t res[5]; unsigned st;
#pragma unroll
  for (int dt = 0; dt < 5; ++dt) {
    const int d = 80 * dg + 16 * dt + 4 * g;
    const char* rp = ws + OFF_H8 + ((size_t)(d >> 3) * SL + t) * 16 + (d & 4) * 2;
    asm volatile("global_load_dwordx2 %0, %1, off" : "=v"(res[dt]) : "v"(rp) : "memory");
  }
  asm volatile("global_load_dword %0, %1, off" : "=v"(st) : "v"(ws + OFF_G2 + (size_t)t * 8) : "memory");
  f4_t acc[5];
#pragma unroll
  for (int dt = 0; dt < 5; ++dt) acc[dt] = f4_t{0.f, 0.f, 0.f, 0.f};
  b8_t A0[5], A1[5];
  auto rdA = [&](b8_t (&A)[5], unsigned off) {
    const char* sp = smem + off + lane * 16;
#pragma unroll
    for (int dt = 0; dt < 5; ++dt) A[dt] = *(const b8_t*)(sp + dt * 1024);
  };
  bar();
  if (w == 0) STAMP(3, 1);
  rdA(A0, 0);
  unsigned so = 0;
  auto kstep = [&](auto mode, auto idx, b8_t (&Ac)[5], b8_t (&An)[5], const char* hnext) {
    constexpr int MODE = decltype(mode)::value, i = decltype(idx)::value, kk = i % 3;
    if (kk == 0) bar();
    unsigned nx = so + (kk + 1) * 5120;
    if (kk == 2) { so += D_SLOT; if (so == D_NSLOT * D_SLOT) so = 0; nx = so; }
    SB;
    if (!(MODE == 2 && i == 8)) rdA(An, nx);
    asm volatile("s_waitcnt vmcnt(%1)" : "+v"(R[i]) : "i"(MODE == 0 ? 14 : MODE == 1 ? 8 : 8 - i) : "memory");
    const b8_t B = __builtin_bit_cast(b8_t, R[i]);
#pragma unroll
    for (int dt = 0; dt < 5; ++dt) acc[dt] = __builtin_amdgcn_mfma_f32_16x16x32_bf16(Ac[dt], B, acc[dt], 0, 0, 0);
    SB;
    if (MODE != 2) asm volatile("global_load_dwordx4 %0, %1, %2" : "+v"(R[i]) : "v"(voff), "s"(hnext + (size_t)i * 1024) : "memory");
    SB;
  };
  auto round = [&](auto mode, b8_t (&P)[5], b8_t (&Q)[5], const char* hnext) {
    kstep(mode, IC(0), P, Q, hnext); kstep(mode, IC(1), Q, P, hnext); kstep(mode, IC(2), P, Q, hnext);
    kstep(mode, IC(3), Q, P, hnext); kstep(mode, IC(4), P, Q, hnext); kstep(mode, IC(5), Q, P, hnext);
    kstep(mode, IC(6), P, Q, hnext); kstep(mode, IC(7), Q, P, hnext); kstep(mode, IC(8), P, Q, hnext);
  };
  round(IC(0), A0, A1, hp + 9 * 1024);
#pragma unroll 1
  for (int r = 1; r < 5; r += 2) {
    round(IC(1), A1, A0, hp + (size_t)(9 * (r + 1)) * 1024);
    round(IC(1), A0, A1, hp + (size_t)(9 * (r + 2)) * 1024);
  }
  round(IC(2), A1, A0, hp);
  if (w == 0) STAMP(3, 2);
  asm volatile("s_waitcnt vmcnt(0)" : "+v"(res[0]), "+v"(res[1]), "+v"(res[2]), "+v"(res[3]), "+v"(res[4]), "+v"(st)::"memory");
  const float sd = bf_hi(st), r2 = 1.0f / (sd * sd);
#pragma unroll
  for (int dt = 0; dt < 5; ++dt) {
    const int d = 80 * dg + 16 * dt + 4 * g;
    const float h0 = bf_lo(res[dt].x), h1 = bf_hi(res[dt].x), h2 = bf_lo(res[dt].y), h3 = bf_hi(res[dt].y);
    st8(out + (size_t)t * DM + d, u2_t{pk_bf16(h0 + r2 * acc[dt][0], h1 + r2 * acc[dt][1]), pk_bf16(h2 + r2 * acc[dt][2], h3 + r2 * acc[dt][3])});
  }
  if (w == 0) STAMP(3, 7);
}

extern "C" void kernel_launch(void* const* d_in, const int* in_sizes, int n_in, void* d_out, int out_size, void* d_ws,
                              size_t ws_size, hipStream_t stream) {
  char* ws = (char*)d_ws;
  (void)hipFuncSetAttribute((const void*)kA, hipFuncAttributeMaxDynamicSharedMemorySize, A_LDS);
  (void)hipFuncSetAttribute((const void*)kC, hipFuncAttributeMaxDynamicSharedMemorySize, C_LDS);
  (void)hipFuncSetAttribute((const void*)kD, hipFuncAttributeMaxDynamicSharedMemorySize, D_LDS);
  kA<<<256, 1024, A_LDS, stream>>>((const u16*)d_in[0], (const u16*)d_in[1], (const u16*)d_in[2], (const u16*)d_in[3], (const u16*)d_in[4],
                                   (const u16*)d_in[5], (const u16*)d_in[6], (const u16*)d_in[7], (const u16*)d_in[12], ws);
  kB<<<256, 512, B_LDS, stream>>>((const u16*)d_in[8], (const u16*)d_in[10], (const u16*)d_in[11], ws);
  kC<<<256, 512, C_LDS, stream>>>((const u16*)d_in[10], (const u16*)d_in[11], ws);
  kD<<<256, 512, D_LDS, stream>>>(ws, (u16*)d_out);
}
```

```cpp
#define CODEPF
#include <hip/hip_runtime.h>
#include <hip/hip_bf16.h>
#include <stdint.h>
#include <type_traits>

typedef unsigned short u16;
typedef _Float16 h2_t __attribute__((ext_vector_type(2)));
typedef _Float16 h8_t __attribute__((ext_vector_type(8)));
typedef __bf16 b2_t __attribute__((ext_vector_type(2)));
typedef __bf16 b8_t __attribute__((ext_vector_type(8)));
typedef float f2_t __attribute__((ext_vector_type(2)));
typedef float f4_t __attribute__((ext_vector_type(4)));
typedef unsigned u2_t __attribute__((ext_vector_type(2)));
typedef unsigned u4_t __attribute__((ext_vector_type(4)));
typedef short s4_t __attribute__((ext_vector_type(4)));
typedef __attribute__((address_space(1))) unsigned long long gu64;
#define LAS __attribute__((address_space(3)))
#define SB __builtin_amdgcn_sched_barrier(0)
#define IC(n) std::integral_constant<int, n>{}

constexpr int SL = 2048;
constexpr int DM = 640;
constexpr int FF = 1728;

constexpr size_t OFF_X4 = 0;
constexpr size_t OFF_S1 = 0x280000;
constexpr size_t OFF_KD = 0x290000;
constexpr size_t OFF_INVS = 0x590000;
constexpr size_t OFF_H8 = 0x5A0000;
constexpr size_t OFF_HH = 0x820000;
constexpr size_t OFF_WP2 = 0xF00000;
constexpr size_t OFF_CS = 0x1120000;
constexpr size_t OFF_G2 = 0x1130000;
constexpr int KRS = 1344;
constexpr int KDS = 4864;
constexpr int KD_O = 2240;
constexpr int KD_DR = 4384;

#define STAMP(k, i)

__device__ __forceinline__ float bf_lo(unsigned d) { return __uint_as_float(d << 16); }
__device__ __forceinline__ float bf_hi(unsigned d) { return __uint_as_float(d & 0xffff0000u); }
__device__ __forceinline__ float bf2f(u16 v) { return __uint_as_float((unsigned)v << 16); }
__device__ __forceinline__ unsigned pk_bf16(float a, float b) {
  f2_t v = {a, b};
  return __builtin_bit_cast(unsigned, __builtin_convertvector(v, b2_t));
}
__device__ __forceinline__ unsigned pk_f16(float a, float b) {
  f2_t v = {a, b};
  return __builtin_bit_cast(unsigned, __builtin_convertvector(v, h2_t));
}
__device__ __forceinline__ float rbf(float a) { return bf_lo(pk_bf16(a, 0.f)); }
template <int N> __device__ __forceinline__ float dpp_ror_add(float v) {
  int r = __builtin_amdgcn_update_dpp(0, __float_as_int(v), 0x120 + N, 0xf, 0xf, false);
  return v + __int_as_float(r);
}
__device__ __forceinline__ float row16_sum(float v) {
  v = dpp_ror_add<8>(v); v = dpp_ror_add<4>(v); v = dpp_ror_add<2>(v); v = dpp_ror_add<1>(v);
  return v;
}
__device__ __forceinline__ float wave_sum(float v) {
  v = row16_sum(v);
  const int b = __float_as_int(v);
  return (__int_as_float(__builtin_amdgcn_readlane(b, 0)) + __int_as_float(__builtin_amdgcn_readlane(b, 16))) +
         (__int_as_float(__builtin_amdgcn_readlane(b, 32)) + __int_as_float(__builtin_amdgcn_readlane(b, 48)));
}
template <typename T> __device__ __forceinline__ T ldnt(const T* p) { return __builtin_nontemporal_load(p); }
__device__ __forceinline__ void wave_fence() {
  __builtin_amdgcn_fence(__ATOMIC_RELEASE, "wavefront");
  __builtin_amdgcn_wave_barrier();
  __builtin_amdgcn_fence(__ATOMIC_ACQUIRE, "wavefront");
}
__device__ __forceinline__ s4_t tr_read(const char* p) {
  return __builtin_amdgcn_ds_read_tr16_b64_v4i16((LAS s4_t*)p);
}
__device__ __forceinline__ void glds16(const void* sbase, unsigned voff, unsigned lds_addr) {
  asm volatile("s_mov_b32 m0, %2\n\ts_nop 0\n\tglobal_load_lds_dwordx4 %0, %1" ::"v"(voff), "s"(sbase), "s"(lds_addr) : "memory");
}
template <int N> __device__ __forceinline__ void wait_vm() { asm volatile("s_waitcnt vmcnt(%0)" ::"i"(N) : "memory"); }
__device__ __forceinline__ void bar() { asm volatile("s_barrier" ::: "memory"); }
__device__ __forceinline__ float dot2bf(unsigned a, unsigned b, float c) {
  return __builtin_amdgcn_fdot2_f32_bf16(__builtin_bit_cast(b2_t, a), __builtin_bit_cast(b2_t, b), c, false);
}

#define ST_MODE 0
__device__ __forceinline__ void st16(void* p, u4_t v) {
  *(u4_t*)p = v;
}
__device__ __forceinline__ void st8(void* p, u2_t v) {
  *(u2_t*)p = v;
}

struct Cw { f2_t w, ws; };
__device__ __forceinline__ Cw mkw(f2_t w) { Cw r; r.w = w; r.ws = f2_t{-w.y, w.x}; return r; }
__device__ __forceinline__ f2_t cmulw(f2_t a, const Cw& W) {
  return __builtin_elementwise_fma(f2_t{a.y, a.y}, W.ws, f2_t{a.x, a.x} * W.w);
}
__device__ __forceinline__ f2_t cexp_l(float er, float wr, float l) {
  float mag = __builtin_amdgcn_exp2f(er * l);
  float rev = __builtin_amdgcn_fractf(wr * l);
  return f2_t{mag * __builtin_amdgcn_cosf(rev), mag * __builtin_amdgcn_sinf(rev)};
}
__device__ __forceinline__ float pow2_scale(float bound, int target) {
  unsigned e = (__float_as_uint(bound) >> 23) & 0xff;
  int k = target - ((int)e - 126);
  k = k > 126 ? 126 : (k < -126 ? -126 : k);
  return (bound > 0.f) ? __uint_as_float((unsigned)(k + 127) << 23) : 1.0f;
}
__device__ __forceinline__ float pow2_inv(float p) { return __uint_as_float(0x7f000000u - __float_as_uint(p)); }

constexpr int A_KBLK = 214;
constexpr int KG_R = 0, KG_L0 = 4352, KG_L1 = 8704, KG_HW = 8704 + 25 * 272;
constexpr int KG_CH = 2 * KG_HW + 16;
constexpr int A_SIDE = 3 * KG_CH;
constexpr int A_LDS = A_SIDE + 20736;

__device__ __forceinline__ void pack_wd(const u16* __restrict__ wd, char* __restrict__ ws, char* buf, int u, int lane) {
  const int ks = u >> 3, dg = u & 7;
#pragma unroll
  for (int i = 0; i < 5; ++i) {
    const int idx = i * 64 + lane, row = idx / 10, c = idx % 10;
    __builtin_amdgcn_global_load_lds((const unsigned*)((const char*)wd + (size_t)(32 * ks + row) * 1280 + 160 * dg + 16 * c),
                                     (LAS unsigned*)(buf + i * 1024), 16, 0, 2);
  }
  asm volatile("s_waitcnt vmcnt(0)" ::: "memory");
  wave_fence();
  const int g = lane >> 4, q = (lane & 15) >> 2, p = lane & 3;
  char* dst = ws + OFF_WP2 + ((size_t)(dg * 54 + ks) * 5) * 1024 + lane * 16;
#pragma unroll
  for (int dt = 0; dt < 5; ++dt) {
    s4_t aa = tr_read(buf + (4 * g + q) * 160 + 32 * dt + 8 * p);
    s4_t bb = tr_read(buf + (16 + 4 * g + q) * 160 + 32 * dt + 8 * p);
    u2_t ua = __builtin_bit_cast(u2_t, aa), ub = __builtin_bit_cast(u2_t, bb);
    st16(dst + dt * 1024, u4_t{ua.x, ua.y, ub.x, ub.y});
  }
}

extern "C" __global__ __launch_bounds__(1024) void kA(const u16* __restrict__ x, const u16* __restrict__ Are, const u16* __restrict__ Aim,
                                           const u16* __restrict__ Bre, const u16* __restrict__ Bim, const u16* __restrict__ Cre,
                                           const u16* __restrict__ Cim, const u16* __restrict__ logdt, const u16* __restrict__ wd,
                                           char* __restrict__ ws) {
  extern __shared__ __attribute__((aligned(16))) char smem[];
  const int tid = threadIdx.x, lane = tid & 63;
  const int w = __builtin_amdgcn_readfirstlane(tid >> 6);
  if (w == 0) STAMP(0, 0);
  if (blockIdx.x >= A_KBLK) {
    const int u = 344 + (blockIdx.x - A_KBLK) * 16 + w;
    if (u < 432) pack_wd(wd, ws, smem + w * 5120, u, lane);
    if (blockIdx.x >= 248 && w == 0) {
      unsigned long long a0, a1;
      asm volatile("s_getpc_b64 s[20:21]\n\ts_add_u32 s20, s20, kA@rel32@lo+4\n\ts_addc_u32 s21, s21, kA@rel32@hi+12\n\ts_mov_b64 %0, s[20:21]" : "=s"(a0) :: "s20", "s21", "scc");
      asm volatile("s_getpc_b64 s[20:21]\n\ts_add_u32 s20, s20, kD@rel32@lo+4\n\ts_addc_u32 s21, s21, kD@rel32@hi+12\n\ts_mov_b64 %0, s[20:21]" : "=s"(a1) :: "s20", "s21", "scc");
      const unsigned long long lo = a0 < a1 ? a0 : a1, n = ((a0 < a1 ? a1 - a0 : a0 - a1) + 8192) >> 7;
      unsigned sink = 0;
      for (unsigned long long i = lane; i < n; i += 64) { unsigned v; asm volatile("global_load_dword %0, %1, off" : "=v"(v) : "v"(lo + i * 128) : "memory"); sink = v; }
      asm volatile("s_waitcnt vmcnt(0)" :: "v"(sink) : "memory");
    }
    if (w == 0) STAMP(0, 7);
    return;
  }
  if (w >= 12) {
    const int w4 = w - 12;
    if (blockIdx.x >= 128) {
      pack_wd(wd, ws, smem + A_SIDE + w4 * 5120, (blockIdx.x - 128) * 4 + w4, lane);
      bar();
      return;
    }
    const int t0 = blockIdx.x * 16;
    const int r = lane >> 4, c = lane & 15, rl = w4 * 4 + r;
    char* xs = smem + A_SIDE;
    const u4_t* p = (const u4_t*)(x + (size_t)(t0 + rl) * DM);
    u4_t v[5];
#pragma unroll
    for (int i = 0; i < 5; ++i) v[i] = ldnt(p + c + 16 * i);
    if (w4 == 0 && lane < 16) ((unsigned long long*)(ws + OFF_G2))[t0 + lane] = 0ull;
    float s = 0.f;
#pragma unroll
    for (int i = 0; i < 5; ++i)
#pragma unroll
      for (int j = 0; j < 4; ++j) s += bf_lo(v[i][j]) + bf_hi(v[i][j]);
#pragma unroll
    for (int i = 0; i < 5; ++i) *(u4_t*)(xs + rl * 1296 + (c + 16 * i) * 16) = v[i];
    const float mean = row16_sum(s) * (1.0f / DM);
    float q = 0.f;
#pragma unroll
    for (int i = 0; i < 5; ++i)
#pragma unroll
      for (int j = 0; j < 4; ++j) {
        float a = bf_lo(v[i][j]) - mean, b = bf_hi(v[i][j]) - mean;
        q += a * a; q += b * b;
      }
    const float var = row16_sum(q) * (1.0f / DM);
    if (c == 0) {
      float mb = rbf(mean), sd = rbf(sqrtf(rbf(rbf(var) + 1e-5f)));
      ((unsigned*)(ws + OFF_S1))[t0 + rl] = (__float_as_uint(mb) >> 16) | (__float_as_uint(sd) & 0xffff0000u);
    }
    __syncthreads();
    const int t4 = w4 * 64 + lane;
#pragma unroll
    for (int it = 0; it < 5; ++it) {
      const int item = t4 + 256 * it, q4 = item >> 3, tp = item & 7;
      u2_t a = *(const u2_t*)(xs + (2 * tp) * 1296 + 8 * q4);
      u2_t b = *(const u2_t*)(xs + (2 * tp + 1) * 1296 + 8 * q4);
      st16(ws + OFF_X4 + ((size_t)q4 * SL + t0 + 2 * tp) * 8, u4_t{a.x, a.y, b.x, b.y});
    }
    if (w4 == 0) STAMP(0, 6);
    return;
  }
  const int cl = w >> 2, part = (w >> 1) & 1, hw = w & 1;
  const int chr = blockIdx.x * 3 + cl, ch = min(chr, DM - 1);
  char* chb = smem + cl * KG_CH;
  char* hb = chb + hw * KG_HW;
  const int n = ch * 128 + hw * 64 + lane;
  const u16 i_are = ldnt(Are + n), i_aim = ldnt(Aim + n), i_bre = ldnt(Bre + n), i_bim = ldnt(Bim + n), i_cre = ldnt(Cre + n), i_cim = ldnt(Cim + n);
  const float dt = rbf(expf(bf2f(logdt[ch])));
  const float are = dt * bf2f(i_are), aim = dt * bf2f(i_aim);
  const float bre = dt * bf2f(i_bre), bim = dt * bf2f(i_bim);
  const float cre = bf2f(i_cre), cim = bf2f(i_cim);
  f2_t c = {cre * bre - cim * bim, cre * bim + cim * bre};
  const float er = are * 1.44269504088896341f, wr = aim * 0.15915494309189535f;
  const float bnd = (fabsf(c.x) + fabsf(c.y)) * fmaxf(1.0f, __builtin_amdgcn_exp2f(er * 2048.f));
  const float bsum = wave_sum(bnd);
  if (w == 0) STAMP(0, 1);
  const float Sw = pow2_scale(bsum, 15);
  c *= Sw;
  const Cw z1 = mkw(cexp_l(er, wr, 1.f)), w128 = mkw(cexp_l(er, wr, 128.f));
  auto put = [&](char* buf, int row, f2_t v) { *(unsigned*)(buf + row * 272 + 4 * lane) = pk_f16(v.x, -v.y); };
  if (part == 0) {
    const Cw w8 = mkw(cexp_l(er, wr, 8.f));
    f2_t R = w8.w, v0 = c, v1 = cmulw(c, w128);
    *(unsigned*)(hb + KG_R + 4 * lane) = pk_f16(1.f, 0.f);
#pragma unroll
    for (int i = 0; i < 8; ++i) {
      *(unsigned*)(hb + KG_R + (2 * i + 1) * 272 + 4 * lane) = pk_f16(R.x, R.y);
      if (i < 7) {
        R = cmulw(R, w8);
        *(unsigned*)(hb + KG_R + (2 * i + 2) * 272 + 4 * lane) = pk_f16(R.x, R.y);
        R = cmulw(R, w8);
      }
      put(hb + KG_L0, i, v0); put(hb + KG_L0, 8 + i, v1);
      if (i < 7) { v0 = cmulw(v0, z1); v1 = cmulw(v1, z1); }
    }
  } else {
    const Cw z2 = mkw(cmulw(z1.w, z1));
    const Cw z4 = mkw(cmulw(z2.w, z2));
    f2_t T = cmulw(c, w128);
#pragma unroll
    for (int i1 = 2; i1 < 17; ++i1) {
      T = cmulw(T, w128);
      if (i1 < 4) {
        f2_t v = T;
#pragma unroll
        for (int i0 = 0; i0 < 4; ++i0) { put(hb + KG_L1, 4 * (i1 - 2) + i0, v); if (i0 < 3) v = cmulw(v, z2); }
      } else if (i1 < 8) {
        put(hb + KG_L1, 8 + 2 * (i1 - 4), T); put(hb + KG_L1, 8 + 2 * (i1 - 4) + 1, cmulw(T, z4));
      } else {
        put(hb + KG_L1, 16 + (i1 - 8), T);
      }
    }
  }
  if (part == 0 && lane == 0) ((float*)(chb + 2 * KG_HW))[hw] = bsum;
  __syncthreads();
  if (w == 0) STAMP(0, 2);
  if (part == 1 && hw == 1) return;
  const int tile = part ? 1 : 2 * hw;
  const int fr = lane & 15, kg = lane >> 4;
  const int row = (tile == 2) ? 16 + min(fr, 8) : fr;
  const int lo = (tile == 0) ? KG_L0 : KG_L1;
  f4_t acc[2];
#pragma unroll
  for (int h = 0; h < 2; ++h) {
    const char* hbb = chb + h * KG_HW;
    f4_t a = {0.f, 0.f, 0.f, 0.f};
#pragma unroll
    for (int ks = 0; ks < 4; ++ks) {
      const h8_t Bf = *(const h8_t*)(hbb + KG_R + fr * 272 + 64 * ks + 16 * kg);
      const h8_t Af = *(const h8_t*)(hbb + lo + row * 272 + 64 * ks + 16 * kg);
      a = __builtin_amdgcn_mfma_f32_16x16x32_f16(Af, Bf, a, 0, 0, 0);
    }
    acc[h] = a;
  }
  const float b0 = ((const float*)(chb + 2 * KG_HW))[0], b1 = ((const float*)(chb + 2 * KG_HW))[1];
  const float Sc = pow2_scale(b0 + b1, 15);
  const f4_t kv = acc[0] * (pow2_inv(pow2_scale(b0, 15)) * Sc) + acc[1] * (pow2_inv(pow2_scale(b1, 15)) * Sc);
  const unsigned p01 = pk_f16(kv[0], kv[1]), p23 = pk_f16(kv[2], kv[3]);
  char* kr = ws + OFF_KD + (size_t)ch * KRS;
  if (tile == 0) {
    *(u2_t*)(kr + 2 * (128 * (kg >> 1) + 4 * (kg & 1) + 8 * fr)) = u2_t{p01, p23};
    if (lane == 0) ((float*)(ws + OFF_INVS))[ch] = pow2_inv(Sc);
  } else if (tile == 1) {
    if (kg < 2) {
      *(u2_t*)(kr + 2 * (256 + 64 * kg + 4 * fr)) = u2_t{p01, p23};
    } else {
      *(unsigned*)(kr + 2 * (384 + 64 * (kg - 2) + 2 * fr)) = p01;
      *(unsigned*)(kr + 2 * (384 + 64 * (kg - 2) + 32 + 2 * fr)) = p23;
    }
  } else {
    u16* k3 = (u16*)kr + 512 + 64 * kg + fr;
    if (kg < 2) { k3[0] = (u16)p01; k3[16] = (u16)(p01 >> 16); k3[32] = (u16)p23; k3[48] = (u16)(p23 >> 16); }
    else if (lane == 32) k3[0] = (u16)p01;
  }
  if (w == 0) STAMP(0, 7);
}

constexpr int B_CONV = 160;
constexpr int B_KOFF = 0;
constexpr int B_UOFF = 4 * KDS;
constexpr int B_UCH = 4608;
constexpr int B_YCH = 10240;
constexpr int B_LDS = 40960;
#define B_DELAY 0

struct BFrag { h8_t B[8]; u2_t A[5][2]; };
__device__ __forceinline__ void mfma_f16(f4_t& d, u4_t a, h8_t b) {
  asm volatile("v_mfma_f32_16x16x32_f16 %0, %1, %2, %0" : "+v"(d) : "v"(a), "v"(b));
}
__device__ __forceinline__ void mfma_f16_0(f4_t& d, u4_t a, h8_t b) {
  asm volatile("v_mfma_f32_16x16x32_f16 %0, %1, %2, 0" : "=&v"(d) : "v"(a), "v"(b));
}

extern "C" __global__ __launch_bounds__(512) void kB(const u16* __restrict__ dskip, const u16* __restrict__ wg, const u16* __restrict__ wu,
                                          char* __restrict__ ws) {
  extern __shared__ __attribute__((aligned(16))) char smem[];
  const int tid = threadIdx.x, lane = tid & 63;
  const int w = __builtin_amdgcn_readfirstlane(tid >> 6);
  if (w == 0) STAMP(1, 0);
  if (blockIdx.x >= B_CONV) {
    const int xcd = blockIdx.x & 7, rk = (blockIdx.x - B_CONV) >> 3;
    const int g4 = xcd >> 1, a = xcd & 1, nl = (g4 < 3) ? 7 : 6;
    if (rk >= nl) return;
#pragma unroll
    for (int i = 0; i < B_DELAY; ++i) __builtin_amdgcn_s_sleep(16);
    const int jl = 7 * g4 + rk;
    const char* pm = (const char*)(a ? wu : wg) + (size_t)(80 * w + (lane >> 3)) * (FF * 2) + 128 * jl + (lane & 7) * 16;
    u4_t v[10];
#pragma unroll
    for (int i = 0; i < 10; ++i) v[i] = ldnt((const u4_t*)(pm + (size_t)i * 8 * (FF * 2)));
    const char* po = (const char*)(a ? wg : wu) + (size_t)(80 * w) * (FF * 2) + 128 * jl;
    unsigned t0v, t1v;
    asm volatile("global_load_dword %0, %1, off nt" : "=v"(t0v) : "v"(po + (size_t)lane * (FF * 2)) : "memory");
    asm volatile("global_load_dword %0, %1, off nt" : "=v"(t1v) : "v"(po + (size_t)(64 + (lane & 15)) * (FF * 2)) : "memory");
    f2_t s[4];
#pragma unroll
    for (int e = 0; e < 4; ++e) s[e] = f2_t{0.f, 0.f};
#pragma unroll
    for (int i = 0; i < 10; ++i)
#pragma unroll
      for (int jj = 0; jj < 4; ++jj) s[jj] += f2_t{bf_lo(v[i][jj]), bf_hi(v[i][jj])};
    float* pp = (float*)smem;
    *(f4_t*)(pp + (w * 8 + (lane >> 3)) * 68 + (lane & 7) * 8) = f4_t{s[0].x, s[0].y, s[1].x, s[1].y};
    *(f4_t*)(pp + (w * 8 + (lane >> 3)) * 68 + (lane & 7) * 8 + 4) = f4_t{s[2].x, s[2].y, s[3].x, s[3].y};
    __syncthreads();
    float acc = 0.f;
#pragma unroll
    for (int i = 0; i < 8; ++i) acc += pp[(w * 8 + i) * 68 + lane];
    __syncthreads();
    pp[w * 64 + lane] = acc;
    __syncthreads();
    if (w == 0) {
      float t = 0.f;
#pragma unroll
      for (int i = 0; i < 8; ++i) t += pp[i * 64 + lane];
      ((float*)(ws + OFF_CS))[a * FF + 64 * jl + lane] = t;
    }
    asm volatile("s_waitcnt vmcnt(0)" ::"v"(t0v), "v"(t1v) : "memory");
    if (w == 0) STAMP(1, 7);
    return;
  }
  const int a_ = blockIdx.x >> 3, x_ = blockIdx.x & 7;
  const int q = 16 * (a_ >> 1) + 2 * x_ + (a_ & 1);
  u4_t xv[2]; u2_t sv[2];
#pragma unroll
  for (int it = 0; it < 2; ++it) {
    const int j = tid + 512 * it;
    xv[it] = *(const u4_t*)(ws + OFF_X4 + ((size_t)q * SL + 2 * j) * 8);
    sv[it] = *(const u2_t*)(ws + OFF_S1 + 8 * j);
  }
  const int kch = tid >> 7, kc = tid & 127;
  const char* kr = ws + OFF_KD + (size_t)(4 * q + kch) * KRS;
  const int e0 = (kc < 32) ? 8 * kc : (kc < 64) ? 256 + 4 * (kc - 32) : 384 + 2 * (kc - 64);
  const u4_t kd4 = *(const u4_t*)(kr + 2 * e0);
  const unsigned kd5 = *(const unsigned*)(kr + 2 * e0 + 16);
  const unsigned kfirst = *(const u16*)kr, k1024 = *(const u16*)(kr + 2 * 512);
  const int dl = 127 - kc;
  const unsigned kfa = *(const u16*)(kr + 2 * (512 + max(2 * dl - 127, 0))), kfb = *(const u16*)(kr + 2 * (512 + max(2 * dl - 126, 0)));
  float dk[4];
#pragma unroll
  for (int c = 0; c < 4; ++c) dk[c] = bf2f(dskip[4 * q + c]);
  const float invs = ((const float*)(ws + OFF_INVS))[4 * q + (w & 3)];
  *(unsigned*)(smem + B_UOFF + (tid >> 7) * B_UCH + 4096 + 4 * (tid & 127)) = 0u;
  const int c = lane & 15, kg = lane >> 4;
  h8_t band9, band7;
#pragma unroll
  for (int j = 0; j < 8; ++j) {
    const int k = 8 * kg + 7 - j;
    band9[j] = (k >= c + 4 && k <= c + 12) ? (_Float16)1.0f : (_Float16)0.0f;
    band7[j] = (k >= c + 5 && k <= c + 11) ? (_Float16)1.0f : (_Float16)0.0f;
  }
  {
    char* ki = smem + B_KOFF + kch * KDS;
    const unsigned k0 = kd4[0] & 0xffffu;
    unsigned o0, o1, o2, o3, nx;
    if (kc < 32) { o0 = kd4[0]; o1 = kd4[1]; o2 = kd4[2]; o3 = kd4[3]; nx = kd5; }
    else if (kc < 64) {
      const unsigned k2 = kd4[0] >> 16, k4 = kd4[1] & 0xffffu, k6 = kd4[1] >> 16, k8 = kd4[2] & 0xffffu;
      o0 = k0 * 0x10001u; o1 = k2 | (k4 << 16); o2 = k4 * 0x10001u; o3 = k6 | (k8 << 16); nx = k8;
    } else {
      const unsigned k4 = kd4[0] >> 16, k8 = kd4[1] & 0xffffu;
      o0 = k0 * 0x10001u; o1 = k0 | (k4 << 16); o2 = k4 * 0x10001u; o3 = k8 * 0x10001u; nx = k8;
    }
    *(u4_t*)(ki + 16 * (kc + 2)) = u4_t{o0, o1, o2, o3};
    *(u4_t*)(ki + KD_O + 16 * (kc + 2)) = u4_t{__builtin_amdgcn_alignbit(o1, o0, 16), __builtin_amdgcn_alignbit(o2, o1, 16),
                                              __builtin_amdgcn_alignbit(o3, o2, 16), __builtin_amdgcn_alignbit(nx, o3, 16)};
    if (kc < 6) {
      const int cp = (kc < 2) ? kc : 128 + kc;
      const unsigned kk = k1024 * 0x10001u;
      const u4_t e = (kc == 2) ? u4_t{kk, kk, k1024, 0u} : u4_t{0u, 0u, 0u, 0u};
      const u4_t o = (kc == 2) ? u4_t{kk, kk, 0u, 0u} : u4_t{0u, 0u, 0u, (kc == 1) ? (kfirst << 16) : 0u};
      *(u4_t*)(ki + 16 * cp) = e;
      *(u4_t*)(ki + KD_O + 16 * cp) = o;
    }
    if (kc < 80) *(unsigned*)(ki + KD_DR + 4 * kc) = (dl >= 64) ? (kfa | (kfb << 16)) : 0u;
  }
  asm volatile("" : "+v"(band9), "+v"(band7));
  float z[2][8];
  if (w == 0) { STAMP(1, 1); }
#pragma unroll
  for (int it = 0; it < 2; ++it) {
    const int j = tid + 512 * it;
    const float m0 = bf_lo(sv[it].x), r0 = __builtin_amdgcn_rcpf(bf_hi(sv[it].x)), m1 = bf_lo(sv[it].y), r1 = __builtin_amdgcn_rcpf(bf_hi(sv[it].y));
    float x0[4] = {bf_lo(xv[it].x), bf_hi(xv[it].x), bf_lo(xv[it].y), bf_hi(xv[it].y)};
    float x1[4] = {bf_lo(xv[it].z), bf_hi(xv[it].z), bf_lo(xv[it].w), bf_hi(xv[it].w)};
#pragma unroll
    for (int cc = 0; cc < 4; ++cc) {
      const float u0 = (x0[cc] - m0) * r0, u1 = (x1[cc] - m1) * r1;
      *(unsigned*)(smem + B_UOFF + cc * B_UCH + 4 * (1023 - j)) = pk_f16(u1, u0);
      z[it][cc] = x0[cc] + u0 * dk[cc];
      z[it][4 + cc] = x1[cc] + u1 * dk[cc];
    }
  }
  __syncthreads();
  if (w == 0) STAMP(1, 2);
  f4_t acc[8];
  if (w < 4) {
    const char* ub = smem + B_UOFF + w * B_UCH + 4112 - 32 * c - 16 * kg - 512 * 7;
    const char* kb = smem + B_KOFF + w * KDS + ((c & 1) ? 0 : KD_O) + 4 * (12 + ((c + 1) >> 1) - 4 * kg);
    const unsigned* dr = (const unsigned*)(smem + B_KOFF + w * KDS + KD_DR) + (127 - c + 4 * kg);
    const char* uw = smem + B_UOFF + w * B_UCH + 4112 - 32 * c - 16 * kg;
    auto rdB = [&](BFrag& F, int i0, int p) { F.B[p] = *(const h8_t*)(ub + 512 * (7 - p) + 64 * i0); };
    auto rdA = [&](BFrag& F, int i0, int i1, int half) {
      const unsigned* ap = (const unsigned*)(kb + 64 * i0 + 512 * i1) + 2 * half;
      F.A[i1][half] = u2_t{ap[0], ap[1]};
    };
    auto frag = [&](const u2_t (&a)[2]) { return u4_t{a[0].x, a[0].y, a[1].x, a[1].y}; };
    auto rdnext = [&](BFrag& F, int i0, int m) {
      if (m < 8) rdB(F, i0, m); else if (m < 16) rdA(F, i0, (m - 8) >> 1, (m - 8) & 1);
    };
    auto step = [&](auto i0c, const BFrag& F, BFrag& N, auto nextc) {
      constexpr int I0 = decltype(i0c)::value, NX = decltype(nextc)::value;
      int m = 0;
#pragma unroll
      for (int i1 = 0; i1 < 5; ++i1) {
        if (i1 == 4 && I0 != 0) continue;
        const u4_t a = frag(F.A[i1]);
#pragma unroll
        for (int t = i1; t < 8; ++t) {
          if (I0 == 0 && i1 == 0) mfma_f16_0(acc[t], a, F.B[t]); else mfma_f16(acc[t], a, F.B[t - i1]);
          if (NX >= 0) rdnext(N, NX, m);
          SB;
          ++m;
        }
      }
    };
    BFrag F0, F1;
    h8_t U9[4], U7[4];
#pragma unroll
    for (int bt = 0; bt < 4; ++bt) { U9[bt] = *(const h8_t*)(uw - 512 * bt + 16); U7[bt] = *(const h8_t*)(uw - 512 * bt); }
#pragma unroll
    for (int m = 0; m < 16; ++m) rdnext(F0, 0, m);
    rdA(F0, 0, 4, 0); rdA(F0, 0, 4, 1);
    SB;
    f4_t X9[4], X7[4];
#pragma unroll
    for (int bt = 0; bt < 4; ++bt) { mfma_f16_0(X9[bt], __builtin_bit_cast(u4_t, U9[bt]), band9); mfma_f16_0(X7[bt], __builtin_bit_cast(u4_t, U7[bt]), band7); }
    SB;
    step(IC(0), F0, F1, IC(1));
    h8_t Vf[4]; u4_t Tf[4];
    {
      int m = 0;
#pragma unroll
      for (int i1 = 0; i1 < 4; ++i1) {
        const u4_t a = frag(F1.A[i1]);
#pragma unroll
        for (int t = i1; t < 8; ++t) {
          mfma_f16(acc[t], a, F1.B[t - i1]);
          rdnext(F0, 2, m);
          if (m < 4) {
            const u4_t vu = {pk_f16(X7[m][0], X9[m][0]), pk_f16(X7[m][1], X9[m][1]), pk_f16(X7[m][2], X9[m][2]), pk_f16(X7[m][3], X9[m][3])};
            Vf[m] = __builtin_bit_cast(h8_t, vu);
          }
          if (m >= 16 && m < 20) { const unsigned* tp = dr - 16 * (m - 16 + 4); Tf[m - 16] = u4_t{tp[0], tp[1], tp[2], tp[3]}; }
          SB;
          ++m;
        }
      }
    }
    step(IC(2), F0, F1, IC(3));
#pragma unroll
    for (int bt = 0; bt < 4; ++bt)
#pragma unroll
      for (int at = 4 + bt; at < 8; ++at) mfma_f16(acc[at], __builtin_bit_cast(u4_t, Vf[bt]), __builtin_bit_cast(h8_t, Tf[at - bt - 4]));
    SB;
    step(IC(3), F1, F0, IC(4));
    step(IC(4), F0, F1, IC(5));
    step(IC(5), F1, F0, IC(6));
    step(IC(6), F0, F1, IC(7));
    step(IC(7), F1, F0, IC(-1));
    asm volatile("s_nop 7\n\ts_nop 1" : "+v"(acc[0]), "+v"(acc[1]), "+v"(acc[2]), "+v"(acc[3]), "+v"(acc[4]), "+v"(acc[5]), "+v"(acc[6]), "+v"(acc[7]));
  }
  if (w == 0) STAMP(1, 3);
  __syncthreads();
  if (w < 4) {
#pragma unroll
    for (int t = 0; t < 8; ++t) *(f4_t*)(smem + w * B_YCH + t * 1280 + kg * 320 + c * 16) = acc[t] * invs;
  }
  __syncthreads();
#pragma unroll
  for (int it = 0; it < 2; ++it) {
    const int j = tid + 512 * it, t = 2 * j;
    const int yo = (t >> 8) * 1280 + ((t >> 2) & 3) * 320 + ((t >> 4) & 15) * 16 + (t & 3) * 4;
    float h0[4], h1[4];
#pragma unroll
    for (int cc = 0; cc < 4; ++cc) {
      f2_t y = *(const f2_t*)(smem + cc * B_YCH + yo);
      h0[cc] = z[it][cc] + y.x;
      h1[cc] = z[it][4 + cc] + y.y;
    }
    char* dst = ws + OFF_H8 + ((size_t)(q >> 1) * SL + t) * 16 + (q & 1) * 8;
    st8(dst, u2_t{pk_bf16(h0[0], h0[1]), pk_bf16(h0[2], h0[3])});
    st8(dst + 16, u2_t{pk_bf16(h1[0], h1[1]), pk_bf16(h1[2], h1[3])});
  }
  if (w == 0) STAMP(1, 7);
}

constexpr int C_T = 240;
constexpr int C_SLOT = 23552, C_NSLOT = 6, C_GOFF = 15360;
constexpr int C_STAT = C_NSLOT * C_SLOT;
constexpr int C_CS = C_STAT + 960;
constexpr int C_LDS = C_CS + 512;
constexpr int C_STEPS = 20;
constexpr size_t OFF_DUMMY = 0x1140000;

__device__ __forceinline__ unsigned ln_pack(float sum, float sq) {
  const float mean = sum * (1.0f / DM);
  const float var = fmaxf(sq * (1.0f / DM) - mean * mean, 0.f);
  const float mb = rbf(mean), sd = rbf(sqrtf(rbf(rbf(var) + 1e-5f)));
  return (__float_as_uint(mb) >> 16) | (__float_as_uint(sd) & 0xffff0000u);
}
__device__ __forceinline__ unsigned ln_row(const char* h8, int t) {
  float s = 0.f, q = 0.f;
  for (int pl = 0; pl < 80; ++pl) {
    const u4_t v = *(const u4_t*)(h8 + (size_t)pl * 32768 + t * 16);
    for (int j = 0; j < 4; ++j) { const float a = bf_lo(v[j]), b = bf_hi(v[j]); s += a + b; q += a * a + b * b; }
  }
  return ln_pack(s, q);
}

template <int NDMA>
__device__ __forceinline__ void c_loader(char* smem, char* ws, const char* wbase, int w4, int lane, int t0, int jline, int xcd) {
  const unsigned lds0 = (unsigned)(size_t)(LAS char*)smem;
  const char* h8 = ws + OFF_H8;
  unsigned voffH[4], voffW[2];
#pragma unroll
  for (int k = 0; k < 2; ++k) {
    const int r = 8 * (2 * (w4 & 1) + k) + (lane >> 3), c = lane & 7;
    const int f = ((r >> 1) & 1) | ((r >> 2) & 2);
    voffW[k] = r * (FF * 2) + 128 * jline + (((c >> 1) ^ f) * 32) + (c & 1) * 16;
  }
#pragma unroll
  for (int k = 0; k < 4; ++k) {
    const int idx = 64 * (w4 + 4 * k) + lane, pl = idx / C_T, tt = idx % C_T;
    const int t = min(t0 + tt, SL - 1);
    voffH[k] = pl * 32768 + t * 16;
  }
  const unsigned ldsW = C_GOFF + (w4 >> 1) * 4096 + (w4 & 1) * 2048;
  auto issue = [&](int step, unsigned so) {
#pragma unroll
    for (int k = 0; k < 2; ++k) glds16(wbase + (size_t)step * (32 * FF * 2), voffW[k], lds0 + so + ldsW + k * 1024);
#pragma unroll
    for (int k = 0; k < NDMA - 2; ++k) glds16(h8 + (size_t)step * (4 * 32768), voffH[k], lds0 + so + (w4 + 4 * k) * 1024);
  };
  constexpr unsigned S = C_SLOT;
  issue(0, 0); issue(1, S); issue(2, 2 * S); issue(3, 3 * S);
  wait_vm<3 * NDMA>(); bar();
  issue(4, 4 * S);
  wait_vm<2 * NDMA>(); bar();
  issue(5, 5 * S); issue(6, 0);
  unsigned sw = S;
#pragma unroll 1
  for (int j = 1; j < 7; ++j) {
    wait_vm<2 * NDMA>();
    bar();
    issue(2 * j + 5, sw);
    sw += S; if (sw == C_NSLOT * S) sw = 0;
    issue(2 * j + 6, sw);
    sw += S; if (sw == C_NSLOT * S) sw = 0;
  }
  wait_vm<2 * NDMA>(); bar();
  issue(19, sw);
  const int tl = min(60 * w4 + lane, C_T - 1), tg = min(t0 + tl, SL - 1);
  const gu64* gp = (const gu64*)(ws + OFF_G2) + tg;
  const float* csp = (const float*)(ws + OFF_CS) + ((lane >> 4) & 1) * FF + 64 * jline + 16 * w4 + (lane & 15);
  const char* tb = ws + OFF_WP2 + (size_t)(4 * (xcd & 1)) * (54 * 5120);
  const int tl0 = ((int)(blockIdx.x >> 3) * 4 + w4) * 68;
  u2_t gv; float csv; unsigned tv0, tv1;
  asm volatile("global_load_dwordx2 %0, %1, off sc1" : "=v"(gv) : "v"(gp) : "memory");
  asm volatile("global_load_dword %0, %1, off" : "=v"(csv) : "v"(csp) : "memory");
  asm volatile("global_load_dword %0, %1, off" : "=v"(tv0) : "v"(tb + (size_t)min(tl0 + lane, 8639) * 128) : "memory");
  asm volatile("global_load_dword %0, %1, off" : "=v"(tv1) : "v"(tb + (size_t)min(tl0 + 64 + (lane & 3), 8639) * 128) : "memory");
  wait_vm<NDMA + 4>(); bar();
  asm volatile("s_waitcnt vmcnt(0)" : "+v"(gv), "+v"(csv), "+v"(tv0), "+v"(tv1)::"memory");
  bar();
  {
    bool ok = gv.y == 1u;
    for (int spin = 0; spin < 4096 && !__all(ok); ++spin) {
      const unsigned long long g = __hip_atomic_load(gp, __ATOMIC_RELAXED, __HIP_MEMORY_SCOPE_AGENT);
      gv.x = (unsigned)g; ok = (unsigned)(g >> 32) == 1u;
      __builtin_amdgcn_s_sleep(2);
    }
    if (!__all(ok)) gv.x = ln_row(h8, tg);
    if (lane < 60) *(float*)(smem + C_STAT + (60 * w4 + lane) * 4) = bf_lo(gv.x);
    if (lane < 32) *(float*)(smem + C_CS + (lane >> 4) * 256 + (16 * w4 + (lane & 15)) * 4) = csv;
  }
  asm volatile("s_waitcnt lgkmcnt(0)" ::: "memory");
  bar();
}

struct CFrag { b8_t A[2][2]; b8_t B[8]; };

extern "C" __global__ __launch_bounds__(512, 2) void kC(const u16* __restrict__ wg, const u16* __restrict__ wu, char* __restrict__ ws) {
  extern __shared__ __attribute__((aligned(16))) char smem[];
  const int tid = threadIdx.x, lane = tid & 63;
  const int w = __builtin_amdgcn_readfirstlane(tid >> 6);
  int panel, jline;
  const int xcd = blockIdx.x & 7, rank = blockIdx.x >> 3;
  {
    const int g4 = xcd >> 1, a = xcd & 1, nl = (g4 < 3) ? 7 : 6, total = 9 * nl, n0 = (total + 1) >> 1;
    if (a == 0 ? rank >= n0 : rank >= total - n0) {
      const int sp = (xcd < 6) ? (xcd >> 1) : 3 + 5 * (xcd - 6) + (rank - 27);
      const int wv = sp * 8 + w;
      if (w == 0) STAMP(2, 0);
      for (int tile = wv; tile < 128; tile += 104) {
        const int t = 16 * tile + (lane & 15);
        const char* hp = ws + OFF_H8 + (size_t)(lane >> 4) * 32768 + t * 16;
        u4_t v[20];
#pragma unroll
        for (int i = 0; i < 20; ++i) v[i] = *(const u4_t*)(hp + (size_t)i * (4 * 32768));
        float s0 = 0.f, s1 = 0.f, q0 = 0.f, q1 = 0.f;
        unsigned k_both = 0x3f803f80u;
        asm volatile("" : "+s"(k_both));
#pragma unroll
        for (int i = 0; i < 20; ++i) {
          s0 = dot2bf(v[i][0], k_both, s0); q0 = dot2bf(v[i][0], v[i][0], q0);
          s1 = dot2bf(v[i][1], k_both, s1); q1 = dot2bf(v[i][1], v[i][1], q1);
          s0 = dot2bf(v[i][2], k_both, s0); q0 = dot2bf(v[i][2], v[i][2], q0);
          s1 = dot2bf(v[i][3], k_both, s1); q1 = dot2bf(v[i][3], v[i][3], q1);
        }
        float s = s0 + s1, q = q0 + q1;
        s += __shfl_xor(s, 16); q += __shfl_xor(q, 16);
        s += __shfl_xor(s, 32); q += __shfl_xor(q, 32);
        if (lane < 16)
          __hip_atomic_store((gu64*)(ws + OFF_G2) + t, (1ull << 32) | ln_pack(s, q), __ATOMIC_RELAXED, __HIP_MEMORY_SCOPE_AGENT);
      }
      if (w == 0) STAMP(2, 7);
      return;
    }
    const int idx = rank + a * n0;
    panel = idx / nl; jline = 7 * g4 + idx % nl;
  }
  const int t0 = panel * C_T;
  if (w == 0) STAMP(2, 0);
  if (w >= 4) {
    const int w4 = w - 4;
    const char* wbase = (const char*)((w4 >> 1) ? wu : wg);
    if (w4 < 3) c_loader<6>(smem, ws, wbase, w4, lane, t0, jline, xcd);
    else c_loader<5>(smem, ws, wbase, w4, lane, t0, jline, xcd);
    return;
  }
  const int s = w >> 1, th = w & 1;
  const int fr = lane & 15, kg = lane >> 4, q = (lane & 15) >> 2, p = lane & 3;
  const unsigned hb0 = kg * 3840 + fr * 16 + 256 * 7;
  const unsigned hb1 = kg * 3840 + fr * 16 + 256 * (th ? 8 : 0);
  unsigned wa[2];
#pragma unroll
  for (int v = 0; v < 2; ++v) {
    const int sub = 2 * s + (v ? 1 - th : th);
    const int f = (q >> 1) | (2 * (kg & 1));
    wa[v] = C_GOFF + (8 * kg + q) * 128 + ((sub ^ f) * 32) + 8 * p;
  }
  f4_t ag[8][2], au[8][2];
#pragma unroll
  for (int u = 0; u < 8; ++u)
#pragma unroll
    for (int v = 0; v < 2; ++v) { ag[u][v] = f4_t{0.f, 0.f, 0.f, 0.f}; au[u][v] = f4_t{0.f, 0.f, 0.f, 0.f}; }
  auto load = [&](CFrag& F, unsigned so) {
    const char* sp = smem + so;
#pragma unroll
    for (int v = 0; v < 2; ++v)
#pragma unroll
      for (int m = 0; m < 2; ++m) {
        s4_t lo = tr_read(sp + wa[v] + m * 4096), hi = tr_read(sp + wa[v] + m * 4096 + 512);
        u2_t a = __builtin_bit_cast(u2_t, lo), b = __builtin_bit_cast(u2_t, hi);
        F.A[v][m] = __builtin_bit_cast(b8_t, (u4_t{a.x, a.y, b.x, b.y}));
      }
    F.B[0] = *(const b8_t*)(sp + hb0);
#pragma unroll
    for (int u = 1; u < 8; ++u) F.B[u] = *(const b8_t*)(sp + hb1 + 256 * (u - 1));
  };
  auto mma = [&](const CFrag& F) {
    ag[0][0] = __builtin_amdgcn_mfma_f32_16x16x32_bf16(F.A[0][0], F.B[0], ag[0][0], 0, 0, 0);
    au[0][0] = __builtin_amdgcn_mfma_f32_16x16x32_bf16(F.A[0][1], F.B[0], au[0][0], 0, 0, 0);
#pragma unroll
    for (int u = 1; u < 8; ++u)
#pragma unroll
      for (int v = 0; v < 2; ++v) {
        ag[u][v] = __builtin_amdgcn_mfma_f32_16x16x32_bf16(F.A[v][0], F.B[u], ag[u][v], 0, 0, 0);
        au[u][v] = __builtin_amdgcn_mfma_f32_16x16x32_bf16(F.A[v][1], F.B[u], au[u][v], 0, 0, 0);
      }
  };
  auto interleave = [&]() {
#pragma unroll
    for (int i = 0; i < 16; ++i) {
      __builtin_amdgcn_sched_group_barrier(0x008, 1, 0);
      __builtin_amdgcn_sched_group_barrier(0x100, 1, 0);
    }
    __builtin_amdgcn_sched_group_barrier(0x008, 14, 0);
  };
  CFrag F0, F1;
  bar();
  if (w == 0) STAMP(2, 1);
  load(F0, 0);
  unsigned sn = C_SLOT;
  SB;
#pragma unroll 1
  for (int j = 0; j < 9; ++j) {
    asm volatile("s_waitcnt lgkmcnt(0)" ::: "memory");
    bar(); SB;
    load(F1, sn);
    mma(F0);
    interleave();
    SB;
    sn += C_SLOT; if (sn == C_NSLOT * C_SLOT) sn = 0;
    load(F0, sn);
    mma(F1);
    interleave();
    SB;
    sn += C_SLOT; if (sn == C_NSLOT * C_SLOT) sn = 0;
  }
  asm volatile("s_waitcnt lgkmcnt(0)" ::: "memory");
  bar(); SB;
  load(F1, sn);
  mma(F0);
  interleave();
  SB;
  bar(); SB;
  f4_t csg[2], csu[2];
  float mu[8];
#pragma unroll
  for (int v = 0; v < 2; ++v) {
    const int sub = 2 * s + (v ? 1 - th : th);
    csg[v] = *(const f4_t*)(smem + C_CS + (16 * sub + 4 * kg) * 4);
    csu[v] = *(const f4_t*)(smem + C_CS + 256 + (16 * sub + 4 * kg) * 4);
  }
#pragma unroll
  for (int u = 0; u < 8; ++u) {
    const int tile = (u == 0) ? 7 : (th ? 7 + u : u - 1);
    mu[u] = *(const float*)(smem + C_STAT + (16 * tile + fr) * 4);
  }
  mma(F1);
  SB;
  if (w == 0) STAMP(2, 2);
  char* hh = ws + OFF_HH + (size_t)(2 * jline + s) * 1024 + lane * 16;
  char* sink = ws + OFF_DUMMY + lane * 16;
  auto epi = [&](auto thc) {
    constexpr int TH = decltype(thc)::value;
#pragma unroll
    for (int u = 0; u < 8; ++u) {
      const int tile = (u == 0) ? 7 : (TH ? 7 + u : u - 1);
      const int t16 = 15 * panel + tile;
      char* dst = (t16 < 128) ? hh + (size_t)t16 * (54 * 1024) : sink;
      u2_t o[2];
#pragma unroll
      for (int v = 0; v < 2; ++v) {
        if (u == 0 && v == 1) continue;
        const f4_t r = (ag[u][v] - mu[u] * csg[v]) * (au[u][v] - mu[u] * csu[v]);
        o[v] = u2_t{pk_bf16(r[0], r[1]), pk_bf16(r[2], r[3])};
      }
      if (u == 0) st8(dst + 8 * TH, o[0]);
      else st16(dst, u4_t{o[TH].x, o[TH].y, o[1 - TH].x, o[1 - TH].y});
    }
  };
  if (th) epi(IC(1)); else epi(IC(0));
  if (w == 0) STAMP(2, 7);
}

constexpr int D_SLOT = 15360, D_NSLOT = 9, D_STEPS = 18;
constexpr int D_LDS = D_NSLOT * D_SLOT;

template <int NDMA>
__device__ __forceinline__ void d_loader(char* smem, const char* wp, int w4, int lane) {
  const unsigned lds0 = (unsigned)(size_t)(LAS char*)smem;
  const unsigned voff = lane * 16;
  auto issue = [&](int step, unsigned so) {
#pragma unroll
    for (int k = 0; k < NDMA; ++k) {
      const int i = w4 + 4 * k;
      glds16(wp + (size_t)step * D_SLOT + i * 1024, voff, lds0 + so + i * 1024);
    }
  };
  constexpr unsigned S = D_SLOT;
  issue(0, 0); issue(1, S); issue(2, 2 * S);
  wait_vm<2 * NDMA>(); bar();
  issue(3, 3 * S); issue(4, 4 * S); issue(5, 5 * S);
  wait_vm<4 * NDMA>(); bar();
  issue(6, 6 * S); issue(7, 7 * S);
  wait_vm<5 * NDMA>(); bar();
  issue(8, 8 * S); issue(9, 0);
  unsigned sw = S;
#pragma unroll 1
  for (int b = 2; b < 10; ++b) {
    wait_vm<6 * NDMA>();
    bar();
    issue(b + 8, sw);
    sw += S; if (sw == D_NSLOT * S) sw = 0;
  }
  wait_vm<6 * NDMA>(); bar();
  wait_vm<5 * NDMA>(); bar();
  wait_vm<4 * NDMA>(); bar();
  wait_vm<3 * NDMA>(); bar();
  wait_vm<2 * NDMA>(); bar();
  wait_vm<1 * NDMA>(); bar();
  wait_vm<0>(); bar();
  bar();
}

extern "C" __global__ __launch_bounds__(512, 2) void kD(char* __restrict__ ws, u16* __restrict__ out) {
  extern __shared__ __attribute__((aligned(16))) char smem[];
  const int tid = threadIdx.x, lane = tid & 63;
  const int w = __builtin_amdgcn_readfirstlane(tid >> 6);
  const int xcd = blockIdx.x & 7, rank = blockIdx.x >> 3;
  const int it = 8 * (xcd >> 1) + (rank & 7), dg = 4 * (xcd & 1) + (rank >> 3);
  if (w == 0) STAMP(3, 0);
  if (w >= 4) {
    const int w4 = w - 4;
    const char* wp = ws + OFF_WP2 + (size_t)dg * (54 * 5120);
    if (w4 < 3) d_loader<4>(smem, wp, w4, lane);
    else d_loader<3>(smem, wp, w4, lane);
    return;
  }
  const int fr = lane & 15, g = lane >> 4;
  const int t = 64 * it + 16 * w + fr;
  const char* hp = ws + OFF_HH + (size_t)(4 * it + w) * (54 * 1024);
  const unsigned voff = lane * 16;
  u4_t R[18];
#pragma unroll
  for (int i = 0; i < 9; ++i)
    asm volatile("global_load_dwordx4 %0, %1, %2" : "=v"(R[i]) : "v"(voff), "s"(hp + (size_t)i * 1024) : "memory");
  u2_t res[5]; unsigned st;
  f4_t acc[5];
#pragma unroll
  for (int dt = 0; dt < 5; ++dt) acc[dt] = f4_t{0.f, 0.f, 0.f, 0.f};
  b8_t A0[5], A1[5];
  auto rdA = [&](b8_t (&A)[5], unsigned off) {
    const char* sp = smem + off + lane * 16;
#pragma unroll
    for (int dt = 0; dt < 5; ++dt) A[dt] = *(const b8_t*)(sp + dt * 1024);
  };
  bar();
  if (w == 0) STAMP(3, 1);
  rdA(A0, 0);
  unsigned so = 0;
  auto kstep = [&](auto mode, auto idx, b8_t (&Ac)[5], b8_t (&An)[5], const char* hnext) {
    constexpr int MODE = decltype(mode)::value, i = decltype(idx)::value, kk = i % 3;
    if (kk == 0) bar();
    unsigned nx = so + (kk + 1) * 5120;
    if (kk == 2) { so += D_SLOT; if (so == D_NSLOT * D_SLOT) so = 0; nx = so; }
    SB;
    if (!(MODE == 2 && i == 17)) rdA(An, nx);
    constexpr int NV = (MODE == 0) ? (i < 9 ? 8 + i : 26 - i) : (MODE == 1) ? (i < 9 ? 25 - i : 17) : (i == 0 ? 17 : 23 - i);
    asm volatile("s_waitcnt vmcnt(%1)" : "+v"(R[i]) : "i"(NV) : "memory");
    const b8_t B = __builtin_bit_cast(b8_t, R[i]);
#pragma unroll
    for (int dt = 0; dt < 5; ++dt) acc[dt] = __builtin_amdgcn_mfma_f32_16x16x32_bf16(Ac[dt], B, acc[dt], 0, 0, 0);
    SB;
    if (MODE == 0 && i < 9)
      asm volatile("global_load_dwordx4 %0, %1, %2" : "=v"(R[i < 9 ? 9 + i : 0]) : "v"(voff), "s"(hp + (size_t)(9 + i) * 1024) : "memory");
    if (MODE != 2) asm volatile("global_load_dwordx4 %0, %1, %2" : "+v"(R[i]) : "v"(voff), "s"(hnext + (size_t)i * 1024) : "memory");
    if (MODE == 2 && i == 0) {
#pragma unroll
      for (int dt = 0; dt < 5; ++dt) {
        const int d = 80 * dg + 16 * dt + 4 * g;
        const char* rp = ws + OFF_H8 + ((size_t)(d >> 3) * SL + t) * 16 + (d & 4) * 2;
        asm volatile("global_load_dwordx2 %0, %1, off" : "=v"(res[dt]) : "v"(rp) : "memory");
      }
      asm volatile("global_load_dword %0, %1, off" : "=v"(st) : "v"(ws + OFF_G2 + (size_t)t * 8) : "memory");
    }
    SB;
  };
  auto round = [&](auto mode, const char* hnext) {
    kstep(mode, IC(0), A0, A1, hnext); kstep(mode, IC(1), A1, A0, hnext); kstep(mode, IC(2), A0, A1, hnext);
    kstep(mode, IC(3), A1, A0, hnext); kstep(mode, IC(4), A0, A1, hnext); kstep(mode, IC(5), A1, A0, hnext);
    kstep(mode, IC(6), A0, A1, hnext); kstep(mode, IC(7), A1, A0, hnext); kstep(mode, IC(8), A0, A1, hnext);
    kstep(mode, IC(9), A1, A0, hnext); kstep(mode, IC(10), A0, A1, hnext); kstep(mode, IC(11), A1, A0, hnext);
    kstep(mode, IC(12), A0, A1, hnext); kstep(mode, IC(13), A1, A0, hnext); kstep(mode, IC(14), A0, A1, hnext);
    kstep(mode, IC(15), A1, A0, hnext); kstep(mode, IC(16), A0, A1, hnext); kstep(mode, IC(17), A1, A0, hnext);
  };
  round(IC(0), hp + 18 * 1024);
  round(IC(1), hp + 36 * 1024);
  round(IC(2), hp);
  if (w == 0) STAMP(3, 2);
  asm volatile("s_waitcnt vmcnt(0)" : "+v"(res[0]), "+v"(res[1]), "+v"(res[2]), "+v"(res[3]), "+v"(res[4]), "+v"(st)::"memory");
  const float sd = bf_hi(st), r2 = 1.0f / (sd * sd);
#pragma unroll
  for (int dt = 0; dt < 5; ++dt) {
    const int d = 80 * dg + 16 * dt + 4 * g;
    const float h0 = bf_lo(res[dt].x), h1 = bf_hi(res[dt].x), h2 = bf_lo(res[dt].y), h3 = bf_hi(res[dt].y);
    st8(out + (size_t)t * DM + d, u2_t{pk_bf16(h0 + r2 * acc[dt][0], h1 + r2 * acc[dt][1]), pk_bf16(h2 + r2 * acc[dt][2], h3 + r2 * acc[dt][3])});
  }
  if (w == 0) STAMP(3, 7);
}

extern "C" void kernel_launch(void* const* d_in, const int* in_sizes, int n_in, void* d_out, int out_size, void* d_ws,
                              size_t ws_size, hipStream_t stream) {
  char* ws = (char*)d_ws;
  (void)hipFuncSetAttribute((const void*)kA, hipFuncAttributeMaxDynamicSharedMemorySize, A_LDS);
  (void)hipFuncSetAttribute((const void*)kC, hipFuncAttributeMaxDynamicSharedMemorySize, C_LDS);
  (void)hipFuncSetAttribute((const void*)kD, hipFuncAttributeMaxDynamicSharedMemorySize, D_LDS);
  kA<<<256, 1024, A_LDS, stream>>>((const u16*)d_in[0], (const u16*)d_in[1], (const u16*)d_in[2], (const u16*)d_in[3], (const u16*)d_in[4],
                                   (const u16*)d_in[5], (const u16*)d_in[6], (const u16*)d_in[7], (const u16*)d_in[12], ws);
  kB<<<256, 512, B_LDS, stream>>>((const u16*)d_in[8], (const u16*)d_in[10], (const u16*)d_in[11], ws);
  kC<<<256, 512, C_LDS, stream>>>((const u16*)d_in[10], (const u16*)d_in[11], ws);
  kD<<<256, 512, D_LDS, stream>>>(ws, (u16*)d_out);
}
```

```cpp
#define CODEPF
#define TLBPF 65536
#include <hip/hip_runtime.h>
#include <hip/hip_bf16.h>
#include <stdint.h>
#include <type_traits>

typedef unsigned short u16;
typedef _Float16 h2_t __attribute__((ext_vector_type(2)));
typedef _Float16 h8_t __attribute__((ext_vector_type(8)));
typedef __bf16 b2_t __attribute__((ext_vector_type(2)));
typedef __bf16 b8_t __attribute__((ext_vector_type(8)));
typedef float f2_t __attribute__((ext_vector_type(2)));
typedef float f4_t __attribute__((ext_vector_type(4)));
typedef unsigned u2_t __attribute__((ext_vector_type(2)));
typedef unsigned u4_t __attribute__((ext_vector_type(4)));
typedef short s4_t __attribute__((ext_vector_type(4)));
typedef __attribute__((address_space(1))) unsigned long long gu64;
#define LAS __attribute__((address_space(3)))
#define SB __builtin_amdgcn_sched_barrier(0)
#define IC(n) std::integral_constant<int, n>{}

constexpr int SL = 2048;
constexpr int DM = 640;
constexpr int FF = 1728;

constexpr size_t OFF_X4 = 0;
constexpr size_t OFF_S1 = 0x280000;
constexpr size_t OFF_KD = 0x290000;
constexpr size_t OFF_INVS = 0x590000;
constexpr size_t OFF_H8 = 0x5A0000;
constexpr size_t OFF_HH = 0x820000;
constexpr size_t OFF_WP2 = 0xF00000;
constexpr size_t OFF_CS = 0x1120000;
constexpr size_t OFF_G2 = 0x1130000;
constexpr int KRS = 1344;
constexpr int KDS = 4864;
constexpr int KD_O = 2240;
constexpr int KD_DR = 4384;

#define STAMP(k, i)

__device__ __forceinline__ float bf_lo(unsigned d) { return __uint_as_float(d << 16); }
__device__ __forceinline__ float bf_hi(unsigned d) { return __uint_as_float(d & 0xffff0000u); }
__device__ __forceinline__ float bf2f(u16 v) { return __uint_as_float((unsigned)v << 16); }
__device__ __forceinline__ unsigned pk_bf16(float a, float b) {
  f2_t v = {a, b};
  return __builtin_bit_cast(unsigned, __builtin_convertvector(v, b2_t));
}
__device__ __forceinline__ unsigned pk_f16(float a, float b) {
  f2_t v = {a, b};
  return __builtin_bit_cast(unsigned, __builtin_convertvector(v, h2_t));
}
__device__ __forceinline__ float rbf(float a) { return bf_lo(pk_bf16(a, 0.f)); }
template <int N> __device__ __forceinline__ float dpp_ror_add(float v) {
  int r = __builtin_amdgcn_update_dpp(0, __float_as_int(v), 0x120 + N, 0xf, 0xf, false);
  return v + __int_as_float(r);
}
__device__ __forceinline__ float row16_sum(float v) {
  v = dpp_ror_add<8>(v); v = dpp_ror_add<4>(v); v = dpp_ror_add<2>(v); v = dpp_ror_add<1>(v);
  return v;
}
__device__ __forceinline__ float wave_sum(float v) {
  v = row16_sum(v);
  const int b = __float_as_int(v);
  return (__int_as_float(__builtin_amdgcn_readlane(b, 0)) + __int_as_float(__builtin_amdgcn_readlane(b, 16))) +
         (__int_as_float(__builtin_amdgcn_readlane(b, 32)) + __int_as_float(__builtin_amdgcn_readlane(b, 48)));
}
template <typename T> __device__ __forceinline__ T ldnt(const T* p) { return __builtin_nontemporal_load(p); }
__device__ __forceinline__ void wave_fence() {
  __builtin_amdgcn_fence(__ATOMIC_RELEASE, "wavefront");
  __builtin_amdgcn_wave_barrier();
  __builtin_amdgcn_fence(__ATOMIC_ACQUIRE, "wavefront");
}
__device__ __forceinline__ s4_t tr_read(const char* p) {
  return __builtin_amdgcn_ds_read_tr16_b64_v4i16((LAS s4_t*)p);
}
__device__ __forceinline__ void glds16(const void* sbase, unsigned voff, unsigned lds_addr) {
  asm volatile("s_mov_b32 m0, %2\n\ts_nop 0\n\tglobal_load_lds_dwordx4 %0, %1" ::"v"(voff), "s"(sbase), "s"(lds_addr) : "memory");
}
template <int N> __device__ __forceinline__ void wait_vm() { asm volatile("s_waitcnt vmcnt(%0)" ::"i"(N) : "memory"); }
__device__ __forceinline__ void bar() { asm volatile("s_barrier" ::: "memory"); }
__device__ __forceinline__ float dot2bf(unsigned a, unsigned b, float c) {
  return __builtin_amdgcn_fdot2_f32_bf16(__builtin_bit_cast(b2_t, a), __builtin_bit_cast(b2_t, b), c, false);
}

#define ST_MODE 0
__device__ __forceinline__ void st16(void* p, u4_t v) {
  *(u4_t*)p = v;
}
__device__ __forceinline__ void st8(void* p, u2_t v) {
  *(u2_t*)p = v;
}

struct Cw { f2_t w, ws; };
__device__ __forceinline__ Cw mkw(f2_t w) { Cw r; r.w = w; r.ws = f2_t{-w.y, w.x}; return r; }
__device__ __forceinline__ f2_t cmulw(f2_t a, const Cw& W) {
  return __builtin_elementwise_fma(f2_t{a.y, a.y}, W.ws, f2_t{a.x, a.x} * W.w);
}
__device__ __forceinline__ f2_t cexp_l(float er, float wr, float l) {
  float mag = __builtin_amdgcn_exp2f(er * l);
  float rev = __builtin_amdgcn_fractf(wr * l);
  return f2_t{mag * __builtin_amdgcn_cosf(rev), mag * __builtin_amdgcn_sinf(rev)};
}
__device__ __forceinline__ float pow2_scale(float bound, int target) {
  unsigned e = (__float_as_uint(bound) >> 23) & 0xff;
  int k = target - ((int)e - 126);
  k = k > 126 ? 126 : (k < -126 ? -126 : k);
  return (bound > 0.f) ? __uint_as_float((unsigned)(k + 127) << 23) : 1.0f;
}
__device__ __forceinline__ float pow2_inv(float p) { return __uint_as_float(0x7f000000u - __float_as_uint(p)); }

constexpr int A_KBLK = 214;
constexpr int KG_R = 0, KG_L0 = 4352, KG_L1 = 8704, KG_HW = 8704 + 25 * 272;
constexpr int KG_CH = 2 * KG_HW + 16;
constexpr int A_SIDE = 3 * KG_CH;
constexpr int A_LDS = A_SIDE + 20736;

__device__ __forceinline__ void pack_wd(const u16* __restrict__ wd, char* __restrict__ ws, char* buf, int u, int lane) {
  const int ks = u >> 3, dg = u & 7;
#pragma unroll
  for (int i = 0; i < 5; ++i) {
    const int idx = i * 64 + lane, row = idx / 10, c = idx % 10;
    __builtin_amdgcn_global_load_lds((const unsigned*)((const char*)wd + (size_t)(32 * ks + row) * 1280 + 160 * dg + 16 * c),
                                     (LAS unsigned*)(buf + i * 1024), 16, 0, 2);
  }
  asm volatile("s_waitcnt vmcnt(0)" ::: "memory");
  wave_fence();
  const int g = lane >> 4, q = (lane & 15) >> 2, p = lane & 3;
  char* dst = ws + OFF_WP2 + ((size_t)(dg * 54 + ks) * 5) * 1024 + lane * 16;
#pragma unroll
  for (int dt = 0; dt < 5; ++dt) {
    s4_t aa = tr_read(buf + (4 * g + q) * 160 + 32 * dt + 8 * p);
    s4_t bb = tr_read(buf + (16 + 4 * g + q) * 160 + 32 * dt + 8 * p);
    u2_t ua = __builtin_bit_cast(u2_t, aa), ub = __builtin_bit_cast(u2_t, bb);
    st16(dst + dt * 1024, u4_t{ua.x, ua.y, ub.x, ub.y});
  }
}

extern "C" __global__ __launch_bounds__(1024) void kA(const u16* __restrict__ x, const u16* __restrict__ Are, const u16* __restrict__ Aim,
                                           const u16* __restrict__ Bre, const u16* __restrict__ Bim, const u16* __restrict__ Cre,
                                           const u16* __restrict__ Cim, const u16* __restrict__ logdt, const u16* __restrict__ wd,
                                           const u16* __restrict__ wg, const u16* __restrict__ wu, const u16* __restrict__ dskip,
                                           const u16* __restrict__ outp, char* __restrict__ ws) {
  extern __shared__ __attribute__((aligned(16))) char smem[];
  const int tid = threadIdx.x, lane = tid & 63;
  const int w = __builtin_amdgcn_readfirstlane(tid >> 6);
  if (w == 0) STAMP(0, 0);
  if (blockIdx.x >= A_KBLK) {
    const int u = 344 + (blockIdx.x - A_KBLK) * 16 + w;
    if (u < 432) pack_wd(wd, ws, smem + w * 5120, u, lane);
    if (blockIdx.x >= 248 && w == 0) {
      unsigned long long a0, a1;
      asm volatile("s_getpc_b64 s[20:21]\n\ts_add_u32 s20, s20, kA@rel32@lo+4\n\ts_addc_u32 s21, s21, kA@rel32@hi+12\n\ts_mov_b64 %0, s[20:21]" : "=s"(a0) :: "s20", "s21", "scc");
      asm volatile("s_getpc_b64 s[20:21]\n\ts_add_u32 s20, s20, kD@rel32@lo+4\n\ts_addc_u32 s21, s21, kD@rel32@hi+12\n\ts_mov_b64 %0, s[20:21]" : "=s"(a1) :: "s20", "s21", "scc");
      const unsigned long long lo = a0 < a1 ? a0 : a1, n = ((a0 < a1 ? a1 - a0 : a0 - a1) + 8192) >> 7;
      unsigned sink = 0;
      for (unsigned long long i = lane; i < n; i += 64) { unsigned v; asm volatile("global_load_dword %0, %1, off" : "=v"(v) : "v"(lo + i * 128) : "memory"); sink = v; }
      asm volatile("s_waitcnt vmcnt(0)" :: "v"(sink) : "memory");
    }
    if (blockIdx.x >= 248 && w == 1) {
      unsigned v0, v1, v2, v3, v4, v5, v6, v7, v8;
      const char* p;
      constexpr size_t G = TLBPF;
      p = ws + (size_t)min((size_t)lane * G, (size_t)0x1150000 - 4);
      asm volatile("global_load_dword %0, %1, off nt" : "=v"(v0) : "v"(p) : "memory");
      p = ws + (size_t)min((size_t)(lane + 64) * G, (size_t)0x1150000 - 4);
      asm volatile("global_load_dword %0, %1, off nt" : "=v"(v1) : "v"(p) : "memory");
      p = ws + (size_t)min((size_t)(lane + 128) * G, (size_t)0x1150000 - 4);
      asm volatile("global_load_dword %0, %1, off nt" : "=v"(v2) : "v"(p) : "memory");
      p = ws + (size_t)min((size_t)(lane + 192) * G, (size_t)0x1150000 - 4);
      asm volatile("global_load_dword %0, %1, off nt" : "=v"(v3) : "v"(p) : "memory");
      p = ws + (size_t)min((size_t)(lane + 256) * G, (size_t)0x1150000 - 4);
      asm volatile("global_load_dword %0, %1, off nt" : "=v"(v4) : "v"(p) : "memory");
      p = (const char*)wg + min((size_t)lane * G, (size_t)DM * FF * 2 - 4);
      asm volatile("global_load_dword %0, %1, off nt" : "=v"(v5) : "v"(p) : "memory");
      p = (const char*)wu + min((size_t)lane * G, (size_t)DM * FF * 2 - 4);
      asm volatile("global_load_dword %0, %1, off nt" : "=v"(v6) : "v"(p) : "memory");
      p = (const char*)outp + min((size_t)lane * G, (size_t)SL * DM * 2 - 4);
      asm volatile("global_load_dword %0, %1, off nt" : "=v"(v7) : "v"(p) : "memory");
      p = (const char*)dskip;
      asm volatile("global_load_dword %0, %1, off nt" : "=v"(v8) : "v"(p) : "memory");
      asm volatile("s_waitcnt vmcnt(0)" :: "v"(v0), "v"(v1), "v"(v2), "v"(v3), "v"(v4), "v"(v5), "v"(v6), "v"(v7), "v"(v8) : "memory");
    }
    if (w == 0) STAMP(0, 7);
    return;
  }
  if (w >= 12) {
    const int w4 = w - 12;
    if (blockIdx.x >= 128) {
      pack_wd(wd, ws, smem + A_SIDE + w4 * 5120, (blockIdx.x - 128) * 4 + w4, lane);
      bar();
      return;
    }
    const int t0 = blockIdx.x * 16;
    const int r = lane >> 4, c = lane & 15, rl = w4 * 4 + r;
    char* xs = smem + A_SIDE;
    const u4_t* p = (const u4_t*)(x + (size_t)(t0 + rl) * DM);
    u4_t v[5];
#pragma unroll
    for (int i = 0; i < 5; ++i) v[i] = ldnt(p + c + 16 * i);
    if (w4 == 0 && lane < 16) ((unsigned long long*)(ws + OFF_G2))[t0 + lane] = 0ull;
    float s = 0.f;
#pragma unroll
    for (int i = 0; i < 5; ++i)
#pragma unroll
      for (int j = 0; j < 4; ++j) s += bf_lo(v[i][j]) + bf_hi(v[i][j]);
#pragma unroll
    for (int i = 0; i < 5; ++i) *(u4_t*)(xs + rl * 1296 + (c + 16 * i) * 16) = v[i];
    const float mean = row16_sum(s) * (1.0f / DM);
    float q = 0.f;
#pragma unroll
    for (int i = 0; i < 5; ++i)
#pragma unroll
      for (int j = 0; j < 4; ++j) {
        float a = bf_lo(v[i][j]) - mean, b = bf_hi(v[i][j]) - mean;
        q += a * a; q += b * b;
      }
    const float var = row16_sum(q) * (1.0f / DM);
    if (c == 0) {
      float mb = rbf(mean), sd = rbf(sqrtf(rbf(rbf(var) + 1e-5f)));
      ((unsigned*)(ws + OFF_S1))[t0 + rl] = (__float_as_uint(mb) >> 16) | (__float_as_uint(sd) & 0xffff0000u);
    }
    __syncthreads();
    const int t4 = w4 * 64 + lane;
#pragma unroll
    for (int it = 0; it < 5; ++it) {
      const int item = t4 + 256 * it, q4 = item >> 3, tp = item & 7;
      u2_t a = *(const u2_t*)(xs + (2 * tp) * 1296 + 8 * q4);
      u2_t b = *(const u2_t*)(xs + (2 * tp + 1) * 1296 + 8 * q4);
      st16(ws + OFF_X4 + ((size_t)q4 * SL + t0 + 2 * tp) * 8, u4_t{a.x, a.y, b.x, b.y});
    }
    if (w4 == 0) STAMP(0, 6);
    return;
  }
  const int cl = w >> 2, part = (w >> 1) & 1, hw = w & 1;
  const int chr = blockIdx.x * 3 + cl, ch = min(chr, DM - 1);
  char* chb = smem + cl * KG_CH;
  char* hb = chb + hw * KG_HW;
  const int n = ch * 128 + hw * 64 + lane;
  const u16 i_are = ldnt(Are + n), i_aim = ldnt(Aim + n), i_bre = ldnt(Bre + n), i_bim = ldnt(Bim + n), i_cre = ldnt(Cre + n), i_cim = ldnt(Cim + n);
  const float dt = rbf(expf(bf2f(logdt[ch])));
  const float are = dt * bf2f(i_are), aim = dt * bf2f(i_aim);
  const float bre = dt * bf2f(i_bre), bim = dt * bf2f(i_bim);
  const float cre = bf2f(i_cre), cim = bf2f(i_cim);
  f2_t c = {cre * bre - cim * bim, cre * bim + cim * bre};
  const float er = are * 1.44269504088896341f, wr = aim * 0.15915494309189535f;
  const float bnd = (fabsf(c.x) + fabsf(c.y)) * fmaxf(1.0f, __builtin_amdgcn_exp2f(er * 2048.f));
  const float bsum = wave_sum(bnd);
  if (w == 0) STAMP(0, 1);
  const float Sw = pow2_scale(bsum, 15);
  c *= Sw;
  const Cw z1 = mkw(cexp_l(er, wr, 1.f)), w128 = mkw(cexp_l(er, wr, 128.f));
  auto put = [&](char* buf, int row, f2_t v) { *(unsigned*)(buf + row * 272 + 4 * lane) = pk_f16(v.x, -v.y); };
  if (part == 0) {
    const Cw w8 = mkw(cexp_l(er, wr, 8.f));
    f2_t R = w8.w, v0 = c, v1 = cmulw(c, w128);
    *(unsigned*)(hb + KG_R + 4 * lane) = pk_f16(1.f, 0.f);
#pragma unroll
    for (int i = 0; i < 8; ++i) {
      *(unsigned*)(hb + KG_R + (2 * i + 1) * 272 + 4 * lane) = pk_f16(R.x, R.y);
      if (i < 7) {
        R = cmulw(R, w8);
        *(unsigned*)(hb + KG_R + (2 * i + 2) * 272 + 4 * lane) = pk_f16(R.x, R.y);
        R = cmulw(R, w8);
      }
      put(hb + KG_L0, i, v0); put(hb + KG_L0, 8 + i, v1);
      if (i < 7) { v0 = cmulw(v0, z1); v1 = cmulw(v1, z1); }
    }
  } else {
    const Cw z2 = mkw(cmulw(z1.w, z1));
    const Cw z4 = mkw(cmulw(z2.w, z2));
    f2_t T = cmulw(c, w128);
#pragma unroll
    for (int i1 = 2; i1 < 17; ++i1) {
      T = cmulw(T, w128);
      if (i1 < 4) {
        f2_t v = T;
#pragma unroll
        for (int i0 = 0; i0 < 4; ++i0) { put(hb + KG_L1, 4 * (i1 - 2) + i0, v); if (i0 < 3) v = cmulw(v, z2); }
      } else if (i1 < 8) {
        put(hb + KG_L1, 8 + 2 * (i1 - 4), T); put(hb + KG_L1, 8 + 2 * (i1 - 4) + 1, cmulw(T, z4));
      } else {
        put(hb + KG_L1, 16 + (i1 - 8), T);
      }
    }
  }
  if (part == 0 && lane == 0) ((float*)(chb + 2 * KG_HW))[hw] = bsum;
  __syncthreads();
  if (w == 0) STAMP(0, 2);
  if (part == 1 && hw == 1) return;
  const int tile = part ? 1 : 2 * hw;
  const int fr = lane & 15, kg = lane >> 4;
  const int row = (tile == 2) ? 16 + min(fr, 8) : fr;
  const int lo = (tile == 0) ? KG_L0 : KG_L1;
  f4_t acc[2];
#pragma unroll
  for (int h = 0; h < 2; ++h) {
    const char* hbb = chb + h * KG_HW;
    f4_t a = {0.f, 0.f, 0.f, 0.f};
#pragma unroll
    for (int ks = 0; ks < 4; ++ks) {
      const h8_t Bf = *(const h8_t*)(hbb + KG_R + fr * 272 + 64 * ks + 16 * kg);
      const h8_t Af = *(const h8_t*)(hbb + lo + row * 272 + 64 * ks + 16 * kg);
      a = __builtin_amdgcn_mfma_f32_16x16x32_f16(Af, Bf, a, 0, 0, 0);
    }
    acc[h] = a;
  }
  const float b0 = ((const float*)(chb + 2 * KG_HW))[0], b1 = ((const float*)(chb + 2 * KG_HW))[1];
  const float Sc = pow2_scale(b0 + b1, 15);
  const f4_t kv = acc[0] * (pow2_inv(pow2_scale(b0, 15)) * Sc) + acc[1] * (pow2_inv(pow2_scale(b1, 15)) * Sc);
  const unsigned p01 = pk_f16(kv[0], kv[1]), p23 = pk_f16(kv[2], kv[3]);
  char* kr = ws + OFF_KD + (size_t)ch * KRS;
  if (tile == 0) {
    *(u2_t*)(kr + 2 * (128 * (kg >> 1) + 4 * (kg & 1) + 8 * fr)) = u2_t{p01, p23};
    if (lane == 0) ((float*)(ws + OFF_INVS))[ch] = pow2_inv(Sc);
  } else if (tile == 1) {
    if (kg < 2) {
      *(u2_t*)(kr + 2 * (256 + 64 * kg + 4 * fr)) = u2_t{p01, p23};
    } else {
      *(unsigned*)(kr + 2 * (384 + 64 * (kg - 2) + 2 * fr)) = p01;
      *(unsigned*)(kr + 2 * (384 + 64 * (kg - 2) + 32 + 2 * fr)) = p23;
    }
  } else {
    u16* k3 = (u16*)kr + 512 + 64 * kg + fr;
    if (kg < 2) { k3[0] = (u16)p01; k3[16] = (u16)(p01 >> 16); k3[32] = (u16)p23; k3[48] = (u16)(p23 >> 16); }
    else if (lane == 32) k3[0] = (u16)p01;
  }
  if (w == 0) STAMP(0, 7);
}

constexpr int B_CONV = 160;
constexpr int B_KOFF = 0;
constexpr int B_UOFF = 4 * KDS;
constexpr int B_UCH = 4608;
constexpr int B_YCH = 10240;
constexpr int B_LDS = 40960;
#define B_DELAY 0

struct BFrag { h8_t B[8]; u2_t A[5][2]; };
__device__ __forceinline__ void mfma_f16(f4_t& d, u4_t a, h8_t b) {
  asm volatile("v_mfma_f32_16x16x32_f16 %0, %1, %2, %0" : "+v"(d) : "v"(a), "v"(b));
}
__device__ __forceinline__ void mfma_f16_0(f4_t& d, u4_t a, h8_t b) {
  asm volatile("v_mfma_f32_16x16x32_f16 %0, %1, %2, 0" : "=&v"(d) : "v"(a), "v"(b));
}

extern "C" __global__ __launch_bounds__(512) void kB(const u16* __restrict__ dskip, const u16* __restrict__ wg, const u16* __restrict__ wu,
                                          char* __restrict__ ws) {
  extern __shared__ __attribute__((aligned(16))) char smem[];
  const int tid = threadIdx.x, lane = tid & 63;
  const int w = __builtin_amdgcn_readfirstlane(tid >> 6);
  if (w == 0) STAMP(1, 0);
  if (blockIdx.x >= B_CONV) {
    const int xcd = blockIdx.x & 7, rk = (blockIdx.x - B_CONV) >> 3;
    const int g4 = xcd >> 1, a = xcd & 1, nl = (g4 < 3) ? 7 : 6;
    if (rk >= nl) return;
#pragma unroll
    for (int i = 0; i < B_DELAY; ++i) __builtin_amdgcn_s_sleep(16);
    const int jl = 7 * g4 + rk;
    const char* pm = (const char*)(a ? wu : wg) + (size_t)(80 * w + (lane >> 3)) * (FF * 2) + 128 * jl + (lane & 7) * 16;
    u4_t v[10];
#pragma unroll
    for (int i = 0; i < 10; ++i) v[i] = ldnt((const u4_t*)(pm + (size_t)i * 8 * (FF * 2)));
    const char* po = (const char*)(a ? wg : wu) + (size_t)(80 * w) * (FF * 2) + 128 * jl;
    unsigned t0v, t1v;
    asm volatile("global_load_dword %0, %1, off nt" : "=v"(t0v) : "v"(po + (size_t)lane * (FF * 2)) : "memory");
    asm volatile("global_load_dword %0, %1, off nt" : "=v"(t1v) : "v"(po + (size_t)(64 + (lane & 15)) * (FF * 2)) : "memory");
    f2_t s[4];
#pragma unroll
    for (int e = 0; e < 4; ++e) s[e] = f2_t{0.f, 0.f};
#pragma unroll
    for (int i = 0; i < 10; ++i)
#pragma unroll
      for (int jj = 0; jj < 4; ++jj) s[jj] += f2_t{bf_lo(v[i][jj]), bf_hi(v[i][jj])};
    float* pp = (float*)smem;
    *(f4_t*)(pp + (w * 8 + (lane >> 3)) * 68 + (lane & 7) * 8) = f4_t{s[0].x, s[0].y, s[1].x, s[1].y};
    *(f4_t*)(pp + (w * 8 + (lane >> 3)) * 68 + (lane & 7) * 8 + 4) = f4_t{s[2].x, s[2].y, s[3].x, s[3].y};
    __syncthreads();
    float acc = 0.f;
#pragma unroll
    for (int i = 0; i < 8; ++i) acc += pp[(w * 8 + i) * 68 + lane];
    __syncthreads();
    pp[w * 64 + lane] = acc;
    __syncthreads();
    if (w == 0) {
      float t = 0.f;
#pragma unroll
      for (int i = 0; i < 8; ++i) t += pp[i * 64 + lane];
      ((float*)(ws + OFF_CS))[a * FF + 64 * jl + lane] = t;
    }
    asm volatile("s_waitcnt vmcnt(0)" ::"v"(t0v), "v"(t1v) : "memory");
    if (w == 0) STAMP(1, 7);
    return;
  }
  const int a_ = blockIdx.x >> 3, x_ = blockIdx.x & 7;
  const int q = 16 * (a_ >> 1) + 2 * x_ + (a_ & 1);
  u4_t xv[2]; u2_t sv[2];
#pragma unroll
  for (int it = 0; it < 2; ++it) {
    const int j = tid + 512 * it;
    xv[it] = *(const u4_t*)(ws + OFF_X4 + ((size_t)q * SL + 2 * j) * 8);
    sv[it] = *(const u2_t*)(ws + OFF_S1 + 8 * j);
  }
  const int kch = tid >> 7, kc = tid & 127;
  const char* kr = ws + OFF_KD + (size_t)(4 * q + kch) * KRS;
  const int e0 = (kc < 32) ? 8 * kc : (kc < 64) ? 256 + 4 * (kc - 32) : 384 + 2 * (kc - 64);
  const u4_t kd4 = *(const u4_t*)(kr + 2 * e0);
  const unsigned kd5 = *(const unsigned*)(kr + 2 * e0 + 16);
  const unsigned kfirst = *(const u16*)kr, k1024 = *(const u16*)(kr + 2 * 512);
  const int dl = 127 - kc;
  const unsigned kfa = *(const u16*)(kr + 2 * (512 + max(2 * dl - 127, 0))), kfb = *(const u16*)(kr + 2 * (512 + max(2 * dl - 126, 0)));
  float dk[4];
#pragma unroll
  for (int c = 0; c < 4; ++c) dk[c] = bf2f(dskip[4 * q + c]);
  const float invs = ((const float*)(ws + OFF_INVS))[4 * q + (w & 3)];
  *(unsigned*)(smem + B_UOFF + (tid >> 7) * B_UCH + 4096 + 4 * (tid & 127)) = 0u;
  const int c = lane & 15, kg = lane >> 4;
  h8_t band9, band7;
#pragma unroll
  for (int j = 0; j < 8; ++j) {
    const int k = 8 * kg + 7 - j;
    band9[j] = (k >= c + 4 && k <= c + 12) ? (_Float16)1.0f : (_Float16)0.0f;
    band7[j] = (k >= c + 5 && k <= c + 11) ? (_Float16)1.0f : (_Float16)0.0f;
  }
  {
    char* ki = smem + B_KOFF + kch * KDS;
    const unsigned k0 = kd4[0] & 0xffffu;
    unsigned o0, o1, o2, o3, nx;
    if (kc < 32) { o0 = kd4[0]; o1 = kd4[1]; o2 = kd4[2]; o3 = kd4[3]; nx = kd5; }
    else if (kc < 64) {
      const unsigned k2 = kd4[0] >> 16, k4 = kd4[1] & 0xffffu, k6 = kd4[1] >> 16, k8 = kd4[2] & 0xffffu;
      o0 = k0 * 0x10001u; o1 = k2 | (k4 << 16); o2 = k4 * 0x10001u; o3 = k6 | (k8 << 16); nx = k8;
    } else {
      const unsigned k4 = kd4[0] >> 16, k8 = kd4[1] & 0xffffu;
      o0 = k0 * 0x10001u; o1 = k0 | (k4 << 16); o2 = k4 * 0x10001u; o3 = k8 * 0x10001u; nx = k8;
    }
    *(u4_t*)(ki + 16 * (kc + 2)) = u4_t{o0, o1, o2, o3};
    *(u4_t*)(ki + KD_O + 16 * (kc + 2)) = u4_t{__builtin_amdgcn_alignbit(o1, o0, 16), __builtin_amdgcn_alignbit(o2, o1, 16),
                                              __builtin_amdgcn_alignbit(o3, o2, 16), __builtin_amdgcn_alignbit(nx, o3, 16)};
    if (kc < 6) {
      const int cp = (kc < 2) ? kc : 128 + kc;
      const unsigned kk = k1024 * 0x10001u;
      const u4_t e = (kc == 2) ? u4_t{kk, kk, k1024, 0u} : u4_t{0u, 0u, 0u, 0u};
      const u4_t o = (kc == 2) ? u4_t{kk, kk, 0u, 0u} : u4_t{0u, 0u, 0u, (kc == 1) ? (kfirst << 16) : 0u};
      *(u4_t*)(ki + 16 * cp) = e;
      *(u4_t*)(ki + KD_O + 16 * cp) = o;
    }
    if (kc < 80) *(unsigned*)(ki + KD_DR + 4 * kc) = (dl >= 64) ? (kfa | (kfb << 16)) : 0u;
  }
  asm volatile("" : "+v"(band9), "+v"(band7));
  float z[2][8];
  if (w == 0) { STAMP(1, 1); }
#pragma unroll
  for (int it = 0; it < 2; ++it) {
    const int j = tid + 512 * it;
    const float m0 = bf_lo(sv[it].x), r0 = __builtin_amdgcn_rcpf(bf_hi(sv[it].x)), m1 = bf_lo(sv[it].y), r1 = __builtin_amdgcn_rcpf(bf_hi(sv[it].y));
    float x0[4] = {bf_lo(xv[it].x), bf_hi(xv[it].x), bf_lo(xv[it].y), bf_hi(xv[it].y)};
    float x1[4] = {bf_lo(xv[it].z), bf_hi(xv[it].z), bf_lo(xv[it].w), bf_hi(xv[it].w)};
#pragma unroll
    for (int cc = 0; cc < 4; ++cc) {
      const float u0 = (x0[cc] - m0) * r0, u1 = (x1[cc] - m1) * r1;
      *(unsigned*)(smem + B_UOFF + cc * B_UCH + 4 * (1023 - j)) = pk_f16(u1, u0);
      z[it][cc] = x0[cc] + u0 * dk[cc];
      z[it][4 + cc] = x1[cc] + u1 * dk[cc];
    }
  }
  __syncthreads();
  if (w == 0) STAMP(1, 2);
  f4_t acc[8];
  if (w < 4) {
    const char* ub = smem + B_UOFF + w * B_UCH + 4112 - 32 * c - 16 * kg - 512 * 7;
    const char* kb = smem + B_KOFF + w * KDS + ((c & 1) ? 0 : KD_O) + 4 * (12 + ((c + 1) >> 1) - 4 * kg);
    const unsigned* dr = (const unsigned*)(smem + B_KOFF + w * KDS + KD_DR) + (127 - c + 4 * kg);
    const char* uw = smem + B_UOFF + w * B_UCH + 4112 - 32 * c - 16 * kg;
    auto rdB = [&](BFrag& F, int i0, int p) { F.B[p] = *(const h8_t*)(ub + 512 * (7 - p) + 64 * i0); };
    auto rdA = [&](BFrag& F, int i0, int i1, int half) {
      const unsigned* ap = (const unsigned*)(kb + 64 * i0 + 512 * i1) + 2 * half;
      F.A[i1][half] = u2_t{ap[0], ap[1]};
    };
    auto frag = [&](const u2_t (&a)[2]) { return u4_t{a[0].x, a[0].y, a[1].x, a[1].y}; };
    auto rdnext = [&](BFrag& F, int i0, int m) {
      if (m < 8) rdB(F, i0, m); else if (m < 16) rdA(F, i0, (m - 8) >> 1, (m - 8) & 1);
    };
    auto step = [&](auto i0c, const BFrag& F, BFrag& N, auto nextc) {
      constexpr int I0 = decltype(i0c)::value, NX = decltype(nextc)::value;
      int m = 0;
#pragma unroll
      for (int i1 = 0; i1 < 5; ++i1) {
        if (i1 == 4 && I0 != 0) continue;
        const u4_t a = frag(F.A[i1]);
#pragma unroll
        for (int t = i1; t < 8; ++t) {
          if (I0 == 0 && i1 == 0) mfma_f16_0(acc[t], a, F.B[t]); else mfma_f16(acc[t], a, F.B[t - i1]);
          if (NX >= 0) rdnext(N, NX, m);
          SB;
          ++m;
        }
      }
    };
    BFrag F0, F1;
    h8_t U9[4], U7[4];
#pragma unroll
    for (int bt = 0; bt < 4; ++bt) { U9[bt] = *(const h8_t*)(uw - 512 * bt + 16); U7[bt] = *(const h8_t*)(uw - 512 * bt); }
#pragma unroll
    for (int m = 0; m < 16; ++m) rdnext(F0, 0, m);
    rdA(F0, 0, 4, 0); rdA(F0, 0, 4, 1);
    SB;
    f4_t X9[4], X7[4];
#pragma unroll
    for (int bt = 0; bt < 4; ++bt) { mfma_f16_0(X9[bt], __builtin_bit_cast(u4_t, U9[bt]), band9); mfma_f16_0(X7[bt], __builtin_bit_cast(u4_t, U7[bt]), band7); }
    SB;
    step(IC(0), F0, F1, IC(1));
    h8_t Vf[4]; u4_t Tf[4];
    {
      int m = 0;
#pragma unroll
      for (int i1 = 0; i1 < 4; ++i1) {
        const u4_t a = frag(F1.A[i1]);
#pragma unroll
        for (int t = i1; t < 8; ++t) {
          mfma_f16(acc[t], a, F1.B[t - i1]);
          rdnext(F0, 2, m);
          if (m < 4) {
            const u4_t vu = {pk_f16(X7[m][0], X9[m][0]), pk_f16(X7[m][1], X9[m][1]), pk_f16(X7[m][2], X9[m][2]), pk_f16(X7[m][3], X9[m][3])};
            Vf[m] = __builtin_bit_cast(h8_t, vu);
          }
          if (m >= 16 && m < 20) { const unsigned* tp = dr - 16 * (m - 16 + 4); Tf[m - 16] = u4_t{tp[0], tp[1], tp[2], tp[3]}; }
          SB;
          ++m;
        }
      }
    }
    step(IC(2), F0, F1, IC(3));
#pragma unroll
    for (int bt = 0; bt < 4; ++bt)
#pragma unroll
      for (int at = 4 + bt; at < 8; ++at) mfma_f16(acc[at], __builtin_bit_cast(u4_t, Vf[bt]), __builtin_bit_cast(h8_t, Tf[at - bt - 4]));
    SB;
    step(IC(3), F1, F0, IC(4));
    step(IC(4), F0, F1, IC(5));
    step(IC(5), F1, F0, IC(6));
    step(IC(6), F0, F1, IC(7));
    step(IC(7), F1, F0, IC(-1));
    asm volatile("s_nop 7\n\ts_nop 1" : "+v"(acc[0]), "+v"(acc[1]), "+v"(acc[2]), "+v"(acc[3]), "+v"(acc[4]), "+v"(acc[5]), "+v"(acc[6]), "+v"(acc[7]));
  }
  if (w == 0) STAMP(1, 3);
  __syncthreads();
  if (w < 4) {
#pragma unroll
    for (int t = 0; t < 8; ++t) *(f4_t*)(smem + w * B_YCH + t * 1280 + kg * 320 + c * 16) = acc[t] * invs;
  }
  __syncthreads();
#pragma unroll
  for (int it = 0; it < 2; ++it) {
    const int j = tid + 512 * it, t = 2 * j;
    const int yo = (t >> 8) * 1280 + ((t >> 2) & 3) * 320 + ((t >> 4) & 15) * 16 + (t & 3) * 4;
    float h0[4], h1[4];
#pragma unroll
    for (int cc = 0; cc < 4; ++cc) {
      f2_t y = *(const f2_t*)(smem + cc * B_YCH + yo);
      h0[cc] = z[it][cc] + y.x;
      h1[cc] = z[it][4 + cc] + y.y;
    }
    char* dst = ws + OFF_H8 + ((size_t)(q >> 1) * SL + t) * 16 + (q & 1) * 8;
    st8(dst, u2_t{pk_bf16(h0[0], h0[1]), pk_bf16(h0[2], h0[3])});
    st8(dst + 16, u2_t{pk_bf16(h1[0], h1[1]), pk_bf16(h1[2], h1[3])});
  }
  if (w == 0) STAMP(1, 7);
}

constexpr int C_T = 240;
constexpr int C_SLOT = 23552, C_NSLOT = 6, C_GOFF = 15360;
constexpr int C_STAT = C_NSLOT * C_SLOT;
constexpr int C_CS = C_STAT + 960;
constexpr int C_LDS = C_CS + 512;
constexpr int C_STEPS = 20;
constexpr size_t OFF_DUMMY = 0x1140000;

__device__ __forceinline__ unsigned ln_pack(float sum, float sq) {
  const float mean = sum * (1.0f / DM);
  const float var = fmaxf(sq * (1.0f / DM) - mean * mean, 0.f);
  const float mb = rbf(mean), sd = rbf(sqrtf(rbf(rbf(var) + 1e-5f)));
  return (__float_as_uint(mb) >> 16) | (__float_as_uint(sd) & 0xffff0000u);
}
__device__ __forceinline__ unsigned ln_row(const char* h8, int t) {
  float s = 0.f, q = 0.f;
  for (int pl = 0; pl < 80; ++pl) {
    const u4_t v = *(const u4_t*)(h8 + (size_t)pl * 32768 + t * 16);
    for (int j = 0; j < 4; ++j) { const float a = bf_lo(v[j]), b = bf_hi(v[j]); s += a + b; q += a * a + b * b; }
  }
  return ln_pack(s, q);
}

template <int NDMA>
__device__ __forceinline__ void c_loader(char* smem, char* ws, const char* wbase, int w4, int lane, int t0, int jline, int xcd) {
  const unsigned lds0 = (unsigned)(size_t)(LAS char*)smem;
  const char* h8 = ws + OFF_H8;
  unsigned voffH[4], voffW[2];
#pragma unroll
  for (int k = 0; k < 2; ++k) {
    const int r = 8 * (2 * (w4 & 1) + k) + (lane >> 3), c = lane & 7;
    const int f = ((r >> 1) & 1) | ((r >> 2) & 2);
    voffW[k] = r * (FF * 2) + 128 * jline + (((c >> 1) ^ f) * 32) + (c & 1) * 16;
  }
#pragma unroll
  for (int k = 0; k < 4; ++k) {
    const int idx = 64 * (w4 + 4 * k) + lane, pl = idx / C_T, tt = idx % C_T;
    const int t = min(t0 + tt, SL - 1);
    voffH[k] = pl * 32768 + t * 16;
  }
  const unsigned ldsW = C_GOFF + (w4 >> 1) * 4096 + (w4 & 1) * 2048;
  auto issue = [&](int step, unsigned so) {
#pragma unroll
    for (int k = 0; k < 2; ++k) glds16(wbase + (size_t)step * (32 * FF * 2), voffW[k], lds0 + so + ldsW + k * 1024);
#pragma unroll
    for (int k = 0; k < NDMA - 2; ++k) glds16(h8 + (size_t)step * (4 * 32768), voffH[k], lds0 + so + (w4 + 4 * k) * 1024);
  };
  constexpr unsigned S = C_SLOT;
  issue(0, 0); issue(1, S); issue(2, 2 * S); issue(3, 3 * S);
  wait_vm<3 * NDMA>(); bar();
  issue(4, 4 * S);
  wait_vm<2 * NDMA>(); bar();
  issue(5, 5 * S); issue(6, 0);
  unsigned sw = S;
#pragma unroll 1
  for (int j = 1; j < 7; ++j) {
    wait_vm<2 * NDMA>();
    bar();
    issue(2 * j + 5, sw);
    sw += S; if (sw == C_NSLOT * S) sw = 0;
    issue(2 * j + 6, sw);
    sw += S; if (sw == C_NSLOT * S) sw = 0;
  }
  wait_vm<2 * NDMA>(); bar();
  issue(19, sw);
  const int tl = min(60 * w4 + lane, C_T - 1), tg = min(t0 + tl, SL - 1);
  const gu64* gp = (const gu64*)(ws + OFF_G2) + tg;
  const float* csp = (const float*)(ws + OFF_CS) + ((lane >> 4) & 1) * FF + 64 * jline + 16 * w4 + (lane & 15);
  const char* tb = ws + OFF_WP2 + (size_t)(4 * (xcd & 1)) * (54 * 5120);
  const int tl0 = ((int)(blockIdx.x >> 3) * 4 + w4) * 68;
  u2_t gv; float csv; unsigned tv0, tv1;
  asm volatile("global_load_dwordx2 %0, %1, off sc1" : "=v"(gv) : "v"(gp) : "memory");
  asm volatile("global_load_dword %0, %1, off" : "=v"(csv) : "v"(csp) : "memory");
  asm volatile("global_load_dword %0, %1, off" : "=v"(tv0) : "v"(tb + (size_t)min(tl0 + lane, 8639) * 128) : "memory");
  asm volatile("global_load_dword %0, %1, off" : "=v"(tv1) : "v"(tb + (size_t)min(tl0 + 64 + (lane & 3), 8639) * 128) : "memory");
  wait_vm<NDMA + 4>(); bar();
  asm volatile("s_waitcnt vmcnt(0)" : "+v"(gv), "+v"(csv), "+v"(tv0), "+v"(tv1)::"memory");
  bar();
  {
    bool ok = gv.y == 1u;
    for (int spin = 0; spin < 4096 && !__all(ok); ++spin) {
      const unsigned long long g = __hip_atomic_load(gp, __ATOMIC_RELAXED, __HIP_MEMORY_SCOPE_AGENT);
      gv.x = (unsigned)g; ok = (unsigned)(g >> 32) == 1u;
      __builtin_amdgcn_s_sleep(2);
    }
    if (!__all(ok)) gv.x = ln_row(h8, tg);
    if (lane < 60) *(float*)(smem + C_STAT + (60 * w4 + lane) * 4) = bf_lo(gv.x);
    if (lane < 32) *(float*)(smem + C_CS + (lane >> 4) * 256 + (16 * w4 + (lane & 15)) * 4) = csv;
  }
  asm volatile("s_waitcnt lgkmcnt(0)" ::: "memory");
  bar();
}

struct CFrag { b8_t A[2][2]; b8_t B[8]; };

extern "C" __global__ __launch_bounds__(512, 2) void kC(const u16* __restrict__ wg, const u16* __restrict__ wu, char* __restrict__ ws) {
  extern __shared__ __attribute__((aligned(16))) char smem[];
  const int tid = threadIdx.x, lane = tid & 63;
  const int w = __builtin_amdgcn_readfirstlane(tid >> 6);
  int panel, jline;
  const int xcd = blockIdx.x & 7, rank = blockIdx.x >> 3;
  {
    const int g4 = xcd >> 1, a = xcd & 1, nl = (g4 < 3) ? 7 : 6, total = 9 * nl, n0 = (total + 1) >> 1;
    if (a == 0 ? rank >= n0 : rank >= total - n0) {
      const int sp = (xcd < 6) ? (xcd >> 1) : 3 + 5 * (xcd - 6) + (rank - 27);
      const int wv = sp * 8 + w;
      if (w == 0) STAMP(2, 0);
      for (int tile = wv; tile < 128; tile += 104) {
        const int t = 16 * tile + (lane & 15);
        const char* hp = ws + OFF_H8 + (size_t)(lane >> 4) * 32768 + t * 16;
        u4_t v[20];
#pragma unroll
        for (int i = 0; i < 20; ++i) v[i] = *(const u4_t*)(hp + (size_t)i * (4 * 32768));
        float s0 = 0.f, s1 = 0.f, q0 = 0.f, q1 = 0.f;
        unsigned k_both = 0x3f803f80u;
        asm volatile("" : "+s"(k_both));
#pragma unroll
        for (int i = 0; i < 20; ++i) {
          s0 = dot2bf(v[i][0], k_both, s0); q0 = dot2bf(v[i][0], v[i][0], q0);
          s1 = dot2bf(v[i][1], k_both, s1); q1 = dot2bf(v[i][1], v[i][1], q1);
          s0 = dot2bf(v[i][2], k_both, s0); q0 = dot2bf(v[i][2], v[i][2], q0);
          s1 = dot2bf(v[i][3], k_both, s1); q1 = dot2bf(v[i][3], v[i][3], q1);
        }
        float s = s0 + s1, q = q0 + q1;
        s += __shfl_xor(s, 16); q += __shfl_xor(q, 16);
        s += __shfl_xor(s, 32); q += __shfl_xor(q, 32);
        if (lane < 16)
          __hip_atomic_store((gu64*)(ws + OFF_G2) + t, (1ull << 32) | ln_pack(s, q), __ATOMIC_RELAXED, __HIP_MEMORY_SCOPE_AGENT);
      }
      if (w == 0) STAMP(2, 7);
      return;
    }
    const int idx = rank + a * n0;
    panel = idx / nl; jline = 7 * g4 + idx % nl;
  }
  const int t0 = panel * C_T;
  if (w == 0) STAMP(2, 0);
  if (w >= 4) {
    const int w4 = w - 4;
    const char* wbase = (const char*)((w4 >> 1) ? wu : wg);
    if (w4 < 3) c_loader<6>(smem, ws, wbase, w4, lane, t0, jline, xcd);
    else c_loader<5>(smem, ws, wbase, w4, lane, t0, jline, xcd);
    return;
  }
  const int s = w >> 1, th = w & 1;
  const int fr = lane & 15, kg = lane >> 4, q = (lane & 15) >> 2, p = lane & 3;
  const unsigned hb0 = kg * 3840 + fr * 16 + 256 * 7;
  const unsigned hb1 = kg * 3840 + fr * 16 + 256 * (th ? 8 : 0);
  unsigned wa[2];
#pragma unroll
  for (int v = 0; v < 2; ++v) {
    const int sub = 2 * s + (v ? 1 - th : th);
    const int f = (q >> 1) | (2 * (kg & 1));
    wa[v] = C_GOFF + (8 * kg + q) * 128 + ((sub ^ f) * 32) + 8 * p;
  }
  f4_t ag[8][2], au[8][2];
#pragma unroll
  for (int u = 0; u < 8; ++u)
#pragma unroll
    for (int v = 0; v < 2; ++v) { ag[u][v] = f4_t{0.f, 0.f, 0.f, 0.f}; au[u][v] = f4_t{0.f, 0.f, 0.f, 0.f}; }
  auto load = [&](CFrag& F, unsigned so) {
    const char* sp = smem + so;
#pragma unroll
    for (int v = 0; v < 2; ++v)
#pragma unroll
      for (int m = 0; m < 2; ++m) {
        s4_t lo = tr_read(sp + wa[v] + m * 4096), hi = tr_read(sp + wa[v] + m * 4096 + 512);
        u2_t a = __builtin_bit_cast(u2_t, lo), b = __builtin_bit_cast(u2_t, hi);
        F.A[v][m] = __builtin_bit_cast(b8_t, (u4_t{a.x, a.y, b.x, b.y}));
      }
    F.B[0] = *(const b8_t*)(sp + hb0);
#pragma unroll
    for (int u = 1; u < 8; ++u) F.B[u] = *(const b8_t*)(sp + hb1 + 256 * (u - 1));
  };
  auto mma = [&](const CFrag& F) {
    ag[0][0] = __builtin_amdgcn_mfma_f32_16x16x32_bf16(F.A[0][0], F.B[0], ag[0][0], 0, 0, 0);
    au[0][0] = __builtin_amdgcn_mfma_f32_16x16x32_bf16(F.A[0][1], F.B[0], au[0][0], 0, 0, 0);
#pragma unroll
    for (int u = 1; u < 8; ++u)
#pragma unroll
      for (int v = 0; v < 2; ++v) {
        ag[u][v] = __builtin_amdgcn_mfma_f32_16x16x32_bf16(F.A[v][0], F.B[u], ag[u][v], 0, 0, 0);
        au[u][v] = __builtin_amdgcn_mfma_f32_16x16x32_bf16(F.A[v][1], F.B[u], au[u][v], 0, 0, 0);
      }
  };
  auto interleave = [&]() {
#pragma unroll
    for (int i = 0; i < 16; ++i) {
      __builtin_amdgcn_sched_group_barrier(0x008, 1, 0);
      __builtin_amdgcn_sched_group_barrier(0x100, 1, 0);
    }
    __builtin_amdgcn_sched_group_barrier(0x008, 14, 0);
  };
  CFrag F0, F1;
  bar();
  if (w == 0) STAMP(2, 1);
  load(F0, 0);
  unsigned sn = C_SLOT;
  SB;
#pragma unroll 1
  for (int j = 0; j < 9; ++j) {
    asm volatile("s_waitcnt lgkmcnt(0)" ::: "memory");
    bar(); SB;
    load(F1, sn);
    mma(F0);
    interleave();
    SB;
    sn += C_SLOT; if (sn == C_NSLOT * C_SLOT) sn = 0;
    load(F0, sn);
    mma(F1);
    interleave();
    SB;
    sn += C_SLOT; if (sn == C_NSLOT * C_SLOT) sn = 0;
  }
  asm volatile("s_waitcnt lgkmcnt(0)" ::: "memory");
  bar(); SB;
  load(F1, sn);
  mma(F0);
  interleave();
  SB;
  bar(); SB;
  f4_t csg[2], csu[2];
  float mu[8];
#pragma unroll
  for (int v = 0; v < 2; ++v) {
    const int sub = 2 * s + (v ? 1 - th : th);
    csg[v] = *(const f4_t*)(smem + C_CS + (16 * sub + 4 * kg) * 4);
    csu[v] = *(const f4_t*)(smem + C_CS + 256 + (16 * sub + 4 * kg) * 4);
  }
#pragma unroll
  for (int u = 0; u < 8; ++u) {
    const int tile = (u == 0) ? 7 : (th ? 7 + u : u - 1);
    mu[u] = *(const float*)(smem + C_STAT + (16 * tile + fr) * 4);
  }
  mma(F1);
  SB;
  if (w == 0) STAMP(2, 2);
  char* hh = ws + OFF_HH + (size_t)(2 * jline + s) * 1024 + lane * 16;
  char* sink = ws + OFF_DUMMY + lane * 16;
  auto epi = [&](auto thc) {
    constexpr int TH = decltype(thc)::value;
#pragma unroll
    for (int u = 0; u < 8; ++u) {
      const int tile = (u == 0) ? 7 : (TH ? 7 + u : u - 1);
      const int t16 = 15 * panel + tile;
      char* dst = (t16 < 128) ? hh + (size_t)t16 * (54 * 1024) : sink;
      u2_t o[2];
#pragma unroll
      for (int v = 0; v < 2; ++v) {
        if (u == 0 && v == 1) continue;
        const f4_t r = (ag[u][v] - mu[u] * csg[v]) * (au[u][v] - mu[u] * csu[v]);
        o[v] = u2_t{pk_bf16(r[0], r[1]), pk_bf16(r[2], r[3])};
      }
      if (u == 0) st8(dst + 8 * TH, o[0]);
      else st16(dst, u4_t{o[TH].x, o[TH].y, o[1 - TH].x, o[1 - TH].y});
    }
  };
  if (th) epi(IC(1)); else epi(IC(0));
  if (w == 0) STAMP(2, 7);
}

constexpr int D_SLOT = 15360, D_NSLOT = 9, D_STEPS = 18;
constexpr int D_LDS = D_NSLOT * D_SLOT;

template <int NDMA>
__device__ __forceinline__ void d_loader(char* smem, const char* wp, int w4, int lane) {
  const unsigned lds0 = (unsigned)(size_t)(LAS char*)smem;
  const unsigned voff = lane * 16;
  auto issue = [&](int step, unsigned so) {
#pragma unroll
    for (int k = 0; k < NDMA; ++k) {
      const int i = w4 + 4 * k;
      glds16(wp + (size_t)step * D_SLOT + i * 1024, voff, lds0 + so + i * 1024);
    }
  };
  constexpr unsigned S = D_SLOT;
  issue(0, 0); issue(1, S); issue(2, 2 * S);
  wait_vm<2 * NDMA>(); bar();
  issue(3, 3 * S); issue(4, 4 * S); issue(5, 5 * S);
  wait_vm<4 * NDMA>(); bar();
  issue(6, 6 * S); issue(7, 7 * S);
  wait_vm<5 * NDMA>(); bar();
  issue(8, 8 * S); issue(9, 0);
  unsigned sw = S;
#pragma unroll 1
  for (int b = 2; b < 10; ++b) {
    wait_vm<6 * NDMA>();
    bar();
    issue(b + 8, sw);
    sw += S; if (sw == D_NSLOT * S) sw = 0;
  }
  wait_vm<6 * NDMA>(); bar();
  wait_vm<5 * NDMA>(); bar();
  wait_vm<4 * NDMA>(); bar();
  wait_vm<3 * NDMA>(); bar();
  wait_vm<2 * NDMA>(); bar();
  wait_vm<1 * NDMA>(); bar();
  wait_vm<0>(); bar();
  bar();
}

extern "C" __global__ __launch_bounds__(512, 2) void kD(char* __restrict__ ws, u16* __restrict__ out) {
  extern __shared__ __attribute__((aligned(16))) char smem[];
  const int tid = threadIdx.x, lane = tid & 63;
  const int w = __builtin_amdgcn_readfirstlane(tid >> 6);
  const int xcd = blockIdx.x & 7, rank = blockIdx.x >> 3;
  const int it = 8 * (xcd >> 1) + (rank & 7), dg = 4 * (xcd & 1) + (rank >> 3);
  if (w == 0) STAMP(3, 0);
  if (w >= 4) {
    const int w4 = w - 4;
    const char* wp = ws + OFF_WP2 + (size_t)dg * (54 * 5120);
    if (w4 < 3) d_loader<4>(smem, wp, w4, lane);
    else d_loader<3>(smem, wp, w4, lane);
    return;
  }
  const int fr = lane & 15, g = lane >> 4;
  const int t = 64 * it + 16 * w + fr;
  const char* hp = ws + OFF_HH + (size_t)(4 * it + w) * (54 * 1024);
  const unsigned voff = lane * 16;
  u4_t R[18];
#pragma unroll
  for (int i = 0; i < 9; ++i)
    asm volatile("global_load_dwordx4 %0, %1, %2" : "=v"(R[i]) : "v"(voff), "s"(hp + (size_t)i * 1024) : "memory");
  u2_t res[5]; unsigned st;
  f4_t acc[5];
#pragma unroll
  for (int dt = 0; dt < 5; ++dt) acc[dt] = f4_t{0.f, 0.f, 0.f, 0.f};
  b8_t A0[5], A1[5];
  auto rdA = [&](b8_t (&A)[5], unsigned off) {
    const char* sp = smem + off + lane * 16;
#pragma unroll
    for (int dt = 0; dt < 5; ++dt) A[dt] = *(const b8_t*)(sp + dt * 1024);
  };
  bar();
  if (w == 0) STAMP(3, 1);
  rdA(A0, 0);
  unsigned so = 0;
  auto kstep = [&](auto mode, auto idx, b8_t (&Ac)[5], b8_t (&An)[5], const char* hnext) {
    constexpr int MODE = decltype(mode)::value, i = decltype(idx)::value, kk = i % 3;
    if (kk == 0) bar();
    unsigned nx = so + (kk + 1) * 5120;
    if (kk == 2) { so += D_SLOT; if (so == D_NSLOT * D_SLOT) so = 0; nx = so; }
    SB;
    if (!(MODE == 2 && i == 17)) rdA(An, nx);
    constexpr int NV = (MODE == 0) ? (i < 9 ? 8 + i : 26 - i) : (MODE == 1) ? (i < 9 ? 25 - i : 17) : (i == 0 ? 17 : 23 - i);
    asm volatile("s_waitcnt vmcnt(%1)" : "+v"(R[i]) : "i"(NV) : "memory");
    const b8_t B = __builtin_bit_cast(b8_t, R[i]);
#pragma unroll
    for (int dt = 0; dt < 5; ++dt) acc[dt] = __builtin_amdgcn_mfma_f32_16x16x32_bf16(Ac[dt], B, acc[dt], 0, 0, 0);
    SB;
    if (MODE == 0 && i < 9)
      asm volatile("global_load_dwordx4 %0, %1, %2" : "=v"(R[i < 9 ? 9 + i : 0]) : "v"(voff), "s"(hp + (size_t)(9 + i) * 1024) : "memory");
    if (MODE != 2) asm volatile("global_load_dwordx4 %0, %1, %2" : "+v"(R[i]) : "v"(voff), "s"(hnext + (size_t)i * 1024) : "memory");
    if (MODE == 2 && i == 0) {
#pragma unroll
      for (int dt = 0; dt < 5; ++dt) {
        const int d = 80 * dg + 16 * dt + 4 * g;
        const char* rp = ws + OFF_H8 + ((size_t)(d >> 3) * SL + t) * 16 + (d & 4) * 2;
        asm volatile("global_load_dwordx2 %0, %1, off" : "=v"(res[dt]) : "v"(rp) : "memory");
      }
      asm volatile("global_load_dword %0, %1, off" : "=v"(st) : "v"(ws + OFF_G2 + (size_t)t * 8) : "memory");
    }
    SB;
  };
  auto round = [&](auto mode, const char* hnext) {
    kstep(mode, IC(0), A0, A1, hnext); kstep(mode, IC(1), A1, A0, hnext); kstep(mode, IC(2), A0, A1, hnext);
    kstep(mode, IC(3), A1, A0, hnext); kstep(mode, IC(4), A0, A1, hnext); kstep(mode, IC(5), A1, A0, hnext);
    kstep(mode, IC(6), A0, A1, hnext); kstep(mode, IC(7), A1, A0, hnext); kstep(mode, IC(8), A0, A1, hnext);
    kstep(mode, IC(9), A1, A0, hnext); kstep(mode, IC(10), A0, A1, hnext); kstep(mode, IC(11), A1, A0, hnext);
    kstep(mode, IC(12), A0, A1, hnext); kstep(mode, IC(13), A1, A0, hnext); kstep(mode, IC(14), A0, A1, hnext);
    kstep(mode, IC(15), A1, A0, hnext); kstep(mode, IC(16), A0, A1, hnext); kstep(mode, IC(17), A1, A0, hnext);
  };
  round(IC(0), hp + 18 * 1024);
  round(IC(1), hp + 36 * 1024);
  round(IC(2), hp);
  if (w == 0) STAMP(3, 2);
  asm volatile("s_waitcnt vmcnt(0)" : "+v"(res[0]), "+v"(res[1]), "+v"(res[2]), "+v"(res[3]), "+v"(res[4]), "+v"(st)::"memory");
  const float sd = bf_hi(st), r2 = 1.0f / (sd * sd);
#pragma unroll
  for (int dt = 0; dt < 5; ++dt) {
    const int d = 80 * dg + 16 * dt + 4 * g;
    const float h0 = bf_lo(res[dt].x), h1 = bf_hi(res[dt].x), h2 = bf_lo(res[dt].y), h3 = bf_hi(res[dt].y);
    st8(out + (size_t)t * DM + d, u2_t{pk_bf16(h0 + r2 * acc[dt][0], h1 + r2 * acc[dt][1]), pk_bf16(h2 + r2 * acc[dt][2], h3 + r2 * acc[dt][3])});
  }
  if (w == 0) STAMP(3, 7);
}

extern "C" void kernel_launch(void* const* d_in, const int* in_sizes, int n_in, void* d_out, int out_size, void* d_ws,
                              size_t ws_size, hipStream_t stream) {
  char* ws = (char*)d_ws;
  (void)hipFuncSetAttribute((const void*)kA, hipFuncAttributeMaxDynamicSharedMemorySize, A_LDS);
  (void)hipFuncSetAttribute((const void*)kC, hipFuncAttributeMaxDynamicSharedMemorySize, C_LDS);
  (void)hipFuncSetAttribute((const void*)kD, hipFuncAttributeMaxDynamicSharedMemorySize, D_LDS);
  kA<<<256, 1024, A_LDS, stream>>>((const u16*)d_in[0], (const u16*)d_in[1], (const u16*)d_in[2], (const u16*)d_in[3], (const u16*)d_in[4],
                                   (const u16*)d_in[5], (const u16*)d_in[6], (const u16*)d_in[7], (const u16*)d_in[12],
                                   (const u16*)d_in[10], (const u16*)d_in[11], (const u16*)d_in[8], (const u16*)d_out, ws);
  kB<<<256, 512, B_LDS, stream>>>((const u16*)d_in[8], (const u16*)d_in[10], (const u16*)d_in[11], ws);
  kC<<<256, 512, C_LDS, stream>>>((const u16*)d_in[10], (const u16*)d_in[11], ws);
  kD<<<256, 512, D_LDS, stream>>>(ws, (u16*)d_out);
}
```

```cpp
#define CODEPF
#define TLBPF 65536
#define SELFPF 8
#include <hip/hip_runtime.h>
#include <hip/hip_bf16.h>
#include <stdint.h>
#include <type_traits>

typedef unsigned short u16;
typedef _Float16 h2_t __attribute__((ext_vector_type(2)));
typedef _Float16 h8_t __attribute__((ext_vector_type(8)));
typedef __bf16 b2_t __attribute__((ext_vector_type(2)));
typedef __bf16 b8_t __attribute__((ext_vector_type(8)));
typedef float f2_t __attribute__((ext_vector_type(2)));
typedef float f4_t __attribute__((ext_vector_type(4)));
typedef unsigned u2_t __attribute__((ext_vector_type(2)));
typedef unsigned u4_t __attribute__((ext_vector_type(4)));
typedef short s4_t __attribute__((ext_vector_type(4)));
typedef __attribute__((address_space(1))) unsigned long long gu64;
#define LAS __attribute__((address_space(3)))
#define SB __builtin_amdgcn_sched_barrier(0)
#define IC(n) std::integral_constant<int, n>{}

constexpr int SL = 2048;
constexpr int DM = 640;
constexpr int FF = 1728;

constexpr size_t OFF_X4 = 0;
constexpr size_t OFF_S1 = 0x280000;
constexpr size_t OFF_KD = 0x290000;
constexpr size_t OFF_INVS = 0x590000;
constexpr size_t OFF_H8 = 0x5A0000;
constexpr size_t OFF_HH = 0x820000;
constexpr size_t OFF_WP2 = 0xF00000;
constexpr size_t OFF_CS = 0x1120000;
constexpr size_t OFF_G2 = 0x1130000;
constexpr int KRS = 1344;
constexpr int KDS = 4864;
constexpr int KD_O = 2240;
constexpr int KD_DR = 4384;

#define STAMP(k, i)

__device__ __forceinline__ float bf_lo(unsigned d) { return __uint_as_float(d << 16); }
__device__ __forceinline__ float bf_hi(unsigned d) { return __uint_as_float(d & 0xffff0000u); }
__device__ __forceinline__ float bf2f(u16 v) { return __uint_as_float((unsigned)v << 16); }
__device__ __forceinline__ unsigned pk_bf16(float a, float b) {
  f2_t v = {a, b};
  return __builtin_bit_cast(unsigned, __builtin_convertvector(v, b2_t));
}
__device__ __forceinline__ unsigned pk_f16(float a, float b) {
  f2_t v = {a, b};
  return __builtin_bit_cast(unsigned, __builtin_convertvector(v, h2_t));
}
__device__ __forceinline__ float rbf(float a) { return bf_lo(pk_bf16(a, 0.f)); }
template <int N> __device__ __forceinline__ float dpp_ror_add(float v) {
  int r = __builtin_amdgcn_update_dpp(0, __float_as_int(v), 0x120 + N, 0xf, 0xf, false);
  return v + __int_as_float(r);
}
__device__ __forceinline__ float row16_sum(float v) {
  v = dpp_ror_add<8>(v); v = dpp_ror_add<4>(v); v = dpp_ror_add<2>(v); v = dpp_ror_add<1>(v);
  return v;
}
__device__ __forceinline__ float wave_sum(float v) {
  v = row16_sum(v);
  const int b = __float_as_int(v);
  return (__int_as_float(__builtin_amdgcn_readlane(b, 0)) + __int_as_float(__builtin_amdgcn_readlane(b, 16))) +
         (__int_as_float(__builtin_amdgcn_readlane(b, 32)) + __int_as_float(__builtin_amdgcn_readlane(b, 48)));
}
template <typename T> __device__ __forceinline__ T ldnt(const T* p) { return __builtin_nontemporal_load(p); }
__device__ __forceinline__ void wave_fence() {
  __builtin_amdgcn_fence(__ATOMIC_RELEASE, "wavefront");
  __builtin_amdgcn_wave_barrier();
  __builtin_amdgcn_fence(__ATOMIC_ACQUIRE, "wavefront");
}
__device__ __forceinline__ s4_t tr_read(const char* p) {
  return __builtin_amdgcn_ds_read_tr16_b64_v4i16((LAS s4_t*)p);
}
__device__ __forceinline__ void glds16(const void* sbase, unsigned voff, unsigned lds_addr) {
  asm volatile("s_mov_b32 m0, %2\n\ts_nop 0\n\tglobal_load_lds_dwordx4 %0, %1" ::"v"(voff), "s"(sbase), "s"(lds_addr) : "memory");
}
template <int N> __device__ __forceinline__ void wait_vm() { asm volatile("s_waitcnt vmcnt(%0)" ::"i"(N) : "memory"); }
__device__ __forceinline__ void bar() { asm volatile("s_barrier" ::: "memory"); }
__device__ __forceinline__ float dot2bf(unsigned a, unsigned b, float c) {
  return __builtin_amdgcn_fdot2_f32_bf16(__builtin_bit_cast(b2_t, a), __builtin_bit_cast(b2_t, b), c, false);
}

#define ST_MODE 0
__device__ __forceinline__ void st16(void* p, u4_t v) {
  *(u4_t*)p = v;
}
__device__ __forceinline__ void st8(void* p, u2_t v) {
  *(u2_t*)p = v;
}

struct Cw { f2_t w, ws; };
__device__ __forceinline__ Cw mkw(f2_t w) { Cw r; r.w = w; r.ws = f2_t{-w.y, w.x}; return r; }
__device__ __forceinline__ f2_t cmulw(f2_t a, const Cw& W) {
  return __builtin_elementwise_fma(f2_t{a.y, a.y}, W.ws, f2_t{a.x, a.x} * W.w);
}
__device__ __forceinline__ f2_t cexp_l(float er, float wr, float l) {
  float mag = __builtin_amdgcn_exp2f(er * l);
  float rev = __builtin_amdgcn_fractf(wr * l);
  return f2_t{mag * __builtin_amdgcn_cosf(rev), mag * __builtin_amdgcn_sinf(rev)};
}
__device__ __forceinline__ float pow2_scale(float bound, int target) {
  unsigned e = (__float_as_uint(bound) >> 23) & 0xff;
  int k = target - ((int)e - 126);
  k = k > 126 ? 126 : (k < -126 ? -126 : k);
  return (bound > 0.f) ? __uint_as_float((unsigned)(k + 127) << 23) : 1.0f;
}
__device__ __forceinline__ float pow2_inv(float p) { return __uint_as_float(0x7f000000u - __float_as_uint(p)); }

constexpr int A_KBLK = 214;
constexpr int KG_R = 0, KG_L0 = 4352, KG_L1 = 8704, KG_HW = 8704 + 25 * 272;
constexpr int KG_CH = 2 * KG_HW + 16;
constexpr int A_SIDE = 3 * KG_CH;
constexpr int A_LDS = A_SIDE + 20736;

__device__ __forceinline__ void pack_wd(const u16* __restrict__ wd, char* __restrict__ ws, char* buf, int u, int lane) {
  const int ks = u >> 3, dg = u & 7;
#pragma unroll
  for (int i = 0; i < 5; ++i) {
    const int idx = i * 64 + lane, row = idx / 10, c = idx % 10;
    __builtin_amdgcn_global_load_lds((const unsigned*)((const char*)wd + (size_t)(32 * ks + row) * 1280 + 160 * dg + 16 * c),
                                     (LAS unsigned*)(buf + i * 1024), 16, 0, 2);
  }
  asm volatile("s_waitcnt vmcnt(0)" ::: "memory");
  wave_fence();
  const int g = lane >> 4, q = (lane & 15) >> 2, p = lane & 3;
  char* dst = ws + OFF_WP2 + ((size_t)(dg * 54 + ks) * 5) * 1024 + lane * 16;
#pragma unroll
  for (int dt = 0; dt < 5; ++dt) {
    s4_t aa = tr_read(buf + (4 * g + q) * 160 + 32 * dt + 8 * p);
    s4_t bb = tr_read(buf + (16 + 4 * g + q) * 160 + 32 * dt + 8 * p);
    u2_t ua = __builtin_bit_cast(u2_t, aa), ub = __builtin_bit_cast(u2_t, bb);
    st16(dst + dt * 1024, u4_t{ua.x, ua.y, ub.x, ub.y});
  }
}

extern "C" __global__ __launch_bounds__(1024) void kA(const u16* __restrict__ x, const u16* __restrict__ Are, const u16* __restrict__ Aim,
                                           const u16* __restrict__ Bre, const u16* __restrict__ Bim, const u16* __restrict__ Cre,
                                           const u16* __restrict__ Cim, const u16* __restrict__ logdt, const u16* __restrict__ wd,
                                           const u16* __restrict__ wg, const u16* __restrict__ wu, const u16* __restrict__ dskip,
                                           const u16* __restrict__ outp, char* __restrict__ ws) {
  extern __shared__ __attribute__((aligned(16))) char smem[];
  const int tid = threadIdx.x, lane = tid & 63;
  const int w = __builtin_amdgcn_readfirstlane(tid >> 6);
  unsigned pf0 = 0, pf1 = 0;
  if (blockIdx.x < SELFPF && w == 15) {
    unsigned long long a0;
    asm volatile("s_getpc_b64 s[20:21]\n\ts_add_u32 s20, s20, kA@rel32@lo+4\n\ts_addc_u32 s21, s21, kA@rel32@hi+12\n\ts_mov_b64 %0, s[20:21]" : "=s"(a0) :: "s20", "s21", "scc");
    asm volatile("global_load_dword %0, %1, off" : "=v"(pf0) : "v"(a0 + lane * 64) : "memory");
    asm volatile("global_load_dword %0, %1, off" : "=v"(pf1) : "v"(a0 + 4096 + lane * 64) : "memory");
  }
  if (w == 0) STAMP(0, 0);
  if (blockIdx.x >= A_KBLK) {
    const int u = 344 + (blockIdx.x - A_KBLK) * 16 + w;
    if (u < 432) pack_wd(wd, ws, smem + w * 5120, u, lane);
    if (blockIdx.x >= 248 && w == 0) {
      unsigned long long a0, a1;
      asm volatile("s_getpc_b64 s[20:21]\n\ts_add_u32 s20, s20, kA@rel32@lo+4\n\ts_addc_u32 s21, s21, kA@rel32@hi+12\n\ts_mov_b64 %0, s[20:21]" : "=s"(a0) :: "s20", "s21", "scc");
      asm volatile("s_getpc_b64 s[20:21]\n\ts_add_u32 s20, s20, kD@rel32@lo+4\n\ts_addc_u32 s21, s21, kD@rel32@hi+12\n\ts_mov_b64 %0, s[20:21]" : "=s"(a1) :: "s20", "s21", "scc");
      const unsigned long long lo = a0 < a1 ? a0 : a1, n = ((a0 < a1 ? a1 - a0 : a0 - a1) + 8192) >> 7;
      unsigned sink = 0;
      for (unsigned long long i = lane; i < n; i += 64) { unsigned v; asm volatile("global_load_dword %0, %1, off" : "=v"(v) : "v"(lo + i * 128) : "memory"); sink = v; }
      asm volatile("s_waitcnt vmcnt(0)" :: "v"(sink) : "memory");
    }
    if (blockIdx.x >= 248 && w == 1) {
      unsigned v0, v1, v2, v3, v4, v5, v6, v7, v8;
      const char* p;
      constexpr size_t G = TLBPF;
      p = ws + (size_t)min((size_t)lane * G, (size_t)0x1150000 - 4);
      asm volatile("global_load_dword %0, %1, off nt" : "=v"(v0) : "v"(p) : "memory");
      p = ws + (size_t)min((size_t)(lane + 64) * G, (size_t)0x1150000 - 4);
      asm volatile("global_load_dword %0, %1, off nt" : "=v"(v1) : "v"(p) : "memory");
      p = ws + (size_t)min((size_t)(lane + 128) * G, (size_t)0x1150000 - 4);
      asm volatile("global_load_dword %0, %1, off nt" : "=v"(v2) : "v"(p) : "memory");
      p = ws + (size_t)min((size_t)(lane + 192) * G, (size_t)0x1150000 - 4);
      asm volatile("global_load_dword %0, %1, off nt" : "=v"(v3) : "v"(p) : "memory");
      p = ws + (size_t)min((size_t)(lane + 256) * G, (size_t)0x1150000 - 4);
      asm volatile("global_load_dword %0, %1, off nt" : "=v"(v4) : "v"(p) : "memory");
      p = (const char*)wg + min((size_t)lane * G, (size_t)DM * FF * 2 - 4);
      asm volatile("global_load_dword %0, %1, off nt" : "=v"(v5) : "v"(p) : "memory");
      p = (const char*)wu + min((size_t)lane * G, (size_t)DM * FF * 2 - 4);
      asm volatile("global_load_dword %0, %1, off nt" : "=v"(v6) : "v"(p) : "memory");
      p = (const char*)outp + min((size_t)lane * G, (size_t)SL * DM * 2 - 4);
      asm volatile("global_load_dword %0, %1, off nt" : "=v"(v7) : "v"(p) : "memory");
      p = (const char*)dskip;
      asm volatile("global_load_dword %0, %1, off nt" : "=v"(v8) : "v"(p) : "memory");
      asm volatile("s_waitcnt vmcnt(0)" :: "v"(v0), "v"(v1), "v"(v2), "v"(v3), "v"(v4), "v"(v5), "v"(v6), "v"(v7), "v"(v8) : "memory");
    }
    if (w == 0) STAMP(0, 7);
    return;
  }
  if (w >= 12) {
    const int w4 = w - 12;
    if (blockIdx.x >= 128) {
      pack_wd(wd, ws, smem + A_SIDE + w4 * 5120, (blockIdx.x - 128) * 4 + w4, lane);
      bar();
      return;
    }
    const int t0 = blockIdx.x * 16;
    const int r = lane >> 4, c = lane & 15, rl = w4 * 4 + r;
    char* xs = smem + A_SIDE;
    const u4_t* p = (const u4_t*)(x + (size_t)(t0 + rl) * DM);
    u4_t v[5];
#pragma unroll
    for (int i = 0; i < 5; ++i) v[i] = ldnt(p + c + 16 * i);
    if (w4 == 0 && lane < 16) ((unsigned long long*)(ws + OFF_G2))[t0 + lane] = 0ull;
    float s = 0.f;
#pragma unroll
    for (int i = 0; i < 5; ++i)
#pragma unroll
      for (int j = 0; j < 4; ++j) s += bf_lo(v[i][j]) + bf_hi(v[i][j]);
#pragma unroll
    for (int i = 0; i < 5; ++i) *(u4_t*)(xs + rl * 1296 + (c + 16 * i) * 16) = v[i];
    asm volatile("" :: "v"(pf0), "v"(pf1));
    const float mean = row16_sum(s) * (1.0f / DM);
    float q = 0.f;
#pragma unroll
    for (int i = 0; i < 5; ++i)
#pragma unroll
      for (int j = 0; j < 4; ++j) {
        float a = bf_lo(v[i][j]) - mean, b = bf_hi(v[i][j]) - mean;
        q += a * a; q += b * b;
      }
    const float var = row16_sum(q) * (1.0f / DM);
    if (c == 0) {
      float mb = rbf(mean), sd = rbf(sqrtf(rbf(rbf(var) + 1e-5f)));
      ((unsigned*)(ws + OFF_S1))[t0 + rl] = (__float_as_uint(mb) >> 16) | (__float_as_uint(sd) & 0xffff0000u);
    }
    __syncthreads();
    const int t4 = w4 * 64 + lane;
#pragma unroll
    for (int it = 0; it < 5; ++it) {
      const int item = t4 + 256 * it, q4 = item >> 3, tp = item & 7;
      u2_t a = *(const u2_t*)(xs + (2 * tp) * 1296 + 8 * q4);
      u2_t b = *(const u2_t*)(xs + (2 * tp + 1) * 1296 + 8 * q4);
      st16(ws + OFF_X4 + ((size_t)q4 * SL + t0 + 2 * tp) * 8, u4_t{a.x, a.y, b.x, b.y});
    }
    if (w4 == 0) STAMP(0, 6);
    return;
  }
  const int cl = w >> 2, part = (w >> 1) & 1, hw = w & 1;
  const int chr = blockIdx.x * 3 + cl, ch = min(chr, DM - 1);
  char* chb = smem + cl * KG_CH;
  char* hb = chb + hw * KG_HW;
  const int n = ch * 128 + hw * 64 + lane;
  const u16 i_are = ldnt(Are + n), i_aim = ldnt(Aim + n), i_bre = ldnt(Bre + n), i_bim = ldnt(Bim + n), i_cre = ldnt(Cre + n), i_cim = ldnt(Cim + n);
  const float dt = rbf(expf(bf2f(logdt[ch])));
  const float are = dt * bf2f(i_are), aim = dt * bf2f(i_aim);
  const float bre = dt * bf2f(i_bre), bim = dt * bf2f(i_bim);
  const float cre = bf2f(i_cre), cim = bf2f(i_cim);
  f2_t c = {cre * bre - cim * bim, cre * bim + cim * bre};
  const float er = are * 1.44269504088896341f, wr = aim * 0.15915494309189535f;
  const float bnd = (fabsf(c.x) + fabsf(c.y)) * fmaxf(1.0f, __builtin_amdgcn_exp2f(er * 2048.f));
  const float bsum = wave_sum(bnd);
  if (w == 0) STAMP(0, 1);
  const float Sw = pow2_scale(bsum, 15);
  c *= Sw;
  const Cw z1 = mkw(cexp_l(er, wr, 1.f)), w128 = mkw(cexp_l(er, wr, 128.f));
  auto put = [&](char* buf, int row, f2_t v) { *(unsigned*)(buf + row * 272 + 4 * lane) = pk_f16(v.x, -v.y); };
  if (part == 0) {
    const Cw w8 = mkw(cexp_l(er, wr, 8.f));
    f2_t R = w8.w, v0 = c, v1 = cmulw(c, w128);
    *(unsigned*)(hb + KG_R + 4 * lane) = pk_f16(1.f, 0.f);
#pragma unroll
    for (int i = 0; i < 8; ++i) {
      *(unsigned*)(hb + KG_R + (2 * i + 1) * 272 + 4 * lane) = pk_f16(R.x, R.y);
      if (i < 7) {
        R = cmulw(R, w8);
        *(unsigned*)(hb + KG_R + (2 * i + 2) * 272 + 4 * lane) = pk_f16(R.x, R.y);
        R = cmulw(R, w8);
      }
      put(hb + KG_L0, i, v0); put(hb + KG_L0, 8 + i, v1);
      if (i < 7) { v0 = cmulw(v0, z1); v1 = cmulw(v1, z1); }
    }
  } else {
    const Cw z2 = mkw(cmulw(z1.w, z1));
    const Cw z4 = mkw(cmulw(z2.w, z2));
    f2_t T = cmulw(c, w128);
#pragma unroll
    for (int i1 = 2; i1 < 17; ++i1) {
      T = cmulw(T, w128);
      if (i1 < 4) {
        f2_t v = T;
#pragma unroll
        for (int i0 = 0; i0 < 4; ++i0) { put(hb + KG_L1, 4 * (i1 - 2) + i0, v); if (i0 < 3) v = cmulw(v, z2); }
      } else if (i1 < 8) {
        put(hb + KG_L1, 8 + 2 * (i1 - 4), T); put(hb + KG_L1, 8 + 2 * (i1 - 4) + 1, cmulw(T, z4));
      } else {
        put(hb + KG_L1, 16 + (i1 - 8), T);
      }
    }
  }
  if (part == 0 && lane == 0) ((float*)(chb + 2 * KG_HW))[hw] = bsum;
  __syncthreads();
  if (w == 0) STAMP(0, 2);
  if (part == 1 && hw == 1) return;
  const int tile = part ? 1 : 2 * hw;
  const int fr = lane & 15, kg = lane >> 4;
  const int row = (tile == 2) ? 16 + min(fr, 8) : fr;
  const int lo = (tile == 0) ? KG_L0 : KG_L1;
  f4_t acc[2];
#pragma unroll
  for (int h = 0; h < 2; ++h) {
    const char* hbb = chb + h * KG_HW;
    f4_t a = {0.f, 0.f, 0.f, 0.f};
#pragma unroll
    for (int ks = 0; ks < 4; ++ks) {
      const h8_t Bf = *(const h8_t*)(hbb + KG_R + fr * 272 + 64 * ks + 16 * kg);
      const h8_t Af = *(const h8_t*)(hbb + lo + row * 272 + 64 * ks + 16 * kg);
      a = __builtin_amdgcn_mfma_f32_16x16x32_f16(Af, Bf, a, 0, 0, 0);
    }
    acc[h] = a;
  }
  const float b0 = ((const float*)(chb + 2 * KG_HW))[0], b1 = ((const float*)(chb + 2 * KG_HW))[1];
  const float Sc = pow2_scale(b0 + b1, 15);
  const f4_t kv = acc[0] * (pow2_inv(pow2_scale(b0, 15)) * Sc) + acc[1] * (pow2_inv(pow2_scale(b1, 15)) * Sc);
  const unsigned p01 = pk_f16(kv[0], kv[1]), p23 = pk_f16(kv[2], kv[3]);
  char* kr = ws + OFF_KD + (size_t)ch * KRS;
  if (tile == 0) {
    *(u2_t*)(kr + 2 * (128 * (kg >> 1) + 4 * (kg & 1) + 8 * fr)) = u2_t{p01, p23};
    if (lane == 0) ((float*)(ws + OFF_INVS))[ch] = pow2_inv(Sc);
  } else if (tile == 1) {
    if (kg < 2) {
      *(u2_t*)(kr + 2 * (256 + 64 * kg + 4 * fr)) = u2_t{p01, p23};
    } else {
      *(unsigned*)(kr + 2 * (384 + 64 * (kg - 2) + 2 * fr)) = p01;
      *(unsigned*)(kr + 2 * (384 + 64 * (kg - 2) + 32 + 2 * fr)) = p23;
    }
  } else {
    u16* k3 = (u16*)kr + 512 + 64 * kg + fr;
    if (kg < 2) { k3[0] = (u16)p01; k3[16] = (u16)(p01 >> 16); k3[32] = (u16)p23; k3[48] = (u16)(p23 >> 16); }
    else if (lane == 32) k3[0] = (u16)p01;
  }
  if (w == 0) STAMP(0, 7);
}

constexpr int B_CONV = 160;
constexpr int B_KOFF = 0;
constexpr int B_UOFF = 4 * KDS;
constexpr int B_UCH = 4608;
constexpr int B_YCH = 10240;
constexpr int B_LDS = 40960;
#define B_DELAY 0

struct BFrag { h8_t B[8]; u2_t A[5][2]; };
__device__ __forceinline__ void mfma_f16(f4_t& d, u4_t a, h8_t b) {
  asm volatile("v_mfma_f32_16x16x32_f16 %0, %1, %2, %0" : "+v"(d) : "v"(a), "v"(b));
}
__device__ __forceinline__ void mfma_f16_0(f4_t& d, u4_t a, h8_t b) {
  asm volatile("v_mfma_f32_16x16x32_f16 %0, %1, %2, 0" : "=&v"(d) : "v"(a), "v"(b));
}

extern "C" __global__ __launch_bounds__(512) void kB(const u16* __restrict__ dskip, const u16* __restrict__ wg, const u16* __restrict__ wu,
                                          char* __restrict__ ws) {
  extern __shared__ __attribute__((aligned(16))) char smem[];
  const int tid = threadIdx.x, lane = tid & 63;
  const int w = __builtin_amdgcn_readfirstlane(tid >> 6);
  if (w == 0) STAMP(1, 0);
  if (blockIdx.x >= B_CONV) {
    const int xcd = blockIdx.x & 7, rk = (blockIdx.x - B_CONV) >> 3;
    const int g4 = xcd >> 1, a = xcd & 1, nl = (g4 < 3) ? 7 : 6;
    if (rk >= nl) return;
#pragma unroll
    for (int i = 0; i < B_DELAY; ++i) __builtin_amdgcn_s_sleep(16);
    const int jl = 7 * g4 + rk;
    const char* pm = (const char*)(a ? wu : wg) + (size_t)(80 * w + (lane >> 3)) * (FF * 2) + 128 * jl + (lane & 7) * 16;
    u4_t v[10];
#pragma unroll
    for (int i = 0; i < 10; ++i) v[i] = ldnt((const u4_t*)(pm + (size_t)i * 8 * (FF * 2)));
    const char* po = (const char*)(a ? wg : wu) + (size_t)(80 * w) * (FF * 2) + 128 * jl;
    unsigned t0v, t1v;
    asm volatile("global_load_dword %0, %1, off nt" : "=v"(t0v) : "v"(po + (size_t)lane * (FF * 2)) : "memory");
    asm volatile("global_load_dword %0, %1, off nt" : "=v"(t1v) : "v"(po + (size_t)(64 + (lane & 15)) * (FF * 2)) : "memory");
    f2_t s[4];
#pragma unroll
    for (int e = 0; e < 4; ++e) s[e] = f2_t{0.f, 0.f};
#pragma unroll
    for (int i = 0; i < 10; ++i)
#pragma unroll
      for (int jj = 0; jj < 4; ++jj) s[jj] += f2_t{bf_lo(v[i][jj]), bf_hi(v[i][jj])};
    float* pp = (float*)smem;
    *(f4_t*)(pp + (w * 8 + (lane >> 3)) * 68 + (lane & 7) * 8) = f4_t{s[0].x, s[0].y, s[1].x, s[1].y};
    *(f4_t*)(pp + (w * 8 + (lane >> 3)) * 68 + (lane & 7) * 8 + 4) = f4_t{s[2].x, s[2].y, s[3].x, s[3].y};
    __syncthreads();
    float acc = 0.f;
#pragma unroll
    for (int i = 0; i < 8; ++i) acc += pp[(w * 8 + i) * 68 + lane];
    __syncthreads();
    pp[w * 64 + lane] = acc;
    __syncthreads();
    if (w == 0) {
      float t = 0.f;
#pragma unroll
      for (int i = 0; i < 8; ++i) t += pp[i * 64 + lane];
      ((float*)(ws + OFF_CS))[a * FF + 64 * jl + lane] = t;
    }
    asm volatile("s_waitcnt vmcnt(0)" ::"v"(t0v), "v"(t1v) : "memory");
    if (w == 0) STAMP(1, 7);
    return;
  }
  const int a_ = blockIdx.x >> 3, x_ = blockIdx.x & 7;
  const int q = 16 * (a_ >> 1) + 2 * x_ + (a_ & 1);
  u4_t xv[2]; u2_t sv[2];
#pragma unroll
  for (int it = 0; it < 2; ++it) {
    const int j = tid + 512 * it;
    xv[it] = *(const u4_t*)(ws + OFF_X4 + ((size_t)q * SL + 2 * j) * 8);
    sv[it] = *(const u2_t*)(ws + OFF_S1 + 8 * j);
  }
  const int kch = tid >> 7, kc = tid & 127;
  const char* kr = ws + OFF_KD + (size_t)(4 * q + kch) * KRS;
  const int e0 = (kc < 32) ? 8 * kc : (kc < 64) ? 256 + 4 * (kc - 32) : 384 + 2 * (kc - 64);
  const u4_t kd4 = *(const u4_t*)(kr + 2 * e0);
  const unsigned kd5 = *(const unsigned*)(kr + 2 * e0 + 16);
  const unsigned kfirst = *(const u16*)kr, k1024 = *(const u16*)(kr + 2 * 512);
  const int dl = 127 - kc;
  const unsigned kfa = *(const u16*)(kr + 2 * (512 + max(2 * dl - 127, 0))), kfb = *(const u16*)(kr + 2 * (512 + max(2 * dl - 126, 0)));
  float dk[4];
#pragma unroll
  for (int c = 0; c < 4; ++c) dk[c] = bf2f(dskip[4 * q + c]);
  const float invs = ((const float*)(ws + OFF_INVS))[4 * q + (w & 3)];
  *(unsigned*)(smem + B_UOFF + (tid >> 7) * B_UCH + 4096 + 4 * (tid & 127)) = 0u;
  const int c = lane & 15, kg = lane >> 4;
  h8_t band9, band7;
#pragma unroll
  for (int j = 0; j < 8; ++j) {
    const int k = 8 * kg + 7 - j;
    band9[j] = (k >= c + 4 && k <= c + 12) ? (_Float16)1.0f : (_Float16)0.0f;
    band7[j] = (k >= c + 5 && k <= c + 11) ? (_Float16)1.0f : (_Float16)0.0f;
  }
  {
    char* ki = smem + B_KOFF + kch * KDS;
    const unsigned k0 = kd4[0] & 0xffffu;
    unsigned o0, o1, o2, o3, nx;
    if (kc < 32) { o0 = kd4[0]; o1 = kd4[1]; o2 = kd4[2]; o3 = kd4[3]; nx = kd5; }
    else if (kc < 64) {
      const unsigned k2 = kd4[0] >> 16, k4 = kd4[1] & 0xffffu, k6 = kd4[1] >> 16, k8 = kd4[2] & 0xffffu;
      o0 = k0 * 0x10001u; o1 = k2 | (k4 << 16); o2 = k4 * 0x10001u; o3 = k6 | (k8 << 16); nx = k8;
    } else {
      const unsigned k4 = kd4[0] >> 16, k8 = kd4[1] & 0xffffu;
      o0 = k0 * 0x10001u; o1 = k0 | (k4 << 16); o2 = k4 * 0x10001u; o3 = k8 * 0x10001u; nx = k8;
    }
    *(u4_t*)(ki + 16 * (kc + 2)) = u4_t{o0, o1, o2, o3};
    *(u4_t*)(ki + KD_O + 16 * (kc + 2)) = u4_t{__builtin_amdgcn_alignbit(o1, o0, 16), __builtin_amdgcn_alignbit(o2, o1, 16),
                                              __builtin_amdgcn_alignbit(o3, o2, 16), __builtin_amdgcn_alignbit(nx, o3, 16)};
    if (kc < 6) {
      const int cp = (kc < 2) ? kc : 128 + kc;
      const unsigned kk = k1024 * 0x10001u;
      const u4_t e = (kc == 2) ? u4_t{kk, kk, k1024, 0u} : u4_t{0u, 0u, 0u, 0u};
      const u4_t o = (kc == 2) ? u4_t{kk, kk, 0u, 0u} : u4_t{0u, 0u, 0u, (kc == 1) ? (kfirst << 16) : 0u};
      *(u4_t*)(ki + 16 * cp) = e;
      *(u4_t*)(ki + KD_O + 16 * cp) = o;
    }
    if (kc < 80) *(unsigned*)(ki + KD_DR + 4 * kc) = (dl >= 64) ? (kfa | (kfb << 16)) : 0u;
  }
  asm volatile("" : "+v"(band9), "+v"(band7));
  float z[2][8];
  if (w == 0) { STAMP(1, 1); }
#pragma unroll
  for (int it = 0; it < 2; ++it) {
    const int j = tid + 512 * it;
    const float m0 = bf_lo(sv[it].x), r0 = __builtin_amdgcn_rcpf(bf_hi(sv[it].x)), m1 = bf_lo(sv[it].y), r1 = __builtin_amdgcn_rcpf(bf_hi(sv[it].y));
    float x0[4] = {bf_lo(xv[it].x), bf_hi(xv[it].x), bf_lo(xv[it].y), bf_hi(xv[it].y)};
    float x1[4] = {bf_lo(xv[it].z), bf_hi(xv[it].z), bf_lo(xv[it].w), bf_hi(xv[it].w)};
#pragma unroll
    for (int cc = 0; cc < 4; ++cc) {
      const float u0 = (x0[cc] - m0) * r0, u1 = (x1[cc] - m1) * r1;
      *(unsigned*)(smem + B_UOFF + cc * B_UCH + 4 * (1023 - j)) = pk_f16(u1, u0);
      z[it][cc] = x0[cc] + u0 * dk[cc];
      z[it][4 + cc] = x1[cc] + u1 * dk[cc];
    }
  }
  __syncthreads();
  if (w == 0) STAMP(1, 2);
  f4_t acc[8];
  if (w < 4) {
    const char* ub = smem + B_UOFF + w * B_UCH + 4112 - 32 * c - 16 * kg - 512 * 7;
    const char* kb = smem + B_KOFF + w * KDS + ((c & 1) ? 0 : KD_O) + 4 * (12 + ((c + 1) >> 1) - 4 * kg);
    const unsigned* dr = (const unsigned*)(smem + B_KOFF + w * KDS + KD_DR) + (127 - c + 4 * kg);
    const char* uw = smem + B_UOFF + w * B_UCH + 4112 - 32 * c - 16 * kg;
    auto rdB = [&](BFrag& F, int i0, int p) { F.B[p] = *(const h8_t*)(ub + 512 * (7 - p) + 64 * i0); };
    auto rdA = [&](BFrag& F, int i0, int i1, int half) {
      const unsigned* ap = (const unsigned*)(kb + 64 * i0 + 512 * i1) + 2 * half;
      F.A[i1][half] = u2_t{ap[0], ap[1]};
    };
    auto frag = [&](const u2_t (&a)[2]) { return u4_t{a[0].x, a[0].y, a[1].x, a[1].y}; };
    auto rdnext = [&](BFrag& F, int i0, int m) {
      if (m < 8) rdB(F, i0, m); else if (m < 16) rdA(F, i0, (m - 8) >> 1, (m - 8) & 1);
    };
    auto step = [&](auto i0c, const BFrag& F, BFrag& N, auto nextc) {
      constexpr int I0 = decltype(i0c)::value, NX = decltype(nextc)::value;
      int m = 0;
#pragma unroll
      for (int i1 = 0; i1 < 5; ++i1) {
        if (i1 == 4 && I0 != 0) continue;
        const u4_t a = frag(F.A[i1]);
#pragma unroll
        for (int t = i1; t < 8; ++t) {
          if (I0 == 0 && i1 == 0) mfma_f16_0(acc[t], a, F.B[t]); else mfma_f16(acc[t], a, F.B[t - i1]);
          if (NX >= 0) rdnext(N, NX, m);
          SB;
          ++m;
        }
      }
    };
    BFrag F0, F1;
    h8_t U9[4], U7[4];
#pragma unroll
    for (int bt = 0; bt < 4; ++bt) { U9[bt] = *(const h8_t*)(uw - 512 * bt + 16); U7[bt] = *(const h8_t*)(uw - 512 * bt); }
#pragma unroll
    for (int m = 0; m < 16; ++m) rdnext(F0, 0, m);
    rdA(F0, 0, 4, 0); rdA(F0, 0, 4, 1);
    SB;
    f4_t X9[4], X7[4];
#pragma unroll
    for (int bt = 0; bt < 4; ++bt) { mfma_f16_0(X9[bt], __builtin_bit_cast(u4_t, U9[bt]), band9); mfma_f16_0(X7[bt], __builtin_bit_cast(u4_t, U7[bt]), band7); }
    SB;
    step(IC(0), F0, F1, IC(1));
    h8_t Vf[4]; u4_t Tf[4];
    {
      int m = 0;
#pragma unroll
      for (int i1 = 0; i1 < 4; ++i1) {
        const u4_t a = frag(F1.A[i1]);
#pragma unroll
        for (int t = i1; t < 8; ++t) {
          mfma_f16(acc[t], a, F1.B[t - i1]);
          rdnext(F0, 2, m);
          if (m < 4) {
            const u4_t vu = {pk_f16(X7[m][0], X9[m][0]), pk_f16(X7[m][1], X9[m][1]), pk_f16(X7[m][2], X9[m][2]), pk_f16(X7[m][3], X9[m][3])};
            Vf[m] = __builtin_bit_cast(h8_t, vu);
          }
          if (m >= 16 && m < 20) { const unsigned* tp = dr - 16 * (m - 16 + 4); Tf[m - 16] = u4_t{tp[0], tp[1], tp[2], tp[3]}; }
          SB;
          ++m;
        }
      }
    }
    step(IC(2), F0, F1, IC(3));
#pragma unroll
    for (int bt = 0; bt < 4; ++bt)
#pragma unroll
      for (int at = 4 + bt; at < 8; ++at) mfma_f16(acc[at], __builtin_bit_cast(u4_t, Vf[bt]), __builtin_bit_cast(h8_t, Tf[at - bt - 4]));
    SB;
    step(IC(3), F1, F0, IC(4));
    step(IC(4), F0, F1, IC(5));
    step(IC(5), F1, F0, IC(6));
    step(IC(6), F0, F1, IC(7));
    step(IC(7), F1, F0, IC(-1));
    asm volatile("s_nop 7\n\ts_nop 1" : "+v"(acc[0]), "+v"(acc[1]), "+v"(acc[2]), "+v"(acc[3]), "+v"(acc[4]), "+v"(acc[5]), "+v"(acc[6]), "+v"(acc[7]));
  }
  if (w == 0) STAMP(1, 3);
  __syncthreads();
  if (w < 4) {
#pragma unroll
    for (int t = 0; t < 8; ++t) *(f4_t*)(smem + w * B_YCH + t * 1280 + kg * 320 + c * 16) = acc[t] * invs;
  }
  __syncthreads();
#pragma unroll
  for (int it = 0; it < 2; ++it) {
    const int j = tid + 512 * it, t = 2 * j;
    const int yo = (t >> 8) * 1280 + ((t >> 2) & 3) * 320 + ((t >> 4) & 15) * 16 + (t & 3) * 4;
    float h0[4], h1[4];
#pragma unroll
    for (int cc = 0; cc < 4; ++cc) {
      f2_t y = *(const f2_t*)(smem + cc * B_YCH + yo);
      h0[cc] = z[it][cc] + y.x;
      h1[cc] = z[it][4 + cc] + y.y;
    }
    char* dst = ws + OFF_H8 + ((size_t)(q >> 1) * SL + t) * 16 + (q & 1) * 8;
    st8(dst, u2_t{pk_bf16(h0[0], h0[1]), pk_bf16(h0[2], h0[3])});
    st8(dst + 16, u2_t{pk_bf16(h1[0], h1[1]), pk_bf16(h1[2], h1[3])});
  }
  if (w == 0) STAMP(1, 7);
}

constexpr int C_T = 240;
constexpr int C_SLOT = 23552, C_NSLOT = 6, C_GOFF = 15360;
constexpr int C_STAT = C_NSLOT * C_SLOT;
constexpr int C_CS = C_STAT + 960;
constexpr int C_LDS = C_CS + 512;
constexpr int C_STEPS = 20;
constexpr size_t OFF_DUMMY = 0x1140000;

__device__ __forceinline__ unsigned ln_pack(float sum, float sq) {
  const float mean = sum * (1.0f / DM);
  const float var = fmaxf(sq * (1.0f / DM) - mean * mean, 0.f);
  const float mb = rbf(mean), sd = rbf(sqrtf(rbf(rbf(var) + 1e-5f)));
  return (__float_as_uint(mb) >> 16) | (__float_as_uint(sd) & 0xffff0000u);
}
__device__ __forceinline__ unsigned ln_row(const char* h8, int t) {
  float s = 0.f, q = 0.f;
  for (int pl = 0; pl < 80; ++pl) {
    const u4_t v = *(const u4_t*)(h8 + (size_t)pl * 32768 + t * 16);
    for (int j = 0; j < 4; ++j) { const float a = bf_lo(v[j]), b = bf_hi(v[j]); s += a + b; q += a * a + b * b; }
  }
  return ln_pack(s, q);
}

template <int NDMA>
__device__ __forceinline__ void c_loader(char* smem, char* ws, const char* wbase, int w4, int lane, int t0, int jline, int xcd) {
  const unsigned lds0 = (unsigned)(size_t)(LAS char*)smem;
  const char* h8 = ws + OFF_H8;
  unsigned voffH[4], voffW[2];
#pragma unroll
  for (int k = 0; k < 2; ++k) {
    const int r = 8 * (2 * (w4 & 1) + k) + (lane >> 3), c = lane & 7;
    const int f = ((r >> 1) & 1) | ((r >> 2) & 2);
    voffW[k] = r * (FF * 2) + 128 * jline + (((c >> 1) ^ f) * 32) + (c & 1) * 16;
  }
#pragma unroll
  for (int k = 0; k < 4; ++k) {
    const int idx = 64 * (w4 + 4 * k) + lane, pl = idx / C_T, tt = idx % C_T;
    const int t = min(t0 + tt, SL - 1);
    voffH[k] = pl * 32768 + t * 16;
  }
  const unsigned ldsW = C_GOFF + (w4 >> 1) * 4096 + (w4 & 1) * 2048;
  auto issue = [&](int step, unsigned so) {
#pragma unroll
    for (int k = 0; k < 2; ++k) glds16(wbase + (size_t)step * (32 * FF * 2), voffW[k], lds0 + so + ldsW + k * 1024);
#pragma unroll
    for (int k = 0; k < NDMA - 2; ++k) glds16(h8 + (size_t)step * (4 * 32768), voffH[k], lds0 + so + (w4 + 4 * k) * 1024);
  };
  constexpr unsigned S = C_SLOT;
  issue(0, 0); issue(1, S); issue(2, 2 * S); issue(3, 3 * S);
  wait_vm<3 * NDMA>(); bar();
  issue(4, 4 * S);
  wait_vm<2 * NDMA>(); bar();
  issue(5, 5 * S); issue(6, 0);
  unsigned sw = S;
#pragma unroll 1
  for (int j = 1; j < 7; ++j) {
    wait_vm<2 * NDMA>();
    bar();
    issue(2 * j + 5, sw);
    sw += S; if (sw == C_NSLOT * S) sw = 0;
    issue(2 * j + 6, sw);
    sw += S; if (sw == C_NSLOT * S) sw = 0;
  }
  wait_vm<2 * NDMA>(); bar();
  issue(19, sw);
  const int tl = min(60 * w4 + lane, C_T - 1), tg = min(t0 + tl, SL - 1);
  const gu64* gp = (const gu64*)(ws + OFF_G2) + tg;
  const float* csp = (const float*)(ws + OFF_CS) + ((lane >> 4) & 1) * FF + 64 * jline + 16 * w4 + (lane & 15);
  const char* tb = ws + OFF_WP2 + (size_t)(4 * (xcd & 1)) * (54 * 5120);
  const int tl0 = ((int)(blockIdx.x >> 3) * 4 + w4) * 68;
  u2_t gv; float csv; unsigned tv0, tv1;
  asm volatile("global_load_dwordx2 %0, %1, off sc1" : "=v"(gv) : "v"(gp) : "memory");
  asm volatile("global_load_dword %0, %1, off" : "=v"(csv) : "v"(csp) : "memory");
  asm volatile("global_load_dword %0, %1, off" : "=v"(tv0) : "v"(tb + (size_t)min(tl0 + lane, 8639) * 128) : "memory");
  asm volatile("global_load_dword %0, %1, off" : "=v"(tv1) : "v"(tb + (size_t)min(tl0 + 64 + (lane & 3), 8639) * 128) : "memory");
  wait_vm<NDMA + 4>(); bar();
  asm volatile("s_waitcnt vmcnt(0)" : "+v"(gv), "+v"(csv), "+v"(tv0), "+v"(tv1)::"memory");
  bar();
  {
    bool ok = gv.y == 1u;
    for (int spin = 0; spin < 4096 && !__all(ok); ++spin) {
      const unsigned long long g = __hip_atomic_load(gp, __ATOMIC_RELAXED, __HIP_MEMORY_SCOPE_AGENT);
      gv.x = (unsigned)g; ok = (unsigned)(g >> 32) == 1u;
      __builtin_amdgcn_s_sleep(2);
    }
    if (!__all(ok)) gv.x = ln_row(h8, tg);
    if (lane < 60) *(float*)(smem + C_STAT + (60 * w4 + lane) * 4) = bf_lo(gv.x);
    if (lane < 32) *(float*)(smem + C_CS + (lane >> 4) * 256 + (16 * w4 + (lane & 15)) * 4) = csv;
  }
  asm volatile("s_waitcnt lgkmcnt(0)" ::: "memory");
  bar();
}

struct CFrag { b8_t A[2][2]; b8_t B[8]; };

extern "C" __global__ __launch_bounds__(512, 2) void kC(const u16* __restrict__ wg, const u16* __restrict__ wu, char* __restrict__ ws) {
  extern __shared__ __attribute__((aligned(16))) char smem[];
  const int tid = threadIdx.x, lane = tid & 63;
  const int w = __builtin_amdgcn_readfirstlane(tid >> 6);
  int panel, jline;
  const int xcd = blockIdx.x & 7, rank = blockIdx.x >> 3;
  {
    const int g4 = xcd >> 1, a = xcd & 1, nl = (g4 < 3) ? 7 : 6, total = 9 * nl, n0 = (total + 1) >> 1;
    if (a == 0 ? rank >= n0 : rank >= total - n0) {
      const int sp = (xcd < 6) ? (xcd >> 1) : 3 + 5 * (xcd - 6) + (rank - 27);
      const int wv = sp * 8 + w;
      if (w == 0) STAMP(2, 0);
      for (int tile = wv; tile < 128; tile += 104) {
        const int t = 16 * tile + (lane & 15);
        const char* hp = ws + OFF_H8 + (size_t)(lane >> 4) * 32768 + t * 16;
        u4_t v[20];
#pragma unroll
        for (int i = 0; i < 20; ++i) v[i] = *(const u4_t*)(hp + (size_t)i * (4 * 32768));
        float s0 = 0.f, s1 = 0.f, q0 = 0.f, q1 = 0.f;
        unsigned k_both = 0x3f803f80u;
        asm volatile("" : "+s"(k_both));
#pragma unroll
        for (int i = 0; i < 20; ++i) {
          s0 = dot2bf(v[i][0], k_both, s0); q0 = dot2bf(v[i][0], v[i][0], q0);
          s1 = dot2bf(v[i][1], k_both, s1); q1 = dot2bf(v[i][1], v[i][1], q1);
          s0 = dot2bf(v[i][2], k_both, s0); q0 = dot2bf(v[i][2], v[i][2], q0);
          s1 = dot2bf(v[i][3], k_both, s1); q1 = dot2bf(v[i][3], v[i][3], q1);
        }
        float s = s0 + s1, q = q0 + q1;
        s += __shfl_xor(s, 16); q += __shfl_xor(q, 16);
        s += __shfl_xor(s, 32); q += __shfl_xor(q, 32);
        if (lane < 16)
          __hip_atomic_store((gu64*)(ws + OFF_G2) + t, (1ull << 32) | ln_pack(s, q), __ATOMIC_RELAXED, __HIP_MEMORY_SCOPE_AGENT);
      }
      if (w == 0) STAMP(2, 7);
      return;
    }
    const int idx = rank + a * n0;
    panel = idx / nl; jline = 7 * g4 + idx % nl;
  }
  const int t0 = panel * C_T;
  if (w == 0) STAMP(2, 0);
  if (w >= 4) {
    const int w4 = w - 4;
    const char* wbase = (const char*)((w4 >> 1) ? wu : wg);
    if (w4 < 3) c_loader<6>(smem, ws, wbase, w4, lane, t0, jline, xcd);
    else c_loader<5>(smem, ws, wbase, w4, lane, t0, jline, xcd);
    return;
  }
  const int s = w >> 1, th = w & 1;
  const int fr = lane & 15, kg = lane >> 4, q = (lane & 15) >> 2, p = lane & 3;
  const unsigned hb0 = kg * 3840 + fr * 16 + 256 * 7;
  const unsigned hb1 = kg * 3840 + fr * 16 + 256 * (th ? 8 : 0);
  unsigned wa[2];
#pragma unroll
  for (int v = 0; v < 2; ++v) {
    const int sub = 2 * s + (v ? 1 - th : th);
    const int f = (q >> 1) | (2 * (kg & 1));
    wa[v] = C_GOFF + (8 * kg + q) * 128 + ((sub ^ f) * 32) + 8 * p;
  }
  f4_t ag[8][2], au[8][2];
#pragma unroll
  for (int u = 0; u < 8; ++u)
#pragma unroll
    for (int v = 0; v < 2; ++v) { ag[u][v] = f4_t{0.f, 0.f, 0.f, 0.f}; au[u][v] = f4_t{0.f, 0.f, 0.f, 0.f}; }
  auto load = [&](CFrag& F, unsigned so) {
    const char* sp = smem + so;
#pragma unroll
    for (int v = 0; v < 2; ++v)
#pragma unroll
      for (int m = 0; m < 2; ++m) {
        s4_t lo = tr_read(sp + wa[v] + m * 4096), hi = tr_read(sp + wa[v] + m * 4096 + 512);
        u2_t a = __builtin_bit_cast(u2_t, lo), b = __builtin_bit_cast(u2_t, hi);
        F.A[v][m] = __builtin_bit_cast(b8_t, (u4_t{a.x, a.y, b.x, b.y}));
      }
    F.B[0] = *(const b8_t*)(sp + hb0);
#pragma unroll
    for (int u = 1; u < 8; ++u) F.B[u] = *(const b8_t*)(sp + hb1 + 256 * (u - 1));
  };
  auto mma = [&](const CFrag& F) {
    ag[0][0] = __builtin_amdgcn_mfma_f32_16x16x32_bf16(F.A[0][0], F.B[0], ag[0][0], 0, 0, 0);
    au[0][0] = __builtin_amdgcn_mfma_f32_16x16x32_bf16(F.A[0][1], F.B[0], au[0][0], 0, 0, 0);
#pragma unroll
    for (int u = 1; u < 8; ++u)
#pragma unroll
      for (int v = 0; v < 2; ++v) {
        ag[u][v] = __builtin_amdgcn_mfma_f32_16x16x32_bf16(F.A[v][0], F.B[u], ag[u][v], 0, 0, 0);
        au[u][v] = __builtin_amdgcn_mfma_f32_16x16x32_bf16(F.A[v][1], F.B[u], au[u][v], 0, 0, 0);
      }
  };
  auto interleave = [&]() {
#pragma unroll
    for (int i = 0; i < 16; ++i) {
      __builtin_amdgcn_sched_group_barrier(0x008, 1, 0);
      __builtin_amdgcn_sched_group_barrier(0x100, 1, 0);
    }
    __builtin_amdgcn_sched_group_barrier(0x008, 14, 0);
  };
  CFrag F0, F1;
  bar();
  if (w == 0) STAMP(2, 1);
  load(F0, 0);
  unsigned sn = C_SLOT;
  SB;
#pragma unroll 1
  for (int j = 0; j < 9; ++j) {
    asm volatile("s_waitcnt lgkmcnt(0)" ::: "memory");
    bar(); SB;
    load(F1, sn);
    mma(F0);
    interleave();
    SB;
    sn += C_SLOT; if (sn == C_NSLOT * C_SLOT) sn = 0;
    load(F0, sn);
    mma(F1);
    interleave();
    SB;
    sn += C_SLOT; if (sn == C_NSLOT * C_SLOT) sn = 0;
  }
  asm volatile("s_waitcnt lgkmcnt(0)" ::: "memory");
  bar(); SB;
  load(F1, sn);
  mma(F0);
  interleave();
  SB;
  bar(); SB;
  f4_t csg[2], csu[2];
  float mu[8];
#pragma unroll
  for (int v = 0; v < 2; ++v) {
    const int sub = 2 * s + (v ? 1 - th : th);
    csg[v] = *(const f4_t*)(smem + C_CS + (16 * sub + 4 * kg) * 4);
    csu[v] = *(const f4_t*)(smem + C_CS + 256 + (16 * sub + 4 * kg) * 4);
  }
#pragma unroll
  for (int u = 0; u < 8; ++u) {
    const int tile = (u == 0) ? 7 : (th ? 7 + u : u - 1);
    mu[u] = *(const float*)(smem + C_STAT + (16 * tile + fr) * 4);
  }
  mma(F1);
  SB;
  if (w == 0) STAMP(2, 2);
  char* hh = ws + OFF_HH + (size_t)(2 * jline + s) * 1024 + lane * 16;
  char* sink = ws + OFF_DUMMY + lane * 16;
  auto epi = [&](auto thc) {
    constexpr int TH = decltype(thc)::value;
#pragma unroll
    for (int u = 0; u < 8; ++u) {
      const int tile = (u == 0) ? 7 : (TH ? 7 + u : u - 1);
      const int t16 = 15 * panel + tile;
      char* dst = (t16 < 128) ? hh + (size_t)t16 * (54 * 1024) : sink;
      u2_t o[2];
#pragma unroll
      for (int v = 0; v < 2; ++v) {
        if (u == 0 && v == 1) continue;
        const f4_t r = (ag[u][v] - mu[u] * csg[v]) * (au[u][v] - mu[u] * csu[v]);
        o[v] = u2_t{pk_bf16(r[0], r[1]), pk_bf16(r[2], r[3])};
      }
      if (u == 0) st8(dst + 8 * TH, o[0]);
      else st16(dst, u4_t{o[TH].x, o[TH].y, o[1 - TH].x, o[1 - TH].y});
    }
  };
  if (th) epi(IC(1)); else epi(IC(0));
  if (w == 0) STAMP(2, 7);
}

constexpr int D_SLOT = 15360, D_NSLOT = 9, D_STEPS = 18;
constexpr int D_LDS = D_NSLOT * D_SLOT;

template <int NDMA>
__device__ __forceinline__ void d_loader(char* smem, const char* wp, int w4, int lane) {
  const unsigned lds0 = (unsigned)(size_t)(LAS char*)smem;
  const unsigned voff = lane * 16;
  auto issue = [&](int step, unsigned so) {
#pragma unroll
    for (int k = 0; k < NDMA; ++k) {
      const int i = w4 + 4 * k;
      glds16(wp + (size_t)step * D_SLOT + i * 1024, voff, lds0 + so + i * 1024);
    }
  };
  constexpr unsigned S = D_SLOT;
  issue(0, 0); issue(1, S); issue(2, 2 * S);
  wait_vm<2 * NDMA>(); bar();
  issue(3, 3 * S); issue(4, 4 * S); issue(5, 5 * S);
  wait_vm<4 * NDMA>(); bar();
  issue(6, 6 * S); issue(7, 7 * S);
  wait_vm<5 * NDMA>(); bar();
  issue(8, 8 * S); issue(9, 0);
  unsigned sw = S;
#pragma unroll 1
  for (int b = 2; b < 10; ++b) {
    wait_vm<6 * NDMA>();
    bar();
    issue(b + 8, sw);
    sw += S; if (sw == D_NSLOT * S) sw = 0;
  }
  wait_vm<6 * NDMA>(); bar();
  wait_vm<5 * NDMA>(); bar();
  wait_vm<4 * NDMA>(); bar();
  wait_vm<3 * NDMA>(); bar();
  wait_vm<2 * NDMA>(); bar();
  wait_vm<1 * NDMA>(); bar();
  wait_vm<0>(); bar();
  bar();
}

extern "C" __global__ __launch_bounds__(512, 2) void kD(char* __restrict__ ws, u16* __restrict__ out) {
  extern __shared__ __attribute__((aligned(16))) char smem[];
  const int tid = threadIdx.x, lane = tid & 63;
  const int w = __builtin_amdgcn_readfirstlane(tid >> 6);
  const int xcd = blockIdx.x & 7, rank = blockIdx.x >> 3;
  const int it = 8 * (xcd >> 1) + (rank & 7), dg = 4 * (xcd & 1) + (rank >> 3);
  if (w == 0) STAMP(3, 0);
  if (w >= 4) {
    const int w4 = w - 4;
    const char* wp = ws + OFF_WP2 + (size_t)dg * (54 * 5120);
    if (w4 < 3) d_loader<4>(smem, wp, w4, lane);
    else d_loader<3>(smem, wp, w4, lane);
    return;
  }
  const int fr = lane & 15, g = lane >> 4;
  const int t = 64 * it + 16 * w + fr;
  const char* hp = ws + OFF_HH + (size_t)(4 * it + w) * (54 * 1024);
  const unsigned voff = lane * 16;
  u4_t R[18];
#pragma unroll
  for (int i = 0; i < 9; ++i)
    asm volatile("global_load_dwordx4 %0, %1, %2" : "=v"(R[i]) : "v"(voff), "s"(hp + (size_t)i * 1024) : "memory");
  u2_t res[5]; unsigned st;
  f4_t acc[5];
#pragma unroll
  for (int dt = 0; dt < 5; ++dt) acc[dt] = f4_t{0.f, 0.f, 0.f, 0.f};
  b8_t A0[5], A1[5];
  auto rdA = [&](b8_t (&A)[5], unsigned off) {
    const char* sp = smem + off + lane * 16;
#pragma unroll
    for (int dt = 0; dt < 5; ++dt) A[dt] = *(const b8_t*)(sp + dt * 1024);
  };
  bar();
  if (w == 0) STAMP(3, 1);
  rdA(A0, 0);
  unsigned so = 0;
  auto kstep = [&](auto mode, auto idx, b8_t (&Ac)[5], b8_t (&An)[5], const char* hnext) {
    constexpr int MODE = decltype(mode)::value, i = decltype(idx)::value, kk = i % 3;
    if (kk == 0) bar();
    unsigned nx = so + (kk + 1) * 5120;
    if (kk == 2) { so += D_SLOT; if (so == D_NSLOT * D_SLOT) so = 0; nx = so; }
    SB;
    if (!(MODE == 2 && i == 17)) rdA(An, nx);
    constexpr int NV = (MODE == 0) ? (i < 9 ? 8 + i : 26 - i) : (MODE == 1) ? (i < 9 ? 25 - i : 17) : (i == 0 ? 17 : 23 - i);
    asm volatile("s_waitcnt vmcnt(%1)" : "+v"(R[i]) : "i"(NV) : "memory");
    const b8_t B = __builtin_bit_cast(b8_t, R[i]);
#pragma unroll
    for (int dt = 0; dt < 5; ++dt) acc[dt] = __builtin_amdgcn_mfma_f32_16x16x32_bf16(Ac[dt], B, acc[dt], 0, 0, 0);
    SB;
    if (MODE == 0 && i < 9)
      asm volatile("global_load_dwordx4 %0, %1, %2" : "=v"(R[i < 9 ? 9 + i : 0]) : "v"(voff), "s"(hp + (size_t)(9 + i) * 1024) : "memory");
    if (MODE != 2) asm volatile("global_load_dwordx4 %0, %1, %2" : "+v"(R[i]) : "v"(voff), "s"(hnext + (size_t)i * 1024) : "memory");
    if (MODE == 2 && i == 0) {
#pragma unroll
      for (int dt = 0; dt < 5; ++dt) {
        const int d = 80 * dg + 16 * dt + 4 * g;
        const char* rp = ws + OFF_H8 + ((size_t)(d >> 3) * SL + t) * 16 + (d & 4) * 2;
        asm volatile("global_load_dwordx2 %0, %1, off" : "=v"(res[dt]) : "v"(rp) : "memory");
      }
      asm volatile("global_load_dword %0, %1, off" : "=v"(st) : "v"(ws + OFF_G2 + (size_t)t * 8) : "memory");
    }
    SB;
  };
  auto round = [&](auto mode, const char* hnext) {
    kstep(mode, IC(0), A0, A1, hnext); kstep(mode, IC(1), A1, A0, hnext); kstep(mode, IC(2), A0, A1, hnext);
    kstep(mode, IC(3), A1, A0, hnext); kstep(mode, IC(4), A0, A1, hnext); kstep(mode, IC(5), A1, A0, hnext);
    kstep(mode, IC(6), A0, A1, hnext); kstep(mode, IC(7), A1, A0, hnext); kstep(mode, IC(8), A0, A1, hnext);
    kstep(mode, IC(9), A1, A0, hnext); kstep(mode, IC(10), A0, A1, hnext); kstep(mode, IC(11), A1, A0, hnext);
    kstep(mode, IC(12), A0, A1, hnext); kstep(mode, IC(13), A1, A0, hnext); kstep(mode, IC(14), A0, A1, hnext);
    kstep(mode, IC(15), A1, A0, hnext); kstep(mode, IC(16), A0, A1, hnext); kstep(mode, IC(17), A1, A0, hnext);
  };
  round(IC(0), hp + 18 * 1024);
  round(IC(1), hp + 36 * 1024);
  round(IC(2), hp);
  if (w == 0) STAMP(3, 2);
  asm volatile("s_waitcnt vmcnt(0)" : "+v"(res[0]), "+v"(res[1]), "+v"(res[2]), "+v"(res[3]), "+v"(res[4]), "+v"(st)::"memory");
  const float sd = bf_hi(st), r2 = 1.0f / (sd * sd);
#pragma unroll
  for (int dt = 0; dt < 5; ++dt) {
    const int d = 80 * dg + 16 * dt + 4 * g;
    const float h0 = bf_lo(res[dt].x), h1 = bf_hi(res[dt].x), h2 = bf_lo(res[dt].y), h3 = bf_hi(res[dt].y);
    st8(out + (size_t)t * DM + d, u2_t{pk_bf16(h0 + r2 * acc[dt][0], h1 + r2 * acc[dt][1]), pk_bf16(h2 + r2 * acc[dt][2], h3 + r2 * acc[dt][3])});
  }
  if (w == 0) STAMP(3, 7);
}

extern "C" void kernel_launch(void* const* d_in, const int* in_sizes, int n_in, void* d_out, int out_size, void* d_ws,
                              size_t ws_size, hipStream_t stream) {
  char* ws = (char*)d_ws;
  (void)hipFuncSetAttribute((const void*)kA, hipFuncAttributeMaxDynamicSharedMemorySize, A_LDS);
  (void)hipFuncSetAttribute((const void*)kC, hipFuncAttributeMaxDynamicSharedMemorySize, C_LDS);
  (void)hipFuncSetAttribute((const void*)kD, hipFuncAttributeMaxDynamicSharedMemorySize, D_LDS);
  kA<<<256, 1024, A_LDS, stream>>>((const u16*)d_in[0], (const u16*)d_in[1], (const u16*)d_in[2], (const u16*)d_in[3], (const u16*)d_in[4],
                                   (const u16*)d_in[5], (const u16*)d_in[6], (const u16*)d_in[7], (const u16*)d_in[12],
                                   (const u16*)d_in[10], (const u16*)d_in[11], (const u16*)d_in[8], (const u16*)d_out, ws);
  kB<<<256, 512, B_LDS, stream>>>((const u16*)d_in[8], (const u16*)d_in[10], (const u16*)d_in[11], ws);
  kC<<<256, 512, C_LDS, stream>>>((const u16*)d_in[10], (const u16*)d_in[11], ws);
  kD<<<256, 512, D_LDS, stream>>>(ws, (u16*)d_out);
}
```

```cpp
#define CODEPF
#define TLBPF 65536
#define STAMPS
#define PROBE 0
#define PROBE_M 0
#include <hip/hip_runtime.h>
#include <hip/hip_bf16.h>
#include <stdint.h>
#include <type_traits>

typedef unsigned short u16;
typedef _Float16 h2_t __attribute__((ext_vector_type(2)));
typedef _Float16 h8_t __attribute__((ext_vector_type(8)));
typedef __bf16 b2_t __attribute__((ext_vector_type(2)));
typedef __bf16 b8_t __attribute__((ext_vector_type(8)));
typedef float f2_t __attribute__((ext_vector_type(2)));
typedef float f4_t __attribute__((ext_vector_type(4)));
typedef unsigned u2_t __attribute__((ext_vector_type(2)));
typedef unsigned u4_t __attribute__((ext_vector_type(4)));
typedef short s4_t __attribute__((ext_vector_type(4)));
typedef __attribute__((address_space(1))) unsigned long long gu64;
#define LAS __attribute__((address_space(3)))
#define SB __builtin_amdgcn_sched_barrier(0)
#define IC(n) std::integral_constant<int, n>{}

constexpr int SL = 2048;
constexpr int DM = 640;
constexpr int FF = 1728;

constexpr size_t OFF_X4 = 0;
constexpr size_t OFF_S1 = 0x280000;
constexpr size_t OFF_KD = 0x290000;
constexpr size_t OFF_INVS = 0x590000;
constexpr size_t OFF_H8 = 0x5A0000;
constexpr size_t OFF_HH = 0x820000;
constexpr size_t OFF_WP2 = 0xF00000;
constexpr size_t OFF_CS = 0x1120000;
constexpr size_t OFF_G2 = 0x1130000;
constexpr int KRS = 1344;
constexpr int KDS = 4864;
constexpr int KD_O = 2240;
constexpr int KD_DR = 4384;

constexpr size_t OFF_DBG = 0x2000000;
#define STAMP(k, i) do { if ((threadIdx.x & 63) == 0) ((unsigned long long*)(ws + OFF_DBG))[((k) * 512 + blockIdx.x) * 16 + (i)] = __builtin_amdgcn_s_memrealtime(); } while (0)

__device__ __forceinline__ float bf_lo(unsigned d) { return __uint_as_float(d << 16); }
__device__ __forceinline__ float bf_hi(unsigned d) { return __uint_as_float(d & 0xffff0000u); }
__device__ __forceinline__ float bf2f(u16 v) { return __uint_as_float((unsigned)v << 16); }
__device__ __forceinline__ unsigned pk_bf16(float a, float b) {
  f2_t v = {a, b};
  return __builtin_bit_cast(unsigned, __builtin_convertvector(v, b2_t));
}
__device__ __forceinline__ unsigned pk_f16(float a, float b) {
  f2_t v = {a, b};
  return __builtin_bit_cast(unsigned, __builtin_convertvector(v, h2_t));
}
__device__ __forceinline__ float rbf(float a) { return bf_lo(pk_bf16(a, 0.f)); }
template <int N> __device__ __forceinline__ float dpp_ror_add(float v) {
  int r = __builtin_amdgcn_update_dpp(0, __float_as_int(v), 0x120 + N, 0xf, 0xf, false);
  return v + __int_as_float(r);
}
__device__ __forceinline__ float row16_sum(float v) {
  v = dpp_ror_add<8>(v); v = dpp_ror_add<4>(v); v = dpp_ror_add<2>(v); v = dpp_ror_add<1>(v);
  return v;
}
__device__ __forceinline__ float wave_sum(float v) {
  v = row16_sum(v);
  const int b = __float_as_int(v);
  return (__int_as_float(__builtin_amdgcn_readlane(b, 0)) + __int_as_float(__builtin_amdgcn_readlane(b, 16))) +
         (__int_as_float(__builtin_amdgcn_readlane(b, 32)) + __int_as_float(__builtin_amdgcn_readlane(b, 48)));
}
template <typename T> __device__ __forceinline__ T ldnt(const T* p) { return __builtin_nontemporal_load(p); }
__device__ __forceinline__ void wave_fence() {
  __builtin_amdgcn_fence(__ATOMIC_RELEASE, "wavefront");
  __builtin_amdgcn_wave_barrier();
  __builtin_amdgcn_fence(__ATOMIC_ACQUIRE, "wavefront");
}
__device__ __forceinline__ s4_t tr_read(const char* p) {
  return __builtin_amdgcn_ds_read_tr16_b64_v4i16((LAS s4_t*)p);
}
__device__ __forceinline__ void glds16(const void* sbase, unsigned voff, unsigned lds_addr) {
  asm volatile("s_mov_b32 m0, %2\n\ts_nop 0\n\tglobal_load_lds_dwordx4 %0, %1" ::"v"(voff), "s"(sbase), "s"(lds_addr) : "memory");
}
template <int N> __device__ __forceinline__ void wait_vm() { asm volatile("s_waitcnt vmcnt(%0)" ::"i"(N) : "memory"); }
__device__ __forceinline__ void bar() { asm volatile("s_barrier" ::: "memory"); }
__device__ __forceinline__ float dot2bf(unsigned a, unsigned b, float c) {
  return __builtin_amdgcn_fdot2_f32_bf16(__builtin_bit_cast(b2_t, a), __builtin_bit_cast(b2_t, b), c, false);
}

#define ST_MODE 0
__device__ __forceinline__ void st16(void* p, u4_t v) {
  *(u4_t*)p = v;
}
__device__ __forceinline__ void st8(void* p, u2_t v) {
  *(u2_t*)p = v;
}

struct Cw { f2_t w, ws; };
__device__ __forceinline__ Cw mkw(f2_t w) { Cw r; r.w = w; r.ws = f2_t{-w.y, w.x}; return r; }
__device__ __forceinline__ f2_t cmulw(f2_t a, const Cw& W) {
  return __builtin_elementwise_fma(f2_t{a.y, a.y}, W.ws, f2_t{a.x, a.x} * W.w);
}
__device__ __forceinline__ f2_t cexp_l(float er, float wr, float l) {
  float mag = __builtin_amdgcn_exp2f(er * l);
  float rev = __builtin_amdgcn_fractf(wr * l);
  return f2_t{mag * __builtin_amdgcn_cosf(rev), mag * __builtin_amdgcn_sinf(rev)};
}
__device__ __forceinline__ float pow2_scale(float bound, int target) {
  unsigned e = (__float_as_uint(bound) >> 23) & 0xff;
  int k = target - ((int)e - 126);
  k = k > 126 ? 126 : (k < -126 ? -126 : k);
  return (bound > 0.f) ? __uint_as_float((unsigned)(k + 127) << 23) : 1.0f;
}
__device__ __forceinline__ float pow2_inv(float p) { return __uint_as_float(0x7f000000u - __float_as_uint(p)); }

constexpr int A_KBLK = 214;
constexpr int KG_R = 0, KG_L0 = 4352, KG_L1 = 8704, KG_HW = 8704 + 25 * 272;
constexpr int KG_CH = 2 * KG_HW + 16;
constexpr int A_SIDE = 3 * KG_CH;
constexpr int A_LDS = A_SIDE + 20736;

__device__ __forceinline__ void pack_wd(const u16* __restrict__ wd, char* __restrict__ ws, char* buf, int u, int lane) {
  const int ks = u >> 3, dg = u & 7;
#pragma unroll
  for (int i = 0; i < 5; ++i) {
    const int idx = i * 64 + lane, row = idx / 10, c = idx % 10;
    __builtin_amdgcn_global_load_lds((const unsigned*)((const char*)wd + (size_t)(32 * ks + row) * 1280 + 160 * dg + 16 * c),
                                     (LAS unsigned*)(buf + i * 1024), 16, 0, 2);
  }
  asm volatile("s_waitcnt vmcnt(0)" ::: "memory");
  wave_fence();
  const int g = lane >> 4, q = (lane & 15) >> 2, p = lane & 3;
  char* dst = ws + OFF_WP2 + ((size_t)(dg * 54 + ks) * 5) * 1024 + lane * 16;
#pragma unroll
  for (int dt = 0; dt < 5; ++dt) {
    s4_t aa = tr_read(buf + (4 * g + q) * 160 + 32 * dt + 8 * p);
    s4_t bb = tr_read(buf + (16 + 4 * g + q) * 160 + 32 * dt + 8 * p);
    u2_t ua = __builtin_bit_cast(u2_t, aa), ub = __builtin_bit_cast(u2_t, bb);
    st16(dst + dt * 1024, u4_t{ua.x, ua.y, ub.x, ub.y});
  }
}

extern "C" __global__ __launch_bounds__(1024) void kA(const u16* __restrict__ x, const u16* __restrict__ Are, const u16* __restrict__ Aim,
                                           const u16* __restrict__ Bre, const u16* __restrict__ Bim, const u16* __restrict__ Cre,
                                           const u16* __restrict__ Cim, const u16* __restrict__ logdt, const u16* __restrict__ wd,
                                           const u16* __restrict__ wg, const u16* __restrict__ wu, const u16* __restrict__ dskip,
                                           const u16* __restrict__ outp, char* __restrict__ ws) {
  extern __shared__ __attribute__((aligned(16))) char smem[];
  const int tid = threadIdx.x, lane = tid & 63;
  const int w = __builtin_amdgcn_readfirstlane(tid >> 6);
  if (w == 0) STAMP(0, 0);
  if (blockIdx.x >= A_KBLK) {
    const int u = 344 + (blockIdx.x - A_KBLK) * 16 + w;
    if (u < 432) pack_wd(wd, ws, smem + w * 5120, u, lane);
    if (blockIdx.x >= 248 && w == 0) {
      unsigned long long a0, a1;
      asm volatile("s_getpc_b64 s[20:21]\n\ts_add_u32 s20, s20, kA@rel32@lo+4\n\ts_addc_u32 s21, s21, kA@rel32@hi+12\n\ts_mov_b64 %0, s[20:21]" : "=s"(a0) :: "s20", "s21", "scc");
      asm volatile("s_getpc_b64 s[20:21]\n\ts_add_u32 s20, s20, kD@rel32@lo+4\n\ts_addc_u32 s21, s21, kD@rel32@hi+12\n\ts_mov_b64 %0, s[20:21]" : "=s"(a1) :: "s20", "s21", "scc");
      const unsigned long long lo = a0 < a1 ? a0 : a1, n = ((a0 < a1 ? a1 - a0 : a0 - a1) + 8192) >> 7;
      unsigned sink = 0;
      for (unsigned long long i = lane; i < n; i += 64) { unsigned v; asm volatile("global_load_dword %0, %1, off" : "=v"(v) : "v"(lo + i * 128) : "memory"); sink = v; }
      asm volatile("s_waitcnt vmcnt(0)" :: "v"(sink) : "memory");
    }
    if (blockIdx.x >= 248 && w == 1) {
      unsigned v0, v1, v2, v3, v4, v5, v6, v7, v8;
      const char* p;
      constexpr size_t G = TLBPF;
      p = ws + (size_t)min((size_t)lane * G, (size_t)0x1150000 - 4);
      asm volatile("global_load_dword %0, %1, off nt" : "=v"(v0) : "v"(p) : "memory");
      p = ws + (size_t)min((size_t)(lane + 64) * G, (size_t)0x1150000 - 4);
      asm volatile("global_load_dword %0, %1, off nt" : "=v"(v1) : "v"(p) : "memory");
      p = ws + (size_t)min((size_t)(lane + 128) * G, (size_t)0x1150000 - 4);
      asm volatile("global_load_dword %0, %1, off nt" : "=v"(v2) : "v"(p) : "memory");
      p = ws + (size_t)min((size_t)(lane + 192) * G, (size_t)0x1150000 - 4);
      asm volatile("global_load_dword %0, %1, off nt" : "=v"(v3) : "v"(p) : "memory");
      p = ws + (size_t)min((size_t)(lane + 256) * G, (size_t)0x1150000 - 4);
      asm volatile("global_load_dword %0, %1, off nt" : "=v"(v4) : "v"(p) : "memory");
      p = (const char*)wg + min((size_t)lane * G, (size_t)DM * FF * 2 - 4);
      asm volatile("global_load_dword %0, %1, off nt" : "=v"(v5) : "v"(p) : "memory");
      p = (const char*)wu + min((size_t)lane * G, (size_t)DM * FF * 2 - 4);
      asm volatile("global_load_dword %0, %1, off nt" : "=v"(v6) : "v"(p) : "memory");
      p = (const char*)outp + min((size_t)lane * G, (size_t)SL * DM * 2 - 4);
      asm volatile("global_load_dword %0, %1, off nt" : "=v"(v7) : "v"(p) : "memory");
      p = (const char*)dskip;
      asm volatile("global_load_dword %0, %1, off nt" : "=v"(v8) : "v"(p) : "memory");
      asm volatile("s_waitcnt vmcnt(0)" :: "v"(v0), "v"(v1), "v"(v2), "v"(v3), "v"(v4), "v"(v5), "v"(v6), "v"(v7), "v"(v8) : "memory");
    }
    if (w == 0) STAMP(0, 7);
    return;
  }
  if (w >= 12) {
    const int w4 = w - 12;
    if (blockIdx.x >= 128) {
      pack_wd(wd, ws, smem + A_SIDE + w4 * 5120, (blockIdx.x - 128) * 4 + w4, lane);
      bar();
      return;
    }
    const int t0 = blockIdx.x * 16;
    const int r = lane >> 4, c = lane & 15, rl = w4 * 4 + r;
    char* xs = smem + A_SIDE;
    const u4_t* p = (const u4_t*)(x + (size_t)(t0 + rl) * DM);
    u4_t v[5];
#pragma unroll
    for (int i = 0; i < 5; ++i) v[i] = ldnt(p + c + 16 * i);
    if (w4 == 0 && lane < 16) ((unsigned long long*)(ws + OFF_G2))[t0 + lane] = 0ull;
    float s = 0.f;
#pragma unroll
    for (int i = 0; i < 5; ++i)
#pragma unroll
      for (int j = 0; j < 4; ++j) s += bf_lo(v[i][j]) + bf_hi(v[i][j]);
#pragma unroll
    for (int i = 0; i < 5; ++i) *(u4_t*)(xs + rl * 1296 + (c + 16 * i) * 16) = v[i];
    const float mean = row16_sum(s) * (1.0f / DM);
    float q = 0.f;
#pragma unroll
    for (int i = 0; i < 5; ++i)
#pragma unroll
      for (int j = 0; j < 4; ++j) {
        float a = bf_lo(v[i][j]) - mean, b = bf_hi(v[i][j]) - mean;
        q += a * a; q += b * b;
      }
    const float var = row16_sum(q) * (1.0f / DM);
    if (c == 0) {
      float mb = rbf(mean), sd = rbf(sqrtf(rbf(rbf(var) + 1e-5f)));
      ((unsigned*)(ws + OFF_S1))[t0 + rl] = (__float_as_uint(mb) >> 16) | (__float_as_uint(sd) & 0xffff0000u);
    }
    __syncthreads();
    const int t4 = w4 * 64 + lane;
#pragma unroll
    for (int it = 0; it < 5; ++it) {
      const int item = t4 + 256 * it, q4 = item >> 3, tp = item & 7;
      u2_t a = *(const u2_t*)(xs + (2 * tp) * 1296 + 8 * q4);
      u2_t b = *(const u2_t*)(xs + (2 * tp + 1) * 1296 + 8 * q4);
      st16(ws + OFF_X4 + ((size_t)q4 * SL + t0 + 2 * tp) * 8, u4_t{a.x, a.y, b.x, b.y});
    }
    if (w4 == 0) STAMP(0, 6);
    return;
  }
  const int cl = w >> 2, part = (w >> 1) & 1, hw = w & 1;
  const int chr = blockIdx.x * 3 + cl, ch = min(chr, DM - 1);
  char* chb = smem + cl * KG_CH;
  char* hb = chb + hw * KG_HW;
  const int n = ch * 128 + hw * 64 + lane;
  const u16 i_are = ldnt(Are + n), i_aim = ldnt(Aim + n), i_bre = ldnt(Bre + n), i_bim = ldnt(Bim + n), i_cre = ldnt(Cre + n), i_cim = ldnt(Cim + n);
  const float dt = rbf(expf(bf2f(logdt[ch])));
  const float are = dt * bf2f(i_are), aim = dt * bf2f(i_aim);
  const float bre = dt * bf2f(i_bre), bim = dt * bf2f(i_bim);
  const float cre = bf2f(i_cre), cim = bf2f(i_cim);
  f2_t c = {cre * bre - cim * bim, cre * bim + cim * bre};
  const float er = are * 1.44269504088896341f, wr = aim * 0.15915494309189535f;
  const float bnd = (fabsf(c.x) + fabsf(c.y)) * fmaxf(1.0f, __builtin_amdgcn_exp2f(er * 2048.f));
  const float bsum = wave_sum(bnd);
  if (w == 0) STAMP(0, 1);
  const float Sw = pow2_scale(bsum, 15);
  c *= Sw;
  const Cw z1 = mkw(cexp_l(er, wr, 1.f)), w128 = mkw(cexp_l(er, wr, 128.f));
  auto put = [&](char* buf, int row, f2_t v) { *(unsigned*)(buf + row * 272 + 4 * lane) = pk_f16(v.x, -v.y); };
  if (part == 0) {
    const Cw w8 = mkw(cexp_l(er, wr, 8.f));
    f2_t R = w8.w, v0 = c, v1 = cmulw(c, w128);
    *(unsigned*)(hb + KG_R + 4 * lane) = pk_f16(1.f, 0.f);
#pragma unroll
    for (int i = 0; i < 8; ++i) {
      *(unsigned*)(hb + KG_R + (2 * i + 1) * 272 + 4 * lane) = pk_f16(R.x, R.y);
      if (i < 7) {
        R = cmulw(R, w8);
        *(unsigned*)(hb + KG_R + (2 * i + 2) * 272 + 4 * lane) = pk_f16(R.x, R.y);
        R = cmulw(R, w8);
      }
      put(hb + KG_L0, i, v0); put(hb + KG_L0, 8 + i, v1);
      if (i < 7) { v0 = cmulw(v0, z1); v1 = cmulw(v1, z1); }
    }
  } else {
    const Cw z2 = mkw(cmulw(z1.w, z1));
    const Cw z4 = mkw(cmulw(z2.w, z2));
    f2_t T = cmulw(c, w128);
#pragma unroll
    for (int i1 = 2; i1 < 17; ++i1) {
      T = cmulw(T, w128);
      if (i1 < 4) {
        f2_t v = T;
#pragma unroll
        for (int i0 = 0; i0 < 4; ++i0) { put(hb + KG_L1, 4 * (i1 - 2) + i0, v); if (i0 < 3) v = cmulw(v, z2); }
      } else if (i1 < 8) {
        put(hb + KG_L1, 8 + 2 * (i1 - 4), T); put(hb + KG_L1, 8 + 2 * (i1 - 4) + 1, cmulw(T, z4));
      } else {
        put(hb + KG_L1, 16 + (i1 - 8), T);
      }
    }
  }
  if (part == 0 && lane == 0) ((float*)(chb + 2 * KG_HW))[hw] = bsum;
  __syncthreads();
  if (w == 0) STAMP(0, 2);
  if (part == 1 && hw == 1) return;
  const int tile = part ? 1 : 2 * hw;
  const int fr = lane & 15, kg = lane >> 4;
  const int row = (tile == 2) ? 16 + min(fr, 8) : fr;
  const int lo = (tile == 0) ? KG_L0 : KG_L1;
  f4_t acc[2];
#pragma unroll
  for (int h = 0; h < 2; ++h) {
    const char* hbb = chb + h * KG_HW;
    f4_t a = {0.f, 0.f, 0.f, 0.f};
#pragma unroll
    for (int ks = 0; ks < 4; ++ks) {
      const h8_t Bf = *(const h8_t*)(hbb + KG_R + fr * 272 + 64 * ks + 16 * kg);
      const h8_t Af = *(const h8_t*)(hbb + lo + row * 272 + 64 * ks + 16 * kg);
      a = __builtin_amdgcn_mfma_f32_16x16x32_f16(Af, Bf, a, 0, 0, 0);
    }
    acc[h] = a;
  }
  const float b0 = ((const float*)(chb + 2 * KG_HW))[0], b1 = ((const float*)(chb + 2 * KG_HW))[1];
  const float Sc = pow2_scale(b0 + b1, 15);
  const f4_t kv = acc[0] * (pow2_inv(pow2_scale(b0, 15)) * Sc) + acc[1] * (pow2_inv(pow2_scale(b1, 15)) * Sc);
  const unsigned p01 = pk_f16(kv[0], kv[1]), p23 = pk_f16(kv[2], kv[3]);
  char* kr = ws + OFF_KD + (size_t)ch * KRS;
  if (tile == 0) {
    *(u2_t*)(kr + 2 * (128 * (kg >> 1) + 4 * (kg & 1) + 8 * fr)) = u2_t{p01, p23};
    if (lane == 0) ((float*)(ws + OFF_INVS))[ch] = pow2_inv(Sc);
  } else if (tile == 1) {
    if (kg < 2) {
      *(u2_t*)(kr + 2 * (256 + 64 * kg + 4 * fr)) = u2_t{p01, p23};
    } else {
      *(unsigned*)(kr + 2 * (384 + 64 * (kg - 2) + 2 * fr)) = p01;
      *(unsigned*)(kr + 2 * (384 + 64 * (kg - 2) + 32 + 2 * fr)) = p23;
    }
  } else {
    u16* k3 = (u16*)kr + 512 + 64 * kg + fr;
    if (kg < 2) { k3[0] = (u16)p01; k3[16] = (u16)(p01 >> 16); k3[32] = (u16)p23; k3[48] = (u16)(p23 >> 16); }
    else if (lane == 32) k3[0] = (u16)p01;
  }
  if (w == 0) STAMP(0, 7);
}

constexpr int B_CONV = 160;
constexpr int B_KOFF = 0;
constexpr int B_UOFF = 4 * KDS;
constexpr int B_UCH = 4608;
constexpr int B_YCH = 10240;
constexpr int B_LDS = 40960;
#define B_DELAY 0

struct BFrag { h8_t B[8]; u2_t A[5][2]; };
__device__ __forceinline__ void mfma_f16(f4_t& d, u4_t a, h8_t b) {
  asm volatile("v_mfma_f32_16x16x32_f16 %0, %1, %2, %0" : "+v"(d) : "v"(a), "v"(b));
}
__device__ __forceinline__ void mfma_f16_0(f4_t& d, u4_t a, h8_t b) {
  asm volatile("v_mfma_f32_16x16x32_f16 %0, %1, %2, 0" : "=&v"(d) : "v"(a), "v"(b));
}

extern "C" __global__ __launch_bounds__(512) void kB(const u16* __restrict__ dskip, const u16* __restrict__ wg, const u16* __restrict__ wu,
                                          char* __restrict__ ws) {
  extern __shared__ __attribute__((aligned(16))) char smem[];
  const int tid = threadIdx.x, lane = tid & 63;
  const int w = __builtin_amdgcn_readfirstlane(tid >> 6);
  if (w == 0) STAMP(1, 0);
  if (blockIdx.x >= B_CONV) {
    const int xcd = blockIdx.x & 7, rk = (blockIdx.x - B_CONV) >> 3;
    const int g4 = xcd >> 1, a = xcd & 1, nl = (g4 < 3) ? 7 : 6;
    if (rk >= nl) return;
#pragma unroll
    for (int i = 0; i < B_DELAY; ++i) __builtin_amdgcn_s_sleep(16);
    const int jl = 7 * g4 + rk;
    const char* pm = (const char*)(a ? wu : wg) + (size_t)(80 * w + (lane >> 3)) * (FF * 2) + 128 * jl + (lane & 7) * 16;
    u4_t v[10];
#pragma unroll
    for (int i = 0; i < 10; ++i) v[i] = ldnt((const u4_t*)(pm + (size_t)i * 8 * (FF * 2)));
    const char* po = (const char*)(a ? wg : wu) + (size_t)(80 * w) * (FF * 2) + 128 * jl;
    unsigned t0v, t1v;
    asm volatile("global_load_dword %0, %1, off nt" : "=v"(t0v) : "v"(po + (size_t)lane * (FF * 2)) : "memory");
    asm volatile("global_load_dword %0, %1, off nt" : "=v"(t1v) : "v"(po + (size_t)(64 + (lane & 15)) * (FF * 2)) : "memory");
    f2_t s[4];
#pragma unroll
    for (int e = 0; e < 4; ++e) s[e] = f2_t{0.f, 0.f};
#pragma unroll
    for (int i = 0; i < 10; ++i)
#pragma unroll
      for (int jj = 0; jj < 4; ++jj) s[jj] += f2_t{bf_lo(v[i][jj]), bf_hi(v[i][jj])};
    float* pp = (float*)smem;
    *(f4_t*)(pp + (w * 8 + (lane >> 3)) * 68 + (lane & 7) * 8) = f4_t{s[0].x, s[0].y, s[1].x, s[1].y};
    *(f4_t*)(pp + (w * 8 + (lane >> 3)) * 68 + (lane & 7) * 8 + 4) = f4_t{s[2].x, s[2].y, s[3].x, s[3].y};
    __syncthreads();
    float acc = 0.f;
#pragma unroll
    for (int i = 0; i < 8; ++i) acc += pp[(w * 8 + i) * 68 + lane];
    __syncthreads();
    pp[w * 64 + lane] = acc;
    __syncthreads();
    if (w == 0) {
      float t = 0.f;
#pragma unroll
      for (int i = 0; i < 8; ++i) t += pp[i * 64 + lane];
      ((float*)(ws + OFF_CS))[a * FF + 64 * jl + lane] = t;
    }
    asm volatile("s_waitcnt vmcnt(0)" ::"v"(t0v), "v"(t1v) : "memory");
    if (w == 0) STAMP(1, 7);
    return;
  }
  const int a_ = blockIdx.x >> 3, x_ = blockIdx.x & 7;
  const int q = 16 * (a_ >> 1) + 2 * x_ + (a_ & 1);
  u4_t xv[2]; u2_t sv[2];
#pragma unroll
  for (int it = 0; it < 2; ++it) {
    const int j = tid + 512 * it;
    xv[it] = *(const u4_t*)(ws + OFF_X4 + ((size_t)q * SL + 2 * j) * 8);
    sv[it] = *(const u2_t*)(ws + OFF_S1 + 8 * j);
  }
  const int kch = tid >> 7, kc = tid & 127;
  const char* kr = ws + OFF_KD + (size_t)(4 * q + kch) * KRS;
  const int e0 = (kc < 32) ? 8 * kc : (kc < 64) ? 256 + 4 * (kc - 32) : 384 + 2 * (kc - 64);
  const u4_t kd4 = *(const u4_t*)(kr + 2 * e0);
  const unsigned kd5 = *(const unsigned*)(kr + 2 * e0 + 16);
  const unsigned kfirst = *(const u16*)kr, k1024 = *(const u16*)(kr + 2 * 512);
  const int dl = 127 - kc;
  const unsigned kfa = *(const u16*)(kr + 2 * (512 + max(2 * dl - 127, 0))), kfb = *(const u16*)(kr + 2 * (512 + max(2 * dl - 126, 0)));
  float dk[4];
#pragma unroll
  for (int c = 0; c < 4; ++c) dk[c] = bf2f(dskip[4 * q + c]);
  const float invs = ((const float*)(ws + OFF_INVS))[4 * q + (w & 3)];
  *(unsigned*)(smem + B_UOFF + (tid >> 7) * B_UCH + 4096 + 4 * (tid & 127)) = 0u;
  const int c = lane & 15, kg = lane >> 4;
  h8_t band9, band7;
#pragma unroll
  for (int j = 0; j < 8; ++j) {
    const int k = 8 * kg + 7 - j;
    band9[j] = (k >= c + 4 && k <= c + 12) ? (_Float16)1.0f : (_Float16)0.0f;
    band7[j] = (k >= c + 5 && k <= c + 11) ? (_Float16)1.0f : (_Float16)0.0f;
  }
  {
    char* ki = smem + B_KOFF + kch * KDS;
    const unsigned k0 = kd4[0] & 0xffffu;
    unsigned o0, o1, o2, o3, nx;
    if (kc < 32) { o0 = kd4[0]; o1 = kd4[1]; o2 = kd4[2]; o3 = kd4[3]; nx = kd5; }
    else if (kc < 64) {
      const unsigned k2 = kd4[0] >> 16, k4 = kd4[1] & 0xffffu, k6 = kd4[1] >> 16, k8 = kd4[2] & 0xffffu;
      o0 = k0 * 0x10001u; o1 = k2 | (k4 << 16); o2 = k4 * 0x10001u; o3 = k6 | (k8 << 16); nx = k8;
    } else {
      const unsigned k4 = kd4[0] >> 16, k8 = kd4[1] & 0xffffu;
      o0 = k0 * 0x10001u; o1 = k0 | (k4 << 16); o2 = k4 * 0x10001u; o3 = k8 * 0x10001u; nx = k8;
    }
    *(u4_t*)(ki + 16 * (kc + 2)) = u4_t{o0, o1, o2, o3};
    *(u4_t*)(ki + KD_O + 16 * (kc + 2)) = u4_t{__builtin_amdgcn_alignbit(o1, o0, 16), __builtin_amdgcn_alignbit(o2, o1, 16),
                                              __builtin_amdgcn_alignbit(o3, o2, 16), __builtin_amdgcn_alignbit(nx, o3, 16)};
    if (kc < 6) {
      const int cp = (kc < 2) ? kc : 128 + kc;
      const unsigned kk = k1024 * 0x10001u;
      const u4_t e = (kc == 2) ? u4_t{kk, kk, k1024, 0u} : u4_t{0u, 0u, 0u, 0u};
      const u4_t o = (kc == 2) ? u4_t{kk, kk, 0u, 0u} : u4_t{0u, 0u, 0u, (kc == 1) ? (kfirst << 16) : 0u};
      *(u4_t*)(ki + 16 * cp) = e;
      *(u4_t*)(ki + KD_O + 16 * cp) = o;
    }
    if (kc < 80) *(unsigned*)(ki + KD_DR + 4 * kc) = (dl >= 64) ? (kfa | (kfb << 16)) : 0u;
  }
  asm volatile("" : "+v"(band9), "+v"(band7));
  float z[2][8];
  if (w == 0) { STAMP(1, 1); }
#pragma unroll
  for (int it = 0; it < 2; ++it) {
    const int j = tid + 512 * it;
    const float m0 = bf_lo(sv[it].x), r0 = __builtin_amdgcn_rcpf(bf_hi(sv[it].x)), m1 = bf_lo(sv[it].y), r1 = __builtin_amdgcn_rcpf(bf_hi(sv[it].y));
    float x0[4] = {bf_lo(xv[it].x), bf_hi(xv[it].x), bf_lo(xv[it].y), bf_hi(xv[it].y)};
    float x1[4] = {bf_lo(xv[it].z), bf_hi(xv[it].z), bf_lo(xv[it].w), bf_hi(xv[it].w)};
#pragma unroll
    for (int cc = 0; cc < 4; ++cc) {
      const float u0 = (x0[cc] - m0) * r0, u1 = (x1[cc] - m1) * r1;
      *(unsigned*)(smem + B_UOFF + cc * B_UCH + 4 * (1023 - j)) = pk_f16(u1, u0);
      z[it][cc] = x0[cc] + u0 * dk[cc];
      z[it][4 + cc] = x1[cc] + u1 * dk[cc];
    }
  }
  __syncthreads();
  if (w == 0) STAMP(1, 2);
  f4_t acc[8];
  if (w < 4) {
    const char* ub = smem + B_UOFF + w * B_UCH + 4112 - 32 * c - 16 * kg - 512 * 7;
    const char* kb = smem + B_KOFF + w * KDS + ((c & 1) ? 0 : KD_O) + 4 * (12 + ((c + 1) >> 1) - 4 * kg);
    const unsigned* dr = (const unsigned*)(smem + B_KOFF + w * KDS + KD_DR) + (127 - c + 4 * kg);
    const char* uw = smem + B_UOFF + w * B_UCH + 4112 - 32 * c - 16 * kg;
    auto rdB = [&](BFrag& F, int i0, int p) { F.B[p] = *(const h8_t*)(ub + 512 * (7 - p) + 64 * i0); };
    auto rdA = [&](BFrag& F, int i0, int i1, int half) {
      const unsigned* ap = (const unsigned*)(kb + 64 * i0 + 512 * i1) + 2 * half;
      F.A[i1][half] = u2_t{ap[0], ap[1]};
    };
    auto frag = [&](const u2_t (&a)[2]) { return u4_t{a[0].x, a[0].y, a[1].x, a[1].y}; };
    auto rdnext = [&](BFrag& F, int i0, int m) {
      if (m < 8) rdB(F, i0, m); else if (m < 16) rdA(F, i0, (m - 8) >> 1, (m - 8) & 1);
    };
    auto step = [&](auto i0c, const BFrag& F, BFrag& N, auto nextc) {
      constexpr int I0 = decltype(i0c)::value, NX = decltype(nextc)::value;
      int m = 0;
#pragma unroll
      for (int i1 = 0; i1 < 5; ++i1) {
        if (i1 == 4 && I0 != 0) continue;
        const u4_t a = frag(F.A[i1]);
#pragma unroll
        for (int t = i1; t < 8; ++t) {
          if (I0 == 0 && i1 == 0) mfma_f16_0(acc[t], a, F.B[t]); else mfma_f16(acc[t], a, F.B[t - i1]);
          if (NX >= 0) rdnext(N, NX, m);
          SB;
          ++m;
        }
      }
    };
    BFrag F0, F1;
    h8_t U9[4], U7[4];
#pragma unroll
    for (int bt = 0; bt < 4; ++bt) { U9[bt] = *(const h8_t*)(uw - 512 * bt + 16); U7[bt] = *(const h8_t*)(uw - 512 * bt); }
#pragma unroll
    for (int m = 0; m < 16; ++m) rdnext(F0, 0, m);
    rdA(F0, 0, 4, 0); rdA(F0, 0, 4, 1);
    SB;
    f4_t X9[4], X7[4];
#pragma unroll
    for (int bt = 0; bt < 4; ++bt) { mfma_f16_0(X9[bt], __builtin_bit_cast(u4_t, U9[bt]), band9); mfma_f16_0(X7[bt], __builtin_bit_cast(u4_t, U7[bt]), band7); }
    SB;
    step(IC(0), F0, F1, IC(1));
    h8_t Vf[4]; u4_t Tf[4];
    {
      int m = 0;
#pragma unroll
      for (int i1 = 0; i1 < 4; ++i1) {
        const u4_t a = frag(F1.A[i1]);
#pragma unroll
        for (int t = i1; t < 8; ++t) {
          mfma_f16(acc[t], a, F1.B[t - i1]);
          rdnext(F0, 2, m);
          if (m < 4) {
            const u4_t vu = {pk_f16(X7[m][0], X9[m][0]), pk_f16(X7[m][1], X9[m][1]), pk_f16(X7[m][2], X9[m][2]), pk_f16(X7[m][3], X9[m][3])};
            Vf[m] = __builtin_bit_cast(h8_t, vu);
          }
          if (m >= 16 && m < 20) { const unsigned* tp = dr - 16 * (m - 16 + 4); Tf[m - 16] = u4_t{tp[0], tp[1], tp[2], tp[3]}; }
          SB;
          ++m;
        }
      }
    }
    step(IC(2), F0, F1, IC(3));
#pragma unroll
    for (int bt = 0; bt < 4; ++bt)
#pragma unroll
      for (int at = 4 + bt; at < 8; ++at) mfma_f16(acc[at], __builtin_bit_cast(u4_t, Vf[bt]), __builtin_bit_cast(h8_t, Tf[at - bt - 4]));
    SB;
    step(IC(3), F1, F0, IC(4));
    step(IC(4), F0, F1, IC(5));
    step(IC(5), F1, F0, IC(6));
    step(IC(6), F0, F1, IC(7));
    step(IC(7), F1, F0, IC(-1));
    asm volatile("s_nop 7\n\ts_nop 1" : "+v"(acc[0]), "+v"(acc[1]), "+v"(acc[2]), "+v"(acc[3]), "+v"(acc[4]), "+v"(acc[5]), "+v"(acc[6]), "+v"(acc[7]));
  }
  if (w == 0) STAMP(1, 3);
  __syncthreads();
  if (w < 4) {
#pragma unroll
    for (int t = 0; t < 8; ++t) *(f4_t*)(smem + w * B_YCH + t * 1280 + kg * 320 + c * 16) = acc[t] * invs;
  }
  __syncthreads();
#pragma unroll
  for (int it = 0; it < 2; ++it) {
    const int j = tid + 512 * it, t = 2 * j;
    const int yo = (t >> 8) * 1280 + ((t >> 2) & 3) * 320 + ((t >> 4) & 15) * 16 + (t & 3) * 4;
    float h0[4], h1[4];
#pragma unroll
    for (int cc = 0; cc < 4; ++cc) {
      f2_t y = *(const f2_t*)(smem + cc * B_YCH + yo);
      h0[cc] = z[it][cc] + y.x;
      h1[cc] = z[it][4 + cc] + y.y;
    }
    char* dst = ws + OFF_H8 + ((size_t)(q >> 1) * SL + t) * 16 + (q & 1) * 8;
    st8(dst, u2_t{pk_bf16(h0[0], h0[1]), pk_bf16(h0[2], h0[3])});
    st8(dst + 16, u2_t{pk_bf16(h1[0], h1[1]), pk_bf16(h1[2], h1[3])});
  }
  if (w == 0) STAMP(1, 7);
}

constexpr int C_T = 240;
constexpr int C_SLOT = 23552, C_NSLOT = 6, C_GOFF = 15360;
constexpr int C_STAT = C_NSLOT * C_SLOT;
constexpr int C_CS = C_STAT + 960;
constexpr int C_LDS = C_CS + 512;
constexpr int C_STEPS = 20;
constexpr size_t OFF_DUMMY = 0x1140000;

__device__ __forceinline__ unsigned ln_pack(float sum, float sq) {
  const float mean = sum * (1.0f / DM);
  const float var = fmaxf(sq * (1.0f / DM) - mean * mean, 0.f);
  const float mb = rbf(mean), sd = rbf(sqrtf(rbf(rbf(var) + 1e-5f)));
  return (__float_as_uint(mb) >> 16) | (__float_as_uint(sd) & 0xffff0000u);
}
__device__ __forceinline__ unsigned ln_row(const char* h8, int t) {
  float s = 0.f, q = 0.f;
  for (int pl = 0; pl < 80; ++pl) {
    const u4_t v = *(const u4_t*)(h8 + (size_t)pl * 32768 + t * 16);
    for (int j = 0; j < 4; ++j) { const float a = bf_lo(v[j]), b = bf_hi(v[j]); s += a + b; q += a * a + b * b; }
  }
  return ln_pack(s, q);
}

template <int NDMA>
__device__ __forceinline__ void c_loader(char* smem, char* ws, const char* wbase, int w4, int lane, int t0, int jline, int xcd) {
  const unsigned lds0 = (unsigned)(size_t)(LAS char*)smem;
  const char* h8 = ws + OFF_H8;
  unsigned voffH[4], voffW[2];
#pragma unroll
  for (int k = 0; k < 2; ++k) {
    const int r = 8 * (2 * (w4 & 1) + k) + (lane >> 3), c = lane & 7;
    const int f = ((r >> 1) & 1) | ((r >> 2) & 2);
    voffW[k] = r * (FF * 2) + 128 * jline + (((c >> 1) ^ f) * 32) + (c & 1) * 16;
  }
#pragma unroll
  for (int k = 0; k < 4; ++k) {
    const int idx = 64 * (w4 + 4 * k) + lane, pl = idx / C_T, tt = idx % C_T;
    const int t = min(t0 + tt, SL - 1);
    voffH[k] = pl * 32768 + t * 16;
  }
  const unsigned ldsW = C_GOFF + (w4 >> 1) * 4096 + (w4 & 1) * 2048;
  auto issue = [&](int step, unsigned so) {
#pragma unroll
    for (int k = 0; k < 2; ++k) glds16(wbase + (size_t)step * (32 * FF * 2), voffW[k], lds0 + so + ldsW + k * 1024);
#pragma unroll
    for (int k = 0; k < NDMA - 2; ++k) glds16(h8 + (size_t)step * (4 * 32768), voffH[k], lds0 + so + (w4 + 4 * k) * 1024);
  };
  constexpr unsigned S = C_SLOT;
  issue(0, 0); issue(1, S); issue(2, 2 * S); issue(3, 3 * S);
  wait_vm<3 * NDMA>(); bar();
  issue(4, 4 * S);
  wait_vm<2 * NDMA>(); bar();
  issue(5, 5 * S); issue(6, 0);
  unsigned sw = S;
#pragma unroll 1
  for (int j = 1; j < 7; ++j) {
    wait_vm<2 * NDMA>();
    bar();
    issue(2 * j + 5, sw);
    sw += S; if (sw == C_NSLOT * S) sw = 0;
    issue(2 * j + 6, sw);
    sw += S; if (sw == C_NSLOT * S) sw = 0;
  }
  wait_vm<2 * NDMA>(); bar();
  issue(19, sw);
  const int tl = min(60 * w4 + lane, C_T - 1), tg = min(t0 + tl, SL - 1);
  const gu64* gp = (const gu64*)(ws + OFF_G2) + tg;
  const float* csp = (const float*)(ws + OFF_CS) + ((lane >> 4) & 1) * FF + 64 * jline + 16 * w4 + (lane & 15);
  const char* tb = ws + OFF_WP2 + (size_t)(4 * (xcd & 1)) * (54 * 5120);
  const int tl0 = ((int)(blockIdx.x >> 3) * 4 + w4) * 68;
  u2_t gv; float csv; unsigned tv0, tv1;
  asm volatile("global_load_dwordx2 %0, %1, off sc1" : "=v"(gv) : "v"(gp) : "memory");
  asm volatile("global_load_dword %0, %1, off" : "=v"(csv) : "v"(csp) : "memory");
  asm volatile("global_load_dword %0, %1, off" : "=v"(tv0) : "v"(tb + (size_t)min(tl0 + lane, 8639) * 128) : "memory");
  asm volatile("global_load_dword %0, %1, off" : "=v"(tv1) : "v"(tb + (size_t)min(tl0 + 64 + (lane & 3), 8639) * 128) : "memory");
  wait_vm<NDMA + 4>(); bar();
  asm volatile("s_waitcnt vmcnt(0)" : "+v"(gv), "+v"(csv), "+v"(tv0), "+v"(tv1)::"memory");
  bar();
  {
    bool ok = gv.y == 1u;
    for (int spin = 0; spin < 4096 && !__all(ok); ++spin) {
      const unsigned long long g = __hip_atomic_load(gp, __ATOMIC_RELAXED, __HIP_MEMORY_SCOPE_AGENT);
      gv.x = (unsigned)g; ok = (unsigned)(g >> 32) == 1u;
      __builtin_amdgcn_s_sleep(2);
    }
    if (!__all(ok)) gv.x = ln_row(h8, tg);
    if (lane < 60) *(float*)(smem + C_STAT + (60 * w4 + lane) * 4) = bf_lo(gv.x);
    if (lane < 32) *(float*)(smem + C_CS + (lane >> 4) * 256 + (16 * w4 + (lane & 15)) * 4) = csv;
  }
  asm volatile("s_waitcnt lgkmcnt(0)" ::: "memory");
  bar();
}

struct CFrag { b8_t A[2][2]; b8_t B[8]; };

extern "C" __global__ __launch_bounds__(512, 2) void kC(const u16* __restrict__ wg, const u16* __restrict__ wu, char* __restrict__ ws) {
  extern __shared__ __attribute__((aligned(16))) char smem[];
  const int tid = threadIdx.x, lane = tid & 63;
  const int w = __builtin_amdgcn_readfirstlane(tid >> 6);
  int panel, jline;
  const int xcd = blockIdx.x & 7, rank = blockIdx.x >> 3;
  {
    const int g4 = xcd >> 1, a = xcd & 1, nl = (g4 < 3) ? 7 : 6, total = 9 * nl, n0 = (total + 1) >> 1;
    if (a == 0 ? rank >= n0 : rank >= total - n0) {
      const int sp = (xcd < 6) ? (xcd >> 1) : 3 + 5 * (xcd - 6) + (rank - 27);
      const int wv = sp * 8 + w;
      if (w == 0) STAMP(2, 0);
      for (int tile = wv; tile < 128; tile += 104) {
        const int t = 16 * tile + (lane & 15);
        const char* hp = ws + OFF_H8 + (size_t)(lane >> 4) * 32768 + t * 16;
        u4_t v[20];
#pragma unroll
        for (int i = 0; i < 20; ++i) v[i] = *(const u4_t*)(hp + (size_t)i * (4 * 32768));
        float s0 = 0.f, s1 = 0.f, q0 = 0.f, q1 = 0.f;
        unsigned k_both = 0x3f803f80u;
        asm volatile("" : "+s"(k_both));
#pragma unroll
        for (int i = 0; i < 20; ++i) {
          s0 = dot2bf(v[i][0], k_both, s0); q0 = dot2bf(v[i][0], v[i][0], q0);
          s1 = dot2bf(v[i][1], k_both, s1); q1 = dot2bf(v[i][1], v[i][1], q1);
          s0 = dot2bf(v[i][2], k_both, s0); q0 = dot2bf(v[i][2], v[i][2], q0);
          s1 = dot2bf(v[i][3], k_both, s1); q1 = dot2bf(v[i][3], v[i][3], q1);
        }
        float s = s0 + s1, q = q0 + q1;
        s += __shfl_xor(s, 16); q += __shfl_xor(q, 16);
        s += __shfl_xor(s, 32); q += __shfl_xor(q, 32);
        if (lane < 16)
          __hip_atomic_store((gu64*)(ws + OFF_G2) + t, (1ull << 32) | ln_pack(s, q), __ATOMIC_RELAXED, __HIP_MEMORY_SCOPE_AGENT);
      }
      if (w == 0) STAMP(2, 7);
      return;
    }
    const int idx = rank + a * n0;
    panel = idx / nl; jline = 7 * g4 + idx % nl;
  }
  const int t0 = panel * C_T;
  if (w == 0) STAMP(2, 0);
  if (w >= 4) {
    const int w4 = w - 4;
    const char* wbase = (const char*)((w4 >> 1) ? wu : wg);
    if (w4 < 3) c_loader<6>(smem, ws, wbase, w4, lane, t0, jline, xcd);
    else c_loader<5>(smem, ws, wbase, w4, lane, t0, jline, xcd);
    return;
  }
  const int s = w >> 1, th = w & 1;
  const int fr = lane & 15, kg = lane >> 4, q = (lane & 15) >> 2, p = lane & 3;
  const unsigned hb0 = kg * 3840 + fr * 16 + 256 * 7;
  const unsigned hb1 = kg * 3840 + fr * 16 + 256 * (th ? 8 : 0);
  unsigned wa[2];
#pragma unroll
  for (int v = 0; v < 2; ++v) {
    const int sub = 2 * s + (v ? 1 - th : th);
    const int f = (q >> 1) | (2 * (kg & 1));
    wa[v] = C_GOFF + (8 * kg + q) * 128 + ((sub ^ f) * 32) + 8 * p;
  }
  f4_t ag[8][2], au[8][2];
#pragma unroll
  for (int u = 0; u < 8; ++u)
#pragma unroll
    for (int v = 0; v < 2; ++v) { ag[u][v] = f4_t{0.f, 0.f, 0.f, 0.f}; au[u][v] = f4_t{0.f, 0.f, 0.f, 0.f}; }
  auto load = [&](CFrag& F, unsigned so) {
    const char* sp = smem + so;
#pragma unroll
    for (int v = 0; v < 2; ++v)
#pragma unroll
      for (int m = 0; m < 2; ++m) {
        s4_t lo = tr_read(sp + wa[v] + m * 4096), hi = tr_read(sp + wa[v] + m * 4096 + 512);
        u2_t a = __builtin_bit_cast(u2_t, lo), b = __builtin_bit_cast(u2_t, hi);
        F.A[v][m] = __builtin_bit_cast(b8_t, (u4_t{a.x, a.y, b.x, b.y}));
      }
    F.B[0] = *(const b8_t*)(sp + hb0);
#pragma unroll
    for (int u = 1; u < 8; ++u) F.B[u] = *(const b8_t*)(sp + hb1 + 256 * (u - 1));
  };
  auto mma = [&](const CFrag& F) {
    ag[0][0] = __builtin_amdgcn_mfma_f32_16x16x32_bf16(F.A[0][0], F.B[0], ag[0][0], 0, 0, 0);
    au[0][0] = __builtin_amdgcn_mfma_f32_16x16x32_bf16(F.A[0][1], F.B[0], au[0][0], 0, 0, 0);
#pragma unroll
    for (int u = 1; u < 8; ++u)
#pragma unroll
      for (int v = 0; v < 2; ++v) {
        ag[u][v] = __builtin_amdgcn_mfma_f32_16x16x32_bf16(F.A[v][0], F.B[u], ag[u][v], 0, 0, 0);
        au[u][v] = __builtin_amdgcn_mfma_f32_16x16x32_bf16(F.A[v][1], F.B[u], au[u][v], 0, 0, 0);
      }
  };
  auto interleave = [&]() {
#pragma unroll
    for (int i = 0; i < 16; ++i) {
      __builtin_amdgcn_sched_group_barrier(0x008, 1, 0);
      __builtin_amdgcn_sched_group_barrier(0x100, 1, 0);
    }
    __builtin_amdgcn_sched_group_barrier(0x008, 14, 0);
  };
  CFrag F0, F1;
  bar();
  if (w == 0) STAMP(2, 1);
  load(F0, 0);
  unsigned sn = C_SLOT;
  SB;
#pragma unroll 1
  for (int j = 0; j < 9; ++j) {
    asm volatile("s_waitcnt lgkmcnt(0)" ::: "memory");
    bar(); SB;
    load(F1, sn);
    mma(F0);
    interleave();
    SB;
    sn += C_SLOT; if (sn == C_NSLOT * C_SLOT) sn = 0;
    load(F0, sn);
    mma(F1);
    interleave();
    SB;
    sn += C_SLOT; if (sn == C_NSLOT * C_SLOT) sn = 0;
  }
  asm volatile("s_waitcnt lgkmcnt(0)" ::: "memory");
  bar(); SB;
  load(F1, sn);
  mma(F0);
  interleave();
  SB;
  bar(); SB;
  f4_t csg[2], csu[2];
  float mu[8];
#pragma unroll
  for (int v = 0; v < 2; ++v) {
    const int sub = 2 * s + (v ? 1 - th : th);
    csg[v] = *(const f4_t*)(smem + C_CS + (16 * sub + 4 * kg) * 4);
    csu[v] = *(const f4_t*)(smem + C_CS + 256 + (16 * sub + 4 * kg) * 4);
  }
#pragma unroll
  for (int u = 0; u < 8; ++u) {
    const int tile = (u == 0) ? 7 : (th ? 7 + u : u - 1);
    mu[u] = *(const float*)(smem + C_STAT + (16 * tile + fr) * 4);
  }
  mma(F1);
  SB;
  if (w == 0) STAMP(2, 2);
  char* hh = ws + OFF_HH + (size_t)(2 * jline + s) * 1024 + lane * 16;
  char* sink = ws + OFF_DUMMY + lane * 16;
  auto epi = [&](auto thc) {
    constexpr int TH = decltype(thc)::value;
#pragma unroll
    for (int u = 0; u < 8; ++u) {
      const int tile = (u == 0) ? 7 : (TH ? 7 + u : u - 1);
      const int t16 = 15 * panel + tile;
      char* dst = (t16 < 128) ? hh + (size_t)t16 * (54 * 1024) : sink;
      u2_t o[2];
#pragma unroll
      for (int v = 0; v < 2; ++v) {
        if (u == 0 && v == 1) continue;
        const f4_t r = (ag[u][v] - mu[u] * csg[v]) * (au[u][v] - mu[u] * csu[v]);
        o[v] = u2_t{pk_bf16(r[0], r[1]), pk_bf16(r[2], r[3])};
      }
      if (u == 0) st8(dst + 8 * TH, o[0]);
      else st16(dst, u4_t{o[TH].x, o[TH].y, o[1 - TH].x, o[1 - TH].y});
    }
  };
  if (th) epi(IC(1)); else epi(IC(0));
  if (w == 0) STAMP(2, 7);
}

constexpr int D_SLOT = 15360, D_NSLOT = 9, D_STEPS = 18;
constexpr int D_LDS = D_NSLOT * D_SLOT;

template <int NDMA>
__device__ __forceinline__ void d_loader(char* smem, const char* wp, int w4, int lane) {
  const unsigned lds0 = (unsigned)(size_t)(LAS char*)smem;
  const unsigned voff = lane * 16;
  auto issue = [&](int step, unsigned so) {
#pragma unroll
    for (int k = 0; k < NDMA; ++k) {
      const int i = w4 + 4 * k;
      glds16(wp + (size_t)step * D_SLOT + i * 1024, voff, lds0 + so + i * 1024);
    }
  };
  constexpr unsigned S = D_SLOT;
  issue(0, 0); issue(1, S); issue(2, 2 * S);
  wait_vm<2 * NDMA>(); bar();
  issue(3, 3 * S); issue(4, 4 * S); issue(5, 5 * S);
  wait_vm<4 * NDMA>(); bar();
  issue(6, 6 * S); issue(7, 7 * S);
  wait_vm<5 * NDMA>(); bar();
  issue(8, 8 * S); issue(9, 0);
  unsigned sw = S;
#pragma unroll 1
  for (int b = 2; b < 10; ++b) {
    wait_vm<6 * NDMA>();
    bar();
    issue(b + 8, sw);
    sw += S; if (sw == D_NSLOT * S) sw = 0;
  }
  wait_vm<6 * NDMA>(); bar();
  wait_vm<5 * NDMA>(); bar();
  wait_vm<4 * NDMA>(); bar();
  wait_vm<3 * NDMA>(); bar();
  wait_vm<2 * NDMA>(); bar();
  wait_vm<1 * NDMA>(); bar();
  wait_vm<0>(); bar();
  bar();
}

extern "C" __global__ __launch_bounds__(512, 2) void kD(char* __restrict__ ws, u16* __restrict__ out) {
  extern __shared__ __attribute__((aligned(16))) char smem[];
  const int tid = threadIdx.x, lane = tid & 63;
  const int w = __builtin_amdgcn_readfirstlane(tid >> 6);
  const int xcd = blockIdx.x & 7, rank = blockIdx.x >> 3;
  const int it = 8 * (xcd >> 1) + (rank & 7), dg = 4 * (xcd & 1) + (rank >> 3);
  if (w == 0) STAMP(3, 0);
  if (w >= 4) {
    const int w4 = w - 4;
    const char* wp = ws + OFF_WP2 + (size_t)dg * (54 * 5120);
    if (w4 < 3) d_loader<4>(smem, wp, w4, lane);
    else d_loader<3>(smem, wp, w4, lane);
    return;
  }
  const int fr = lane & 15, g = lane >> 4;
  const int t = 64 * it + 16 * w + fr;
  const char* hp = ws + OFF_HH + (size_t)(4 * it + w) * (54 * 1024);
  const unsigned voff = lane * 16;
  u4_t R[18];
#pragma unroll
  for (int i = 0; i < 9; ++i)
    asm volatile("global_load_dwordx4 %0, %1, %2" : "=v"(R[i]) : "v"(voff), "s"(hp + (size_t)i * 1024) : "memory");
  u2_t res[5]; unsigned st;
  f4_t acc[5];
#pragma unroll
  for (int dt = 0; dt < 5; ++dt) acc[dt] = f4_t{0.f, 0.f, 0.f, 0.f};
  b8_t A0[5], A1[5];
  auto rdA = [&](b8_t (&A)[5], unsigned off) {
    const char* sp = smem + off + lane * 16;
#pragma unroll
    for (int dt = 0; dt < 5; ++dt) A[dt] = *(const b8_t*)(sp + dt * 1024);
  };
  bar();
  if (w == 0) STAMP(3, 1);
  rdA(A0, 0);
  unsigned so = 0;
  auto kstep = [&](auto mode, auto idx, b8_t (&Ac)[5], b8_t (&An)[5], const char* hnext) {
    constexpr int MODE = decltype(mode)::value, i = decltype(idx)::value, kk = i % 3;
    if (kk == 0) bar();
    unsigned nx = so + (kk + 1) * 5120;
    if (kk == 2) { so += D_SLOT; if (so == D_NSLOT * D_SLOT) so = 0; nx = so; }
    SB;
    if (!(MODE == 2 && i == 17)) rdA(An, nx);
    constexpr int NV = (MODE == 0) ? (i < 9 ? 8 + i : 26 - i) : (MODE == 1) ? (i < 9 ? 25 - i : 17) : (i == 0 ? 17 : 23 - i);
    asm volatile("s_waitcnt vmcnt(%1)" : "+v"(R[i]) : "i"(NV) : "memory");
    const b8_t B = __builtin_bit_cast(b8_t, R[i]);
#pragma unroll
    for (int dt = 0; dt < 5; ++dt) acc[dt] = __builtin_amdgcn_mfma_f32_16x16x32_bf16(Ac[dt], B, acc[dt], 0, 0, 0);
    SB;
    if (MODE == 0 && i < 9)
      asm volatile("global_load_dwordx4 %0, %1, %2" : "=v"(R[i < 9 ? 9 + i : 0]) : "v"(voff), "s"(hp + (size_t)(9 + i) * 1024) : "memory");
    if (MODE != 2) asm volatile("global_load_dwordx4 %0, %1, %2" : "+v"(R[i]) : "v"(voff), "s"(hnext + (size_t)i * 1024) : "memory");
    if (MODE == 2 && i == 0) {
#pragma unroll
      for (int dt = 0; dt < 5; ++dt) {
        const int d = 80 * dg + 16 * dt + 4 * g;
        const char* rp = ws + OFF_H8 + ((size_t)(d >> 3) * SL + t) * 16 + (d & 4) * 2;
        asm volatile("global_load_dwordx2 %0, %1, off" : "=v"(res[dt]) : "v"(rp) : "memory");
      }
      asm volatile("global_load_dword %0, %1, off" : "=v"(st) : "v"(ws + OFF_G2 + (size_t)t * 8) : "memory");
    }
    SB;
  };
  auto round = [&](auto mode, const char* hnext) {
    kstep(mode, IC(0), A0, A1, hnext); kstep(mode, IC(1), A1, A0, hnext); kstep(mode, IC(2), A0, A1, hnext);
    kstep(mode, IC(3), A1, A0, hnext); kstep(mode, IC(4), A0, A1, hnext); kstep(mode, IC(5), A1, A0, hnext);
    kstep(mode, IC(6), A0, A1, hnext); kstep(mode, IC(7), A1, A0, hnext); kstep(mode, IC(8), A0, A1, hnext);
    kstep(mode, IC(9), A1, A0, hnext); kstep(mode, IC(10), A0, A1, hnext); kstep(mode, IC(11), A1, A0, hnext);
    kstep(mode, IC(12), A0, A1, hnext); kstep(mode, IC(13), A1, A0, hnext); kstep(mode, IC(14), A0, A1, hnext);
    kstep(mode, IC(15), A1, A0, hnext); kstep(mode, IC(16), A0, A1, hnext); kstep(mode, IC(17), A1, A0, hnext);
  };
  round(IC(0), hp + 18 * 1024);
  round(IC(1), hp + 36 * 1024);
  round(IC(2), hp);
  if (w == 0) STAMP(3, 2);
  asm volatile("s_waitcnt vmcnt(0)" : "+v"(res[0]), "+v"(res[1]), "+v"(res[2]), "+v"(res[3]), "+v"(res[4]), "+v"(st)::"memory");
  const float sd = bf_hi(st), r2 = 1.0f / (sd * sd);
#pragma unroll
  for (int dt = 0; dt < 5; ++dt) {
    const int d = 80 * dg + 16 * dt + 4 * g;
    const float h0 = bf_lo(res[dt].x), h1 = bf_hi(res[dt].x), h2 = bf_lo(res[dt].y), h3 = bf_hi(res[dt].y);
    st8(out + (size_t)t * DM + d, u2_t{pk_bf16(h0 + r2 * acc[dt][0], h1 + r2 * acc[dt][1]), pk_bf16(h2 + r2 * acc[dt][2], h3 + r2 * acc[dt][3])});
  }
  if (w == 0) STAMP(3, 7);
}

extern "C" __global__ void kS(char* ws) {
  const unsigned long long* h = (const unsigned long long*)(ws + OFF_DBG);
  const int lane = threadIdx.x;
  unsigned long long mn[4], mx[4];
  for (int k = 0; k < 4; ++k) {
    unsigned long long a = ~0ull, b = 0;
    for (int blk = lane; blk < 256; blk += 64) {
      const unsigned long long s0 = h[(k * 512 + blk) * 16], s7 = h[(k * 512 + blk) * 16 + 7];
      a = s0 < a ? s0 : a;
      if (s7 > s0 && s7 - s0 < 5000) b = s7 > b ? s7 : b;
    }
    for (int o = 1; o < 64; o <<= 1) {
      unsigned long long a2 = __shfl_xor(a, o), b2 = __shfl_xor(b, o);
      a = a2 < a ? a2 : a; b = b2 > b ? b2 : b;
    }
    mn[k] = a; mx[k] = b;
  }
  long long q = 0;
  if (PROBE == 1) q = mx[0] - mn[0];
  if (PROBE == 2) q = mn[1] - mx[0];
  if (PROBE == 3) q = mx[1] - mn[1];
  if (PROBE == 4) q = mn[2] - mx[1];
  if (PROBE == 5) q = mx[2] - mn[2];
  if (PROBE == 6) q = mn[3] - mx[2];
  if (PROBE == 7) q = mx[3] - mn[3];
  if (PROBE == 8) q = mx[3] - mn[0];
  if (q < 0 || q > 5000) q = 5000;
  const unsigned long long t0 = __builtin_amdgcn_s_memrealtime();
  while ((long long)(__builtin_amdgcn_s_memrealtime() - t0) < q * PROBE_M) __builtin_amdgcn_s_sleep(1);
}

extern "C" void kernel_launch(void* const* d_in, const int* in_sizes, int n_in, void* d_out, int out_size, void* d_ws,
                              size_t ws_size, hipStream_t stream) {
  char* ws = (char*)d_ws;
  (void)hipFuncSetAttribute((const void*)kA, hipFuncAttributeMaxDynamicSharedMemorySize, A_LDS);
  (void)hipFuncSetAttribute((const void*)kC, hipFuncAttributeMaxDynamicSharedMemorySize, C_LDS);
  (void)hipFuncSetAttribute((const void*)kD, hipFuncAttributeMaxDynamicSharedMemorySize, D_LDS);
  kA<<<256, 1024, A_LDS, stream>>>((const u16*)d_in[0], (const u16*)d_in[1], (const u16*)d_in[2], (const u16*)d_in[3], (const u16*)d_in[4],
                                   (const u16*)d_in[5], (const u16*)d_in[6], (const u16*)d_in[7], (const u16*)d_in[12],
                                   (const u16*)d_in[10], (const u16*)d_in[11], (const u16*)d_in[8], (const u16*)d_out, ws);
  kB<<<256, 512, B_LDS, stream>>>((const u16*)d_in[8], (const u16*)d_in[10], (const u16*)d_in[11], ws);
  kC<<<256, 512, C_LDS, stream>>>((const u16*)d_in[10], (const u16*)d_in[11], ws);
  kD<<<256, 512, D_LDS, stream>>>(ws, (u16*)d_out);
  kS<<<1, 64, 0, stream>>>(ws);
}
```

```cpp
#include <hip/hip_runtime.h>
#include <hip/hip_bf16.h>
#include <stdint.h>
#include <type_traits>

typedef unsigned short u16;
typedef _Float16 h2_t __attribute__((ext_vector_type(2)));
typedef _Float16 h8_t __attribute__((ext_vector_type(8)));
typedef __bf16 b2_t __attribute__((ext_vector_type(2)));
typedef __bf16 b8_t __attribute__((ext_vector_type(8)));
typedef float f2_t __attribute__((ext_vector_type(2)));
typedef float f4_t __attribute__((ext_vector_type(4)));
typedef unsigned u2_t __attribute__((ext_vector_type(2)));
typedef unsigned u4_t __attribute__((ext_vector_type(4)));
typedef short s4_t __attribute__((ext_vector_type(4)));
typedef __attribute__((address_space(1))) unsigned long long gu64;
#define LAS __attribute__((address_space(3)))
#define SB __builtin_amdgcn_sched_barrier(0)
#define IC(n) std::integral_constant<int, n>{}

constexpr int SL = 2048;
constexpr int DM = 640;
constexpr int FF = 1728;

constexpr size_t OFF_G1 = 0;
constexpr size_t OFF_EP = 0x10000;
constexpr size_t OFF_RD = 0x10100;
constexpr size_t OFF_H8 = 0x5A0000;
constexpr size_t OFF_HH = 0x820000;
constexpr size_t OFF_WP2 = 0xF00000;
constexpr size_t OFF_CS = 0x1120000;
constexpr size_t OFF_G2 = 0x1130000;
constexpr int KRS = 1344;
constexpr int KDS = 4864;
constexpr int KD_O = 2240;
constexpr int KD_DR = 4384;

#define STAMP(k, i)

__device__ __forceinline__ float bf_lo(unsigned d) { return __uint_as_float(d << 16); }
__device__ __forceinline__ float bf_hi(unsigned d) { return __uint_as_float(d & 0xffff0000u); }
__device__ __forceinline__ float bf2f(u16 v) { return __uint_as_float((unsigned)v << 16); }
__device__ __forceinline__ unsigned pk_bf16(float a, float b) {
  f2_t v = {a, b};
  return __builtin_bit_cast(unsigned, __builtin_convertvector(v, b2_t));
}
__device__ __forceinline__ unsigned pk_f16(float a, float b) {
  f2_t v = {a, b};
  return __builtin_bit_cast(unsigned, __builtin_convertvector(v, h2_t));
}
__device__ __forceinline__ float rbf(float a) { return bf_lo(pk_bf16(a, 0.f)); }
template <int N> __device__ __forceinline__ float dpp_ror_add(float v) {
  int r = __builtin_amdgcn_update_dpp(0, __float_as_int(v), 0x120 + N, 0xf, 0xf, false);
  return v + __int_as_float(r);
}
__device__ __forceinline__ float row16_sum(float v) {
  v = dpp_ror_add<8>(v); v = dpp_ror_add<4>(v); v = dpp_ror_add<2>(v); v = dpp_ror_add<1>(v);
  return v;
}
__device__ __forceinline__ float wave_sum(float v) {
  v = row16_sum(v);
  const int b = __float_as_int(v);
  return (__int_as_float(__builtin_amdgcn_readlane(b, 0)) + __int_as_float(__builtin_amdgcn_readlane(b, 16))) +
         (__int_as_float(__builtin_amdgcn_readlane(b, 32)) + __int_as_float(__builtin_amdgcn_readlane(b, 48)));
}
template <typename T> __device__ __forceinline__ T ldnt(const T* p) { return __builtin_nontemporal_load(p); }
__device__ __forceinline__ void wave_fence() {
  __builtin_amdgcn_fence(__ATOMIC_RELEASE, "wavefront");
  __builtin_amdgcn_wave_barrier();
  __builtin_amdgcn_fence(__ATOMIC_ACQUIRE, "wavefront");
}
__device__ __forceinline__ s4_t tr_read(const char* p) {
  return __builtin_amdgcn_ds_read_tr16_b64_v4i16((LAS s4_t*)p);
}
__device__ __forceinline__ void glds16(const void* sbase, unsigned voff, unsigned lds_addr) {
  asm volatile("s_mov_b32 m0, %2\n\ts_nop 0\n\tglobal_load_lds_dwordx4 %0, %1" ::"v"(voff), "s"(sbase), "s"(lds_addr) : "memory");
}
template <int N> __device__ __forceinline__ void wait_vm() { asm volatile("s_waitcnt vmcnt(%0)" ::"i"(N) : "memory"); }
__device__ __forceinline__ void bar() { asm volatile("s_barrier" ::: "memory"); }
__device__ __forceinline__ float dot2bf(unsigned a, unsigned b, float c) {
  return __builtin_amdgcn_fdot2_f32_bf16(__builtin_bit_cast(b2_t, a), __builtin_bit_cast(b2_t, b), c, false);
}

#define ST_MODE 0
__device__ __forceinline__ void st16(void* p, u4_t v) {
  *(u4_t*)p = v;
}
__device__ __forceinline__ void st8(void* p, u2_t v) {
  *(u2_t*)p = v;
}

struct Cw { f2_t w, ws; };
__device__ __forceinline__ Cw mkw(f2_t w) { Cw r; r.w = w; r.ws = f2_t{-w.y, w.x}; return r; }
__device__ __forceinline__ f2_t cmulw(f2_t a, const Cw& W) {
  return __builtin_elementwise_fma(f2_t{a.y, a.y}, W.ws, f2_t{a.x, a.x} * W.w);
}
__device__ __forceinline__ f2_t cexp_l(float er, float wr, float l) {
  float mag = __builtin_amdgcn_exp2f(er * l);
  float rev = __builtin_amdgcn_fractf(wr * l);
  return f2_t{mag * __builtin_amdgcn_cosf(rev), mag * __builtin_amdgcn_sinf(rev)};
}
__device__ __forceinline__ float pow2_scale(float bound, int target) {
  unsigned e = (__float_as_uint(bound) >> 23) & 0xff;
  int k = target - ((int)e - 126);
  k = k > 126 ? 126 : (k < -126 ? -126 : k);
  return (bound > 0.f) ? __uint_as_float((unsigned)(k + 127) << 23) : 1.0f;
}
__device__ __forceinline__ float pow2_inv(float p) { return __uint_as_float(0x7f000000u - __float_as_uint(p)); }

constexpr int B_SIDE = 96;
constexpr int KG_R = 0, KG_L0 = 4352, KG_L1 = 8704, KG_W = 8704 + 25 * 272;
constexpr int B_BND = 8 * KG_W;
constexpr int B_KC = B_BND + 64;
constexpr int B_LDS = B_KC + 4 * KRS;
constexpr int B_KOFF = 0;
constexpr int B_UOFF = 4 * KDS;
constexpr int B_UCH = 4608;
constexpr int B_YCH = 10240;
constexpr int B_PACK = 40960;

__device__ __forceinline__ void pack_wd(const u16* __restrict__ wd, char* __restrict__ ws, char* buf, int u, int lane) {
  const int ks = u >> 3, dg = u & 7;
#pragma unroll
  for (int i = 0; i < 5; ++i) {
    const int idx = i * 64 + lane, row = idx / 10, c = idx % 10;
    __builtin_amdgcn_global_load_lds((const unsigned*)((const char*)wd + (size_t)(32 * ks + row) * 1280 + 160 * dg + 16 * c),
                                     (LAS unsigned*)(buf + i * 1024), 16, 0, 2);
  }
  asm volatile("s_waitcnt vmcnt(0)" ::: "memory");
  wave_fence();
  const int g = lane >> 4, q = (lane & 15) >> 2, p = lane & 3;
  char* dst = ws + OFF_WP2 + ((size_t)(dg * 54 + ks) * 5) * 1024 + lane * 16;
#pragma unroll
  for (int dt = 0; dt < 5; ++dt) {
    s4_t aa = tr_read(buf + (4 * g + q) * 160 + 32 * dt + 8 * p);
    s4_t bb = tr_read(buf + (16 + 4 * g + q) * 160 + 32 * dt + 8 * p);
    u2_t ua = __builtin_bit_cast(u2_t, aa), ub = __builtin_bit_cast(u2_t, bb);
    st16(dst + dt * 1024, u4_t{ua.x, ua.y, ub.x, ub.y});
  }
}

struct BFrag { h8_t B[8]; u2_t A[5][2]; };
__device__ __forceinline__ void mfma_f16(f4_t& d, u4_t a, h8_t b) {
  asm volatile("v_mfma_f32_16x16x32_f16 %0, %1, %2, %0" : "+v"(d) : "v"(a), "v"(b));
}
__device__ __forceinline__ void mfma_f16_0(f4_t& d, u4_t a, h8_t b) {
  asm volatile("v_mfma_f32_16x16x32_f16 %0, %1, %2, 0" : "=&v"(d) : "v"(a), "v"(b));
}
__device__ __forceinline__ unsigned ln_fin(float mean, float var) {
  const float mb = rbf(mean), sd = rbf(sqrtf(rbf(rbf(var) + 1e-5f)));
  return (__float_as_uint(mb) >> 16) | (__float_as_uint(sd) & 0xffff0000u);
}
__device__ __forceinline__ unsigned ln1_row(const u16* __restrict__ x, int t) {
  const u4_t* p = (const u4_t*)(x + (size_t)t * DM);
  float s = 0.f, q = 0.f;
  for (int i = 0; i < 80; ++i) { const u4_t v = p[i]; for (int j = 0; j < 4; ++j) s += bf_lo(v[j]) + bf_hi(v[j]); }
  const float mean = s * (1.0f / DM);
  for (int i = 0; i < 80; ++i) {
    const u4_t v = p[i];
    for (int j = 0; j < 4; ++j) { const float a = bf_lo(v[j]) - mean, b = bf_hi(v[j]) - mean; q += a * a; q += b * b; }
  }
  return ln_fin(mean, q * (1.0f / DM));
}
constexpr int SPIN_MAX = 1 << 14;
#define TLBG 524288

extern "C" __global__ __launch_bounds__(512) void kB(const u16* __restrict__ x, const u16* __restrict__ Are, const u16* __restrict__ Aim,
                                                     const u16* __restrict__ Bre, const u16* __restrict__ Bim, const u16* __restrict__ Cre,
                                                     const u16* __restrict__ Cim, const u16* __restrict__ logdt, const u16* __restrict__ dskip,
                                                     const u16* __restrict__ wg, const u16* __restrict__ wu, const u16* __restrict__ wd,
                                                     const u16* __restrict__ outp, char* __restrict__ ws) {
  extern __shared__ __attribute__((aligned(16))) char smem[];
  const int tid = threadIdx.x, lane = tid & 63;
  const int w = __builtin_amdgcn_readfirstlane(tid >> 6);
  const int xcd = blockIdx.x & 7;
  u2_t ep = {0u, 0u};
  if (w == 0) asm volatile("global_load_dwordx2 %0, %1, off sc1" : "=v"(ep) : "v"(ws + OFF_EP) : "memory");
  if (w == 0) STAMP(1, 0);
  auto post_epoch = [&](int off) {
    if (tid == 0) {
      const unsigned long long e = (((unsigned long long)ep.y << 32) | ep.x) + 1ull;
      *(unsigned long long*)(smem + off) = e;
      __hip_atomic_store((gu64*)(ws + OFF_RD) + blockIdx.x, e, __ATOMIC_RELAXED, __HIP_MEMORY_SCOPE_AGENT);
    }
  };
  if (blockIdx.x < B_SIDE) {
    const int rk = blockIdx.x >> 3;
    const int g4 = xcd >> 1, a = xcd & 1, nl = (g4 < 3) ? 7 : 6;
    constexpr int S_EP = 17408;
    unsigned pf[2] = {0u, 0u};
    if (rk >= 8) {
#pragma unroll
      for (int k = 0; k < 2; ++k) {
        const int id = (((rk - 8) * 8 + w) * 2 + k) * 64 + lane;
        asm volatile("global_load_dword %0, %1, off" : "=v"(pf[k]) : "v"((const char*)x + (size_t)(id >> 1) * (DM * 2) + (((160 * xcd) >> 7) + (id & 1)) * 128) : "memory");
      }
    }
    if (rk < 8) {
      const int r = lane >> 4, c = lane & 15, t = (blockIdx.x * 8 + w) * 4 + r;
      const u4_t* p = (const u4_t*)(x + (size_t)t * DM);
      u4_t v[5];
#pragma unroll
      for (int i = 0; i < 5; ++i) v[i] = ldnt(p + c + 16 * i);
      float s = 0.f;
#pragma unroll
      for (int i = 0; i < 5; ++i)
#pragma unroll
        for (int j = 0; j < 4; ++j) s += bf_lo(v[i][j]) + bf_hi(v[i][j]);
      asm volatile("" : "+v"(ep) : "v"(s));
      post_epoch(S_EP);
      const float mean = row16_sum(s) * (1.0f / DM);
      float q = 0.f;
#pragma unroll
      for (int i = 0; i < 5; ++i)
#pragma unroll
        for (int j = 0; j < 4; ++j) {
          const float aa = bf_lo(v[i][j]) - mean, bb = bf_hi(v[i][j]) - mean;
          q += aa * aa; q += bb * bb;
        }
      const unsigned sv = ln_fin(mean, row16_sum(q) * (1.0f / DM));
      __syncthreads();
      const u2_t e = *(const u2_t*)(smem + S_EP);
      if (c == 0) {
        const u4_t g = {sv, e.x, sv, e.y};
        asm volatile("global_store_dwordx4 %0, %1, off sc1\n\ts_nop 1" ::"v"(ws + OFF_G1 + (size_t)t * 16), "v"(g) : "memory");
        ((unsigned long long*)(ws + OFF_G2))[t] = 0ull;
      }
      if (w == 0) STAMP(1, 1);
      if (rk == 4 && w == 0) {
        unsigned long long a0, a1;
        asm volatile("s_getpc_b64 s[20:21]\n\ts_add_u32 s20, s20, kB@rel32@lo+4\n\ts_addc_u32 s21, s21, kB@rel32@hi+12\n\ts_mov_b64 %0, s[20:21]" : "=s"(a0) :: "s20", "s21", "scc");
        asm volatile("s_getpc_b64 s[20:21]\n\ts_add_u32 s20, s20, kD@rel32@lo+4\n\ts_addc_u32 s21, s21, kD@rel32@hi+12\n\ts_mov_b64 %0, s[20:21]" : "=s"(a1) :: "s20", "s21", "scc");
        const unsigned long long lo = a0 < a1 ? a0 : a1, last = ((a0 < a1 ? a1 - a0 : a0 - a1) + 8192) & ~127ull;
        unsigned cf[8];
#pragma unroll
        for (int i = 0; i < 8; ++i) {
          const unsigned long long o = (unsigned long long)(lane + 64 * i) * 128;
          asm volatile("global_load_dword %0, %1, off" : "=v"(cf[i]) : "v"(lo + (o < last ? o : last)) : "memory");
        }
        asm volatile("s_waitcnt vmcnt(0)" ::"v"(cf[0]), "v"(cf[1]), "v"(cf[2]), "v"(cf[3]), "v"(cf[4]), "v"(cf[5]), "v"(cf[6]), "v"(cf[7]) : "memory");
      }
      if (rk == 4 && w == 1) {
        constexpr size_t G = TLBG;
        constexpr int NWS = (0x1150000 + G - 1) / G, NW = (DM * FF * 2 + G - 1) / G;
        unsigned tf[3];
        asm volatile("global_load_dword %0, %1, off nt" : "=v"(tf[0]) : "v"(ws + min((size_t)lane * G, (size_t)0x1150000 - 4)) : "memory");
        const char* wsel = (const char*)((lane & 1) ? wu : wg);
        asm volatile("global_load_dword %0, %1, off nt" : "=v"(tf[1]) : "v"(wsel + min((size_t)(lane >> 1) * G, (size_t)DM * FF * 2 - 4)) : "memory");
        asm volatile("global_load_dword %0, %1, off nt" : "=v"(tf[2]) : "v"((const char*)outp + min((size_t)lane * G, (size_t)SL * DM * 2 - 4)) : "memory");
        static_assert(NWS <= 64 && NW <= 32, "one load per buffer group");
        asm volatile("s_waitcnt vmcnt(0)" ::"v"(tf[0]), "v"(tf[1]), "v"(tf[2]) : "memory");
      }
      if (blockIdx.x == 63 && w == 7) {
        const unsigned long long e64 = ((unsigned long long)e.y << 32) | e.x;
        for (int spin = 0; spin < SPIN_MAX; ++spin) {
          bool ok = true;
#pragma unroll
          for (int i = 0; i < 4; ++i) ok &= __hip_atomic_load((const gu64*)(ws + OFF_RD) + lane + 64 * i, __ATOMIC_RELAXED, __HIP_MEMORY_SCOPE_AGENT) == e64;
          if (__all(ok)) break;
          __builtin_amdgcn_s_sleep(4);
        }
        if (lane == 0) __hip_atomic_store((gu64*)(ws + OFF_EP), e64, __ATOMIC_RELAXED, __HIP_MEMORY_SCOPE_AGENT);
      }
    }
    const int strip = (rk >= 8) ? rk - 8 : 4 + rk;
    if ((rk >= 8 || rk < 3) && strip < nl) {
      const int jl = 7 * g4 + strip;
      const char* pm = (const char*)(a ? wu : wg) + (size_t)(80 * w + (lane >> 3)) * (FF * 2) + 128 * jl + (lane & 7) * 16;
      u4_t v[10];
#pragma unroll
      for (int i = 0; i < 10; ++i) v[i] = ldnt((const u4_t*)(pm + (size_t)i * 8 * (FF * 2)));
      const char* po = (const char*)(a ? wg : wu) + (size_t)(80 * w) * (FF * 2) + 128 * jl;
      unsigned t0v, t1v;
      asm volatile("global_load_dword %0, %1, off nt" : "=v"(t0v) : "v"(po + (size_t)lane * (FF * 2)) : "memory");
      asm volatile("global_load_dword %0, %1, off nt" : "=v"(t1v) : "v"(po + (size_t)(64 + (lane & 15)) * (FF * 2)) : "memory");
      f2_t s[4];
#pragma unroll
      for (int e = 0; e < 4; ++e) s[e] = f2_t{0.f, 0.f};
#pragma unroll
      for (int i = 0; i < 10; ++i)
#pragma unroll
        for (int jj = 0; jj < 4; ++jj) s[jj] += f2_t{bf_lo(v[i][jj]), bf_hi(v[i][jj])};
      if (rk >= 8) {
        asm volatile("" : "+v"(ep) : "v"(s[0]));
#pragma unroll
        for (int i = 0; i < 2; ++i) asm volatile("" : "+v"(pf[i]) : "v"(s[0]));
        post_epoch(S_EP);
      }
      float* pp = (float*)smem;
      *(f4_t*)(pp + (w * 8 + (lane >> 3)) * 68 + (lane & 7) * 8) = f4_t{s[0].x, s[0].y, s[1].x, s[1].y};
      *(f4_t*)(pp + (w * 8 + (lane >> 3)) * 68 + (lane & 7) * 8 + 4) = f4_t{s[2].x, s[2].y, s[3].x, s[3].y};
      __syncthreads();
      float acc = 0.f;
#pragma unroll
      for (int i = 0; i < 8; ++i) acc += pp[(w * 8 + i) * 68 + lane];
      __syncthreads();
      pp[w * 64 + lane] = acc;
      __syncthreads();
      if (w == 0) {
        float t = 0.f;
#pragma unroll
        for (int i = 0; i < 8; ++i) t += pp[i * 64 + lane];
        ((float*)(ws + OFF_CS))[a * FF + 64 * jl + lane] = t;
      }
      asm volatile("s_waitcnt vmcnt(0)" ::"v"(t0v), "v"(t1v) : "memory");
    }
    if (w == 0) STAMP(1, 7);
    return;
  }
  const int cb = blockIdx.x - B_SIDE;
  const int q = 20 * xcd + (cb >> 3);
  const int cl = w >> 1, hw = w & 1, ch = 4 * q + cl;
  const int n = ch * 128 + hw * 64 + lane;
  const u16 i_are = ldnt(Are + n), i_aim = ldnt(Aim + n), i_bre = ldnt(Bre + n), i_bim = ldnt(Bim + n), i_cre = ldnt(Cre + n), i_cim = ldnt(Cim + n);
  const u16 i_dt = logdt[ch];
  u2_t xg[4];
  const unsigned xoff = tid * (2 * DM * 2);
  const char* xb = (const char*)(x + 4 * q);
#define XG(i) asm volatile("global_load_dwordx2 %0, %1, %2 offset:%3" : "=v"(xg[i]) : "v"(xoff), "s"(xb + ((i) >> 1) * (size_t)(1024 * DM * 2)), "i"(((i) & 1) * DM * 2) : "memory")
  const u2_t dkv = *(const u2_t*)(dskip + 4 * q);
  const int c = lane & 15, kg = lane >> 4;
  h8_t band9, band7;
#pragma unroll
  for (int j = 0; j < 8; ++j) {
    const int k = 8 * kg + 7 - j;
    band9[j] = (k >= c + 4 && k <= c + 12) ? (_Float16)1.0f : (_Float16)0.0f;
    band7[j] = (k >= c + 5 && k <= c + 11) ? (_Float16)1.0f : (_Float16)0.0f;
  }
  asm volatile("" : "+v"(band9), "+v"(band7));
  char* hb = smem + w * KG_W;
  float Sw, bsum;
  {
    const float dt = rbf(expf(bf2f(i_dt)));
    const float are = dt * bf2f(i_are), aim = dt * bf2f(i_aim);
    const float bre = dt * bf2f(i_bre), bim = dt * bf2f(i_bim);
    const float cre = bf2f(i_cre), cim = bf2f(i_cim);
    f2_t c0 = {cre * bre - cim * bim, cre * bim + cim * bre};
    const float er = are * 1.44269504088896341f, wr = aim * 0.15915494309189535f;
    const float bnd = (fabsf(c0.x) + fabsf(c0.y)) * fmaxf(1.0f, __builtin_amdgcn_exp2f(er * 2048.f));
    bsum = wave_sum(bnd);
    asm volatile("" : "+v"(ep) : "v"(bnd));
    post_epoch(B_BND + 48);
    if (lane == 0) ((float*)(smem + B_BND))[w] = bsum;
    if (w == 0) STAMP(1, 1);
    Sw = pow2_scale(bsum, 15);
    c0 *= Sw;
    const Cw z1 = mkw(cexp_l(er, wr, 1.f)), w8 = mkw(cexp_l(er, -wr, 8.f)), w128 = mkw(cexp_l(er, wr, 128.f));
    const Cw z2 = mkw(cmulw(z1.w, z1));
    const Cw z4 = mkw(cmulw(z2.w, z2));
    auto put = [&](int off, int row, f2_t v) { *(unsigned*)(hb + off + row * 272 + 4 * lane) = pk_f16(v.x, v.y); };
    f2_t R = w8.w, v0 = c0, v1 = cmulw(c0, w128), T = v1;
    *(unsigned*)(hb + KG_R + 4 * lane) = pk_f16(1.f, 0.f);
#pragma unroll
    for (int i = 0; i < 8; ++i) {
      SB;
      if ((w & 3) == (i & 3)) { if (i < 4) XG(0); else XG(1); }
      SB;
      *(unsigned*)(hb + KG_R + (2 * i + 1) * 272 + 4 * lane) = pk_f16(R.x, R.y);
      if (i < 7) {
        R = cmulw(R, w8);
        *(unsigned*)(hb + KG_R + (2 * i + 2) * 272 + 4 * lane) = pk_f16(R.x, R.y);
        R = cmulw(R, w8);
      }
      put(KG_L0, i, v0); put(KG_L0, 8 + i, v1);
      if (i < 7) { v0 = cmulw(v0, z1); v1 = cmulw(v1, z1); }
#pragma unroll
      for (int k = 0; k < 2; ++k) {
        const int i1 = 2 + 2 * i + k;
        if (i1 <= 16) {
          T = cmulw(T, w128);
          if (i1 < 4) {
            f2_t v = T;
#pragma unroll
            for (int i0 = 0; i0 < 4; ++i0) { put(KG_L1, 4 * (i1 - 2) + i0, v); if (i0 < 3) v = cmulw(v, z2); }
          } else if (i1 < 8) {
            put(KG_L1, 8 + 2 * (i1 - 4), T); put(KG_L1, 8 + 2 * (i1 - 4) + 1, cmulw(T, z4));
          } else {
            put(KG_L1, 16 + (i1 - 8), T);
          }
        }
      }
    }
  }
  SB;
  if (w == 0) STAMP(1, 4);
  if (w < 4) XG(2);
  wave_fence();
  f4_t ka[3];
  {
    h8_t Bf[4];
#pragma unroll
    for (int ks = 0; ks < 4; ++ks) Bf[ks] = *(const h8_t*)(hb + KG_R + c * 272 + 64 * ks + 16 * kg);
#pragma unroll
    for (int t = 0; t < 3; ++t) ka[t] = f4_t{0.f, 0.f, 0.f, 0.f};
#pragma unroll
    for (int ks = 0; ks < 4; ++ks)
#pragma unroll
      for (int t = 0; t < 3; ++t) {
        const int off = (t == 0) ? KG_L0 + c * 272 : (t == 1) ? KG_L1 + c * 272 : KG_L1 + (16 + min(c, 8)) * 272;
        const h8_t Af = *(const h8_t*)(hb + off + 64 * ks + 16 * kg);
        ka[t] = __builtin_amdgcn_mfma_f32_16x16x32_f16(Af, Bf[ks], ka[t], 0, 0, 0);
      }
  }
  SB;
  if (w >= 4) XG(2);
  SB;
  const float iSw = pow2_inv(Sw);
  wave_fence();
  if (hw == 0) *(f4_t*)(hb + lane * 16) = ka[1] * iSw;
  else { *(f4_t*)(hb + lane * 16) = ka[0] * iSw; *(f4_t*)(hb + 1024 + lane * 16) = ka[2] * iSw; }
  __syncthreads();
  if (w == 0) STAMP(1, 5);
  const u2_t ep1 = *(const u2_t*)(smem + B_BND + 48);
  u4_t gr[2][2];
#pragma unroll
  for (int it = 0; it < 2; ++it) {
    const char* gp = ws + OFF_G1 + (size_t)(2 * (tid + 512 * it)) * 16;
    asm volatile("global_load_dwordx4 %0, %1, off sc1" : "=v"(gr[it][0]) : "v"(gp) : "memory");
    asm volatile("global_load_dwordx4 %0, %1, off offset:16 sc1" : "=v"(gr[it][1]) : "v"(gp) : "memory");
  }
  if (w < 4) XG(3);
  {
    const char* pb = smem + (w ^ 1) * KG_W;
    const float Sc = pow2_scale(((const float*)(smem + B_BND))[2 * cl] + ((const float*)(smem + B_BND))[2 * cl + 1], 15);
    char* kr = smem + B_KC + cl * KRS;
    if (hw == 0) {
      const f4_t k0 = (ka[0] * iSw + *(const f4_t*)(pb + lane * 16)) * Sc, k2 = (ka[2] * iSw + *(const f4_t*)(pb + 1024 + lane * 16)) * Sc;
      *(u2_t*)(kr + 2 * (128 * (kg >> 1) + 4 * (kg & 1) + 8 * c)) = u2_t{pk_f16(k0[0], k0[1]), pk_f16(k0[2], k0[3])};
      const unsigned p01 = pk_f16(k2[0], k2[1]), p23 = pk_f16(k2[2], k2[3]);
      u16* k3 = (u16*)kr + 512 + 64 * kg + c;
      if (kg < 2) { k3[0] = (u16)p01; k3[16] = (u16)(p01 >> 16); k3[32] = (u16)p23; k3[48] = (u16)(p23 >> 16); }
      else if (lane == 32) k3[0] = (u16)p01;
      if (lane == 0) ((float*)(smem + B_BND + 32))[cl] = pow2_inv(Sc);
    } else {
      const f4_t k1 = (ka[1] * iSw + *(const f4_t*)(pb + lane * 16)) * Sc;
      const unsigned p01 = pk_f16(k1[0], k1[1]), p23 = pk_f16(k1[2], k1[3]);
      if (kg < 2) {
        *(u2_t*)(kr + 2 * (256 + 64 * kg + 4 * c)) = u2_t{p01, p23};
      } else {
        *(unsigned*)(kr + 2 * (384 + 64 * (kg - 2) + 2 * c)) = p01;
        *(unsigned*)(kr + 2 * (384 + 64 * (kg - 2) + 32 + 2 * c)) = p23;
      }
    }
  }
  SB;
  if (w >= 4) XG(3);
  __syncthreads();
  const float invs = ((const float*)(smem + B_BND + 32))[w & 3];
  {
    const int kch = tid >> 7, kc = tid & 127;
    const char* kr = smem + B_KC + kch * KRS;
    const int e0 = (kc < 32) ? 8 * kc : (kc < 64) ? 256 + 4 * (kc - 32) : 384 + 2 * (kc - 64);
    const unsigned* kp = (const unsigned*)(kr + 2 * e0);
    const unsigned d0 = kp[0], d1 = kp[1], d2 = kp[2], d3 = kp[3], d4 = kp[4];
    const unsigned kfirst = *(const u16*)kr, k1024 = *(const u16*)(kr + 2 * 512);
    const int dl = 127 - kc;
    const unsigned kfa = *(const u16*)(kr + 2 * (512 + max(2 * dl - 127, 0))), kfb = *(const u16*)(kr + 2 * (512 + max(2 * dl - 126, 0)));
    char* ki = smem + B_KOFF + kch * KDS;
    const unsigned k0 = d0 & 0xffffu;
    unsigned o0, o1, o2, o3, nx;
    if (kc < 32) { o0 = d0; o1 = d1; o2 = d2; o3 = d3; nx = d4; }
    else if (kc < 64) {
      const unsigned k2 = d0 >> 16, k4 = d1 & 0xffffu, k6 = d1 >> 16, k8 = d2 & 0xffffu;
      o0 = k0 * 0x10001u; o1 = k2 | (k4 << 16); o2 = k4 * 0x10001u; o3 = k6 | (k8 << 16); nx = k8;
    } else {
      const unsigned k4 = d0 >> 16, k8 = d1 & 0xffffu;
      o0 = k0 * 0x10001u; o1 = k0 | (k4 << 16); o2 = k4 * 0x10001u; o3 = k8 * 0x10001u; nx = k8;
    }
    *(u4_t*)(ki + 16 * (kc + 2)) = u4_t{o0, o1, o2, o3};
    *(u4_t*)(ki + KD_O + 16 * (kc + 2)) = u4_t{__builtin_amdgcn_alignbit(o1, o0, 16), __builtin_amdgcn_alignbit(o2, o1, 16),
                                              __builtin_amdgcn_alignbit(o3, o2, 16), __builtin_amdgcn_alignbit(nx, o3, 16)};
    if (kc < 6) {
      const int cp = (kc < 2) ? kc : 128 + kc;
      const unsigned kk = k1024 * 0x10001u;
      const u4_t e = (kc == 2) ? u4_t{kk, kk, k1024, 0u} : u4_t{0u, 0u, 0u, 0u};
      const u4_t o = (kc == 2) ? u4_t{kk, kk, 0u, 0u} : u4_t{0u, 0u, 0u, (kc == 1) ? (kfirst << 16) : 0u};
      *(u4_t*)(ki + 16 * cp) = e;
      *(u4_t*)(ki + KD_O + 16 * cp) = o;
    }
    if (kc < 80) *(unsigned*)(ki + KD_DR + 4 * kc) = (dl >= 64) ? (kfa | (kfb << 16)) : 0u;
  }
  *(unsigned*)(smem + B_UOFF + (tid >> 7) * B_UCH + 4096 + 4 * (tid & 127)) = 0u;
  asm volatile("s_waitcnt vmcnt(0)" : "+v"(gr[0][0]), "+v"(gr[0][1]), "+v"(gr[1][0]), "+v"(gr[1][1]), "+v"(xg[0]), "+v"(xg[1]), "+v"(xg[2]), "+v"(xg[3])::"memory");
  {
    auto valid = [&](const u4_t& g) { return g[1] == ep1.x && g[3] == ep1.y && g[0] == g[2]; };
    for (int spin = 0; spin < SPIN_MAX; ++spin) {
      if (__all(valid(gr[0][0]) && valid(gr[0][1]) && valid(gr[1][0]) && valid(gr[1][1]))) break;
      __builtin_amdgcn_s_sleep(2);
#pragma unroll
      for (int it = 0; it < 2; ++it) {
        const char* gp = ws + OFF_G1 + (size_t)(2 * (tid + 512 * it)) * 16;
        asm volatile("global_load_dwordx4 %0, %1, off sc1" : "=v"(gr[it][0]) : "v"(gp) : "memory");
        asm volatile("global_load_dwordx4 %0, %1, off offset:16 sc1" : "=v"(gr[it][1]) : "v"(gp) : "memory");
      }
      asm volatile("s_waitcnt vmcnt(0)" : "+v"(gr[0][0]), "+v"(gr[0][1]), "+v"(gr[1][0]), "+v"(gr[1][1])::"memory");
    }
#pragma unroll
    for (int it = 0; it < 2; ++it)
#pragma unroll
      for (int r = 0; r < 2; ++r)
        if (!valid(gr[it][r])) gr[it][r][0] = ln1_row(x, 2 * (tid + 512 * it) + r);
  }
  if (w == 0) STAMP(1, 6);
  const float dk[4] = {bf_lo(dkv.x), bf_hi(dkv.x), bf_lo(dkv.y), bf_hi(dkv.y)};
  float z[2][8];
#pragma unroll
  for (int it = 0; it < 2; ++it) {
    const int j = tid + 512 * it;
    const unsigned s0 = gr[it][0][0], s1 = gr[it][1][0];
    const float m0 = bf_lo(s0), r0 = __builtin_amdgcn_rcpf(bf_hi(s0)), m1 = bf_lo(s1), r1 = __builtin_amdgcn_rcpf(bf_hi(s1));
    const u2_t xa = xg[2 * it], xc = xg[2 * it + 1];
    const float x0[4] = {bf_lo(xa.x), bf_hi(xa.x), bf_lo(xa.y), bf_hi(xa.y)};
    const float x1[4] = {bf_lo(xc.x), bf_hi(xc.x), bf_lo(xc.y), bf_hi(xc.y)};
#pragma unroll
    for (int cc = 0; cc < 4; ++cc) {
      const float u0 = (x0[cc] - m0) * r0, u1 = (x1[cc] - m1) * r1;
      *(unsigned*)(smem + B_UOFF + cc * B_UCH + 4 * (1023 - j)) = pk_f16(u1, u0);
      z[it][cc] = x0[cc] + u0 * dk[cc];
      z[it][4 + cc] = x1[cc] + u1 * dk[cc];
    }
  }
  __syncthreads();
  if (w == 0) STAMP(1, 2);
  if (w >= 4 && w < 7 && cb * 3 + (w - 4) < 432) pack_wd(wd, ws, smem + B_PACK + (w - 4) * 5120, cb * 3 + (w - 4), lane);
  f4_t acc[8];
  if (w < 4) {
    const char* ub = smem + B_UOFF + w * B_UCH + 4112 - 32 * c - 16 * kg - 512 * 7;
    const char* kb = smem + B_KOFF + w * KDS + ((c & 1) ? 0 : KD_O) + 4 * (12 + ((c + 1) >> 1) - 4 * kg);
    const unsigned* dr = (const unsigned*)(smem + B_KOFF + w * KDS + KD_DR) + (127 - c + 4 * kg);
    const char* uw = smem + B_UOFF + w * B_UCH + 4112 - 32 * c - 16 * kg;
    auto rdB = [&](BFrag& F, int i0, int p) { F.B[p] = *(const h8_t*)(ub + 512 * (7 - p) + 64 * i0); };
    auto rdA = [&](BFrag& F, int i0, int i1, int half) {
      const unsigned* ap = (const unsigned*)(kb + 64 * i0 + 512 * i1) + 2 * half;
      F.A[i1][half] = u2_t{ap[0], ap[1]};
    };
    auto frag = [&](const u2_t (&a)[2]) { return u4_t{a[0].x, a[0].y, a[1].x, a[1].y}; };
    auto rdnext = [&](BFrag& F, int i0, int m) {
      if (m < 8) rdB(F, i0, m); else if (m < 16) rdA(F, i0, (m - 8) >> 1, (m - 8) & 1);
    };
    auto step = [&](auto i0c, const BFrag& F, BFrag& N, auto nextc) {
      constexpr int I0 = decltype(i0c)::value, NX = decltype(nextc)::value;
      int m = 0;
#pragma unroll
      for (int i1 = 0; i1 < 5; ++i1) {
        if (i1 == 4 && I0 != 0) continue;
        const u4_t a = frag(F.A[i1]);
#pragma unroll
        for (int t = i1; t < 8; ++t) {
          if (I0 == 0 && i1 == 0) mfma_f16_0(acc[t], a, F.B[t]); else mfma_f16(acc[t], a, F.B[t - i1]);
          if (NX >= 0) rdnext(N, NX, m);
          SB;
          ++m;
        }
      }
    };
    BFrag F0, F1;
    h8_t U9[4], U7[4];
#pragma unroll
    for (int bt = 0; bt < 4; ++bt) { U9[bt] = *(const h8_t*)(uw - 512 * bt + 16); U7[bt] = *(const h8_t*)(uw - 512 * bt); }
#pragma unroll
    for (int m = 0; m < 16; ++m) rdnext(F0, 0, m);
    rdA(F0, 0, 4, 0); rdA(F0, 0, 4, 1);
    SB;
    f4_t X9[4], X7[4];
#pragma unroll
    for (int bt = 0; bt < 4; ++bt) { mfma_f16_0(X9[bt], __builtin_bit_cast(u4_t, U9[bt]), band9); mfma_f16_0(X7[bt], __builtin_bit_cast(u4_t, U7[bt]), band7); }
    SB;
    step(IC(0), F0, F1, IC(1));
    h8_t Vf[4]; u4_t Tf[4];
    {
      int m = 0;
#pragma unroll
      for (int i1 = 0; i1 < 4; ++i1) {
        const u4_t a = frag(F1.A[i1]);
#pragma unroll
        for (int t = i1; t < 8; ++t) {
          mfma_f16(acc[t], a, F1.B[t - i1]);
          rdnext(F0, 2, m);
          if (m < 4) {
            const u4_t vu = {pk_f16(X7[m][0], X9[m][0]), pk_f16(X7[m][1], X9[m][1]), pk_f16(X7[m][2], X9[m][2]), pk_f16(X7[m][3], X9[m][3])};
            Vf[m] = __builtin_bit_cast(h8_t, vu);
          }
          if (m >= 16 && m < 20) { const unsigned* tp = dr - 16 * (m - 16 + 4); Tf[m - 16] = u4_t{tp[0], tp[1], tp[2], tp[3]}; }
          SB;
          ++m;
        }
      }
    }
    step(IC(2), F0, F1, IC(3));
#pragma unroll
    for (int bt = 0; bt < 4; ++bt)
#pragma unroll
      for (int at = 4 + bt; at < 8; ++at) mfma_f16(acc[at], __builtin_bit_cast(u4_t, Vf[bt]), __builtin_bit_cast(h8_t, Tf[at - bt - 4]));
    SB;
    step(IC(3), F1, F0, IC(4));
    step(IC(4), F0, F1, IC(5));
    step(IC(5), F1, F0, IC(6));
    step(IC(6), F0, F1, IC(7));
    step(IC(7), F1, F0, IC(-1));
    asm volatile("s_nop 7\n\ts_nop 1" : "+v"(acc[0]), "+v"(acc[1]), "+v"(acc[2]), "+v"(acc[3]), "+v"(acc[4]), "+v"(acc[5]), "+v"(acc[6]), "+v"(acc[7]));
  }
  if (w == 0) STAMP(1, 3);
  __syncthreads();
  if (w < 4) {
#pragma unroll
    for (int t = 0; t < 8; ++t) *(f4_t*)(smem + w * B_YCH + t * 1280 + kg * 320 + c * 16) = acc[t] * invs;
  }
  __syncthreads();
#pragma unroll
  for (int it = 0; it < 2; ++it) {
    const int j = tid + 512 * it, t = 2 * j;
    const int yo = (t >> 8) * 1280 + ((t >> 2) & 3) * 320 + ((t >> 4) & 15) * 16 + (t & 3) * 4;
    float h0[4], h1[4];
#pragma unroll
    for (int cc = 0; cc < 4; ++cc) {
      f2_t y = *(const f2_t*)(smem + cc * B_YCH + yo);
      h0[cc] = z[it][cc] + y.x;
      h1[cc] = z[it][4 + cc] + y.y;
    }
    char* dst = ws + OFF_H8 + ((size_t)(q >> 1) * SL + t) * 16 + (q & 1) * 8;
    st8(dst, u2_t{pk_bf16(h0[0], h0[1]), pk_bf16(h0[2], h0[3])});
    st8(dst + 16, u2_t{pk_bf16(h1[0], h1[1]), pk_bf16(h1[2], h1[3])});
  }
  if (w == 0) STAMP(1, 7);
}

constexpr int C_T = 240;
constexpr int C_SLOT = 23552, C_NSLOT = 6, C_GOFF = 15360;
constexpr int C_STAT = C_NSLOT * C_SLOT;
constexpr int C_CS = C_STAT + 960;
constexpr int C_LDS = C_CS + 512;
constexpr int C_STEPS = 20;
constexpr size_t OFF_DUMMY = 0x1140000;

__device__ __forceinline__ unsigned ln_pack(float sum, float sq) {
  const float mean = sum * (1.0f / DM);
  return ln_fin(mean, fmaxf(sq * (1.0f / DM) - mean * mean, 0.f));
}
__device__ __forceinline__ unsigned ln_row(const char* h8, int t) {
  float s = 0.f, q = 0.f;
  for (int pl = 0; pl < 80; ++pl) {
    const u4_t v = *(const u4_t*)(h8 + (size_t)pl * 32768 + t * 16);
    for (int j = 0; j < 4; ++j) { const float a = bf_lo(v[j]), b = bf_hi(v[j]); s += a + b; q += a * a + b * b; }
  }
  return ln_pack(s, q);
}

template <int NDMA>
__device__ __forceinline__ void c_loader(char* smem, char* ws, const char* wbase, int w4, int lane, int t0, int jline, int xcd) {
  const unsigned lds0 = (unsigned)(size_t)(LAS char*)smem;
  const char* h8 = ws + OFF_H8;
  unsigned voffH[4], voffW[2];
#pragma unroll
  for (int k = 0; k < 2; ++k) {
    const int r = 8 * (2 * (w4 & 1) + k) + (lane >> 3), c = lane & 7;
    const int f = ((r >> 1) & 1) | ((r >> 2) & 2);
    voffW[k] = r * (FF * 2) + 128 * jline + (((c >> 1) ^ f) * 32) + (c & 1) * 16;
  }
#pragma unroll
  for (int k = 0; k < 4; ++k) {
    const int idx = 64 * (w4 + 4 * k) + lane, pl = idx / C_T, tt = idx % C_T;
    const int t = min(t0 + tt, SL - 1);
    voffH[k] = pl * 32768 + t * 16;
  }
  const unsigned ldsW = C_GOFF + (w4 >> 1) * 4096 + (w4 & 1) * 2048;
  auto issue = [&](int step, unsigned so) {
#pragma unroll
    for (int k = 0; k < 2; ++k) glds16(wbase + (size_t)step * (32 * FF * 2), voffW[k], lds0 + so + ldsW + k * 1024);
#pragma unroll
    for (int k = 0; k < NDMA - 2; ++k) glds16(h8 + (size_t)step * (4 * 32768), voffH[k], lds0 + so + (w4 + 4 * k) * 1024);
  };
  constexpr unsigned S = C_SLOT;
  issue(0, 0); issue(1, S); issue(2, 2 * S); issue(3, 3 * S);
  wait_vm<3 * NDMA>(); bar();
  issue(4, 4 * S);
  wait_vm<2 * NDMA>(); bar();
  issue(5, 5 * S); issue(6, 0);
  unsigned sw = S;
#pragma unroll 1
  for (int j = 1; j < 7; ++j) {
    wait_vm<2 * NDMA>();
    bar();
    issue(2 * j + 5, sw);
    sw += S; if (sw == C_NSLOT * S) sw = 0;
    issue(2 * j + 6, sw);
    sw += S; if (sw == C_NSLOT * S) sw = 0;
  }
  wait_vm<2 * NDMA>(); bar();
  issue(19, sw);
  const int tl = min(60 * w4 + lane, C_T - 1), tg = min(t0 + tl, SL - 1);
  const gu64* gp = (const gu64*)(ws + OFF_G2) + tg;
  const float* csp = (const float*)(ws + OFF_CS) + ((lane >> 4) & 1) * FF + 64 * jline + 16 * w4 + (lane & 15);
  const char* tb = ws + OFF_WP2 + (size_t)(4 * (xcd & 1)) * (54 * 5120);
  const int tl0 = ((int)(blockIdx.x >> 3) * 4 + w4) * 68;
  u2_t gv; float csv; unsigned tv0, tv1;
  asm volatile("global_load_dwordx2 %0, %1, off sc1" : "=v"(gv) : "v"(gp) : "memory");
  asm volatile("global_load_dword %0, %1, off" : "=v"(csv) : "v"(csp) : "memory");
  asm volatile("global_load_dword %0, %1, off" : "=v"(tv0) : "v"(tb + (size_t)min(tl0 + lane, 8639) * 128) : "memory");
  asm volatile("global_load_dword %0, %1, off" : "=v"(tv1) : "v"(tb + (size_t)min(tl0 + 64 + (lane & 3), 8639) * 128) : "memory");
  wait_vm<NDMA + 4>(); bar();
  asm volatile("s_waitcnt vmcnt(0)" : "+v"(gv), "+v"(csv), "+v"(tv0), "+v"(tv1)::"memory");
  bar();
  {
    bool ok = gv.y == 1u;
    for (int spin = 0; spin < 4096 && !__all(ok); ++spin) {
      const unsigned long long g = __hip_atomic_load(gp, __ATOMIC_RELAXED, __HIP_MEMORY_SCOPE_AGENT);
      gv.x = (unsigned)g; ok = (unsigned)(g >> 32) == 1u;
      __builtin_amdgcn_s_sleep(2);
    }
    if (!__all(ok)) gv.x = ln_row(h8, tg);
    if (lane < 60) *(float*)(smem + C_STAT + (60 * w4 + lane) * 4) = bf_lo(gv.x);
    if (lane < 32) *(float*)(smem + C_CS + (lane >> 4) * 256 + (16 * w4 + (lane & 15)) * 4) = csv;
  }
  asm volatile("s_waitcnt lgkmcnt(0)" ::: "memory");
  bar();
}

struct CFrag { b8_t A[2][2]; b8_t B[8]; };

extern "C" __global__ __launch_bounds__(512, 2) void kC(const u16* __restrict__ wg, const u16* __restrict__ wu, char* __restrict__ ws) {
  extern __shared__ __attribute__((aligned(16))) char smem[];
  const int tid = threadIdx.x, lane = tid & 63;
  const int w = __builtin_amdgcn_readfirstlane(tid >> 6);
  int panel, jline;
  const int xcd = blockIdx.x & 7, rank = blockIdx.x >> 3;
  {
    const int g4 = xcd >> 1, a = xcd & 1, nl = (g4 < 3) ? 7 : 6, total = 9 * nl, n0 = (total + 1) >> 1;
    if (a == 0 ? rank >= n0 : rank >= total - n0) {
      const int sp = (xcd < 6) ? (xcd >> 1) : 3 + 5 * (xcd - 6) + (rank - 27);
      const int wv = sp * 8 + w;
      if (w == 0) STAMP(2, 0);
      for (int tile = wv; tile < 128; tile += 104) {
        const int t = 16 * tile + (lane & 15);
        const char* hp = ws + OFF_H8 + (size_t)(lane >> 4) * 32768 + t * 16;
        u4_t v[20];
#pragma unroll
        for (int i = 0; i < 20; ++i) v[i] = *(const u4_t*)(hp + (size_t)i * (4 * 32768));
        float s0 = 0.f, s1 = 0.f, q0 = 0.f, q1 = 0.f;
        unsigned k_both = 0x3f803f80u;
        asm volatile("" : "+s"(k_both));
#pragma unroll
        for (int i = 0; i < 20; ++i) {
          s0 = dot2bf(v[i][0], k_both, s0); q0 = dot2bf(v[i][0], v[i][0], q0);
          s1 = dot2bf(v[i][1], k_both, s1); q1 = dot2bf(v[i][1], v[i][1], q1);
          s0 = dot2bf(v[i][2], k_both, s0); q0 = dot2bf(v[i][2], v[i][2], q0);
          s1 = dot2bf(v[i][3], k_both, s1); q1 = dot2bf(v[i][3], v[i][3], q1);
        }
        float s = s0 + s1, q = q0 + q1;
        s += __shfl_xor(s, 16); q += __shfl_xor(q, 16);
        s += __shfl_xor(s, 32); q += __shfl_xor(q, 32);
        if (lane < 16)
          __hip_atomic_store((gu64*)(ws + OFF_G2) + t, (1ull << 32) | ln_pack(s, q), __ATOMIC_RELAXED, __HIP_MEMORY_SCOPE_AGENT);
      }
      if (w == 0) STAMP(2, 7);
      return;
    }
    const int idx = rank + a * n0;
    panel = idx / nl; jline = 7 * g4 + idx % nl;
  }
  const int t0 = panel * C_T;
  if (w == 0) STAMP(2, 0);
  if (w >= 4) {
    const int w4 = w - 4;
    const char* wbase = (const char*)((w4 >> 1) ? wu : wg);
    if (w4 < 3) c_loader<6>(smem, ws, wbase, w4, lane, t0, jline, xcd);
    else c_loader<5>(smem, ws, wbase, w4, lane, t0, jline, xcd);
    return;
  }
  const int s = w >> 1, th = w & 1;
  const int fr = lane & 15, kg = lane >> 4, q = (lane & 15) >> 2, p = lane & 3;
  const unsigned hb0 = kg * 3840 + fr * 16 + 256 * 7;
  const unsigned hb1 = kg * 3840 + fr * 16 + 256 * (th ? 8 : 0);
  unsigned wa[2];
#pragma unroll
  for (int v = 0; v < 2; ++v) {
    const int sub = 2 * s + (v ? 1 - th : th);
    const int f = (q >> 1) | (2 * (kg & 1));
    wa[v] = C_GOFF + (8 * kg + q) * 128 + ((sub ^ f) * 32) + 8 * p;
  }
  f4_t ag[8][2], au[8][2];
#pragma unroll
  for (int u = 0; u < 8; ++u)
#pragma unroll
    for (int v = 0; v < 2; ++v) { ag[u][v] = f4_t{0.f, 0.f, 0.f, 0.f}; au[u][v] = f4_t{0.f, 0.f, 0.f, 0.f}; }
  auto load = [&](CFrag& F, unsigned so) {
    const char* sp = smem + so;
#pragma unroll
    for (int v = 0; v < 2; ++v)
#pragma unroll
      for (int m = 0; m < 2; ++m) {
        s4_t lo = tr_read(sp + wa[v] + m * 4096), hi = tr_read(sp + wa[v] + m * 4096 + 512);
        u2_t a = __builtin_bit_cast(u2_t, lo), b = __builtin_bit_cast(u2_t, hi);
        F.A[v][m] = __builtin_bit_cast(b8_t, (u4_t{a.x, a.y, b.x, b.y}));
      }
    F.B[0] = *(const b8_t*)(sp + hb0);
#pragma unroll
    for (int u = 1; u < 8; ++u) F.B[u] = *(const b8_t*)(sp + hb1 + 256 * (u - 1));
  };
  auto mma = [&](const CFrag& F) {
    ag[0][0] = __builtin_amdgcn_mfma_f32_16x16x32_bf16(F.A[0][0], F.B[0], ag[0][0], 0, 0, 0);
    au[0][0] = __builtin_amdgcn_mfma_f32_16x16x32_bf16(F.A[0][1], F.B[0], au[0][0], 0, 0, 0);
#pragma unroll
    for (int u = 1; u < 8; ++u)
#pragma unroll
      for (int v = 0; v < 2; ++v) {
        ag[u][v] = __builtin_amdgcn_mfma_f32_16x16x32_bf16(F.A[v][0], F.B[u], ag[u][v], 0, 0, 0);
        au[u][v] = __builtin_amdgcn_mfma_f32_16x16x32_bf16(F.A[v][1], F.B[u], au[u][v], 0, 0, 0);
      }
  };
  auto interleave = [&]() {
#pragma unroll
    for (int i = 0; i < 16; ++i) {
      __builtin_amdgcn_sched_group_barrier(0x008, 1, 0);
      __builtin_amdgcn_sched_group_barrier(0x100, 1, 0);
    }
    __builtin_amdgcn_sched_group_barrier(0x008, 14, 0);
  };
  CFrag F0, F1;
  bar();
  if (w == 0) STAMP(2, 1);
  load(F0, 0);
  unsigned sn = C_SLOT;
  SB;
#pragma unroll 1
  for (int j = 0; j < 9; ++j) {
    asm volatile("s_waitcnt lgkmcnt(0)" ::: "memory");
    bar(); SB;
    load(F1, sn);
    mma(F0);
    interleave();
    SB;
    sn += C_SLOT; if (sn == C_NSLOT * C_SLOT) sn = 0;
    load(F0, sn);
    mma(F1);
    interleave();
    SB;
    sn += C_SLOT; if (sn == C_NSLOT * C_SLOT) sn = 0;
  }
  asm volatile("s_waitcnt lgkmcnt(0)" ::: "memory");
  bar(); SB;
  load(F1, sn);
  mma(F0);
  interleave();
  SB;
  bar(); SB;
  f4_t csg[2], csu[2];
  float mu[8];
#pragma unroll
  for (int v = 0; v < 2; ++v) {
    const int sub = 2 * s + (v ? 1 - th : th);
    csg[v] = *(const f4_t*)(smem + C_CS + (16 * sub + 4 * kg) * 4);
    csu[v] = *(const f4_t*)(smem + C_CS + 256 + (16 * sub + 4 * kg) * 4);
  }
#pragma unroll
  for (int u = 0; u < 8; ++u) {
    const int tile = (u == 0) ? 7 : (th ? 7 + u : u - 1);
    mu[u] = *(const float*)(smem + C_STAT + (16 * tile + fr) * 4);
  }
  mma(F1);
  SB;
  if (w == 0) STAMP(2, 2);
  char* hh = ws + OFF_HH + (size_t)(2 * jline + s) * 1024 + lane * 16;
  char* sink = ws + OFF_DUMMY + lane * 16;
  auto epi = [&](auto thc) {
    constexpr int TH = decltype(thc)::value;
#pragma unroll
    for (int u = 0; u < 8; ++u) {
      const int tile = (u == 0) ? 7 : (TH ? 7 + u : u - 1);
      const int t16 = 15 * panel + tile;
      char* dst = (t16 < 128) ? hh + (size_t)t16 * (54 * 1024) : sink;
      u2_t o[2];
#pragma unroll
      for (int v = 0; v < 2; ++v) {
        if (u == 0 && v == 1) continue;
        const f4_t r = (ag[u][v] - mu[u] * csg[v]) * (au[u][v] - mu[u] * csu[v]);
        o[v] = u2_t{pk_bf16(r[0], r[1]), pk_bf16(r[2], r[3])};
      }
      if (u == 0) st8(dst + 8 * TH, o[0]);
      else st16(dst, u4_t{o[TH].x, o[TH].y, o[1 - TH].x, o[1 - TH].y});
    }
  };
  if (th) epi(IC(1)); else epi(IC(0));
  if (w == 0) STAMP(2, 7);
}

constexpr int D_SLOT = 15360, D_NSLOT = 9, D_STEPS = 18;
constexpr int D_LDS = D_NSLOT * D_SLOT;

template <int NDMA>
__device__ __forceinline__ void d_loader(char* smem, const char* wp, int w4, int lane) {
  const unsigned lds0 = (unsigned)(size_t)(LAS char*)smem;
  const unsigned voff = lane * 16;
  auto issue = [&](int step, unsigned so) {
#pragma unroll
    for (int k = 0; k < NDMA; ++k) {
      const int i = w4 + 4 * k;
      glds16(wp + (size_t)step * D_SLOT + i * 1024, voff, lds0 + so + i * 1024);
    }
  };
  constexpr unsigned S = D_SLOT;
  issue(0, 0); issue(1, S); issue(2, 2 * S);
  wait_vm<2 * NDMA>(); bar();
  issue(3, 3 * S); issue(4, 4 * S); issue(5, 5 * S);
  wait_vm<4 * NDMA>(); bar();
  issue(6, 6 * S); issue(7, 7 * S);
  wait_vm<5 * NDMA>(); bar();
  issue(8, 8 * S); issue(9, 0);
  unsigned sw = S;
#pragma unroll 1
  for (int b = 2; b < 10; ++b) {
    wait_vm<6 * NDMA>();
    bar();
    issue(b + 8, sw);
    sw += S; if (sw == D_NSLOT * S) sw = 0;
  }
  wait_vm<6 * NDMA>(); bar();
  wait_vm<5 * NDMA>(); bar();
  wait_vm<4 * NDMA>(); bar();
  wait_vm<3 * NDMA>(); bar();
  wait_vm<2 * NDMA>(); bar();
  wait_vm<1 * NDMA>(); bar();
  wait_vm<0>(); bar();
  bar();
}

extern "C" __global__ __launch_bounds__(512, 2) void kD(char* __restrict__ ws, u16* __restrict__ out) {
  extern __shared__ __attribute__((aligned(16))) char smem[];
  const int tid = threadIdx.x, lane = tid & 63;
  const int w = __builtin_amdgcn_readfirstlane(tid >> 6);
  const int xcd = blockIdx.x & 7, rank = blockIdx.x >> 3;
  const int it = 8 * (xcd >> 1) + (rank & 7), dg = 4 * (xcd & 1) + (rank >> 3);
  if (w == 0) STAMP(3, 0);
  if (w >= 4) {
    const int w4 = w - 4;
    const char* wp = ws + OFF_WP2 + (size_t)dg * (54 * 5120);
    if (w4 < 3) d_loader<4>(smem, wp, w4, lane);
    else d_loader<3>(smem, wp, w4, lane);
    return;
  }
  const int fr = lane & 15, g = lane >> 4;
  const int t = 64 * it + 16 * w + fr;
  const char* hp = ws + OFF_HH + (size_t)(4 * it + w) * (54 * 1024);
  const unsigned voff = lane * 16;
  u4_t R[18];
#pragma unroll
  for (int i = 0; i < 9; ++i)
    asm volatile("global_load_dwordx4 %0, %1, %2" : "=v"(R[i]) : "v"(voff), "s"(hp + (size_t)i * 1024) : "memory");
  u2_t res[5]; unsigned st;
  f4_t acc[5];
#pragma unroll
  for (int dt = 0; dt < 5; ++dt) acc[dt] = f4_t{0.f, 0.f, 0.f, 0.f};
  b8_t A0[5], A1[5];
  auto rdA = [&](b8_t (&A)[5], unsigned off) {
    const char* sp = smem + off + lane * 16;
#pragma unroll
    for (int dt = 0; dt < 5; ++dt) A[dt] = *(const b8_t*)(sp + dt * 1024);
  };
  bar();
  if (w == 0) STAMP(3, 1);
  rdA(A0, 0);
  unsigned so = 0;
  auto kstep = [&](auto mode, auto idx, b8_t (&Ac)[5], b8_t (&An)[5], const char* hnext) {
    constexpr int MODE = decltype(mode)::value, i = decltype(idx)::value, kk = i % 3;
    if (kk == 0) bar();
    unsigned nx = so + (kk + 1) * 5120;
    if (kk == 2) { so += D_SLOT; if (so == D_NSLOT * D_SLOT) so = 0; nx = so; }
    SB;
    if (!(MODE == 2 && i == 17)) rdA(An, nx);
    constexpr int NV = (MODE == 0) ? (i < 9 ? 8 + i : 26 - i) : (MODE == 1) ? (i < 9 ? 25 - i : 17) : (i == 0 ? 17 : 23 - i);
    asm volatile("s_waitcnt vmcnt(%1)" : "+v"(R[i]) : "i"(NV) : "memory");
    const b8_t B = __builtin_bit_cast(b8_t, R[i]);
#pragma unroll
    for (int dt = 0; dt < 5; ++dt) acc[dt] = __builtin_amdgcn_mfma_f32_16x16x32_bf16(Ac[dt], B, acc[dt], 0, 0, 0);
    SB;
    if (MODE == 0 && i < 9)
      asm volatile("global_load_dwordx4 %0, %1, %2" : "=v"(R[i < 9 ? 9 + i : 0]) : "v"(voff), "s"(hp + (size_t)(9 + i) * 1024) : "memory");
    if (MODE != 2) asm volatile("global_load_dwordx4 %0, %1, %2" : "+v"(R[i]) : "v"(voff), "s"(hnext + (size_t)i * 1024) : "memory");
    if (MODE == 2 && i == 0) {
#pragma unroll
      for (int dt = 0; dt < 5; ++dt) {
        const int d = 80 * dg + 16 * dt + 4 * g;
        const char* rp = ws + OFF_H8 + ((size_t)(d >> 3) * SL + t) * 16 + (d & 4) * 2;
        asm volatile("global_load_dwordx2 %0, %1, off" : "=v"(res[dt]) : "v"(rp) : "memory");
      }
      asm volatile("global_load_dword %0, %1, off" : "=v"(st) : "v"(ws + OFF_G2 + (size_t)t * 8) : "memory");
    }
    SB;
  };
  auto round = [&](auto mode, const char* hnext) {
    kstep(mode, IC(0), A0, A1, hnext); kstep(mode, IC(1), A1, A0, hnext); kstep(mode, IC(2), A0, A1, hnext);
    kstep(mode, IC(3), A1, A0, hnext); kstep(mode, IC(4), A0, A1, hnext); kstep(mode, IC(5), A1, A0, hnext);
    kstep(mode, IC(6), A0, A1, hnext); kstep(mode, IC(7), A1, A0, hnext); kstep(mode, IC(8), A0, A1, hnext);
    kstep(mode, IC(9), A1, A0, hnext); kstep(mode, IC(10), A0, A1, hnext); kstep(mode, IC(11), A1, A0, hnext);
    kstep(mode, IC(12), A0, A1, hnext); kstep(mode, IC(13), A1, A0, hnext); kstep(mode, IC(14), A0, A1, hnext);
    kstep(mode, IC(15), A1, A0, hnext); kstep(mode, IC(16), A0, A1, hnext); kstep(mode, IC(17), A1, A0, hnext);
  };
  round(IC(0), hp + 18 * 1024);
  round(IC(1), hp + 36 * 1024);
  round(IC(2), hp);
  if (w == 0) STAMP(3, 2);
  asm volatile("s_waitcnt vmcnt(0)" : "+v"(res[0]), "+v"(res[1]), "+v"(res[2]), "+v"(res[3]), "+v"(res[4]), "+v"(st)::"memory");
  const float sd = bf_hi(st), r2 = 1.0f / (sd * sd);
#pragma unroll
  for (int dt = 0; dt < 5; ++dt) {
    const int d = 80 * dg + 16 * dt + 4 * g;
    const float h0 = bf_lo(res[dt].x), h1 = bf_hi(res[dt].x), h2 = bf_lo(res[dt].y), h3 = bf_hi(res[dt].y);
    st8(out + (size_t)t * DM + d, u2_t{pk_bf16(h0 + r2 * acc[dt][0], h1 + r2 * acc[dt][1]), pk_bf16(h2 + r2 * acc[dt][2], h3 + r2 * acc[dt][3])});
  }
  if (w == 0) STAMP(3, 7);
}

extern "C" void kernel_launch(void* const* d_in, const int* in_sizes, int n_in, void* d_out, int out_size, void* d_ws,
                              size_t ws_size, hipStream_t stream) {
  char* ws = (char*)d_ws;
  (void)hipFuncSetAttribute((const void*)kB, hipFuncAttributeMaxDynamicSharedMemorySize, B_LDS);
  (void)hipFuncSetAttribute((const void*)kC, hipFuncAttributeMaxDynamicSharedMemorySize, C_LDS);
  (void)hipFuncSetAttribute((const void*)kD, hipFuncAttributeMaxDynamicSharedMemorySize, D_LDS);
  kB<<<256, 512, B_LDS, stream>>>((const u16*)d_in[0], (const u16*)d_in[1], (const u16*)d_in[2], (const u16*)d_in[3], (const u16*)d_in[4],
                                  (const u16*)d_in[5], (const u16*)d_in[6], (const u16*)d_in[7], (const u16*)d_in[8], (const u16*)d_in[10],
                                  (const u16*)d_in[11], (const u16*)d_in[12], (const u16*)d_out, ws);
  kC<<<256, 512, C_LDS, stream>>>((const u16*)d_in[10], (const u16*)d_in[11], ws);
  kD<<<256, 512, D_LDS, stream>>>(ws, (u16*)d_out);
}
```

```cpp
#define STAMPS
#define PROBE 9
#define PROBE_M 4
#include <hip/hip_runtime.h>
#include <hip/hip_bf16.h>
#include <stdint.h>
#include <type_traits>

typedef unsigned short u16;
typedef _Float16 h2_t __attribute__((ext_vector_type(2)));
typedef _Float16 h8_t __attribute__((ext_vector_type(8)));
typedef __bf16 b2_t __attribute__((ext_vector_type(2)));
typedef __bf16 b8_t __attribute__((ext_vector_type(8)));
typedef float f2_t __attribute__((ext_vector_type(2)));
typedef float f4_t __attribute__((ext_vector_type(4)));
typedef unsigned u2_t __attribute__((ext_vector_type(2)));
typedef unsigned u4_t __attribute__((ext_vector_type(4)));
typedef short s4_t __attribute__((ext_vector_type(4)));
typedef __attribute__((address_space(1))) unsigned long long gu64;
#define LAS __attribute__((address_space(3)))
#define SB __builtin_amdgcn_sched_barrier(0)
#define IC(n) std::integral_constant<int, n>{}

constexpr int SL = 2048;
constexpr int DM = 640;
constexpr int FF = 1728;

constexpr size_t OFF_G1 = 0;
constexpr size_t OFF_EP = 0x10000;
constexpr size_t OFF_RD = 0x10100;
constexpr size_t OFF_H8 = 0x5A0000;
constexpr size_t OFF_HH = 0x820000;
constexpr size_t OFF_WP2 = 0xF00000;
constexpr size_t OFF_CS = 0x1120000;
constexpr size_t OFF_G2 = 0x1130000;
constexpr int KRS = 1344;
constexpr int KDS = 4864;
constexpr int KD_O = 2240;
constexpr int KD_DR = 4384;

constexpr size_t OFF_DBG = 0x2000000;
#define STAMP(k, i) do { if ((threadIdx.x & 63) == 0) ((unsigned long long*)(ws + OFF_DBG))[((k) * 512 + blockIdx.x) * 16 + (i)] = __builtin_amdgcn_s_memrealtime(); } while (0)

__device__ __forceinline__ float bf_lo(unsigned d) { return __uint_as_float(d << 16); }
__device__ __forceinline__ float bf_hi(unsigned d) { return __uint_as_float(d & 0xffff0000u); }
__device__ __forceinline__ float bf2f(u16 v) { return __uint_as_float((unsigned)v << 16); }
__device__ __forceinline__ unsigned pk_bf16(float a, float b) {
  f2_t v = {a, b};
  return __builtin_bit_cast(unsigned, __builtin_convertvector(v, b2_t));
}
__device__ __forceinline__ unsigned pk_f16(float a, float b) {
  f2_t v = {a, b};
  return __builtin_bit_cast(unsigned, __builtin_convertvector(v, h2_t));
}
__device__ __forceinline__ float rbf(float a) { return bf_lo(pk_bf16(a, 0.f)); }
template <int N> __device__ __forceinline__ float dpp_ror_add(float v) {
  int r = __builtin_amdgcn_update_dpp(0, __float_as_int(v), 0x120 + N, 0xf, 0xf, false);
  return v + __int_as_float(r);
}
__device__ __forceinline__ float row16_sum(float v) {
  v = dpp_ror_add<8>(v); v = dpp_ror_add<4>(v); v = dpp_ror_add<2>(v); v = dpp_ror_add<1>(v);
  return v;
}
__device__ __forceinline__ float wave_sum(float v) {
  v = row16_sum(v);
  const int b = __float_as_int(v);
  return (__int_as_float(__builtin_amdgcn_readlane(b, 0)) + __int_as_float(__builtin_amdgcn_readlane(b, 16))) +
         (__int_as_float(__builtin_amdgcn_readlane(b, 32)) + __int_as_float(__builtin_amdgcn_readlane(b, 48)));
}
template <typename T> __device__ __forceinline__ T ldnt(const T* p) { return __builtin_nontemporal_load(p); }
__device__ __forceinline__ void wave_fence() {
  __builtin_amdgcn_fence(__ATOMIC_RELEASE, "wavefront");
  __builtin_amdgcn_wave_barrier();
  __builtin_amdgcn_fence(__ATOMIC_ACQUIRE, "wavefront");
}
__device__ __forceinline__ s4_t tr_read(const char* p) {
  return __builtin_amdgcn_ds_read_tr16_b64_v4i16((LAS s4_t*)p);
}
__device__ __forceinline__ void glds16(const void* sbase, unsigned voff, unsigned lds_addr) {
  asm volatile("s_mov_b32 m0, %2\n\ts_nop 0\n\tglobal_load_lds_dwordx4 %0, %1" ::"v"(voff), "s"(sbase), "s"(lds_addr) : "memory");
}
template <int N> __device__ __forceinline__ void wait_vm() { asm volatile("s_waitcnt vmcnt(%0)" ::"i"(N) : "memory"); }
__device__ __forceinline__ void bar() { asm volatile("s_barrier" ::: "memory"); }
__device__ __forceinline__ float dot2bf(unsigned a, unsigned b, float c) {
  return __builtin_amdgcn_fdot2_f32_bf16(__builtin_bit_cast(b2_t, a), __builtin_bit_cast(b2_t, b), c, false);
}

#define ST_MODE 0
__device__ __forceinline__ void st16(void* p, u4_t v) {
  *(u4_t*)p = v;
}
__device__ __forceinline__ void st8(void* p, u2_t v) {
  *(u2_t*)p = v;
}

struct Cw { f2_t w, ws; };
__device__ __forceinline__ Cw mkw(f2_t w) { Cw r; r.w = w; r.ws = f2_t{-w.y, w.x}; return r; }
__device__ __forceinline__ f2_t cmulw(f2_t a, const Cw& W) {
  return __builtin_elementwise_fma(f2_t{a.y, a.y}, W.ws, f2_t{a.x, a.x} * W.w);
}
__device__ __forceinline__ f2_t cexp_l(float er, float wr, float l) {
  float mag = __builtin_amdgcn_exp2f(er * l);
  float rev = __builtin_amdgcn_fractf(wr * l);
  return f2_t{mag * __builtin_amdgcn_cosf(rev), mag * __builtin_amdgcn_sinf(rev)};
}
__device__ __forceinline__ float pow2_scale(float bound, int target) {
  unsigned e = (__float_as_uint(bound) >> 23) & 0xff;
  int k = target - ((int)e - 126);
  k = k > 126 ? 126 : (k < -126 ? -126 : k);
  return (bound > 0.f) ? __uint_as_float((unsigned)(k + 127) << 23) : 1.0f;
}
__device__ __forceinline__ float pow2_inv(float p) { return __uint_as_float(0x7f000000u - __float_as_uint(p)); }

constexpr int B_SIDE = 96;
constexpr int KG_R = 0, KG_L0 = 4352, KG_L1 = 8704, KG_W = 8704 + 25 * 272;
constexpr int B_BND = 8 * KG_W;
constexpr int B_KC = B_BND + 64;
constexpr int B_LDS = B_KC + 4 * KRS;
constexpr int B_KOFF = 0;
constexpr int B_UOFF = 4 * KDS;
constexpr int B_UCH = 4608;
constexpr int B_YCH = 10240;
constexpr int B_PACK = 40960;

__device__ __forceinline__ void pack_wd(const u16* __restrict__ wd, char* __restrict__ ws, char* buf, int u, int lane) {
  const int ks = u >> 3, dg = u & 7;
#pragma unroll
  for (int i = 0; i < 5; ++i) {
    const int idx = i * 64 + lane, row = idx / 10, c = idx % 10;
    __builtin_amdgcn_global_load_lds((const unsigned*)((const char*)wd + (size_t)(32 * ks + row) * 1280 + 160 * dg + 16 * c),
                                     (LAS unsigned*)(buf + i * 1024), 16, 0, 2);
  }
  asm volatile("s_waitcnt vmcnt(0)" ::: "memory");
  wave_fence();
  const int g = lane >> 4, q = (lane & 15) >> 2, p = lane & 3;
  char* dst = ws + OFF_WP2 + ((size_t)(dg * 54 + ks) * 5) * 1024 + lane * 16;
#pragma unroll
  for (int dt = 0; dt < 5; ++dt) {
    s4_t aa = tr_read(buf + (4 * g + q) * 160 + 32 * dt + 8 * p);
    s4_t bb = tr_read(buf + (16 + 4 * g + q) * 160 + 32 * dt + 8 * p);
    u2_t ua = __builtin_bit_cast(u2_t, aa), ub = __builtin_bit_cast(u2_t, bb);
    st16(dst + dt * 1024, u4_t{ua.x, ua.y, ub.x, ub.y});
  }
}

struct BFrag { h8_t B[8]; u2_t A[5][2]; };
__device__ __forceinline__ void mfma_f16(f4_t& d, u4_t a, h8_t b) {
  asm volatile("v_mfma_f32_16x16x32_f16 %0, %1, %2, %0" : "+v"(d) : "v"(a), "v"(b));
}
__device__ __forceinline__ void mfma_f16_0(f4_t& d, u4_t a, h8_t b) {
  asm volatile("v_mfma_f32_16x16x32_f16 %0, %1, %2, 0" : "=&v"(d) : "v"(a), "v"(b));
}
__device__ __forceinline__ unsigned ln_fin(float mean, float var) {
  const float mb = rbf(mean), sd = rbf(sqrtf(rbf(rbf(var) + 1e-5f)));
  return (__float_as_uint(mb) >> 16) | (__float_as_uint(sd) & 0xffff0000u);
}
__device__ __forceinline__ unsigned ln1_row(const u16* __restrict__ x, int t) {
  const u4_t* p = (const u4_t*)(x + (size_t)t * DM);
  float s = 0.f, q = 0.f;
  for (int i = 0; i < 80; ++i) { const u4_t v = p[i]; for (int j = 0; j < 4; ++j) s += bf_lo(v[j]) + bf_hi(v[j]); }
  const float mean = s * (1.0f / DM);
  for (int i = 0; i < 80; ++i) {
    const u4_t v = p[i];
    for (int j = 0; j < 4; ++j) { const float a = bf_lo(v[j]) - mean, b = bf_hi(v[j]) - mean; q += a * a; q += b * b; }
  }
  return ln_fin(mean, q * (1.0f / DM));
}
constexpr int SPIN_MAX = 1 << 14;
#define TLBG 524288

extern "C" __global__ __launch_bounds__(512) void kB(const u16* __restrict__ x, const u16* __restrict__ Are, const u16* __restrict__ Aim,
                                                     const u16* __restrict__ Bre, const u16* __restrict__ Bim, const u16* __restrict__ Cre,
                                                     const u16* __restrict__ Cim, const u16* __restrict__ logdt, const u16* __restrict__ dskip,
                                                     const u16* __restrict__ wg, const u16* __restrict__ wu, const u16* __restrict__ wd,
                                                     const u16* __restrict__ outp, char* __restrict__ ws) {
  extern __shared__ __attribute__((aligned(16))) char smem[];
  const int tid = threadIdx.x, lane = tid & 63;
  const int w = __builtin_amdgcn_readfirstlane(tid >> 6);
  const int xcd = blockIdx.x & 7;
  u2_t ep = {0u, 0u};
  if (w == 0) asm volatile("global_load_dwordx2 %0, %1, off sc1" : "=v"(ep) : "v"(ws + OFF_EP) : "memory");
  if (w == 0) STAMP(1, 0);
  auto post_epoch = [&](int off) {
    if (tid == 0) {
      const unsigned long long e = (((unsigned long long)ep.y << 32) | ep.x) + 1ull;
      *(unsigned long long*)(smem + off) = e;
      __hip_atomic_store((gu64*)(ws + OFF_RD) + blockIdx.x, e, __ATOMIC_RELAXED, __HIP_MEMORY_SCOPE_AGENT);
    }
  };
  if (blockIdx.x < B_SIDE) {
    const int rk = blockIdx.x >> 3;
    const int g4 = xcd >> 1, a = xcd & 1, nl = (g4 < 3) ? 7 : 6;
    constexpr int S_EP = 17408;
    unsigned pf[2] = {0u, 0u};
    if (rk >= 8) {
#pragma unroll
      for (int k = 0; k < 2; ++k) {
        const int id = (((rk - 8) * 8 + w) * 2 + k) * 64 + lane;
        asm volatile("global_load_dword %0, %1, off" : "=v"(pf[k]) : "v"((const char*)x + (size_t)(id >> 1) * (DM * 2) + (((160 * xcd) >> 7) + (id & 1)) * 128) : "memory");
      }
    }
    if (rk < 8) {
      const int r = lane >> 4, c = lane & 15, t = (blockIdx.x * 8 + w) * 4 + r;
      const u4_t* p = (const u4_t*)(x + (size_t)t * DM);
      u4_t v[5];
#pragma unroll
      for (int i = 0; i < 5; ++i) v[i] = ldnt(p + c + 16 * i);
      float s = 0.f;
#pragma unroll
      for (int i = 0; i < 5; ++i)
#pragma unroll
        for (int j = 0; j < 4; ++j) s += bf_lo(v[i][j]) + bf_hi(v[i][j]);
      asm volatile("" : "+v"(ep) : "v"(s));
      post_epoch(S_EP);
      const float mean = row16_sum(s) * (1.0f / DM);
      float q = 0.f;
#pragma unroll
      for (int i = 0; i < 5; ++i)
#pragma unroll
        for (int j = 0; j < 4; ++j) {
          const float aa = bf_lo(v[i][j]) - mean, bb = bf_hi(v[i][j]) - mean;
          q += aa * aa; q += bb * bb;
        }
      const unsigned sv = ln_fin(mean, row16_sum(q) * (1.0f / DM));
      __syncthreads();
      const u2_t e = *(const u2_t*)(smem + S_EP);
      if (c == 0) {
        const u4_t g = {sv, e.x, sv, e.y};
        asm volatile("global_store_dwordx4 %0, %1, off sc1\n\ts_nop 1" ::"v"(ws + OFF_G1 + (size_t)t * 16), "v"(g) : "memory");
        ((unsigned long long*)(ws + OFF_G2))[t] = 0ull;
      }
      if (w == 0) STAMP(1, 1);
      if (rk == 4 && w == 0) {
        unsigned long long a0, a1;
        asm volatile("s_getpc_b64 s[20:21]\n\ts_add_u32 s20, s20, kB@rel32@lo+4\n\ts_addc_u32 s21, s21, kB@rel32@hi+12\n\ts_mov_b64 %0, s[20:21]" : "=s"(a0) :: "s20", "s21", "scc");
        asm volatile("s_getpc_b64 s[20:21]\n\ts_add_u32 s20, s20, kD@rel32@lo+4\n\ts_addc_u32 s21, s21, kD@rel32@hi+12\n\ts_mov_b64 %0, s[20:21]" : "=s"(a1) :: "s20", "s21", "scc");
        const unsigned long long lo = a0 < a1 ? a0 : a1, last = ((a0 < a1 ? a1 - a0 : a0 - a1) + 8192) & ~127ull;
        unsigned cf[8];
#pragma unroll
        for (int i = 0; i < 8; ++i) {
          const unsigned long long o = (unsigned long long)(lane + 64 * i) * 128;
          asm volatile("global_load_dword %0, %1, off" : "=v"(cf[i]) : "v"(lo + (o < last ? o : last)) : "memory");
        }
        asm volatile("s_waitcnt vmcnt(0)" ::"v"(cf[0]), "v"(cf[1]), "v"(cf[2]), "v"(cf[3]), "v"(cf[4]), "v"(cf[5]), "v"(cf[6]), "v"(cf[7]) : "memory");
      }
      if (rk == 4 && w == 1) {
        constexpr size_t G = TLBG;
        constexpr int NWS = (0x1150000 + G - 1) / G, NW = (DM * FF * 2 + G - 1) / G;
        unsigned tf[3];
        asm volatile("global_load_dword %0, %1, off nt" : "=v"(tf[0]) : "v"(ws + min((size_t)lane * G, (size_t)0x1150000 - 4)) : "memory");
        const char* wsel = (const char*)((lane & 1) ? wu : wg);
        asm volatile("global_load_dword %0, %1, off nt" : "=v"(tf[1]) : "v"(wsel + min((size_t)(lane >> 1) * G, (size_t)DM * FF * 2 - 4)) : "memory");
        asm volatile("global_load_dword %0, %1, off nt" : "=v"(tf[2]) : "v"((const char*)outp + min((size_t)lane * G, (size_t)SL * DM * 2 - 4)) : "memory");
        static_assert(NWS <= 64 && NW <= 32, "one load per buffer group");
        asm volatile("s_waitcnt vmcnt(0)" ::"v"(tf[0]), "v"(tf[1]), "v"(tf[2]) : "memory");
      }
      if (blockIdx.x == 63 && w == 7) {
        const unsigned long long e64 = ((unsigned long long)e.y << 32) | e.x;
        for (int spin = 0; spin < SPIN_MAX; ++spin) {
          bool ok = true;
#pragma unroll
          for (int i = 0; i < 4; ++i) ok &= __hip_atomic_load((const gu64*)(ws + OFF_RD) + lane + 64 * i, __ATOMIC_RELAXED, __HIP_MEMORY_SCOPE_AGENT) == e64;
          if (__all(ok)) break;
          __builtin_amdgcn_s_sleep(4);
        }
        if (lane == 0) __hip_atomic_store((gu64*)(ws + OFF_EP), e64, __ATOMIC_RELAXED, __HIP_MEMORY_SCOPE_AGENT);
      }
    }
    const int strip = (rk >= 8) ? rk - 8 : 4 + rk;
    if ((rk >= 8 || rk < 3) && strip < nl) {
      const int jl = 7 * g4 + strip;
      const char* pm = (const char*)(a ? wu : wg) + (size_t)(80 * w + (lane >> 3)) * (FF * 2) + 128 * jl + (lane & 7) * 16;
      u4_t v[10];
#pragma unroll
      for (int i = 0; i < 10; ++i) v[i] = ldnt((const u4_t*)(pm + (size_t)i * 8 * (FF * 2)));
      const char* po = (const char*)(a ? wg : wu) + (size_t)(80 * w) * (FF * 2) + 128 * jl;
      unsigned t0v, t1v;
      asm volatile("global_load_dword %0, %1, off nt" : "=v"(t0v) : "v"(po + (size_t)lane * (FF * 2)) : "memory");
      asm volatile("global_load_dword %0, %1, off nt" : "=v"(t1v) : "v"(po + (size_t)(64 + (lane & 15)) * (FF * 2)) : "memory");
      f2_t s[4];
#pragma unroll
      for (int e = 0; e < 4; ++e) s[e] = f2_t{0.f, 0.f};
#pragma unroll
      for (int i = 0; i < 10; ++i)
#pragma unroll
        for (int jj = 0; jj < 4; ++jj) s[jj] += f2_t{bf_lo(v[i][jj]), bf_hi(v[i][jj])};
      if (rk >= 8) {
        asm volatile("" : "+v"(ep) : "v"(s[0]));
#pragma unroll
        for (int i = 0; i < 2; ++i) asm volatile("" : "+v"(pf[i]) : "v"(s[0]));
        post_epoch(S_EP);
      }
      float* pp = (float*)smem;
      *(f4_t*)(pp + (w * 8 + (lane >> 3)) * 68 + (lane & 7) * 8) = f4_t{s[0].x, s[0].y, s[1].x, s[1].y};
      *(f4_t*)(pp + (w * 8 + (lane >> 3)) * 68 + (lane & 7) * 8 + 4) = f4_t{s[2].x, s[2].y, s[3].x, s[3].y};
      __syncthreads();
      float acc = 0.f;
#pragma unroll
      for (int i = 0; i < 8; ++i) acc += pp[(w * 8 + i) * 68 + lane];
      __syncthreads();
      pp[w * 64 + lane] = acc;
      __syncthreads();
      if (w == 0) {
        float t = 0.f;
#pragma unroll
        for (int i = 0; i < 8; ++i) t += pp[i * 64 + lane];
        ((float*)(ws + OFF_CS))[a * FF + 64 * jl + lane] = t;
      }
      asm volatile("s_waitcnt vmcnt(0)" ::"v"(t0v), "v"(t1v) : "memory");
    }
    if (w == 0) STAMP(1, 7);
    return;
  }
  const int cb = blockIdx.x - B_SIDE;
  const int q = 20 * xcd + (cb >> 3);
  const int cl = w >> 1, hw = w & 1, ch = 4 * q + cl;
  const int n = ch * 128 + hw * 64 + lane;
  const u16 i_are = ldnt(Are + n), i_aim = ldnt(Aim + n), i_bre = ldnt(Bre + n), i_bim = ldnt(Bim + n), i_cre = ldnt(Cre + n), i_cim = ldnt(Cim + n);
  const u16 i_dt = logdt[ch];
  u2_t xg[4];
  const unsigned xoff = tid * (2 * DM * 2);
  const char* xb = (const char*)(x + 4 * q);
#define XG(i) asm volatile("global_load_dwordx2 %0, %1, %2 offset:%3" : "=v"(xg[i]) : "v"(xoff), "s"(xb + ((i) >> 1) * (size_t)(1024 * DM * 2)), "i"(((i) & 1) * DM * 2) : "memory")
  const u2_t dkv = *(const u2_t*)(dskip + 4 * q);
  const int c = lane & 15, kg = lane >> 4;
  h8_t band9, band7;
#pragma unroll
  for (int j = 0; j < 8; ++j) {
    const int k = 8 * kg + 7 - j;
    band9[j] = (k >= c + 4 && k <= c + 12) ? (_Float16)1.0f : (_Float16)0.0f;
    band7[j] = (k >= c + 5 && k <= c + 11) ? (_Float16)1.0f : (_Float16)0.0f;
  }
  asm volatile("" : "+v"(band9), "+v"(band7));
  char* hb = smem + w * KG_W;
  float Sw, bsum;
  {
    const float dt = rbf(expf(bf2f(i_dt)));
    const float are = dt * bf2f(i_are), aim = dt * bf2f(i_aim);
    const float bre = dt * bf2f(i_bre), bim = dt * bf2f(i_bim);
    const float cre = bf2f(i_cre), cim = bf2f(i_cim);
    f2_t c0 = {cre * bre - cim * bim, cre * bim + cim * bre};
    const float er = are * 1.44269504088896341f, wr = aim * 0.15915494309189535f;
    const float bnd = (fabsf(c0.x) + fabsf(c0.y)) * fmaxf(1.0f, __builtin_amdgcn_exp2f(er * 2048.f));
    bsum = wave_sum(bnd);
    asm volatile("" : "+v"(ep) : "v"(bnd));
    post_epoch(B_BND + 48);
    if (lane == 0) ((float*)(smem + B_BND))[w] = bsum;
    if (w == 0) STAMP(1, 1);
    Sw = pow2_scale(bsum, 15);
    c0 *= Sw;
    const Cw z1 = mkw(cexp_l(er, wr, 1.f)), w8 = mkw(cexp_l(er, -wr, 8.f)), w128 = mkw(cexp_l(er, wr, 128.f));
    const Cw z2 = mkw(cmulw(z1.w, z1));
    const Cw z4 = mkw(cmulw(z2.w, z2));
    auto put = [&](int off, int row, f2_t v) { *(unsigned*)(hb + off + row * 272 + 4 * lane) = pk_f16(v.x, v.y); };
    f2_t R = w8.w, v0 = c0, v1 = cmulw(c0, w128), T = v1;
    *(unsigned*)(hb + KG_R + 4 * lane) = pk_f16(1.f, 0.f);
#pragma unroll
    for (int i = 0; i < 8; ++i) {
      SB;
      if ((w & 3) == (i & 3)) { if (i < 4) XG(0); else XG(1); }
      SB;
      *(unsigned*)(hb + KG_R + (2 * i + 1) * 272 + 4 * lane) = pk_f16(R.x, R.y);
      if (i < 7) {
        R = cmulw(R, w8);
        *(unsigned*)(hb + KG_R + (2 * i + 2) * 272 + 4 * lane) = pk_f16(R.x, R.y);
        R = cmulw(R, w8);
      }
      put(KG_L0, i, v0); put(KG_L0, 8 + i, v1);
      if (i < 7) { v0 = cmulw(v0, z1); v1 = cmulw(v1, z1); }
#pragma unroll
      for (int k = 0; k < 2; ++k) {
        const int i1 = 2 + 2 * i + k;
        if (i1 <= 16) {
          T = cmulw(T, w128);
          if (i1 < 4) {
            f2_t v = T;
#pragma unroll
            for (int i0 = 0; i0 < 4; ++i0) { put(KG_L1, 4 * (i1 - 2) + i0, v); if (i0 < 3) v = cmulw(v, z2); }
          } else if (i1 < 8) {
            put(KG_L1, 8 + 2 * (i1 - 4), T); put(KG_L1, 8 + 2 * (i1 - 4) + 1, cmulw(T, z4));
          } else {
            put(KG_L1, 16 + (i1 - 8), T);
          }
        }
      }
    }
  }
  SB;
  if (w == 0) STAMP(1, 4);
  if (w < 4) XG(2);
  wave_fence();
  f4_t ka[3];
  {
    h8_t Bf[4];
#pragma unroll
    for (int ks = 0; ks < 4; ++ks) Bf[ks] = *(const h8_t*)(hb + KG_R + c * 272 + 64 * ks + 16 * kg);
#pragma unroll
    for (int t = 0; t < 3; ++t) ka[t] = f4_t{0.f, 0.f, 0.f, 0.f};
#pragma unroll
    for (int ks = 0; ks < 4; ++ks)
#pragma unroll
      for (int t = 0; t < 3; ++t) {
        const int off = (t == 0) ? KG_L0 + c * 272 : (t == 1) ? KG_L1 + c * 272 : KG_L1 + (16 + min(c, 8)) * 272;
        const h8_t Af = *(const h8_t*)(hb + off + 64 * ks + 16 * kg);
        ka[t] = __builtin_amdgcn_mfma_f32_16x16x32_f16(Af, Bf[ks], ka[t], 0, 0, 0);
      }
  }
  SB;
  if (w >= 4) XG(2);
  SB;
  const float iSw = pow2_inv(Sw);
  wave_fence();
  if (hw == 0) *(f4_t*)(hb + lane * 16) = ka[1] * iSw;
  else { *(f4_t*)(hb + lane * 16) = ka[0] * iSw; *(f4_t*)(hb + 1024 + lane * 16) = ka[2] * iSw; }
  __syncthreads();
  if (w == 0) STAMP(1, 5);
  const u2_t ep1 = *(const u2_t*)(smem + B_BND + 48);
  u4_t gr[2][2];
#pragma unroll
  for (int it = 0; it < 2; ++it) {
    const char* gp = ws + OFF_G1 + (size_t)(2 * (tid + 512 * it)) * 16;
    asm volatile("global_load_dwordx4 %0, %1, off sc1" : "=v"(gr[it][0]) : "v"(gp) : "memory");
    asm volatile("global_load_dwordx4 %0, %1, off offset:16 sc1" : "=v"(gr[it][1]) : "v"(gp) : "memory");
  }
  if (w < 4) XG(3);
  {
    const char* pb = smem + (w ^ 1) * KG_W;
    const float Sc = pow2_scale(((const float*)(smem + B_BND))[2 * cl] + ((const float*)(smem + B_BND))[2 * cl + 1], 15);
    char* kr = smem + B_KC + cl * KRS;
    if (hw == 0) {
      const f4_t k0 = (ka[0] * iSw + *(const f4_t*)(pb + lane * 16)) * Sc, k2 = (ka[2] * iSw + *(const f4_t*)(pb + 1024 + lane * 16)) * Sc;
      *(u2_t*)(kr + 2 * (128 * (kg >> 1) + 4 * (kg & 1) + 8 * c)) = u2_t{pk_f16(k0[0], k0[1]), pk_f16(k0[2], k0[3])};
      const unsigned p01 = pk_f16(k2[0], k2[1]), p23 = pk_f16(k2[2], k2[3]);
      u16* k3 = (u16*)kr + 512 + 64 * kg + c;
      if (kg < 2) { k3[0] = (u16)p01; k3[16] = (u16)(p01 >> 16); k3[32] = (u16)p23; k3[48] = (u16)(p23 >> 16); }
      else if (lane == 32) k3[0] = (u16)p01;
      if (lane == 0) ((float*)(smem + B_BND + 32))[cl] = pow2_inv(Sc);
    } else {
      const f4_t k1 = (ka[1] * iSw + *(const f4_t*)(pb + lane * 16)) * Sc;
      const unsigned p01 = pk_f16(k1[0], k1[1]), p23 = pk_f16(k1[2], k1[3]);
      if (kg < 2) {
        *(u2_t*)(kr + 2 * (256 + 64 * kg + 4 * c)) = u2_t{p01, p23};
      } else {
        *(unsigned*)(kr + 2 * (384 + 64 * (kg - 2) + 2 * c)) = p01;
        *(unsigned*)(kr + 2 * (384 + 64 * (kg - 2) + 32 + 2 * c)) = p23;
      }
    }
  }
  SB;
  if (w >= 4) XG(3);
  __syncthreads();
  const float invs = ((const float*)(smem + B_BND + 32))[w & 3];
  {
    const int kch = tid >> 7, kc = tid & 127;
    const char* kr = smem + B_KC + kch * KRS;
    const int e0 = (kc < 32) ? 8 * kc : (kc < 64) ? 256 + 4 * (kc - 32) : 384 + 2 * (kc - 64);
    const unsigned* kp = (const unsigned*)(kr + 2 * e0);
    const unsigned d0 = kp[0], d1 = kp[1], d2 = kp[2], d3 = kp[3], d4 = kp[4];
    const unsigned kfirst = *(const u16*)kr, k1024 = *(const u16*)(kr + 2 * 512);
    const int dl = 127 - kc;
    const unsigned kfa = *(const u16*)(kr + 2 * (512 + max(2 * dl - 127, 0))), kfb = *(const u16*)(kr + 2 * (512 + max(2 * dl - 126, 0)));
    char* ki = smem + B_KOFF + kch * KDS;
    const unsigned k0 = d0 & 0xffffu;
    unsigned o0, o1, o2, o3, nx;
    if (kc < 32) { o0 = d0; o1 = d1; o2 = d2; o3 = d3; nx = d4; }
    else if (kc < 64) {
      const unsigned k2 = d0 >> 16, k4 = d1 & 0xffffu, k6 = d1 >> 16, k8 = d2 & 0xffffu;
      o0 = k0 * 0x10001u; o1 = k2 | (k4 << 16); o2 = k4 * 0x10001u; o3 = k6 | (k8 << 16); nx = k8;
    } else {
      const unsigned k4 = d0 >> 16, k8 = d1 & 0xffffu;
      o0 = k0 * 0x10001u; o1 = k0 | (k4 << 16); o2 = k4 * 0x10001u; o3 = k8 * 0x10001u; nx = k8;
    }
    *(u4_t*)(ki + 16 * (kc + 2)) = u4_t{o0, o1, o2, o3};
    *(u4_t*)(ki + KD_O + 16 * (kc + 2)) = u4_t{__builtin_amdgcn_alignbit(o1, o0, 16), __builtin_amdgcn_alignbit(o2, o1, 16),
                                              __builtin_amdgcn_alignbit(o3, o2, 16), __builtin_amdgcn_alignbit(nx, o3, 16)};
    if (kc < 6) {
      const int cp = (kc < 2) ? kc : 128 + kc;
      const unsigned kk = k1024 * 0x10001u;
      const u4_t e = (kc == 2) ? u4_t{kk, kk, k1024, 0u} : u4_t{0u, 0u, 0u, 0u};
      const u4_t o = (kc == 2) ? u4_t{kk, kk, 0u, 0u} : u4_t{0u, 0u, 0u, (kc == 1) ? (kfirst << 16) : 0u};
      *(u4_t*)(ki + 16 * cp) = e;
      *(u4_t*)(ki + KD_O + 16 * cp) = o;
    }
    if (kc < 80) *(unsigned*)(ki + KD_DR + 4 * kc) = (dl >= 64) ? (kfa | (kfb << 16)) : 0u;
  }
  *(unsigned*)(smem + B_UOFF + (tid >> 7) * B_UCH + 4096 + 4 * (tid & 127)) = 0u;
  asm volatile("s_waitcnt vmcnt(0)" : "+v"(gr[0][0]), "+v"(gr[0][1]), "+v"(gr[1][0]), "+v"(gr[1][1]), "+v"(xg[0]), "+v"(xg[1]), "+v"(xg[2]), "+v"(xg[3])::"memory");
  {
    auto valid = [&](const u4_t& g) { return g[1] == ep1.x && g[3] == ep1.y && g[0] == g[2]; };
    for (int spin = 0; spin < SPIN_MAX; ++spin) {
      if (__all(valid(gr[0][0]) && valid(gr[0][1]) && valid(gr[1][0]) && valid(gr[1][1]))) break;
      __builtin_amdgcn_s_sleep(2);
#pragma unroll
      for (int it = 0; it < 2; ++it) {
        const char* gp = ws + OFF_G1 + (size_t)(2 * (tid + 512 * it)) * 16;
        asm volatile("global_load_dwordx4 %0, %1, off sc1" : "=v"(gr[it][0]) : "v"(gp) : "memory");
        asm volatile("global_load_dwordx4 %0, %1, off offset:16 sc1" : "=v"(gr[it][1]) : "v"(gp) : "memory");
      }
      asm volatile("s_waitcnt vmcnt(0)" : "+v"(gr[0][0]), "+v"(gr[0][1]), "+v"(gr[1][0]), "+v"(gr[1][1])::"memory");
    }
#pragma unroll
    for (int it = 0; it < 2; ++it)
#pragma unroll
      for (int r = 0; r < 2; ++r)
        if (!valid(gr[it][r])) gr[it][r][0] = ln1_row(x, 2 * (tid + 512 * it) + r);
  }
  if (w == 0) STAMP(1, 6);
  const float dk[4] = {bf_lo(dkv.x), bf_hi(dkv.x), bf_lo(dkv.y), bf_hi(dkv.y)};
  float z[2][8];
#pragma unroll
  for (int it = 0; it < 2; ++it) {
    const int j = tid + 512 * it;
    const unsigned s0 = gr[it][0][0], s1 = gr[it][1][0];
    const float m0 = bf_lo(s0), r0 = __builtin_amdgcn_rcpf(bf_hi(s0)), m1 = bf_lo(s1), r1 = __builtin_amdgcn_rcpf(bf_hi(s1));
    const u2_t xa = xg[2 * it], xc = xg[2 * it + 1];
    const float x0[4] = {bf_lo(xa.x), bf_hi(xa.x), bf_lo(xa.y), bf_hi(xa.y)};
    const float x1[4] = {bf_lo(xc.x), bf_hi(xc.x), bf_lo(xc.y), bf_hi(xc.y)};
#pragma unroll
    for (int cc = 0; cc < 4; ++cc) {
      const float u0 = (x0[cc] - m0) * r0, u1 = (x1[cc] - m1) * r1;
      *(unsigned*)(smem + B_UOFF + cc * B_UCH + 4 * (1023 - j)) = pk_f16(u1, u0);
      z[it][cc] = x0[cc] + u0 * dk[cc];
      z[it][4 + cc] = x1[cc] + u1 * dk[cc];
    }
  }
  __syncthreads();
  if (w == 0) STAMP(1, 2);
  if (w >= 4 && w < 7 && cb * 3 + (w - 4) < 432) pack_wd(wd, ws, smem + B_PACK + (w - 4) * 5120, cb * 3 + (w - 4), lane);
  f4_t acc[8];
  if (w < 4) {
    const char* ub = smem + B_UOFF + w * B_UCH + 4112 - 32 * c - 16 * kg - 512 * 7;
    const char* kb = smem + B_KOFF + w * KDS + ((c & 1) ? 0 : KD_O) + 4 * (12 + ((c + 1) >> 1) - 4 * kg);
    const unsigned* dr = (const unsigned*)(smem + B_KOFF + w * KDS + KD_DR) + (127 - c + 4 * kg);
    const char* uw = smem + B_UOFF + w * B_UCH + 4112 - 32 * c - 16 * kg;
    auto rdB = [&](BFrag& F, int i0, int p) { F.B[p] = *(const h8_t*)(ub + 512 * (7 - p) + 64 * i0); };
    auto rdA = [&](BFrag& F, int i0, int i1, int half) {
      const unsigned* ap = (const unsigned*)(kb + 64 * i0 + 512 * i1) + 2 * half;
      F.A[i1][half] = u2_t{ap[0], ap[1]};
    };
    auto frag = [&](const u2_t (&a)[2]) { return u4_t{a[0].x, a[0].y, a[1].x, a[1].y}; };
    auto rdnext = [&](BFrag& F, int i0, int m) {
      if (m < 8) rdB(F, i0, m); else if (m < 16) rdA(F, i0, (m - 8) >> 1, (m - 8) & 1);
    };
    auto step = [&](auto i0c, const BFrag& F, BFrag& N, auto nextc) {
      constexpr int I0 = decltype(i0c)::value, NX = decltype(nextc)::value;
      int m = 0;
#pragma unroll
      for (int i1 = 0; i1 < 5; ++i1) {
        if (i1 == 4 && I0 != 0) continue;
        const u4_t a = frag(F.A[i1]);
#pragma unroll
        for (int t = i1; t < 8; ++t) {
          if (I0 == 0 && i1 == 0) mfma_f16_0(acc[t], a, F.B[t]); else mfma_f16(acc[t], a, F.B[t - i1]);
          if (NX >= 0) rdnext(N, NX, m);
          SB;
          ++m;
        }
      }
    };
    BFrag F0, F1;
    h8_t U9[4], U7[4];
#pragma unroll
    for (int bt = 0; bt < 4; ++bt) { U9[bt] = *(const h8_t*)(uw - 512 * bt + 16); U7[bt] = *(const h8_t*)(uw - 512 * bt); }
#pragma unroll
    for (int m = 0; m < 16; ++m) rdnext(F0, 0, m);
    rdA(F0, 0, 4, 0); rdA(F0, 0, 4, 1);
    SB;
    f4_t X9[4], X7[4];
#pragma unroll
    for (int bt = 0; bt < 4; ++bt) { mfma_f16_0(X9[bt], __builtin_bit_cast(u4_t, U9[bt]), band9); mfma_f16_0(X7[bt], __builtin_bit_cast(u4_t, U7[bt]), band7); }
    SB;
    step(IC(0), F0, F1, IC(1));
    h8_t Vf[4]; u4_t Tf[4];
    {
      int m = 0;
#pragma unroll
      for (int i1 = 0; i1 < 4; ++i1) {
        const u4_t a = frag(F1.A[i1]);
#pragma unroll
        for (int t = i1; t < 8; ++t) {
          mfma_f16(acc[t], a, F1.B[t - i1]);
          rdnext(F0, 2, m);
          if (m < 4) {
            const u4_t vu = {pk_f16(X7[m][0], X9[m][0]), pk_f16(X7[m][1], X9[m][1]), pk_f16(X7[m][2], X9[m][2]), pk_f16(X7[m][3], X9[m][3])};
            Vf[m] = __builtin_bit_cast(h8_t, vu);
          }
          if (m >= 16 && m < 20) { const unsigned* tp = dr - 16 * (m - 16 + 4); Tf[m - 16] = u4_t{tp[0], tp[1], tp[2], tp[3]}; }
          SB;
          ++m;
        }
      }
    }
    step(IC(2), F0, F1, IC(3));
#pragma unroll
    for (int bt = 0; bt < 4; ++bt)
#pragma unroll
      for (int at = 4 + bt; at < 8; ++at) mfma_f16(acc[at], __builtin_bit_cast(u4_t, Vf[bt]), __builtin_bit_cast(h8_t, Tf[at - bt - 4]));
    SB;
    step(IC(3), F1, F0, IC(4));
    step(IC(4), F0, F1, IC(5));
    step(IC(5), F1, F0, IC(6));
    step(IC(6), F0, F1, IC(7));
    step(IC(7), F1, F0, IC(-1));
    asm volatile("s_nop 7\n\ts_nop 1" : "+v"(acc[0]), "+v"(acc[1]), "+v"(acc[2]), "+v"(acc[3]), "+v"(acc[4]), "+v"(acc[5]), "+v"(acc[6]), "+v"(acc[7]));
  }
  if (w == 0) STAMP(1, 3);
  __syncthreads();
  if (w < 4) {
#pragma unroll
    for (int t = 0; t < 8; ++t) *(f4_t*)(smem + w * B_YCH + t * 1280 + kg * 320 + c * 16) = acc[t] * invs;
  }
  __syncthreads();
#pragma unroll
  for (int it = 0; it < 2; ++it) {
    const int j = tid + 512 * it, t = 2 * j;
    const int yo = (t >> 8) * 1280 + ((t >> 2) & 3) * 320 + ((t >> 4) & 15) * 16 + (t & 3) * 4;
    float h0[4], h1[4];
#pragma unroll
    for (int cc = 0; cc < 4; ++cc) {
      f2_t y = *(const f2_t*)(smem + cc * B_YCH + yo);
      h0[cc] = z[it][cc] + y.x;
      h1[cc] = z[it][4 + cc] + y.y;
    }
    char* dst = ws + OFF_H8 + ((size_t)(q >> 1) * SL + t) * 16 + (q & 1) * 8;
    st8(dst, u2_t{pk_bf16(h0[0], h0[1]), pk_bf16(h0[2], h0[3])});
    st8(dst + 16, u2_t{pk_bf16(h1[0], h1[1]), pk_bf16(h1[2], h1[3])});
  }
  if (w == 0) STAMP(1, 7);
}

constexpr int C_T = 240;
constexpr int C_SLOT = 23552, C_NSLOT = 6, C_GOFF = 15360;
constexpr int C_STAT = C_NSLOT * C_SLOT;
constexpr int C_CS = C_STAT + 960;
constexpr int C_LDS = C_CS + 512;
constexpr int C_STEPS = 20;
constexpr size_t OFF_DUMMY = 0x1140000;

__device__ __forceinline__ unsigned ln_pack(float sum, float sq) {
  const float mean = sum * (1.0f / DM);
  return ln_fin(mean, fmaxf(sq * (1.0f / DM) - mean * mean, 0.f));
}
__device__ __forceinline__ unsigned ln_row(const char* h8, int t) {
  float s = 0.f, q = 0.f;
  for (int pl = 0; pl < 80; ++pl) {
    const u4_t v = *(const u4_t*)(h8 + (size_t)pl * 32768 + t * 16);
    for (int j = 0; j < 4; ++j) { const float a = bf_lo(v[j]), b = bf_hi(v[j]); s += a + b; q += a * a + b * b; }
  }
  return ln_pack(s, q);
}

template <int NDMA>
__device__ __forceinline__ void c_loader(char* smem, char* ws, const char* wbase, int w4, int lane, int t0, int jline, int xcd) {
  const unsigned lds0 = (unsigned)(size_t)(LAS char*)smem;
  const char* h8 = ws + OFF_H8;
  unsigned voffH[4], voffW[2];
#pragma unroll
  for (int k = 0; k < 2; ++k) {
    const int r = 8 * (2 * (w4 & 1) + k) + (lane >> 3), c = lane & 7;
    const int f = ((r >> 1) & 1) | ((r >> 2) & 2);
    voffW[k] = r * (FF * 2) + 128 * jline + (((c >> 1) ^ f) * 32) + (c & 1) * 16;
  }
#pragma unroll
  for (int k = 0; k < 4; ++k) {
    const int idx = 64 * (w4 + 4 * k) + lane, pl = idx / C_T, tt = idx % C_T;
    const int t = min(t0 + tt, SL - 1);
    voffH[k] = pl * 32768 + t * 16;
  }
  const unsigned ldsW = C_GOFF + (w4 >> 1) * 4096 + (w4 & 1) * 2048;
  auto issue = [&](int step, unsigned so) {
#pragma unroll
    for (int k = 0; k < 2; ++k) glds16(wbase + (size_t)step * (32 * FF * 2), voffW[k], lds0 + so + ldsW + k * 1024);
#pragma unroll
    for (int k = 0; k < NDMA - 2; ++k) glds16(h8 + (size_t)step * (4 * 32768), voffH[k], lds0 + so + (w4 + 4 * k) * 1024);
  };
  constexpr unsigned S = C_SLOT;
  issue(0, 0); issue(1, S); issue(2, 2 * S); issue(3, 3 * S);
  wait_vm<3 * NDMA>(); bar();
  issue(4, 4 * S);
  wait_vm<2 * NDMA>(); bar();
  issue(5, 5 * S); issue(6, 0);
  unsigned sw = S;
#pragma unroll 1
  for (int j = 1; j < 7; ++j) {
    wait_vm<2 * NDMA>();
    bar();
    issue(2 * j + 5, sw);
    sw += S; if (sw == C_NSLOT * S) sw = 0;
    issue(2 * j + 6, sw);
    sw += S; if (sw == C_NSLOT * S) sw = 0;
  }
  wait_vm<2 * NDMA>(); bar();
  issue(19, sw);
  const int tl = min(60 * w4 + lane, C_T - 1), tg = min(t0 + tl, SL - 1);
  const gu64* gp = (const gu64*)(ws + OFF_G2) + tg;
  const float* csp = (const float*)(ws + OFF_CS) + ((lane >> 4) & 1) * FF + 64 * jline + 16 * w4 + (lane & 15);
  const char* tb = ws + OFF_WP2 + (size_t)(4 * (xcd & 1)) * (54 * 5120);
  const int tl0 = ((int)(blockIdx.x >> 3) * 4 + w4) * 68;
  u2_t gv; float csv; unsigned tv0, tv1;
  asm volatile("global_load_dwordx2 %0, %1, off sc1" : "=v"(gv) : "v"(gp) : "memory");
  asm volatile("global_load_dword %0, %1, off" : "=v"(csv) : "v"(csp) : "memory");
  asm volatile("global_load_dword %0, %1, off" : "=v"(tv0) : "v"(tb + (size_t)min(tl0 + lane, 8639) * 128) : "memory");
  asm volatile("global_load_dword %0, %1, off" : "=v"(tv1) : "v"(tb + (size_t)min(tl0 + 64 + (lane & 3), 8639) * 128) : "memory");
  wait_vm<NDMA + 4>(); bar();
  asm volatile("s_waitcnt vmcnt(0)" : "+v"(gv), "+v"(csv), "+v"(tv0), "+v"(tv1)::"memory");
  bar();
  {
    bool ok = gv.y == 1u;
    for (int spin = 0; spin < 4096 && !__all(ok); ++spin) {
      const unsigned long long g = __hip_atomic_load(gp, __ATOMIC_RELAXED, __HIP_MEMORY_SCOPE_AGENT);
      gv.x = (unsigned)g; ok = (unsigned)(g >> 32) == 1u;
      __builtin_amdgcn_s_sleep(2);
    }
    if (!__all(ok)) gv.x = ln_row(h8, tg);
    if (lane < 60) *(float*)(smem + C_STAT + (60 * w4 + lane) * 4) = bf_lo(gv.x);
    if (lane < 32) *(float*)(smem + C_CS + (lane >> 4) * 256 + (16 * w4 + (lane & 15)) * 4) = csv;
  }
  asm volatile("s_waitcnt lgkmcnt(0)" ::: "memory");
  bar();
}

struct CFrag { b8_t A[2][2]; b8_t B[8]; };

extern "C" __global__ __launch_bounds__(512, 2) void kC(const u16* __restrict__ wg, const u16* __restrict__ wu, char* __restrict__ ws) {
  extern __shared__ __attribute__((aligned(16))) char smem[];
  const int tid = threadIdx.x, lane = tid & 63;
  const int w = __builtin_amdgcn_readfirstlane(tid >> 6);
  int panel, jline;
  const int xcd = blockIdx.x & 7, rank = blockIdx.x >> 3;
  {
    const int g4 = xcd >> 1, a = xcd & 1, nl = (g4 < 3) ? 7 : 6, total = 9 * nl, n0 = (total + 1) >> 1;
    if (a == 0 ? rank >= n0 : rank >= total - n0) {
      const int sp = (xcd < 6) ? (xcd >> 1) : 3 + 5 * (xcd - 6) + (rank - 27);
      const int wv = sp * 8 + w;
      if (w == 0) STAMP(2, 0);
      for (int tile = wv; tile < 128; tile += 104) {
        const int t = 16 * tile + (lane & 15);
        const char* hp = ws + OFF_H8 + (size_t)(lane >> 4) * 32768 + t * 16;
        u4_t v[20];
#pragma unroll
        for (int i = 0; i < 20; ++i) v[i] = *(const u4_t*)(hp + (size_t)i * (4 * 32768));
        float s0 = 0.f, s1 = 0.f, q0 = 0.f, q1 = 0.f;
        unsigned k_both = 0x3f803f80u;
        asm volatile("" : "+s"(k_both));
#pragma unroll
        for (int i = 0; i < 20; ++i) {
          s0 = dot2bf(v[i][0], k_both, s0); q0 = dot2bf(v[i][0], v[i][0], q0);
          s1 = dot2bf(v[i][1], k_both, s1); q1 = dot2bf(v[i][1], v[i][1], q1);
          s0 = dot2bf(v[i][2], k_both, s0); q0 = dot2bf(v[i][2], v[i][2], q0);
          s1 = dot2bf(v[i][3], k_both, s1); q1 = dot2bf(v[i][3], v[i][3], q1);
        }
        float s = s0 + s1, q = q0 + q1;
        s += __shfl_xor(s, 16); q += __shfl_xor(q, 16);
        s += __shfl_xor(s, 32); q += __shfl_xor(q, 32);
        if (lane < 16)
          __hip_atomic_store((gu64*)(ws + OFF_G2) + t, (1ull << 32) | ln_pack(s, q), __ATOMIC_RELAXED, __HIP_MEMORY_SCOPE_AGENT);
      }
      if (w == 0) STAMP(2, 7);
      return;
    }
    const int idx = rank + a * n0;
    panel = idx / nl; jline = 7 * g4 + idx % nl;
  }
  const int t0 = panel * C_T;
  if (w == 0) STAMP(2, 0);
  if (w >= 4) {
    const int w4 = w - 4;
    const char* wbase = (const char*)((w4 >> 1) ? wu : wg);
    if (w4 < 3) c_loader<6>(smem, ws, wbase, w4, lane, t0, jline, xcd);
    else c_loader<5>(smem, ws, wbase, w4, lane, t0, jline, xcd);
    return;
  }
  const int s = w >> 1, th = w & 1;
  const int fr = lane & 15, kg = lane >> 4, q = (lane & 15) >> 2, p = lane & 3;
  const unsigned hb0 = kg * 3840 + fr * 16 + 256 * 7;
  const unsigned hb1 = kg * 3840 + fr * 16 + 256 * (th ? 8 : 0);
  unsigned wa[2];
#pragma unroll
  for (int v = 0; v < 2; ++v) {
    const int sub = 2 * s + (v ? 1 - th : th);
    const int f = (q >> 1) | (2 * (kg & 1));
    wa[v] = C_GOFF + (8 * kg + q) * 128 + ((sub ^ f) * 32) + 8 * p;
  }
  f4_t ag[8][2], au[8][2];
#pragma unroll
  for (int u = 0; u < 8; ++u)
#pragma unroll
    for (int v = 0; v < 2; ++v) { ag[u][v] = f4_t{0.f, 0.f, 0.f, 0.f}; au[u][v] = f4_t{0.f, 0.f, 0.f, 0.f}; }
  auto load = [&](CFrag& F, unsigned so) {
    const char* sp = smem + so;
#pragma unroll
    for (int v = 0; v < 2; ++v)
#pragma unroll
      for (int m = 0; m < 2; ++m) {
        s4_t lo = tr_read(sp + wa[v] + m * 4096), hi = tr_read(sp + wa[v] + m * 4096 + 512);
        u2_t a = __builtin_bit_cast(u2_t, lo), b = __builtin_bit_cast(u2_t, hi);
        F.A[v][m] = __builtin_bit_cast(b8_t, (u4_t{a.x, a.y, b.x, b.y}));
      }
    F.B[0] = *(const b8_t*)(sp + hb0);
#pragma unroll
    for (int u = 1; u < 8; ++u) F.B[u] = *(const b8_t*)(sp + hb1 + 256 * (u - 1));
  };
  auto mma = [&](const CFrag& F) {
    ag[0][0] = __builtin_amdgcn_mfma_f32_16x16x32_bf16(F.A[0][0], F.B[0], ag[0][0], 0, 0, 0);
    au[0][0] = __builtin_amdgcn_mfma_f32_16x16x32_bf16(F.A[0][1], F.B[0], au[0][0], 0, 0, 0);
#pragma unroll
    for (int u = 1; u < 8; ++u)
#pragma unroll
      for (int v = 0; v < 2; ++v) {
        ag[u][v] = __builtin_amdgcn_mfma_f32_16x16x32_bf16(F.A[v][0], F.B[u], ag[u][v], 0, 0, 0);
        au[u][v] = __builtin_amdgcn_mfma_f32_16x16x32_bf16(F.A[v][1], F.B[u], au[u][v], 0, 0, 0);
      }
  };
  auto interleave = [&]() {
#pragma unroll
    for (int i = 0; i < 16; ++i) {
      __builtin_amdgcn_sched_group_barrier(0x008, 1, 0);
      __builtin_amdgcn_sched_group_barrier(0x100, 1, 0);
    }
    __builtin_amdgcn_sched_group_barrier(0x008, 14, 0);
  };
  CFrag F0, F1;
  bar();
  if (w == 0) STAMP(2, 1);
  load(F0, 0);
  unsigned sn = C_SLOT;
  SB;
#pragma unroll 1
  for (int j = 0; j < 9; ++j) {
    asm volatile("s_waitcnt lgkmcnt(0)" ::: "memory");
    bar(); SB;
    load(F1, sn);
    mma(F0);
    interleave();
    SB;
    sn += C_SLOT; if (sn == C_NSLOT * C_SLOT) sn = 0;
    load(F0, sn);
    mma(F1);
    interleave();
    SB;
    sn += C_SLOT; if (sn == C_NSLOT * C_SLOT) sn = 0;
  }
  asm volatile("s_waitcnt lgkmcnt(0)" ::: "memory");
  bar(); SB;
  load(F1, sn);
  mma(F0);
  interleave();
  SB;
  bar(); SB;
  f4_t csg[2], csu[2];
  float mu[8];
#pragma unroll
  for (int v = 0; v < 2; ++v) {
    const int sub = 2 * s + (v ? 1 - th : th);
    csg[v] = *(const f4_t*)(smem + C_CS + (16 * sub + 4 * kg) * 4);
    csu[v] = *(const f4_t*)(smem + C_CS + 256 + (16 * sub + 4 * kg) * 4);
  }
#pragma unroll
  for (int u = 0; u < 8; ++u) {
    const int tile = (u == 0) ? 7 : (th ? 7 + u : u - 1);
    mu[u] = *(const float*)(smem + C_STAT + (16 * tile + fr) * 4);
  }
  mma(F1);
  SB;
  if (w == 0) STAMP(2, 2);
  char* hh = ws + OFF_HH + (size_t)(2 * jline + s) * 1024 + lane * 16;
  char* sink = ws + OFF_DUMMY + lane * 16;
  auto epi = [&](auto thc) {
    constexpr int TH = decltype(thc)::value;
#pragma unroll
    for (int u = 0; u < 8; ++u) {
      const int tile = (u == 0) ? 7 : (TH ? 7 + u : u - 1);
      const int t16 = 15 * panel + tile;
      char* dst = (t16 < 128) ? hh + (size_t)t16 * (54 * 1024) : sink;
      u2_t o[2];
#pragma unroll
      for (int v = 0; v < 2; ++v) {
        if (u == 0 && v == 1) continue;
        const f4_t r = (ag[u][v] - mu[u] * csg[v]) * (au[u][v] - mu[u] * csu[v]);
        o[v] = u2_t{pk_bf16(r[0], r[1]), pk_bf16(r[2], r[3])};
      }
      if (u == 0) st8(dst + 8 * TH, o[0]);
      else st16(dst, u4_t{o[TH].x, o[TH].y, o[1 - TH].x, o[1 - TH].y});
    }
  };
  if (th) epi(IC(1)); else epi(IC(0));
  if (w == 0) STAMP(2, 7);
}

constexpr int D_SLOT = 15360, D_NSLOT = 9, D_STEPS = 18;
constexpr int D_LDS = D_NSLOT * D_SLOT;

template <int NDMA>
__device__ __forceinline__ void d_loader(char* smem, const char* wp, int w4, int lane) {
  const unsigned lds0 = (unsigned)(size_t)(LAS char*)smem;
  const unsigned voff = lane * 16;
  auto issue = [&](int step, unsigned so) {
#pragma unroll
    for (int k = 0; k < NDMA; ++k) {
      const int i = w4 + 4 * k;
      glds16(wp + (size_t)step * D_SLOT + i * 1024, voff, lds0 + so + i * 1024);
    }
  };
  constexpr unsigned S = D_SLOT;
  issue(0, 0); issue(1, S); issue(2, 2 * S);
  wait_vm<2 * NDMA>(); bar();
  issue(3, 3 * S); issue(4, 4 * S); issue(5, 5 * S);
  wait_vm<4 * NDMA>(); bar();
  issue(6, 6 * S); issue(7, 7 * S);
  wait_vm<5 * NDMA>(); bar();
  issue(8, 8 * S); issue(9, 0);
  unsigned sw = S;
#pragma unroll 1
  for (int b = 2; b < 10; ++b) {
    wait_vm<6 * NDMA>();
    bar();
    issue(b + 8, sw);
    sw += S; if (sw == D_NSLOT * S) sw = 0;
  }
  wait_vm<6 * NDMA>(); bar();
  wait_vm<5 * NDMA>(); bar();
  wait_vm<4 * NDMA>(); bar();
  wait_vm<3 * NDMA>(); bar();
  wait_vm<2 * NDMA>(); bar();
  wait_vm<1 * NDMA>(); bar();
  wait_vm<0>(); bar();
  bar();
}

extern "C" __global__ __launch_bounds__(512, 2) void kD(char* __restrict__ ws, u16* __restrict__ out) {
  extern __shared__ __attribute__((aligned(16))) char smem[];
  const int tid = threadIdx.x, lane = tid & 63;
  const int w = __builtin_amdgcn_readfirstlane(tid >> 6);
  const int xcd = blockIdx.x & 7, rank = blockIdx.x >> 3;
  const int it = 8 * (xcd >> 1) + (rank & 7), dg = 4 * (xcd & 1) + (rank >> 3);
  if (w == 0) STAMP(3, 0);
  if (w >= 4) {
    const int w4 = w - 4;
    const char* wp = ws + OFF_WP2 + (size_t)dg * (54 * 5120);
    if (w4 < 3) d_loader<4>(smem, wp, w4, lane);
    else d_loader<3>(smem, wp, w4, lane);
    return;
  }
  const int fr = lane & 15, g = lane >> 4;
  const int t = 64 * it + 16 * w + fr;
  const char* hp = ws + OFF_HH + (size_t)(4 * it + w) * (54 * 1024);
  const unsigned voff = lane * 16;
  u4_t R[18];
#pragma unroll
  for (int i = 0; i < 9; ++i)
    asm volatile("global_load_dwordx4 %0, %1, %2" : "=v"(R[i]) : "v"(voff), "s"(hp + (size_t)i * 1024) : "memory");
  u2_t res[5]; unsigned st;
  f4_t acc[5];
#pragma unroll
  for (int dt = 0; dt < 5; ++dt) acc[dt] = f4_t{0.f, 0.f, 0.f, 0.f};
  b8_t A0[5], A1[5];
  auto rdA = [&](b8_t (&A)[5], unsigned off) {
    const char* sp = smem + off + lane * 16;
#pragma unroll
    for (int dt = 0; dt < 5; ++dt) A[dt] = *(const b8_t*)(sp + dt * 1024);
  };
  bar();
  if (w == 0) STAMP(3, 1);
  rdA(A0, 0);
  unsigned so = 0;
  auto kstep = [&](auto mode, auto idx, b8_t (&Ac)[5], b8_t (&An)[5], const char* hnext) {
    constexpr int MODE = decltype(mode)::value, i = decltype(idx)::value, kk = i % 3;
    if (kk == 0) bar();
    unsigned nx = so + (kk + 1) * 5120;
    if (kk == 2) { so += D_SLOT; if (so == D_NSLOT * D_SLOT) so = 0; nx = so; }
    SB;
    if (!(MODE == 2 && i == 17)) rdA(An, nx);
    constexpr int NV = (MODE == 0) ? (i < 9 ? 8 + i : 26 - i) : (MODE == 1) ? (i < 9 ? 25 - i : 17) : (i == 0 ? 17 : 23 - i);
    asm volatile("s_waitcnt vmcnt(%1)" : "+v"(R[i]) : "i"(NV) : "memory");
    const b8_t B = __builtin_bit_cast(b8_t, R[i]);
#pragma unroll
    for (int dt = 0; dt < 5; ++dt) acc[dt] = __builtin_amdgcn_mfma_f32_16x16x32_bf16(Ac[dt], B, acc[dt], 0, 0, 0);
    SB;
    if (MODE == 0 && i < 9)
      asm volatile("global_load_dwordx4 %0, %1, %2" : "=v"(R[i < 9 ? 9 + i : 0]) : "v"(voff), "s"(hp + (size_t)(9 + i) * 1024) : "memory");
    if (MODE != 2) asm volatile("global_load_dwordx4 %0, %1, %2" : "+v"(R[i]) : "v"(voff), "s"(hnext + (size_t)i * 1024) : "memory");
    if (MODE == 2 && i == 0) {
#pragma unroll
      for (int dt = 0; dt < 5; ++dt) {
        const int d = 80 * dg + 16 * dt + 4 * g;
        const char* rp = ws + OFF_H8 + ((size_t)(d >> 3) * SL + t) * 16 + (d & 4) * 2;
        asm volatile("global_load_dwordx2 %0, %1, off" : "=v"(res[dt]) : "v"(rp) : "memory");
      }
      asm volatile("global_load_dword %0, %1, off" : "=v"(st) : "v"(ws + OFF_G2 + (size_t)t * 8) : "memory");
    }
    SB;
  };
  auto round = [&](auto mode, const char* hnext) {
    kstep(mode, IC(0), A0, A1, hnext); kstep(mode, IC(1), A1, A0, hnext); kstep(mode, IC(2), A0, A1, hnext);
    kstep(mode, IC(3), A1, A0, hnext); kstep(mode, IC(4), A0, A1, hnext); kstep(mode, IC(5), A1, A0, hnext);
    kstep(mode, IC(6), A0, A1, hnext); kstep(mode, IC(7), A1, A0, hnext); kstep(mode, IC(8), A0, A1, hnext);
    kstep(mode, IC(9), A1, A0, hnext); kstep(mode, IC(10), A0, A1, hnext); kstep(mode, IC(11), A1, A0, hnext);
    kstep(mode, IC(12), A0, A1, hnext); kstep(mode, IC(13), A1, A0, hnext); kstep(mode, IC(14), A0, A1, hnext);
    kstep(mode, IC(15), A1, A0, hnext); kstep(mode, IC(16), A0, A1, hnext); kstep(mode, IC(17), A1, A0, hnext);
  };
  round(IC(0), hp + 18 * 1024);
  round(IC(1), hp + 36 * 1024);
  round(IC(2), hp);
  if (w == 0) STAMP(3, 2);
  asm volatile("s_waitcnt vmcnt(0)" : "+v"(res[0]), "+v"(res[1]), "+v"(res[2]), "+v"(res[3]), "+v"(res[4]), "+v"(st)::"memory");
  const float sd = bf_hi(st), r2 = 1.0f / (sd * sd);
#pragma unroll
  for (int dt = 0; dt < 5; ++dt) {
    const int d = 80 * dg + 16 * dt + 4 * g;
    const float h0 = bf_lo(res[dt].x), h1 = bf_hi(res[dt].x), h2 = bf_lo(res[dt].y), h3 = bf_hi(res[dt].y);
    st8(out + (size_t)t * DM + d, u2_t{pk_bf16(h0 + r2 * acc[dt][0], h1 + r2 * acc[dt][1]), pk_bf16(h2 + r2 * acc[dt][2], h3 + r2 * acc[dt][3])});
  }
  if (w == 0) STAMP(3, 7);
}

extern "C" __global__ void kS(char* ws) {
  const unsigned long long* h = (const unsigned long long*)(ws + OFF_DBG);
  const int lane = threadIdx.x;
  unsigned long long mn[4], mx[4];
  for (int k = 1; k < 4; ++k) {
    unsigned long long a = ~0ull, b = 0;
    for (int blk = lane; blk < 256; blk += 64) {
      const unsigned long long s0 = h[(k * 512 + blk) * 16], s7 = h[(k * 512 + blk) * 16 + 7];
      a = s0 < a ? s0 : a;
      if (s7 > s0 && s7 - s0 < 5000) b = s7 > b ? s7 : b;
    }
    for (int o = 1; o < 64; o <<= 1) {
      unsigned long long a2 = __shfl_xor(a, o), b2 = __shfl_xor(b, o);
      a = a2 < a ? a2 : a; b = b2 > b ? b2 : b;
    }
    mn[k] = a; mx[k] = b;
  }
  const unsigned long long* r = h + (1 * 512 + 100) * 16;
  const unsigned long long r0 = r[0], r1 = r[1], r6 = r[6], r7 = r[7], r4 = r[4];
  long long q = ((mn[1] ^ mn[2] ^ mn[3] ^ mx[1] ^ mx[2] ^ mx[3] ^ r0 ^ r1 ^ r6 ^ r7 ^ r4) == 0x1234567ull) ? 1 : 0;
  if (PROBE == 3) q += mx[1] - mn[1];
  if (PROBE == 4) q += mn[2] - mx[1];
  if (PROBE == 8) q += mx[3] - mn[1];
  if (PROBE == 10) q += r1 - r0;
  if (PROBE == 11) q += r6 - r0;
  if (PROBE == 12) q += r0 - mn[1];
  if (PROBE == 13) q += r4 - r0;
  if (q < 0 || q > 5000) q = 5000;
  const unsigned long long t0 = __builtin_amdgcn_s_memrealtime();
  while ((long long)(__builtin_amdgcn_s_memrealtime() - t0) < q * PROBE_M) __builtin_amdgcn_s_sleep(1);
}

extern "C" void kernel_launch(void* const* d_in, const int* in_sizes, int n_in, void* d_out, int out_size, void* d_ws,
                              size_t ws_size, hipStream_t stream) {
  char* ws = (char*)d_ws;
  (void)hipFuncSetAttribute((const void*)kB, hipFuncAttributeMaxDynamicSharedMemorySize, B_LDS);
  (void)hipFuncSetAttribute((const void*)kC, hipFuncAttributeMaxDynamicSharedMemorySize, C_LDS);
  (void)hipFuncSetAttribute((const void*)kD, hipFuncAttributeMaxDynamicSharedMemorySize, D_LDS);
  kB<<<256, 512, B_LDS, stream>>>((const u16*)d_in[0], (const u16*)d_in[1], (const u16*)d_in[2], (const u16*)d_in[3], (const u16*)d_in[4],
                                  (const u16*)d_in[5], (const u16*)d_in[6], (const u16*)d_in[7], (const u16*)d_in[8], (const u16*)d_in[10],
                                  (const u16*)d_in[11], (const u16*)d_in[12], (const u16*)d_out, ws);
  kC<<<256, 512, C_LDS, stream>>>((const u16*)d_in[10], (const u16*)d_in[11], ws);
  kD<<<256, 512, D_LDS, stream>>>(ws, (u16*)d_out);
  kS<<<1, 64, 0, stream>>>(ws);
}
```

```cpp
#include <hip/hip_runtime.h>
#include <hip/hip_bf16.h>
#include <stdint.h>
#include <type_traits>

typedef unsigned short u16;
typedef _Float16 h2_t __attribute__((ext_vector_type(2)));
typedef _Float16 h8_t __attribute__((ext_vector_type(8)));
typedef __bf16 b2_t __attribute__((ext_vector_type(2)));
typedef __bf16 b8_t __attribute__((ext_vector_type(8)));
typedef float f2_t __attribute__((ext_vector_type(2)));
typedef float f4_t __attribute__((ext_vector_type(4)));
typedef unsigned u2_t __attribute__((ext_vector_type(2)));
typedef unsigned u4_t __attribute__((ext_vector_type(4)));
typedef short s4_t __attribute__((ext_vector_type(4)));
typedef __attribute__((address_space(1))) unsigned long long gu64;
#define LAS __attribute__((address_space(3)))
#define SB __builtin_amdgcn_sched_barrier(0)
#define IC(n) std::integral_constant<int, n>{}

constexpr int SL = 2048;
constexpr int DM = 640;
constexpr int FF = 1728;

constexpr size_t OFF_G1 = 0;
constexpr size_t OFF_EP = 0x10000;
constexpr size_t OFF_RD = 0x10100;
constexpr size_t OFF_H8 = 0x5A0000;
constexpr size_t OFF_HH = 0x820000;
constexpr size_t OFF_WP2 = 0xF00000;
constexpr size_t OFF_CS = 0x1120000;
constexpr size_t OFF_G2 = 0x1130000;
constexpr int KRS = 1344;
constexpr int KDS = 4864;
constexpr int KD_O = 2240;
constexpr int KD_DR = 4384;

#define STAMP(k, i)

__device__ __forceinline__ float bf_lo(unsigned d) { return __uint_as_float(d << 16); }
__device__ __forceinline__ float bf_hi(unsigned d) { return __uint_as_float(d & 0xffff0000u); }
__device__ __forceinline__ float bf2f(u16 v) { return __uint_as_float((unsigned)v << 16); }
__device__ __forceinline__ unsigned pk_bf16(float a, float b) {
  f2_t v = {a, b};
  return __builtin_bit_cast(unsigned, __builtin_convertvector(v, b2_t));
}
__device__ __forceinline__ unsigned pk_f16(float a, float b) {
  f2_t v = {a, b};
  return __builtin_bit_cast(unsigned, __builtin_convertvector(v, h2_t));
}
__device__ __forceinline__ float rbf(float a) { return bf_lo(pk_bf16(a, 0.f)); }
template <int N> __device__ __forceinline__ float dpp_ror_add(float v) {
  int r = __builtin_amdgcn_update_dpp(0, __float_as_int(v), 0x120 + N, 0xf, 0xf, false);
  return v + __int_as_float(r);
}
__device__ __forceinline__ float row16_sum(float v) {
  v = dpp_ror_add<8>(v); v = dpp_ror_add<4>(v); v = dpp_ror_add<2>(v); v = dpp_ror_add<1>(v);
  return v;
}
__device__ __forceinline__ float wave_sum(float v) {
  v = row16_sum(v);
  const int b = __float_as_int(v);
  return (__int_as_float(__builtin_amdgcn_readlane(b, 0)) + __int_as_float(__builtin_amdgcn_readlane(b, 16))) +
         (__int_as_float(__builtin_amdgcn_readlane(b, 32)) + __int_as_float(__builtin_amdgcn_readlane(b, 48)));
}
template <typename T> __device__ __forceinline__ T ldnt(const T* p) { return __builtin_nontemporal_load(p); }
__device__ __forceinline__ void wave_fence() {
  __builtin_amdgcn_fence(__ATOMIC_RELEASE, "wavefront");
  __builtin_amdgcn_wave_barrier();
  __builtin_amdgcn_fence(__ATOMIC_ACQUIRE, "wavefront");
}
__device__ __forceinline__ s4_t tr_read(const char* p) {
  return __builtin_amdgcn_ds_read_tr16_b64_v4i16((LAS s4_t*)p);
}
__device__ __forceinline__ void glds16(const void* sbase, unsigned voff, unsigned lds_addr) {
  asm volatile("s_mov_b32 m0, %2\n\ts_nop 0\n\tglobal_load_lds_dwordx4 %0, %1" ::"v"(voff), "s"(sbase), "s"(lds_addr) : "memory");
}
template <int N> __device__ __forceinline__ void wait_vm() { asm volatile("s_waitcnt vmcnt(%0)" ::"i"(N) : "memory"); }
__device__ __forceinline__ void bar() { asm volatile("s_barrier" ::: "memory"); }
__device__ __forceinline__ float dot2bf(unsigned a, unsigned b, float c) {
  return __builtin_amdgcn_fdot2_f32_bf16(__builtin_bit_cast(b2_t, a), __builtin_bit_cast(b2_t, b), c, false);
}

#define ST_MODE 0
__device__ __forceinline__ void st16(void* p, u4_t v) {
  *(u4_t*)p = v;
}
__device__ __forceinline__ void st8(void* p, u2_t v) {
  *(u2_t*)p = v;
}

struct Cw { f2_t w, ws; };
__device__ __forceinline__ Cw mkw(f2_t w) { Cw r; r.w = w; r.ws = f2_t{-w.y, w.x}; return r; }
__device__ __forceinline__ f2_t cmulw(f2_t a, const Cw& W) {
  return __builtin_elementwise_fma(f2_t{a.y, a.y}, W.ws, f2_t{a.x, a.x} * W.w);
}
__device__ __forceinline__ f2_t cexp_l(float er, float wr, float l) {
  float mag = __builtin_amdgcn_exp2f(er * l);
  float rev = __builtin_amdgcn_fractf(wr * l);
  return f2_t{mag * __builtin_amdgcn_cosf(rev), mag * __builtin_amdgcn_sinf(rev)};
}
__device__ __forceinline__ float pow2_scale(float bound, int target) {
  unsigned e = (__float_as_uint(bound) >> 23) & 0xff;
  int k = target - ((int)e - 126);
  k = k > 126 ? 126 : (k < -126 ? -126 : k);
  return (bound > 0.f) ? __uint_as_float((unsigned)(k + 127) << 23) : 1.0f;
}
__device__ __forceinline__ float pow2_inv(float p) { return __uint_as_float(0x7f000000u - __float_as_uint(p)); }

constexpr int B_SIDE = 96;
constexpr int KG_R = 0, KG_L0 = 4352, KG_L1 = 8704, KG_W = 8704 + 25 * 272;
constexpr int B_BND = 8 * KG_W;
constexpr int B_KC = B_BND + 64;
constexpr int B_LDS = B_KC + 4 * KRS;
constexpr int B_KOFF = 0;
constexpr int B_UOFF = 4 * KDS;
constexpr int B_UCH = 4608;
constexpr int B_YCH = 10240;
constexpr int B_PACK = 40960;

__device__ __forceinline__ void pack_wd(const u16* __restrict__ wd, char* __restrict__ ws, char* buf, int u, int lane) {
  const int ks = u >> 3, dg = u & 7;
#pragma unroll
  for (int i = 0; i < 5; ++i) {
    const int idx = i * 64 + lane, row = idx / 10, c = idx % 10;
    __builtin_amdgcn_global_load_lds((const unsigned*)((const char*)wd + (size_t)(32 * ks + row) * 1280 + 160 * dg + 16 * c),
                                     (LAS unsigned*)(buf + i * 1024), 16, 0, 2);
  }
  asm volatile("s_waitcnt vmcnt(0)" ::: "memory");
  wave_fence();
  const int g = lane >> 4, q = (lane & 15) >> 2, p = lane & 3;
  char* dst = ws + OFF_WP2 + ((size_t)(dg * 54 + ks) * 5) * 1024 + lane * 16;
#pragma unroll
  for (int dt = 0; dt < 5; ++dt) {
    s4_t aa = tr_read(buf + (4 * g + q) * 160 + 32 * dt + 8 * p);
    s4_t bb = tr_read(buf + (16 + 4 * g + q) * 160 + 32 * dt + 8 * p);
    u2_t ua = __builtin_bit_cast(u2_t, aa), ub = __builtin_bit_cast(u2_t, bb);
    const u4_t pv = {ua.x, ua.y, ub.x, ub.y};
    asm volatile("global_store_dwordx4 %0, %1, off sc1\n\ts_nop 1" ::"v"(dst + dt * 1024), "v"(pv) : "memory");
  }
}

struct BFrag { h8_t B[8]; u2_t A[5][2]; };
__device__ __forceinline__ void mfma_f16(f4_t& d, u4_t a, h8_t b) {
  asm volatile("v_mfma_f32_16x16x32_f16 %0, %1, %2, %0" : "+v"(d) : "v"(a), "v"(b));
}
__device__ __forceinline__ void mfma_f16_0(f4_t& d, u4_t a, h8_t b) {
  asm volatile("v_mfma_f32_16x16x32_f16 %0, %1, %2, 0" : "=&v"(d) : "v"(a), "v"(b));
}
__device__ __forceinline__ unsigned ln_fin(float mean, float var) {
  const float mb = rbf(mean), sd = rbf(sqrtf(rbf(rbf(var) + 1e-5f)));
  return (__float_as_uint(mb) >> 16) | (__float_as_uint(sd) & 0xffff0000u);
}
__device__ __forceinline__ unsigned ln1_row(const u16* __restrict__ x, int t) {
  const u4_t* p = (const u4_t*)(x + (size_t)t * DM);
  float s = 0.f, q = 0.f;
  for (int i = 0; i < 80; ++i) { const u4_t v = p[i]; for (int j = 0; j < 4; ++j) s += bf_lo(v[j]) + bf_hi(v[j]); }
  const float mean = s * (1.0f / DM);
  for (int i = 0; i < 80; ++i) {
    const u4_t v = p[i];
    for (int j = 0; j < 4; ++j) { const float a = bf_lo(v[j]) - mean, b = bf_hi(v[j]) - mean; q += a * a; q += b * b; }
  }
  return ln_fin(mean, q * (1.0f / DM));
}
constexpr int SPIN_MAX = 1 << 14;

extern "C" __global__ __launch_bounds__(512) void kB(const u16* __restrict__ x, const u16* __restrict__ Are, const u16* __restrict__ Aim,
                                                     const u16* __restrict__ Bre, const u16* __restrict__ Bim, const u16* __restrict__ Cre,
                                                     const u16* __restrict__ Cim, const u16* __restrict__ logdt, const u16* __restrict__ dskip,
                                                     const u16* __restrict__ wg, const u16* __restrict__ wu, const u16* __restrict__ wd,
                                                     const u16* __restrict__ outp, char* __restrict__ ws) {
  extern __shared__ __attribute__((aligned(16))) char smem[];
  const int tid = threadIdx.x, lane = tid & 63;
  const int w = __builtin_amdgcn_readfirstlane(tid >> 6);
  const int xcd = blockIdx.x & 7;
#define EPLOAD(v) asm volatile("global_load_dwordx2 %0, %1, %2 sc1" : "=v"(v) : "v"(0u), "s"(ws + OFF_EP) : "memory")
  if (w == 0) STAMP(1, 0);
  auto post_epoch = [&](u2_t ep, int off) {
    if (tid == 0) {
      const unsigned long long e = (((unsigned long long)ep.y << 32) | ep.x) + 1ull;
      *(unsigned long long*)(smem + off) = e;
      *(unsigned*)(smem + off + 8) = (unsigned)((e * 0x9E3779B97F4A7C15ull) >> 32);
      __hip_atomic_store((gu64*)(ws + OFF_RD) + blockIdx.x, e, __ATOMIC_RELAXED, __HIP_MEMORY_SCOPE_AGENT);
    }
  };
  if (blockIdx.x < B_SIDE) {
    const int rk = blockIdx.x >> 3;
    const int g4 = xcd >> 1, a = xcd & 1, nl = (g4 < 3) ? 7 : 6;
    constexpr int S_EP = 17408;
    unsigned pf[2] = {0u, 0u};
    if (rk >= 8) {
#pragma unroll
      for (int k = 0; k < 2; ++k) {
        const int id = (((rk - 8) * 8 + w) * 2 + k) * 64 + lane;
        asm volatile("global_load_dword %0, %1, off" : "=v"(pf[k]) : "v"((const char*)x + (size_t)(id >> 1) * (DM * 2) + (((160 * xcd) >> 7) + (id & 1)) * 128) : "memory");
      }
    }
    if (rk < 8) {
      const int r = lane >> 4, c = lane & 15, t = (blockIdx.x * 8 + w) * 4 + r;
      const u4_t* p = (const u4_t*)(x + (size_t)t * DM);
      u2_t ep;
      EPLOAD(ep);
      u4_t v[5];
#pragma unroll
      for (int i = 0; i < 5; ++i) v[i] = ldnt(p + c + 16 * i);
      float s = 0.f;
#pragma unroll
      for (int i = 0; i < 5; ++i)
#pragma unroll
        for (int j = 0; j < 4; ++j) s += bf_lo(v[i][j]) + bf_hi(v[i][j]);
      asm volatile("" : "+v"(ep) : "v"(s));
      post_epoch(ep, S_EP);
      const float mean = row16_sum(s) * (1.0f / DM);
      float q = 0.f;
#pragma unroll
      for (int i = 0; i < 5; ++i)
#pragma unroll
        for (int j = 0; j < 4; ++j) {
          const float aa = bf_lo(v[i][j]) - mean, bb = bf_hi(v[i][j]) - mean;
          q += aa * aa; q += bb * bb;
        }
      const unsigned sv = ln_fin(mean, row16_sum(q) * (1.0f / DM));
      __syncthreads();
      const u2_t e = *(const u2_t*)(smem + S_EP);
      if (c == 0) {
        __hip_atomic_store((gu64*)(ws + OFF_G1) + t, ((unsigned long long)*(const unsigned*)(smem + S_EP + 8) << 32) | sv, __ATOMIC_RELAXED, __HIP_MEMORY_SCOPE_AGENT);
        ((unsigned long long*)(ws + OFF_G2))[t] = 0ull;
      }
      if (w == 0) STAMP(1, 1);
      if (rk == 4 && w == 0) {
        unsigned long long a0, a1;
        asm volatile("s_getpc_b64 s[20:21]\n\ts_add_u32 s20, s20, kB@rel32@lo+4\n\ts_addc_u32 s21, s21, kB@rel32@hi+12\n\ts_mov_b64 %0, s[20:21]" : "=s"(a0) :: "s20", "s21", "scc");
        asm volatile("s_getpc_b64 s[20:21]\n\ts_add_u32 s20, s20, kD@rel32@lo+4\n\ts_addc_u32 s21, s21, kD@rel32@hi+12\n\ts_mov_b64 %0, s[20:21]" : "=s"(a1) :: "s20", "s21", "scc");
        const unsigned long long lo = a0 < a1 ? a0 : a1, last = ((a0 < a1 ? a1 - a0 : a0 - a1) + 8192) & ~127ull;
        unsigned cf[8];
#pragma unroll
        for (int i = 0; i < 8; ++i) {
          const unsigned long long o = (unsigned long long)(lane + 64 * i) * 128;
          asm volatile("global_load_dword %0, %1, off" : "=v"(cf[i]) : "v"(lo + (o < last ? o : last)) : "memory");
        }
        asm volatile("s_waitcnt vmcnt(0)" ::"v"(cf[0]), "v"(cf[1]), "v"(cf[2]), "v"(cf[3]), "v"(cf[4]), "v"(cf[5]), "v"(cf[6]), "v"(cf[7]) : "memory");
      }
      if (blockIdx.x == 63 && w == 7) {
        const unsigned long long e64 = ((unsigned long long)e.y << 32) | e.x;
        for (int spin = 0; spin < SPIN_MAX; ++spin) {
          bool ok = true;
#pragma unroll
          for (int i = 0; i < 4; ++i) ok &= __hip_atomic_load((const gu64*)(ws + OFF_RD) + lane + 64 * i, __ATOMIC_RELAXED, __HIP_MEMORY_SCOPE_AGENT) == e64;
          if (__all(ok)) break;
          __builtin_amdgcn_s_sleep(4);
        }
        if (lane == 0) __hip_atomic_store((gu64*)(ws + OFF_EP), e64, __ATOMIC_RELAXED, __HIP_MEMORY_SCOPE_AGENT);
      }
    }
    const int strip = (rk >= 8) ? rk - 8 : 4 + rk;
    if ((rk >= 8 || rk < 3) && strip < nl) {
      const int jl = 7 * g4 + strip;
      const char* pm = (const char*)(a ? wu : wg) + (size_t)(80 * w + (lane >> 3)) * (FF * 2) + 128 * jl + (lane & 7) * 16;
      u2_t ep;
      EPLOAD(ep);
      u4_t v[10];
#pragma unroll
      for (int i = 0; i < 10; ++i) v[i] = ldnt((const u4_t*)(pm + (size_t)i * 8 * (FF * 2)));
      const char* po = (const char*)(a ? wg : wu) + (size_t)(80 * w) * (FF * 2) + 128 * jl;
      unsigned t0v, t1v;
      asm volatile("global_load_dword %0, %1, off nt" : "=v"(t0v) : "v"(po + (size_t)lane * (FF * 2)) : "memory");
      asm volatile("global_load_dword %0, %1, off nt" : "=v"(t1v) : "v"(po + (size_t)(64 + (lane & 15)) * (FF * 2)) : "memory");
      f2_t s[4];
#pragma unroll
      for (int e = 0; e < 4; ++e) s[e] = f2_t{0.f, 0.f};
#pragma unroll
      for (int i = 0; i < 10; ++i)
#pragma unroll
        for (int jj = 0; jj < 4; ++jj) s[jj] += f2_t{bf_lo(v[i][jj]), bf_hi(v[i][jj])};
      if (rk >= 8) {
        asm volatile("" : "+v"(ep) : "v"(s[0]));
#pragma unroll
        for (int i = 0; i < 2; ++i) asm volatile("" : "+v"(pf[i]) : "v"(s[0]));
        post_epoch(ep, S_EP);
      }
      float* pp = (float*)smem;
      *(f4_t*)(pp + (w * 8 + (lane >> 3)) * 68 + (lane & 7) * 8) = f4_t{s[0].x, s[0].y, s[1].x, s[1].y};
      *(f4_t*)(pp + (w * 8 + (lane >> 3)) * 68 + (lane & 7) * 8 + 4) = f4_t{s[2].x, s[2].y, s[3].x, s[3].y};
      __syncthreads();
      float acc = 0.f;
#pragma unroll
      for (int i = 0; i < 8; ++i) acc += pp[(w * 8 + i) * 68 + lane];
      __syncthreads();
      pp[w * 64 + lane] = acc;
      __syncthreads();
      if (w == 0) {
        float t = 0.f;
#pragma unroll
        for (int i = 0; i < 8; ++i) t += pp[i * 64 + lane];
        ((float*)(ws + OFF_CS))[a * FF + 64 * jl + lane] = t;
      }
      asm volatile("s_waitcnt vmcnt(0)" ::"v"(t0v), "v"(t1v) : "memory");
    }
    if (w == 0) STAMP(1, 7);
    return;
  }
  const int cb = blockIdx.x - B_SIDE;
  const int q = 20 * xcd + (cb >> 3);
  const int cl = w >> 1, hw = w & 1, ch = 4 * q + cl;
  const int n = ch * 128 + hw * 64 + lane;
  u2_t ep;
  EPLOAD(ep);
  const u16 i_are = ldnt(Are + n), i_aim = ldnt(Aim + n), i_bre = ldnt(Bre + n), i_bim = ldnt(Bim + n), i_cre = ldnt(Cre + n), i_cim = ldnt(Cim + n);
  const u16 i_dt = logdt[ch];
  u2_t xg[4];
  const unsigned xoff = tid * (2 * DM * 2);
  const char* xb = (const char*)(x + 4 * q);
#define XG(i) asm volatile("global_load_dwordx2 %0, %1, %2 offset:%3" : "=v"(xg[i]) : "v"(xoff), "s"(xb + ((i) >> 1) * (size_t)(1024 * DM * 2)), "i"(((i) & 1) * DM * 2) : "memory")
  const u2_t dkv = *(const u2_t*)(dskip + 4 * q);
  const int c = lane & 15, kg = lane >> 4;
  h8_t band9, band7;
#pragma unroll
  for (int j = 0; j < 8; ++j) {
    const int k = 8 * kg + 7 - j;
    band9[j] = (k >= c + 4 && k <= c + 12) ? (_Float16)1.0f : (_Float16)0.0f;
    band7[j] = (k >= c + 5 && k <= c + 11) ? (_Float16)1.0f : (_Float16)0.0f;
  }
  asm volatile("" : "+v"(band9), "+v"(band7));
  char* hb = smem + w * KG_W;
  float Sw, bsum;
  {
    const float dt = rbf(expf(bf2f(i_dt)));
    const float are = dt * bf2f(i_are), aim = dt * bf2f(i_aim);
    const float bre = dt * bf2f(i_bre), bim = dt * bf2f(i_bim);
    const float cre = bf2f(i_cre), cim = bf2f(i_cim);
    f2_t c0 = {cre * bre - cim * bim, cre * bim + cim * bre};
    const float er = are * 1.44269504088896341f, wr = aim * 0.15915494309189535f;
    const float bnd = (fabsf(c0.x) + fabsf(c0.y)) * fmaxf(1.0f, __builtin_amdgcn_exp2f(er * 2048.f));
    bsum = wave_sum(bnd);
    asm volatile("" : "+v"(ep) : "v"(bnd));
    post_epoch(ep, B_BND + 48);
    if (lane == 0) ((float*)(smem + B_BND))[w] = bsum;
    if (w == 0) STAMP(1, 1);
    Sw = pow2_scale(bsum, 15);
    c0 *= Sw;
    const Cw z1 = mkw(cexp_l(er, wr, 1.f)), w8 = mkw(cexp_l(er, -wr, 8.f)), w128 = mkw(cexp_l(er, wr, 128.f));
    const Cw z2 = mkw(cmulw(z1.w, z1));
    const Cw z4 = mkw(cmulw(z2.w, z2));
    auto put = [&](int off, int row, f2_t v) { *(unsigned*)(hb + off + row * 272 + 4 * lane) = pk_f16(v.x, v.y); };
    f2_t R = w8.w, v0 = c0, v1 = cmulw(c0, w128), T = v1;
    *(unsigned*)(hb + KG_R + 4 * lane) = pk_f16(1.f, 0.f);
#pragma unroll
    for (int i = 0; i < 8; ++i) {
      SB;
      if ((w & 3) == (i & 3)) { if (i < 4) XG(0); else XG(1); }
      SB;
      *(unsigned*)(hb + KG_R + (2 * i + 1) * 272 + 4 * lane) = pk_f16(R.x, R.y);
      if (i < 7) {
        R = cmulw(R, w8);
        *(unsigned*)(hb + KG_R + (2 * i + 2) * 272 + 4 * lane) = pk_f16(R.x, R.y);
        R = cmulw(R, w8);
      }
      put(KG_L0, i, v0); put(KG_L0, 8 + i, v1);
      if (i < 7) { v0 = cmulw(v0, z1); v1 = cmulw(v1, z1); }
#pragma unroll
      for (int k = 0; k < 2; ++k) {
        const int i1 = 2 + 2 * i + k;
        if (i1 <= 16) {
          T = cmulw(T, w128);
          if (i1 < 4) {
            f2_t v = T;
#pragma unroll
            for (int i0 = 0; i0 < 4; ++i0) { put(KG_L1, 4 * (i1 - 2) + i0, v); if (i0 < 3) v = cmulw(v, z2); }
          } else if (i1 < 8) {
            put(KG_L1, 8 + 2 * (i1 - 4), T); put(KG_L1, 8 + 2 * (i1 - 4) + 1, cmulw(T, z4));
          } else {
            put(KG_L1, 16 + (i1 - 8), T);
          }
        }
      }
    }
  }
  SB;
  if (w == 0) STAMP(1, 4);
  u4_t gr[2];
  const unsigned goff = tid * 16;
#define GRL() do { asm volatile("global_load_dwordx4 %0, %1, %2 sc1" : "=v"(gr[0]) : "v"(goff), "s"(ws + OFF_G1) : "memory"); \
                   asm volatile("global_load_dwordx4 %0, %1, %2 sc1" : "=v"(gr[1]) : "v"(goff), "s"(ws + OFF_G1 + 8192) : "memory"); } while (0)
  GRL();
  if (w < 4) XG(2);
  wave_fence();
  f4_t ka[3];
  {
    h8_t Bf[4];
#pragma unroll
    for (int ks = 0; ks < 4; ++ks) Bf[ks] = *(const h8_t*)(hb + KG_R + c * 272 + 64 * ks + 16 * kg);
#pragma unroll
    for (int t = 0; t < 3; ++t) ka[t] = f4_t{0.f, 0.f, 0.f, 0.f};
#pragma unroll
    for (int ks = 0; ks < 4; ++ks)
#pragma unroll
      for (int t = 0; t < 3; ++t) {
        const int off = (t == 0) ? KG_L0 + c * 272 : (t == 1) ? KG_L1 + c * 272 : KG_L1 + (16 + min(c, 8)) * 272;
        const h8_t Af = *(const h8_t*)(hb + off + 64 * ks + 16 * kg);
        ka[t] = __builtin_amdgcn_mfma_f32_16x16x32_f16(Af, Bf[ks], ka[t], 0, 0, 0);
      }
  }
  SB;
  if (w >= 4) XG(2); else XG(3);
  SB;
  const float iSw = pow2_inv(Sw);
  wave_fence();
  if (hw == 0) *(f4_t*)(hb + lane * 16) = ka[1] * iSw;
  else { *(f4_t*)(hb + lane * 16) = ka[0] * iSw; *(f4_t*)(hb + 1024 + lane * 16) = ka[2] * iSw; }
  __syncthreads();
  if (w == 0) STAMP(1, 5);
  const unsigned etag = *(const unsigned*)(smem + B_BND + 48 + 8);
  if (w >= 4) XG(3);
  {
    const char* pb = smem + (w ^ 1) * KG_W;
    const float Sc = pow2_scale(((const float*)(smem + B_BND))[2 * cl] + ((const float*)(smem + B_BND))[2 * cl + 1], 15);
    char* kr = smem + B_KC + cl * KRS;
    if (hw == 0) {
      const f4_t k0 = (ka[0] * iSw + *(const f4_t*)(pb + lane * 16)) * Sc, k2 = (ka[2] * iSw + *(const f4_t*)(pb + 1024 + lane * 16)) * Sc;
      *(u2_t*)(kr + 2 * (128 * (kg >> 1) + 4 * (kg & 1) + 8 * c)) = u2_t{pk_f16(k0[0], k0[1]), pk_f16(k0[2], k0[3])};
      const unsigned p01 = pk_f16(k2[0], k2[1]), p23 = pk_f16(k2[2], k2[3]);
      u16* k3 = (u16*)kr + 512 + 64 * kg + c;
      if (kg < 2) { k3[0] = (u16)p01; k3[16] = (u16)(p01 >> 16); k3[32] = (u16)p23; k3[48] = (u16)(p23 >> 16); }
      else if (lane == 32) k3[0] = (u16)p01;
      if (lane == 0) ((float*)(smem + B_BND + 32))[cl] = pow2_inv(Sc);
    } else {
      const f4_t k1 = (ka[1] * iSw + *(const f4_t*)(pb + lane * 16)) * Sc;
      const unsigned p01 = pk_f16(k1[0], k1[1]), p23 = pk_f16(k1[2], k1[3]);
      if (kg < 2) {
        *(u2_t*)(kr + 2 * (256 + 64 * kg + 4 * c)) = u2_t{p01, p23};
      } else {
        *(unsigned*)(kr + 2 * (384 + 64 * (kg - 2) + 2 * c)) = p01;
        *(unsigned*)(kr + 2 * (384 + 64 * (kg - 2) + 32 + 2 * c)) = p23;
      }
    }
  }
  __syncthreads();
  const float invs = ((const float*)(smem + B_BND + 32))[w & 3];
  {
    const int kch = tid >> 7, kc = tid & 127;
    const char* kr = smem + B_KC + kch * KRS;
    const int e0 = (kc < 32) ? 8 * kc : (kc < 64) ? 256 + 4 * (kc - 32) : 384 + 2 * (kc - 64);
    const unsigned* kp = (const unsigned*)(kr + 2 * e0);
    const unsigned d0 = kp[0], d1 = kp[1], d2 = kp[2], d3 = kp[3], d4 = kp[4];
    const unsigned kfirst = *(const u16*)kr, k1024 = *(const u16*)(kr + 2 * 512);
    const int dl = 127 - kc;
    const unsigned kfa = *(const u16*)(kr + 2 * (512 + max(2 * dl - 127, 0))), kfb = *(const u16*)(kr + 2 * (512 + max(2 * dl - 126, 0)));
    char* ki = smem + B_KOFF + kch * KDS;
    const unsigned k0 = d0 & 0xffffu;
    unsigned o0, o1, o2, o3, nx;
    if (kc < 32) { o0 = d0; o1 = d1; o2 = d2; o3 = d3; nx = d4; }
    else if (kc < 64) {
      const unsigned k2 = d0 >> 16, k4 = d1 & 0xffffu, k6 = d1 >> 16, k8 = d2 & 0xffffu;
      o0 = k0 * 0x10001u; o1 = k2 | (k4 << 16); o2 = k4 * 0x10001u; o3 = k6 | (k8 << 16); nx = k8;
    } else {
      const unsigned k4 = d0 >> 16, k8 = d1 & 0xffffu;
      o0 = k0 * 0x10001u; o1 = k0 | (k4 << 16); o2 = k4 * 0x10001u; o3 = k8 * 0x10001u; nx = k8;
    }
    *(u4_t*)(ki + 16 * (kc + 2)) = u4_t{o0, o1, o2, o3};
    *(u4_t*)(ki + KD_O + 16 * (kc + 2)) = u4_t{__builtin_amdgcn_alignbit(o1, o0, 16), __builtin_amdgcn_alignbit(o2, o1, 16),
                                              __builtin_amdgcn_alignbit(o3, o2, 16), __builtin_amdgcn_alignbit(nx, o3, 16)};
    if (kc < 6) {
      const int cp = (kc < 2) ? kc : 128 + kc;
      const unsigned kk = k1024 * 0x10001u;
      const u4_t e = (kc == 2) ? u4_t{kk, kk, k1024, 0u} : u4_t{0u, 0u, 0u, 0u};
      const u4_t o = (kc == 2) ? u4_t{kk, kk, 0u, 0u} : u4_t{0u, 0u, 0u, (kc == 1) ? (kfirst << 16) : 0u};
      *(u4_t*)(ki + 16 * cp) = e;
      *(u4_t*)(ki + KD_O + 16 * cp) = o;
    }
    if (kc < 80) *(unsigned*)(ki + KD_DR + 4 * kc) = (dl >= 64) ? (kfa | (kfb << 16)) : 0u;
  }
  *(unsigned*)(smem + B_UOFF + (tid >> 7) * B_UCH + 4096 + 4 * (tid & 127)) = 0u;
  asm volatile("s_waitcnt vmcnt(2)" : "+v"(gr[0]), "+v"(gr[1]), "+v"(xg[0]), "+v"(xg[1])::"memory");
  {
    for (int spin = 0; spin < SPIN_MAX; ++spin) {
      if (__all(gr[0][1] == etag && gr[0][3] == etag && gr[1][1] == etag && gr[1][3] == etag)) break;
      __builtin_amdgcn_s_sleep(2);
      GRL();
      asm volatile("s_waitcnt vmcnt(0)" : "+v"(gr[0]), "+v"(gr[1])::"memory");
    }
#pragma unroll
    for (int it = 0; it < 2; ++it)
#pragma unroll
      for (int r = 0; r < 2; ++r)
        if (gr[it][2 * r + 1] != etag) gr[it][2 * r] = ln1_row(x, 2 * (tid + 512 * it) + r);
  }
  if (w == 0) STAMP(1, 6);
  const float dk[4] = {bf_lo(dkv.x), bf_hi(dkv.x), bf_lo(dkv.y), bf_hi(dkv.y)};
  float z[2][8];
#pragma unroll
  for (int it = 0; it < 2; ++it) {
    if (it == 1) asm volatile("s_waitcnt vmcnt(0)" : "+v"(xg[2]), "+v"(xg[3])::"memory");
    const int j = tid + 512 * it;
    const unsigned s0 = gr[it][0], s1 = gr[it][2];
    const float m0 = bf_lo(s0), r0 = __builtin_amdgcn_rcpf(bf_hi(s0)), m1 = bf_lo(s1), r1 = __builtin_amdgcn_rcpf(bf_hi(s1));
    const u2_t xa = xg[2 * it], xc = xg[2 * it + 1];
    const float x0[4] = {bf_lo(xa.x), bf_hi(xa.x), bf_lo(xa.y), bf_hi(xa.y)};
    const float x1[4] = {bf_lo(xc.x), bf_hi(xc.x), bf_lo(xc.y), bf_hi(xc.y)};
#pragma unroll
    for (int cc = 0; cc < 4; ++cc) {
      const float u0 = (x0[cc] - m0) * r0, u1 = (x1[cc] - m1) * r1;
      *(unsigned*)(smem + B_UOFF + cc * B_UCH + 4 * (1023 - j)) = pk_f16(u1, u0);
      z[it][cc] = x0[cc] + u0 * dk[cc];
      z[it][4 + cc] = x1[cc] + u1 * dk[cc];
    }
  }
  __syncthreads();
  if (w == 0) STAMP(1, 2);
  if (w >= 4 && w < 7 && cb * 3 + (w - 4) < 432) pack_wd(wd, ws, smem + B_PACK + (w - 4) * 5120, cb * 3 + (w - 4), lane);
  f4_t acc[8];
  if (w < 4) {
    const char* ub = smem + B_UOFF + w * B_UCH + 4112 - 32 * c - 16 * kg - 512 * 7;
    const char* kb = smem + B_KOFF + w * KDS + ((c & 1) ? 0 : KD_O) + 4 * (12 + ((c + 1) >> 1) - 4 * kg);
    const unsigned* dr = (const unsigned*)(smem + B_KOFF + w * KDS + KD_DR) + (127 - c + 4 * kg);
    const char* uw = smem + B_UOFF + w * B_UCH + 4112 - 32 * c - 16 * kg;
    auto rdB = [&](BFrag& F, int i0, int p) { F.B[p] = *(const h8_t*)(ub + 512 * (7 - p) + 64 * i0); };
    auto rdA = [&](BFrag& F, int i0, int i1, int half) {
      const unsigned* ap = (const unsigned*)(kb + 64 * i0 + 512 * i1) + 2 * half;
      F.A[i1][half] = u2_t{ap[0], ap[1]};
    };
    auto frag = [&](const u2_t (&a)[2]) { return u4_t{a[0].x, a[0].y, a[1].x, a[1].y}; };
    auto rdnext = [&](BFrag& F, int i0, int m) {
      if (m < 8) rdB(F, i0, m); else if (m < 16) rdA(F, i0, (m - 8) >> 1, (m - 8) & 1);
    };
    auto step = [&](auto i0c, const BFrag& F, BFrag& N, auto nextc) {
      constexpr int I0 = decltype(i0c)::value, NX = decltype(nextc)::value;
      int m = 0;
#pragma unroll
      for (int i1 = 0; i1 < 5; ++i1) {
        if (i1 == 4 && I0 != 0) continue;
        const u4_t a = frag(F.A[i1]);
#pragma unroll
        for (int t = i1; t < 8; ++t) {
          if (I0 == 0 && i1 == 0) mfma_f16_0(acc[t], a, F.B[t]); else mfma_f16(acc[t], a, F.B[t - i1]);
          if (NX >= 0) rdnext(N, NX, m);
          SB;
          ++m;
        }
      }
    };
    BFrag F0, F1;
    h8_t U9[4], U7[4];
#pragma unroll
    for (int bt = 0; bt < 4; ++bt) { U9[bt] = *(const h8_t*)(uw - 512 * bt + 16); U7[bt] = *(const h8_t*)(uw - 512 * bt); }
#pragma unroll
    for (int m = 0; m < 16; ++m) rdnext(F0, 0, m);
    rdA(F0, 0, 4, 0); rdA(F0, 0, 4, 1);
    SB;
    f4_t X9[4], X7[4];
#pragma unroll
    for (int bt = 0; bt < 4; ++bt) { mfma_f16_0(X9[bt], __builtin_bit_cast(u4_t, U9[bt]), band9); mfma_f16_0(X7[bt], __builtin_bit_cast(u4_t, U7[bt]), band7); }
    SB;
    step(IC(0), F0, F1, IC(1));
    h8_t Vf[4]; u4_t Tf[4];
    {
      int m = 0;
#pragma unroll
      for (int i1 = 0; i1 < 4; ++i1) {
        const u4_t a = frag(F1.A[i1]);
#pragma unroll
        for (int t = i1; t < 8; ++t) {
          mfma_f16(acc[t], a, F1.B[t - i1]);
          rdnext(F0, 2, m);
          if (m < 4) {
            const u4_t vu = {pk_f16(X7[m][0], X9[m][0]), pk_f16(X7[m][1], X9[m][1]), pk_f16(X7[m][2], X9[m][2]), pk_f16(X7[m][3], X9[m][3])};
            Vf[m] = __builtin_bit_cast(h8_t, vu);
          }
          if (m >= 16 && m < 20) { const unsigned* tp = dr - 16 * (m - 16 + 4); Tf[m - 16] = u4_t{tp[0], tp[1], tp[2], tp[3]}; }
          SB;
          ++m;
        }
      }
    }
    step(IC(2), F0, F1, IC(3));
#pragma unroll
    for (int bt = 0; bt < 4; ++bt)
#pragma unroll
      for (int at = 4 + bt; at < 8; ++at) mfma_f16(acc[at], __builtin_bit_cast(u4_t, Vf[bt]), __builtin_bit_cast(h8_t, Tf[at - bt - 4]));
    SB;
    step(IC(3), F1, F0, IC(4));
    step(IC(4), F0, F1, IC(5));
    step(IC(5), F1, F0, IC(6));
    step(IC(6), F0, F1, IC(7));
    step(IC(7), F1, F0, IC(-1));
    asm volatile("s_nop 7\n\ts_nop 1" : "+v"(acc[0]), "+v"(acc[1]), "+v"(acc[2]), "+v"(acc[3]), "+v"(acc[4]), "+v"(acc[5]), "+v"(acc[6]), "+v"(acc[7]));
  }
  if (w == 0) STAMP(1, 3);
  bar();
  if (w < 4) {
#pragma unroll
    for (int t = 0; t < 8; ++t) *(f4_t*)(smem + w * B_YCH + t * 1280 + kg * 320 + c * 16) = acc[t] * invs;
  }
  asm volatile("s_waitcnt lgkmcnt(0)" ::: "memory");
  bar();
#pragma unroll
  for (int it = 0; it < 2; ++it) {
    const int j = tid + 512 * it, t = 2 * j;
    const int yo = (t >> 8) * 1280 + ((t >> 2) & 3) * 320 + ((t >> 4) & 15) * 16 + (t & 3) * 4;
    float h0[4], h1[4];
#pragma unroll
    for (int cc = 0; cc < 4; ++cc) {
      f2_t y = *(const f2_t*)(smem + cc * B_YCH + yo);
      h0[cc] = z[it][cc] + y.x;
      h1[cc] = z[it][4 + cc] + y.y;
    }
    char* dst = ws + OFF_H8 + ((size_t)(q >> 1) * SL + t) * 16 + (q & 1) * 8;
    st8(dst, u2_t{pk_bf16(h0[0], h0[1]), pk_bf16(h0[2], h0[3])});
    st8(dst + 16, u2_t{pk_bf16(h1[0], h1[1]), pk_bf16(h1[2], h1[3])});
  }
  if (w == 0) STAMP(1, 7);
}

constexpr int C_T = 240;
constexpr int C_SLOT = 23552, C_NSLOT = 6, C_GOFF = 15360;
constexpr int C_STAT = C_NSLOT * C_SLOT;
constexpr int C_CS = C_STAT + 960;
constexpr int C_LDS = C_CS + 512;
constexpr int C_STEPS = 20;
constexpr size_t OFF_DUMMY = 0x1140000;

__device__ __forceinline__ unsigned ln_pack(float sum, float sq) {
  const float mean = sum * (1.0f / DM);
  return ln_fin(mean, fmaxf(sq * (1.0f / DM) - mean * mean, 0.f));
}
__device__ __forceinline__ unsigned ln_row(const char* h8, int t) {
  float s = 0.f, q = 0.f;
  for (int pl = 0; pl < 80; ++pl) {
    const u4_t v = *(const u4_t*)(h8 + (size_t)pl * 32768 + t * 16);
    for (int j = 0; j < 4; ++j) { const float a = bf_lo(v[j]), b = bf_hi(v[j]); s += a + b; q += a * a + b * b; }
  }
  return ln_pack(s, q);
}

template <int NDMA>
__device__ __forceinline__ void c_loader(char* smem, char* ws, const char* wbase, int w4, int lane, int t0, int jline, int xcd) {
  const unsigned lds0 = (unsigned)(size_t)(LAS char*)smem;
  const char* h8 = ws + OFF_H8;
  unsigned voffH[4], voffW[2];
#pragma unroll
  for (int k = 0; k < 2; ++k) {
    const int r = 8 * (2 * (w4 & 1) + k) + (lane >> 3), c = lane & 7;
    const int f = ((r >> 1) & 1) | ((r >> 2) & 2);
    voffW[k] = r * (FF * 2) + 128 * jline + (((c >> 1) ^ f) * 32) + (c & 1) * 16;
  }
#pragma unroll
  for (int k = 0; k < 4; ++k) {
    const int idx = 64 * (w4 + 4 * k) + lane, pl = idx / C_T, tt = idx % C_T;
    const int t = min(t0 + tt, SL - 1);
    voffH[k] = pl * 32768 + t * 16;
  }
  const unsigned ldsW = C_GOFF + (w4 >> 1) * 4096 + (w4 & 1) * 2048;
  auto issue = [&](int step, unsigned so) {
#pragma unroll
    for (int k = 0; k < 2; ++k) glds16(wbase + (size_t)step * (32 * FF * 2), voffW[k], lds0 + so + ldsW + k * 1024);
#pragma unroll
    for (int k = 0; k < NDMA - 2; ++k) glds16(h8 + (size_t)step * (4 * 32768), voffH[k], lds0 + so + (w4 + 4 * k) * 1024);
  };
  constexpr unsigned S = C_SLOT;
  issue(0, 0); issue(1, S); issue(2, 2 * S); issue(3, 3 * S);
  wait_vm<3 * NDMA>(); bar();
  issue(4, 4 * S);
  wait_vm<2 * NDMA>(); bar();
  issue(5, 5 * S); issue(6, 0);
  unsigned sw = S;
#pragma unroll 1
  for (int j = 1; j < 7; ++j) {
    wait_vm<2 * NDMA>();
    bar();
    issue(2 * j + 5, sw);
    sw += S; if (sw == C_NSLOT * S) sw = 0;
    issue(2 * j + 6, sw);
    sw += S; if (sw == C_NSLOT * S) sw = 0;
  }
  wait_vm<2 * NDMA>(); bar();
  issue(19, sw);
  const int tl = min(60 * w4 + lane, C_T - 1), tg = min(t0 + tl, SL - 1);
  const gu64* gp = (const gu64*)(ws + OFF_G2) + tg;
  const float* csp = (const float*)(ws + OFF_CS) + ((lane >> 4) & 1) * FF + 64 * jline + 16 * w4 + (lane & 15);
  const char* tb = ws + OFF_WP2 + (size_t)(4 * (xcd & 1)) * (54 * 5120);
  const int tl0 = ((int)(blockIdx.x >> 3) * 4 + w4) * 68;
  u2_t gv; float csv; unsigned tv0, tv1;
  asm volatile("global_load_dwordx2 %0, %1, off sc1" : "=v"(gv) : "v"(gp) : "memory");
  asm volatile("global_load_dword %0, %1, off" : "=v"(csv) : "v"(csp) : "memory");
  asm volatile("global_load_dword %0, %1, off" : "=v"(tv0) : "v"(tb + (size_t)min(tl0 + lane, 8639) * 128) : "memory");
  asm volatile("global_load_dword %0, %1, off" : "=v"(tv1) : "v"(tb + (size_t)min(tl0 + 64 + (lane & 3), 8639) * 128) : "memory");
  wait_vm<NDMA + 4>(); bar();
  asm volatile("s_waitcnt vmcnt(0)" : "+v"(gv), "+v"(csv), "+v"(tv0), "+v"(tv1)::"memory");
  bar();
  {
    bool ok = gv.y == 1u;
    for (int spin = 0; spin < 4096 && !__all(ok); ++spin) {
      const unsigned long long g = __hip_atomic_load(gp, __ATOMIC_RELAXED, __HIP_MEMORY_SCOPE_AGENT);
      gv.x = (unsigned)g; ok = (unsigned)(g >> 32) == 1u;
      __builtin_amdgcn_s_sleep(2);
    }
    if (!__all(ok)) gv.x = ln_row(h8, tg);
    if (lane < 60) *(float*)(smem + C_STAT + (60 * w4 + lane) * 4) = bf_lo(gv.x);
    if (lane < 32) *(float*)(smem + C_CS + (lane >> 4) * 256 + (16 * w4 + (lane & 15)) * 4) = csv;
  }
  asm volatile("s_waitcnt lgkmcnt(0)" ::: "memory");
  bar();
}

struct CFrag { b8_t A[2][2]; b8_t B[8]; };

extern "C" __global__ __launch_bounds__(512, 2) void kC(const u16* __restrict__ wg, const u16* __restrict__ wu, char* __restrict__ ws) {
  extern __shared__ __attribute__((aligned(16))) char smem[];
  const int tid = threadIdx.x, lane = tid & 63;
  const int w = __builtin_amdgcn_readfirstlane(tid >> 6);
  int panel, jline;
  const int xcd = blockIdx.x & 7, rank = blockIdx.x >> 3;
  {
    const int g4 = xcd >> 1, a = xcd & 1, nl = (g4 < 3) ? 7 : 6, total = 9 * nl, n0 = (total + 1) >> 1;
    if (a == 0 ? rank >= n0 : rank >= total - n0) {
      const int sp = (xcd < 6) ? (xcd >> 1) : 3 + 5 * (xcd - 6) + (rank - 27);
      const int wv = sp * 8 + w;
      if (w == 0) STAMP(2, 0);
      for (int tile = wv; tile < 128; tile += 104) {
        const int t = 16 * tile + (lane & 15);
        const char* hp = ws + OFF_H8 + (size_t)(lane >> 4) * 32768 + t * 16;
        u4_t v[20];
#pragma unroll
        for (int i = 0; i < 20; ++i) v[i] = *(const u4_t*)(hp + (size_t)i * (4 * 32768));
        float s0 = 0.f, s1 = 0.f, q0 = 0.f, q1 = 0.f;
        unsigned k_both = 0x3f803f80u;
        asm volatile("" : "+s"(k_both));
#pragma unroll
        for (int i = 0; i < 20; ++i) {
          s0 = dot2bf(v[i][0], k_both, s0); q0 = dot2bf(v[i][0], v[i][0], q0);
          s1 = dot2bf(v[i][1], k_both, s1); q1 = dot2bf(v[i][1], v[i][1], q1);
          s0 = dot2bf(v[i][2], k_both, s0); q0 = dot2bf(v[i][2], v[i][2], q0);
          s1 = dot2bf(v[i][3], k_both, s1); q1 = dot2bf(v[i][3], v[i][3], q1);
        }
        float s = s0 + s1, q = q0 + q1;
        s += __shfl_xor(s, 16); q += __shfl_xor(q, 16);
        s += __shfl_xor(s, 32); q += __shfl_xor(q, 32);
        if (lane < 16)
          __hip_atomic_store((gu64*)(ws + OFF_G2) + t, (1ull << 32) | ln_pack(s, q), __ATOMIC_RELAXED, __HIP_MEMORY_SCOPE_AGENT);
      }
      if (w == 0) STAMP(2, 7);
      return;
    }
    const int idx = rank + a * n0;
    panel = idx / nl; jline = 7 * g4 + idx % nl;
  }
  const int t0 = panel * C_T;
  if (w == 0) STAMP(2, 0);
  if (w >= 4) {
    const int w4 = w - 4;
    const char* wbase = (const char*)((w4 >> 1) ? wu : wg);
    if (w4 < 3) c_loader<6>(smem, ws, wbase, w4, lane, t0, jline, xcd);
    else c_loader<5>(smem, ws, wbase, w4, lane, t0, jline, xcd);
    return;
  }
  const int s = w >> 1, th = w & 1;
  const int fr = lane & 15, kg = lane >> 4, q = (lane & 15) >> 2, p = lane & 3;
  const unsigned hb0 = kg * 3840 + fr * 16 + 256 * 7;
  const unsigned hb1 = kg * 3840 + fr * 16 + 256 * (th ? 8 : 0);
  unsigned wa[2];
#pragma unroll
  for (int v = 0; v < 2; ++v) {
    const int sub = 2 * s + (v ? 1 - th : th);
    const int f = (q >> 1) | (2 * (kg & 1));
    wa[v] = C_GOFF + (8 * kg + q) * 128 + ((sub ^ f) * 32) + 8 * p;
  }
  f4_t ag[8][2], au[8][2];
#pragma unroll
  for (int u = 0; u < 8; ++u)
#pragma unroll
    for (int v = 0; v < 2; ++v) { ag[u][v] = f4_t{0.f, 0.f, 0.f, 0.f}; au[u][v] = f4_t{0.f, 0.f, 0.f, 0.f}; }
  auto load = [&](CFrag& F, unsigned so) {
    const char* sp = smem + so;
#pragma unroll
    for (int v = 0; v < 2; ++v)
#pragma unroll
      for (int m = 0; m < 2; ++m) {
        s4_t lo = tr_read(sp + wa[v] + m * 4096), hi = tr_read(sp + wa[v] + m * 4096 + 512);
        u2_t a = __builtin_bit_cast(u2_t, lo), b = __builtin_bit_cast(u2_t, hi);
        F.A[v][m] = __builtin_bit_cast(b8_t, (u4_t{a.x, a.y, b.x, b.y}));
      }
    F.B[0] = *(const b8_t*)(sp + hb0);
#pragma unroll
    for (int u = 1; u < 8; ++u) F.B[u] = *(const b8_t*)(sp + hb1 + 256 * (u - 1));
  };
  auto mma = [&](const CFrag& F) {
    ag[0][0] = __builtin_amdgcn_mfma_f32_16x16x32_bf16(F.A[0][0], F.B[0], ag[0][0], 0, 0, 0);
    au[0][0] = __builtin_amdgcn_mfma_f32_16x16x32_bf16(F.A[0][1], F.B[0], au[0][0], 0, 0, 0);
#pragma unroll
    for (int u = 1; u < 8; ++u)
#pragma unroll
      for (int v = 0; v < 2; ++v) {
        ag[u][v] = __builtin_amdgcn_mfma_f32_16x16x32_bf16(F.A[v][0], F.B[u], ag[u][v], 0, 0, 0);
        au[u][v] = __builtin_amdgcn_mfma_f32_16x16x32_bf16(F.A[v][1], F.B[u], au[u][v], 0, 0, 0);
      }
  };
  auto interleave = [&]() {
#pragma unroll
    for (int i = 0; i < 16; ++i) {
      __builtin_amdgcn_sched_group_barrier(0x008, 1, 0);
      __builtin_amdgcn_sched_group_barrier(0x100, 1, 0);
    }
    __builtin_amdgcn_sched_group_barrier(0x008, 14, 0);
  };
  CFrag F0, F1;
  bar();
  if (w == 0) STAMP(2, 1);
  load(F0, 0);
  unsigned sn = C_SLOT;
  SB;
#pragma unroll 1
  for (int j = 0; j < 9; ++j) {
    asm volatile("s_waitcnt lgkmcnt(0)" ::: "memory");
    bar(); SB;
    load(F1, sn);
    mma(F0);
    interleave();
    SB;
    sn += C_SLOT; if (sn == C_NSLOT * C_SLOT) sn = 0;
    load(F0, sn);
    mma(F1);
    interleave();
    SB;
    sn += C_SLOT; if (sn == C_NSLOT * C_SLOT) sn = 0;
  }
  asm volatile("s_waitcnt lgkmcnt(0)" ::: "memory");
  bar(); SB;
  load(F1, sn);
  mma(F0);
  interleave();
  SB;
  bar(); SB;
  f4_t csg[2], csu[2];
  float mu[8];
#pragma unroll
  for (int v = 0; v < 2; ++v) {
    const int sub = 2 * s + (v ? 1 - th : th);
    csg[v] = *(const f4_t*)(smem + C_CS + (16 * sub + 4 * kg) * 4);
    csu[v] = *(const f4_t*)(smem + C_CS + 256 + (16 * sub + 4 * kg) * 4);
  }
#pragma unroll
  for (int u = 0; u < 8; ++u) {
    const int tile = (u == 0) ? 7 : (th ? 7 + u : u - 1);
    mu[u] = *(const float*)(smem + C_STAT + (16 * tile + fr) * 4);
  }
  mma(F1);
  if (w == 0) STAMP(2, 2);
  char* hh = ws + OFF_HH + (size_t)(2 * jline + s) * 1024 + lane * 16;
  char* sink = ws + OFF_DUMMY + lane * 16;
  auto epi = [&](auto thc) {
    constexpr int TH = decltype(thc)::value;
#pragma unroll
    for (int u = 0; u < 8; ++u) {
      const int tile = (u == 0) ? 7 : (TH ? 7 + u : u - 1);
      const int t16 = 15 * panel + tile;
      char* dst = (t16 < 128) ? hh + (size_t)t16 * (54 * 1024) : sink;
      u2_t o[2];
#pragma unroll
      for (int v = 0; v < 2; ++v) {
        if (u == 0 && v == 1) continue;
        const f4_t r = (ag[u][v] - mu[u] * csg[v]) * (au[u][v] - mu[u] * csu[v]);
        o[v] = u2_t{pk_bf16(r[0], r[1]), pk_bf16(r[2], r[3])};
      }
      if (u == 0) st8(dst + 8 * TH, o[0]);
      else st16(dst, u4_t{o[TH].x, o[TH].y, o[1 - TH].x, o[1 - TH].y});
    }
  };
  if (th) epi(IC(1)); else epi(IC(0));
  if (w == 0) STAMP(2, 7);
}

constexpr int D_SLOT = 15360, D_NSLOT = 9, D_STEPS = 18;
constexpr int D_LDS = D_NSLOT * D_SLOT;

template <int NDMA>
__device__ __forceinline__ void d_loader(char* smem, const char* wp, int w4, int lane) {
  const unsigned lds0 = (unsigned)(size_t)(LAS char*)smem;
  const unsigned voff = lane * 16;
  auto issue = [&](int step, unsigned so) {
#pragma unroll
    for (int k = 0; k < NDMA; ++k) {
      const int i = w4 + 4 * k;
      glds16(wp + (size_t)step * D_SLOT + i * 1024, voff, lds0 + so + i * 1024);
    }
  };
  constexpr unsigned S = D_SLOT;
  issue(0, 0); issue(1, S); issue(2, 2 * S);
  wait_vm<2 * NDMA>(); bar();
  issue(3, 3 * S); issue(4, 4 * S); issue(5, 5 * S);
  wait_vm<3 * NDMA>(); bar();
  issue(6, 6 * S); issue(7, 7 * S); issue(8, 8 * S);
  unsigned sw = 0;
#pragma unroll 1
  for (int j = 1; j < 5; ++j) {
    wait_vm<4 * NDMA>();
    bar();
    issue(2 * j + 7, sw);
    sw += S; if (sw == D_NSLOT * S) sw = 0;
    issue(2 * j + 8, sw);
    sw += S; if (sw == D_NSLOT * S) sw = 0;
  }
  wait_vm<4 * NDMA>(); bar();
  issue(17, sw);
  wait_vm<3 * NDMA>(); bar();
  wait_vm<1 * NDMA>(); bar();
  wait_vm<0>(); bar();
}

extern "C" __global__ __launch_bounds__(512, 2) void kD(char* __restrict__ ws, u16* __restrict__ out) {
  extern __shared__ __attribute__((aligned(16))) char smem[];
  const int tid = threadIdx.x, lane = tid & 63;
  const int w = __builtin_amdgcn_readfirstlane(tid >> 6);
  const int xcd = blockIdx.x & 7, rank = blockIdx.x >> 3;
  const int it = 8 * (xcd >> 1) + (rank & 7), dg = 4 * (xcd & 1) + (rank >> 3);
  if (w == 0) STAMP(3, 0);
  if (w >= 4) {
    const int w4 = w - 4;
    const char* wp = ws + OFF_WP2 + (size_t)dg * (54 * 5120);
    if (w4 < 3) d_loader<4>(smem, wp, w4, lane);
    else d_loader<3>(smem, wp, w4, lane);
    return;
  }
  const int fr = lane & 15, g = lane >> 4;
  const int t = 64 * it + 16 * w + fr;
  const char* hp = ws + OFF_HH + (size_t)(4 * it + w) * (54 * 1024);
  const unsigned voff = lane * 16;
  u4_t R[18];
#pragma unroll
  for (int i = 0; i < 9; ++i)
    asm volatile("global_load_dwordx4 %0, %1, %2" : "=v"(R[i]) : "v"(voff), "s"(hp + (size_t)i * 1024) : "memory");
  u2_t res[5]; unsigned st;
  f4_t acc[5];
#pragma unroll
  for (int dt = 0; dt < 5; ++dt) acc[dt] = f4_t{0.f, 0.f, 0.f, 0.f};
  b8_t A0[5], A1[5];
  auto rdA = [&](b8_t (&A)[5], unsigned off) {
    const char* sp = smem + off + lane * 16;
#pragma unroll
    for (int dt = 0; dt < 5; ++dt) A[dt] = *(const b8_t*)(sp + dt * 1024);
  };
  bar();
  if (w == 0) STAMP(3, 1);
  rdA(A0, 0);
  unsigned so = 0;
  auto kstep = [&](auto mode, auto idx, b8_t (&Ac)[5], b8_t (&An)[5], const char* hnext) {
    constexpr int MODE = decltype(mode)::value, i = decltype(idx)::value, kk = i % 3;
    if (i % 6 == 0) bar();
    unsigned nx = so + (kk + 1) * 5120;
    if (kk == 2) { so += D_SLOT; if (so == D_NSLOT * D_SLOT) so = 0; nx = so; }
    SB;
    if (!(MODE == 2 && i == 17)) rdA(An, nx);
    constexpr int NV = (MODE == 0) ? (i < 9 ? 8 + i : 26 - i) : (MODE == 1) ? (i < 9 ? 25 - i : 17) : (i == 0 ? 17 : 23 - i);
    asm volatile("s_waitcnt vmcnt(%1)" : "+v"(R[i]) : "i"(NV) : "memory");
    const b8_t B = __builtin_bit_cast(b8_t, R[i]);
#pragma unroll
    for (int dt = 0; dt < 5; ++dt) acc[dt] = __builtin_amdgcn_mfma_f32_16x16x32_bf16(Ac[dt], B, acc[dt], 0, 0, 0);
    SB;
    if (MODE == 0 && i < 9)
      asm volatile("global_load_dwordx4 %0, %1, %2" : "=v"(R[i < 9 ? 9 + i : 0]) : "v"(voff), "s"(hp + (size_t)(9 + i) * 1024) : "memory");
    if (MODE != 2) asm volatile("global_load_dwordx4 %0, %1, %2" : "+v"(R[i]) : "v"(voff), "s"(hnext + (size_t)i * 1024) : "memory");
    if (MODE == 2 && i == 0) {
#pragma unroll
      for (int dt = 0; dt < 5; ++dt) {
        const int d = 80 * dg + 16 * dt + 4 * g;
        const char* rp = ws + OFF_H8 + ((size_t)(d >> 3) * SL + t) * 16 + (d & 4) * 2;
        asm volatile("global_load_dwordx2 %0, %1, off" : "=v"(res[dt]) : "v"(rp) : "memory");
      }
      asm volatile("global_load_dword %0, %1, off" : "=v"(st) : "v"(ws + OFF_G2 + (size_t)t * 8) : "memory");
    }
    SB;
  };
  auto round = [&](auto mode, const char* hnext) {
    kstep(mode, IC(0), A0, A1, hnext); kstep(mode, IC(1), A1, A0, hnext); kstep(mode, IC(2), A0, A1, hnext);
    kstep(mode, IC(3), A1, A0, hnext); kstep(mode, IC(4), A0, A1, hnext); kstep(mode, IC(5), A1, A0, hnext);
    kstep(mode, IC(6), A0, A1, hnext); kstep(mode, IC(7), A1, A0, hnext); kstep(mode, IC(8), A0, A1, hnext);
    kstep(mode, IC(9), A1, A0, hnext); kstep(mode, IC(10), A0, A1, hnext); kstep(mode, IC(11), A1, A0, hnext);
    kstep(mode, IC(12), A0, A1, hnext); kstep(mode, IC(13), A1, A0, hnext); kstep(mode, IC(14), A0, A1, hnext);
    kstep(mode, IC(15), A1, A0, hnext); kstep(mode, IC(16), A0, A1, hnext); kstep(mode, IC(17), A1, A0, hnext);
  };
  round(IC(0), hp + 18 * 1024);
  round(IC(1), hp + 36 * 1024);
  round(IC(2), hp);
  if (w == 0) STAMP(3, 2);
  asm volatile("s_waitcnt vmcnt(0)" : "+v"(res[0]), "+v"(res[1]), "+v"(res[2]), "+v"(res[3]), "+v"(res[4]), "+v"(st)::"memory");
  const float sd = bf_hi(st), r2 = 1.0f / (sd * sd);
#pragma unroll
  for (int dt = 0; dt < 5; ++dt) {
    const int d = 80 * dg + 16 * dt + 4 * g;
    const float h0 = bf_lo(res[dt].x), h1 = bf_hi(res[dt].x), h2 = bf_lo(res[dt].y), h3 = bf_hi(res[dt].y);
    st8(out + (size_t)t * DM + d, u2_t{pk_bf16(h0 + r2 * acc[dt][0], h1 + r2 * acc[dt][1]), pk_bf16(h2 + r2 * acc[dt][2], h3 + r2 * acc[dt][3])});
  }
  if (w == 0) STAMP(3, 7);
}

extern "C" void kernel_launch(void* const* d_in, const int* in_sizes, int n_in, void* d_out, int out_size, void* d_ws,
                              size_t ws_size, hipStream_t stream) {
  char* ws = (char*)d_ws;
  (void)hipFuncSetAttribute((const void*)kB, hipFuncAttributeMaxDynamicSharedMemorySize, B_LDS);
  (void)hipFuncSetAttribute((const void*)kC, hipFuncAttributeMaxDynamicSharedMemorySize, C_LDS);
  (void)hipFuncSetAttribute((const void*)kD, hipFuncAttributeMaxDynamicSharedMemorySize, D_LDS);
  kB<<<256, 512, B_LDS, stream>>>((const u16*)d_in[0], (const u16*)d_in[1], (const u16*)d_in[2], (const u16*)d_in[3], (const u16*)d_in[4],
                                  (const u16*)d_in[5], (const u16*)d_in[6], (const u16*)d_in[7], (const u16*)d_in[8], (const u16*)d_in[10],
                                  (const u16*)d_in[11], (const u16*)d_in[12], (const u16*)d_out, ws);
  kC<<<256, 512, C_LDS, stream>>>((const u16*)d_in[10], (const u16*)d_in[11], ws);
  kD<<<256, 512, D_LDS, stream>>>(ws, (u16*)d_out);
}
```

```cpp
#include <hip/hip_runtime.h>
#include <hip/hip_bf16.h>
#include <stdint.h>
#include <type_traits>

typedef unsigned short u16;
typedef _Float16 h2_t __attribute__((ext_vector_type(2)));
typedef _Float16 h8_t __attribute__((ext_vector_type(8)));
typedef __bf16 b2_t __attribute__((ext_vector_type(2)));
typedef __bf16 b8_t __attribute__((ext_vector_type(8)));
typedef float f2_t __attribute__((ext_vector_type(2)));
typedef float f4_t __attribute__((ext_vector_type(4)));
typedef unsigned u2_t __attribute__((ext_vector_type(2)));
typedef unsigned u4_t __attribute__((ext_vector_type(4)));
typedef short s4_t __attribute__((ext_vector_type(4)));
typedef __attribute__((address_space(1))) unsigned long long gu64;
#define LAS __attribute__((address_space(3)))
#define SB __builtin_amdgcn_sched_barrier(0)
#define IC(n) std::integral_constant<int, n>{}

constexpr int SL = 2048;
constexpr int DM = 640;
constexpr int FF = 1728;

constexpr size_t OFF_G1 = 0;
constexpr size_t OFF_EP = 0x10000;
constexpr size_t OFF_RD = 0x10100;
constexpr size_t OFF_H8 = 0x5A0000;
constexpr size_t OFF_HH = 0x820000;
constexpr size_t OFF_WP2 = 0xF00000;
constexpr size_t OFF_CS = 0x1120000;
constexpr size_t OFF_G2 = 0x1130000;
constexpr int KRS = 1344;
constexpr int KDS = 4864;
constexpr int KD_O = 2240;
constexpr int KD_DR = 4384;

__device__ __forceinline__ float bf_lo(unsigned d) { return __uint_as_float(d << 16); }
__device__ __forceinline__ float bf_hi(unsigned d) { return __uint_as_float(d & 0xffff0000u); }
__device__ __forceinline__ float bf2f(u16 v) { return __uint_as_float((unsigned)v << 16); }
__device__ __forceinline__ unsigned pk_bf16(float a, float b) {
  f2_t v = {a, b};
  return __builtin_bit_cast(unsigned, __builtin_convertvector(v, b2_t));
}
__device__ __forceinline__ unsigned pk_f16(float a, float b) {
  f2_t v = {a, b};
  return __builtin_bit_cast(unsigned, __builtin_convertvector(v, h2_t));
}
__device__ __forceinline__ float rbf(float a) { return bf_lo(pk_bf16(a, 0.f)); }
template <int N> __device__ __forceinline__ float dpp_ror_add(float v) {
  int r = __builtin_amdgcn_update_dpp(0, __float_as_int(v), 0x120 + N, 0xf, 0xf, false);
  return v + __int_as_float(r);
}
__device__ __forceinline__ float row16_sum(float v) {
  v = dpp_ror_add<8>(v); v = dpp_ror_add<4>(v); v = dpp_ror_add<2>(v); v = dpp_ror_add<1>(v);
  return v;
}
__device__ __forceinline__ float wave_sum(float v) {
  v = row16_sum(v);
  const int b = __float_as_int(v);
  return (__int_as_float(__builtin_amdgcn_readlane(b, 0)) + __int_as_float(__builtin_amdgcn_readlane(b, 16))) +
         (__int_as_float(__builtin_amdgcn_readlane(b, 32)) + __int_as_float(__builtin_amdgcn_readlane(b, 48)));
}
template <typename T> __device__ __forceinline__ T ldnt(const T* p) { return __builtin_nontemporal_load(p); }
__device__ __forceinline__ void wave_fence() {
  __builtin_amdgcn_fence(__ATOMIC_RELEASE, "wavefront");
  __builtin_amdgcn_wave_barrier();
  __builtin_amdgcn_fence(__ATOMIC_ACQUIRE, "wavefront");
}
__device__ __forceinline__ s4_t tr_read(const char* p) {
  return __builtin_amdgcn_ds_read_tr16_b64_v4i16((LAS s4_t*)p);
}
__device__ __forceinline__ void glds16(const void* sbase, unsigned voff, unsigned lds_addr) {
  asm volatile("s_mov_b32 m0, %2\n\ts_nop 0\n\tglobal_load_lds_dwordx4 %0, %1" ::"v"(voff), "s"(sbase), "s"(lds_addr) : "memory");
}
template <int N> __device__ __forceinline__ void wait_vm() { asm volatile("s_waitcnt vmcnt(%0)" ::"i"(N) : "memory"); }
__device__ __forceinline__ void bar() { asm volatile("s_barrier" ::: "memory"); }
__device__ __forceinline__ float dot2bf(unsigned a, unsigned b, float c) {
  return __builtin_amdgcn_fdot2_f32_bf16(__builtin_bit_cast(b2_t, a), __builtin_bit_cast(b2_t, b), c, false);
}

struct Cw { f2_t w, ws; };
__device__ __forceinline__ Cw mkw(f2_t w) { Cw r; r.w = w; r.ws = f2_t{-w.y, w.x}; return r; }
__device__ __forceinline__ f2_t cmulw(f2_t a, const Cw& W) {
  return __builtin_elementwise_fma(f2_t{a.y, a.y}, W.ws, f2_t{a.x, a.x} * W.w);
}
__device__ __forceinline__ f2_t cexp_l(float er, float wr, float l) {
  float mag = __builtin_amdgcn_exp2f(er * l);
  float rev = __builtin_amdgcn_fractf(wr * l);
  return f2_t{mag * __builtin_amdgcn_cosf(rev), mag * __builtin_amdgcn_sinf(rev)};
}
__device__ __forceinline__ float pow2_scale(float bound, int target) {
  unsigned e = (__float_as_uint(bound) >> 23) & 0xff;
  int k = target - ((int)e - 126);
  k = k > 126 ? 126 : (k < -126 ? -126 : k);
  return (bound > 0.f) ? __uint_as_float((unsigned)(k + 127) << 23) : 1.0f;
}
__device__ __forceinline__ float pow2_inv(float p) { return __uint_as_float(0x7f000000u - __float_as_uint(p)); }

constexpr int B_SIDE = 96;
constexpr int KG_R = 0, KG_L0 = 4352, KG_L1 = 8704, KG_W = 8704 + 25 * 272;
constexpr int B_BND = 8 * KG_W;
constexpr int B_KC = B_BND + 64;
constexpr int B_LDS = B_KC + 4 * KRS;
constexpr int B_KOFF = 0;
constexpr int B_UOFF = 4 * KDS;
constexpr int B_UCH = 4608;
constexpr int B_YCH = 10240;
constexpr int B_PACK = 40960;

__device__ __forceinline__ void pack_wd(const u16* __restrict__ wd, char* __restrict__ ws, char* buf, int u, int lane) {
  const int ks = u >> 3, dg = u & 7;
#pragma unroll
  for (int i = 0; i < 5; ++i) {
    const int idx = i * 64 + lane, row = idx / 10, c = idx % 10;
    __builtin_amdgcn_global_load_lds((const unsigned*)((const char*)wd + (size_t)(32 * ks + row) * 1280 + 160 * dg + 16 * c),
                                     (LAS unsigned*)(buf + i * 1024), 16, 0, 2);
  }
  asm volatile("s_waitcnt vmcnt(0)" ::: "memory");
  wave_fence();
  const int g = lane >> 4, q = (lane & 15) >> 2, p = lane & 3;
  char* dst = ws + OFF_WP2 + ((size_t)(dg * 54 + ks) * 5) * 1024 + lane * 16;
#pragma unroll
  for (int dt = 0; dt < 5; ++dt) {
    s4_t aa = tr_read(buf + (4 * g + q) * 160 + 32 * dt + 8 * p);
    s4_t bb = tr_read(buf + (16 + 4 * g + q) * 160 + 32 * dt + 8 * p);
    u2_t ua = __builtin_bit_cast(u2_t, aa), ub = __builtin_bit_cast(u2_t, bb);
    const u4_t pv = {ua.x, ua.y, ub.x, ub.y};
    asm volatile("global_store_dwordx4 %0, %1, off sc1\n\ts_nop 1" ::"v"(dst + dt * 1024), "v"(pv) : "memory");
  }
}

struct BFrag { h8_t B[8]; u2_t A[5][2]; };
__device__ __forceinline__ void mfma_f16(f4_t& d, u4_t a, h8_t b) {
  asm volatile("v_mfma_f32_16x16x32_f16 %0, %1, %2, %0" : "+v"(d) : "v"(a), "v"(b));
}
__device__ __forceinline__ void mfma_f16_0(f4_t& d, u4_t a, h8_t b) {
  asm volatile("v_mfma_f32_16x16x32_f16 %0, %1, %2, 0" : "=&v"(d) : "v"(a), "v"(b));
}
__device__ __forceinline__ unsigned ln_fin(float mean, float var) {
  const float mb = rbf(mean), sd = rbf(sqrtf(rbf(rbf(var) + 1e-5f)));
  return (__float_as_uint(mb) >> 16) | (__float_as_uint(sd) & 0xffff0000u);
}
__device__ __forceinline__ unsigned ln1_row(const u16* __restrict__ x, int t) {
  const u4_t* p = (const u4_t*)(x + (size_t)t * DM);
  float s = 0.f, q = 0.f;
  for (int i = 0; i < 80; ++i) { const u4_t v = p[i]; for (int j = 0; j < 4; ++j) s += bf_lo(v[j]) + bf_hi(v[j]); }
  const float mean = s * (1.0f / DM);
  for (int i = 0; i < 80; ++i) {
    const u4_t v = p[i];
    for (int j = 0; j < 4; ++j) { const float a = bf_lo(v[j]) - mean, b = bf_hi(v[j]) - mean; q += a * a; q += b * b; }
  }
  return ln_fin(mean, q * (1.0f / DM));
}
constexpr int SPIN_MAX = 1 << 14;

extern "C" __global__ __launch_bounds__(512, 2) void kB(const u16* __restrict__ x, const u16* __restrict__ Are, const u16* __restrict__ Aim,
                                                     const u16* __restrict__ Bre, const u16* __restrict__ Bim, const u16* __restrict__ Cre,
                                                     const u16* __restrict__ Cim, const u16* __restrict__ logdt, const u16* __restrict__ dskip,
                                                     const u16* __restrict__ wg, const u16* __restrict__ wu, const u16* __restrict__ wd,
                                                     char* __restrict__ ws) {
  extern __shared__ __attribute__((aligned(16))) char smem[];
  const int tid = threadIdx.x, lane = tid & 63;
  const int w = __builtin_amdgcn_readfirstlane(tid >> 6);
  const int xcd = blockIdx.x & 7;
#define EPLOAD(v) asm volatile("global_load_dwordx2 %0, %1, %2 sc1" : "=v"(v) : "v"(0u), "s"(ws + OFF_EP) : "memory")
  auto post_epoch = [&](u2_t ep, int off) {
    if (tid == 0) {
      const unsigned long long e = (((unsigned long long)ep.y << 32) | ep.x) + 1ull;
      *(unsigned long long*)(smem + off) = e;
      *(unsigned*)(smem + off + 8) = (unsigned)((e * 0x9E3779B97F4A7C15ull) >> 32);
      __hip_atomic_store((gu64*)(ws + OFF_RD) + blockIdx.x, e, __ATOMIC_RELAXED, __HIP_MEMORY_SCOPE_AGENT);
    }
  };
  if (blockIdx.x < B_SIDE) {
    const int rk = blockIdx.x >> 3;
    const int g4 = xcd >> 1, a = xcd & 1, nl = (g4 < 3) ? 7 : 6;
    constexpr int S_EP = 17408;
    unsigned pf[2] = {0u, 0u};
    if (rk >= 8) {
#pragma unroll
      for (int k = 0; k < 2; ++k) {
        const int id = (((rk - 8) * 8 + w) * 2 + k) * 64 + lane;
        asm volatile("global_load_dword %0, %1, off" : "=v"(pf[k]) : "v"((const char*)x + (size_t)(id >> 1) * (DM * 2) + (((160 * xcd) >> 7) + (id & 1)) * 128) : "memory");
      }
    }
    if (rk < 8) {
      const int r = lane >> 4, c = lane & 15, t = (blockIdx.x * 8 + w) * 4 + r;
      const u4_t* p = (const u4_t*)(x + (size_t)t * DM);
      u2_t ep;
      EPLOAD(ep);
      u4_t v[5];
#pragma unroll
      for (int i = 0; i < 5; ++i) v[i] = ldnt(p + c + 16 * i);
      float s = 0.f;
#pragma unroll
      for (int i = 0; i < 5; ++i)
#pragma unroll
        for (int j = 0; j < 4; ++j) s += bf_lo(v[i][j]) + bf_hi(v[i][j]);
      asm volatile("" : "+v"(ep) : "v"(s));
      post_epoch(ep, S_EP);
      const float mean = row16_sum(s) * (1.0f / DM);
      float q = 0.f;
#pragma unroll
      for (int i = 0; i < 5; ++i)
#pragma unroll
        for (int j = 0; j < 4; ++j) {
          const float aa = bf_lo(v[i][j]) - mean, bb = bf_hi(v[i][j]) - mean;
          q += aa * aa; q += bb * bb;
        }
      const unsigned sv = ln_fin(mean, row16_sum(q) * (1.0f / DM));
      __syncthreads();
      const u2_t e = *(const u2_t*)(smem + S_EP);
      if (c == 0) {
        __hip_atomic_store((gu64*)(ws + OFF_G1) + t, ((unsigned long long)*(const unsigned*)(smem + S_EP + 8) << 32) | sv, __ATOMIC_RELAXED, __HIP_MEMORY_SCOPE_AGENT);
        ((unsigned long long*)(ws + OFF_G2))[t] = 0ull;
      }
      if (rk == 4 && w == 0) {
        unsigned long long a0, a1;
        asm volatile("s_getpc_b64 s[20:21]\n\ts_add_u32 s20, s20, kB@rel32@lo+4\n\ts_addc_u32 s21, s21, kB@rel32@hi+12\n\ts_mov_b64 %0, s[20:21]" : "=s"(a0) :: "s20", "s21", "scc");
        asm volatile("s_getpc_b64 s[20:21]\n\ts_add_u32 s20, s20, kD@rel32@lo+4\n\ts_addc_u32 s21, s21, kD@rel32@hi+12\n\ts_mov_b64 %0, s[20:21]" : "=s"(a1) :: "s20", "s21", "scc");
        const unsigned long long lo = a0 < a1 ? a0 : a1, last = ((a0 < a1 ? a1 - a0 : a0 - a1) + 8192) & ~127ull;
        unsigned cf[8];
#pragma unroll
        for (int i = 0; i < 8; ++i) {
          const unsigned long long o = (unsigned long long)(lane + 64 * i) * 128;
          asm volatile("global_load_dword %0, %1, off" : "=v"(cf[i]) : "v"(lo + (o < last ? o : last)) : "memory");
        }
        asm volatile("s_waitcnt vmcnt(0)" ::"v"(cf[0]), "v"(cf[1]), "v"(cf[2]), "v"(cf[3]), "v"(cf[4]), "v"(cf[5]), "v"(cf[6]), "v"(cf[7]) : "memory");
      }
      if (blockIdx.x == 63 && w == 7) {
        const unsigned long long e64 = ((unsigned long long)e.y << 32) | e.x;
        for (int spin = 0; spin < SPIN_MAX; ++spin) {
          bool ok = true;
#pragma unroll
          for (int i = 0; i < 4; ++i) ok &= __hip_atomic_load((const gu64*)(ws + OFF_RD) + lane + 64 * i, __ATOMIC_RELAXED, __HIP_MEMORY_SCOPE_AGENT) == e64;
          if (__all(ok)) break;
          __builtin_amdgcn_s_sleep(4);
        }
        if (lane == 0) __hip_atomic_store((gu64*)(ws + OFF_EP), e64, __ATOMIC_RELAXED, __HIP_MEMORY_SCOPE_AGENT);
      }
    }
    const int strip = (rk >= 8) ? rk - 8 : 4 + rk;
    if ((rk >= 8 || rk < 3) && strip < nl) {
      const int jl = 7 * g4 + strip;
      const char* pm = (const char*)(a ? wu : wg) + (size_t)(80 * w + (lane >> 3)) * (FF * 2) + 128 * jl + (lane & 7) * 16;
      u2_t ep;
      EPLOAD(ep);
      u4_t v[10];
#pragma unroll
      for (int i = 0; i < 10; ++i) v[i] = ldnt((const u4_t*)(pm + (size_t)i * 8 * (FF * 2)));
      const char* po = (const char*)(a ? wg : wu) + (size_t)(80 * w) * (FF * 2) + 128 * jl;
      unsigned t0v, t1v;
      asm volatile("global_load_dword %0, %1, off nt" : "=v"(t0v) : "v"(po + (size_t)lane * (FF * 2)) : "memory");
      asm volatile("global_load_dword %0, %1, off nt" : "=v"(t1v) : "v"(po + (size_t)(64 + (lane & 15)) * (FF * 2)) : "memory");
      f2_t s[4];
#pragma unroll
      for (int e = 0; e < 4; ++e) s[e] = f2_t{0.f, 0.f};
#pragma unroll
      for (int i = 0; i < 10; ++i)
#pragma unroll
        for (int jj = 0; jj < 4; ++jj) s[jj] += f2_t{bf_lo(v[i][jj]), bf_hi(v[i][jj])};
      asm volatile("" : "+v"(ep), "+v"(pf[0]), "+v"(pf[1]) : "v"(s[0]));
      if (rk >= 8) post_epoch(ep, S_EP);
      float* pp = (float*)smem;
      *(f4_t*)(pp + (w * 8 + (lane >> 3)) * 68 + (lane & 7) * 8) = f4_t{s[0].x, s[0].y, s[1].x, s[1].y};
      *(f4_t*)(pp + (w * 8 + (lane >> 3)) * 68 + (lane & 7) * 8 + 4) = f4_t{s[2].x, s[2].y, s[3].x, s[3].y};
      __syncthreads();
      float acc = 0.f;
#pragma unroll
      for (int i = 0; i < 8; ++i) acc += pp[(w * 8 + i) * 68 + lane];
      __syncthreads();
      pp[w * 64 + lane] = acc;
      __syncthreads();
      if (w == 0) {
        float t = 0.f;
#pragma unroll
        for (int i = 0; i < 8; ++i) t += pp[i * 64 + lane];
        ((float*)(ws + OFF_CS))[a * FF + 64 * jl + lane] = t;
      }
      asm volatile("s_waitcnt vmcnt(0)" ::"v"(t0v), "v"(t1v) : "memory");
    }
    return;
  }
  const int cb = blockIdx.x - B_SIDE;
  const int q = 20 * xcd + (cb >> 3);
  const int cl = w >> 1, hw = w & 1, ch = 4 * q + cl;
  const int n = ch * 128 + hw * 64 + lane;
  u2_t ep;
  EPLOAD(ep);
  const u16 i_are = ldnt(Are + n), i_aim = ldnt(Aim + n), i_bre = ldnt(Bre + n), i_bim = ldnt(Bim + n), i_cre = ldnt(Cre + n), i_cim = ldnt(Cim + n);
  const u16 i_dt = logdt[ch];
  u2_t xg[4];
  const unsigned xoff = tid * (2 * DM * 2);
  const char* xb = (const char*)(x + 4 * q);
#define XG(i) asm volatile("global_load_dwordx2 %0, %1, %2 offset:%3" : "=v"(xg[i]) : "v"(xoff), "s"(xb + ((i) >> 1) * (size_t)(1024 * DM * 2)), "i"(((i) & 1) * DM * 2) : "memory")
  const u2_t dkv = *(const u2_t*)(dskip + 4 * q);
  const int c = lane & 15, kg = lane >> 4;
  h8_t band9, band7;
#pragma unroll
  for (int j = 0; j < 8; ++j) {
    const int k = 8 * kg + 7 - j;
    band9[j] = (k >= c + 4 && k <= c + 12) ? (_Float16)1.0f : (_Float16)0.0f;
    band7[j] = (k >= c + 5 && k <= c + 11) ? (_Float16)1.0f : (_Float16)0.0f;
  }
  asm volatile("" : "+v"(band9), "+v"(band7));
  char* hb = smem + w * KG_W;
  float Sw, bsum;
  {
    const float dt = rbf(expf(bf2f(i_dt)));
    const float are = dt * bf2f(i_are), aim = dt * bf2f(i_aim);
    const float bre = dt * bf2f(i_bre), bim = dt * bf2f(i_bim);
    const float cre = bf2f(i_cre), cim = bf2f(i_cim);
    f2_t c0 = {cre * bre - cim * bim, cre * bim + cim * bre};
    const float er = are * 1.44269504088896341f, wr = aim * 0.15915494309189535f;
    const float bnd = (fabsf(c0.x) + fabsf(c0.y)) * fmaxf(1.0f, __builtin_amdgcn_exp2f(er * 2048.f));
    bsum = wave_sum(bnd);
    asm volatile("" : "+v"(ep) : "v"(bnd));
    post_epoch(ep, B_BND + 48);
    if (lane == 0) ((float*)(smem + B_BND))[w] = bsum;
    Sw = pow2_scale(bsum, 15);
    c0 *= Sw;
    const Cw z1 = mkw(cexp_l(er, wr, 1.f)), w8 = mkw(cexp_l(er, -wr, 8.f)), w128 = mkw(cexp_l(er, wr, 128.f));
    const Cw z2 = mkw(cmulw(z1.w, z1));
    const Cw z4 = mkw(cmulw(z2.w, z2));
    auto put = [&](int off, int row, f2_t v) { *(unsigned*)(hb + off + row * 272 + 4 * lane) = pk_f16(v.x, v.y); };
    f2_t R = w8.w, v0 = c0, v1 = cmulw(c0, w128), T = v1;
    *(unsigned*)(hb + KG_R + 4 * lane) = pk_f16(1.f, 0.f);
#pragma unroll
    for (int i = 0; i < 8; ++i) {
      SB;
      if ((w & 3) == (i & 3)) { if (i < 4) XG(0); else XG(1); }
      SB;
      *(unsigned*)(hb + KG_R + (2 * i + 1) * 272 + 4 * lane) = pk_f16(R.x, R.y);
      if (i < 7) {
        R = cmulw(R, w8);
        *(unsigned*)(hb + KG_R + (2 * i + 2) * 272 + 4 * lane) = pk_f16(R.x, R.y);
        R = cmulw(R, w8);
      }
      put(KG_L0, i, v0); put(KG_L0, 8 + i, v1);
      if (i < 7) { v0 = cmulw(v0, z1); v1 = cmulw(v1, z1); }
#pragma unroll
      for (int k = 0; k < 2; ++k) {
        const int i1 = 2 + 2 * i + k;
        if (i1 <= 16) {
          T = cmulw(T, w128);
          if (i1 < 4) {
            f2_t v = T;
#pragma unroll
            for (int i0 = 0; i0 < 4; ++i0) { put(KG_L1, 4 * (i1 - 2) + i0, v); if (i0 < 3) v = cmulw(v, z2); }
          } else if (i1 < 8) {
            put(KG_L1, 8 + 2 * (i1 - 4), T); put(KG_L1, 8 + 2 * (i1 - 4) + 1, cmulw(T, z4));
          } else {
            put(KG_L1, 16 + (i1 - 8), T);
          }
        }
      }
    }
  }
  SB;
  u4_t gr[2];
  const unsigned goff = tid * 16;
#define GRL() do { asm volatile("global_load_dwordx4 %0, %1, %2 sc1" : "=v"(gr[0]) : "v"(goff), "s"(ws + OFF_G1) : "memory"); \
                   asm volatile("global_load_dwordx4 %0, %1, %2 sc1" : "=v"(gr[1]) : "v"(goff), "s"(ws + OFF_G1 + 8192) : "memory"); } while (0)
  GRL();
  if (w < 4) XG(2);
  wave_fence();
  f4_t ka[3];
  {
    h8_t Bf[4];
#pragma unroll
    for (int ks = 0; ks < 4; ++ks) Bf[ks] = *(const h8_t*)(hb + KG_R + c * 272 + 64 * ks + 16 * kg);
#pragma unroll
    for (int t = 0; t < 3; ++t) ka[t] = f4_t{0.f, 0.f, 0.f, 0.f};
#pragma unroll
    for (int ks = 0; ks < 4; ++ks)
#pragma unroll
      for (int t = 0; t < 3; ++t) {
        const int off = (t == 0) ? KG_L0 + c * 272 : (t == 1) ? KG_L1 + c * 272 : KG_L1 + (16 + min(c, 8)) * 272;
        const h8_t Af = *(const h8_t*)(hb + off + 64 * ks + 16 * kg);
        ka[t] = __builtin_amdgcn_mfma_f32_16x16x32_f16(Af, Bf[ks], ka[t], 0, 0, 0);
      }
  }
  SB;
  if (w >= 4) XG(2); else XG(3);
  SB;
  const float iSw = pow2_inv(Sw);
  wave_fence();
  if (hw == 0) *(f4_t*)(hb + lane * 16) = ka[1] * iSw;
  else { *(f4_t*)(hb + lane * 16) = ka[0] * iSw; *(f4_t*)(hb + 1024 + lane * 16) = ka[2] * iSw; }
  __syncthreads();
  const unsigned etag = *(const unsigned*)(smem + B_BND + 48 + 8);
  if (w >= 4) XG(3);
  {
    const char* pb = smem + (w ^ 1) * KG_W;
    const float Sc = pow2_scale(((const float*)(smem + B_BND))[2 * cl] + ((const float*)(smem + B_BND))[2 * cl + 1], 15);
    char* kr = smem + B_KC + cl * KRS;
    if (hw == 0) {
      const f4_t k0 = (ka[0] * iSw + *(const f4_t*)(pb + lane * 16)) * Sc, k2 = (ka[2] * iSw + *(const f4_t*)(pb + 1024 + lane * 16)) * Sc;
      *(u2_t*)(kr + 2 * (128 * (kg >> 1) + 4 * (kg & 1) + 8 * c)) = u2_t{pk_f16(k0[0], k0[1]), pk_f16(k0[2], k0[3])};
      const unsigned p01 = pk_f16(k2[0], k2[1]), p23 = pk_f16(k2[2], k2[3]);
      u16* k3 = (u16*)kr + 512 + 64 * kg + c;
      if (kg < 2) { k3[0] = (u16)p01; k3[16] = (u16)(p01 >> 16); k3[32] = (u16)p23; k3[48] = (u16)(p23 >> 16); }
      else if (lane == 32) k3[0] = (u16)p01;
      if (lane == 0) ((float*)(smem + B_BND + 32))[cl] = pow2_inv(Sc);
    } else {
      const f4_t k1 = (ka[1] * iSw + *(const f4_t*)(pb + lane * 16)) * Sc;
      const unsigned p01 = pk_f16(k1[0], k1[1]), p23 = pk_f16(k1[2], k1[3]);
      if (kg < 2) {
        *(u2_t*)(kr + 2 * (256 + 64 * kg + 4 * c)) = u2_t{p01, p23};
      } else {
        *(unsigned*)(kr + 2 * (384 + 64 * (kg - 2) + 2 * c)) = p01;
        *(unsigned*)(kr + 2 * (384 + 64 * (kg - 2) + 32 + 2 * c)) = p23;
      }
    }
  }
  __syncthreads();
  const float invs = ((const float*)(smem + B_BND + 32))[w & 3];
  {
    const int kch = tid >> 7, kc = tid & 127;
    const char* kr = smem + B_KC + kch * KRS;
    const int e0 = (kc < 32) ? 8 * kc : (kc < 64) ? 256 + 4 * (kc - 32) : 384 + 2 * (kc - 64);
    const unsigned* kp = (const unsigned*)(kr + 2 * e0);
    const unsigned d0 = kp[0], d1 = kp[1], d2 = kp[2], d3 = kp[3], d4 = kp[4];
    const unsigned kfirst = *(const u16*)kr, k1024 = *(const u16*)(kr + 2 * 512);
    const int dl = 127 - kc;
    const unsigned kfa = *(const u16*)(kr + 2 * (512 + max(2 * dl - 127, 0))), kfb = *(const u16*)(kr + 2 * (512 + max(2 * dl - 126, 0)));
    char* ki = smem + B_KOFF + kch * KDS;
    const unsigned k0 = d0 & 0xffffu;
    unsigned o0, o1, o2, o3, nx;
    if (kc < 32) { o0 = d0; o1 = d1; o2 = d2; o3 = d3; nx = d4; }
    else if (kc < 64) {
      const unsigned k2 = d0 >> 16, k4 = d1 & 0xffffu, k6 = d1 >> 16, k8 = d2 & 0xffffu;
      o0 = k0 * 0x10001u; o1 = k2 | (k4 << 16); o2 = k4 * 0x10001u; o3 = k6 | (k8 << 16); nx = k8;
    } else {
      const unsigned k4 = d0 >> 16, k8 = d1 & 0xffffu;
      o0 = k0 * 0x10001u; o1 = k0 | (k4 << 16); o2 = k4 * 0x10001u; o3 = k8 * 0x10001u; nx = k8;
    }
    *(u4_t*)(ki + 16 * (kc + 2)) = u4_t{o0, o1, o2, o3};
    *(u4_t*)(ki + KD_O + 16 * (kc + 2)) = u4_t{__builtin_amdgcn_alignbit(o1, o0, 16), __builtin_amdgcn_alignbit(o2, o1, 16),
                                              __builtin_amdgcn_alignbit(o3, o2, 16), __builtin_amdgcn_alignbit(nx, o3, 16)};
    if (kc < 6) {
      const int cp = (kc < 2) ? kc : 128 + kc;
      const unsigned kk = k1024 * 0x10001u;
      const u4_t e = (kc == 2) ? u4_t{kk, kk, k1024, 0u} : u4_t{0u, 0u, 0u, 0u};
      const u4_t o = (kc == 2) ? u4_t{kk, kk, 0u, 0u} : u4_t{0u, 0u, 0u, (kc == 1) ? (kfirst << 16) : 0u};
      *(u4_t*)(ki + 16 * cp) = e;
      *(u4_t*)(ki + KD_O + 16 * cp) = o;
    }
    if (kc < 80) *(unsigned*)(ki + KD_DR + 4 * kc) = (dl >= 64) ? (kfa | (kfb << 16)) : 0u;
  }
  *(unsigned*)(smem + B_UOFF + (tid >> 7) * B_UCH + 4096 + 4 * (tid & 127)) = 0u;
  asm volatile("s_waitcnt vmcnt(2)" : "+v"(gr[0]), "+v"(gr[1]), "+v"(xg[0]), "+v"(xg[1])::"memory");
  {
    for (int spin = 0; spin < SPIN_MAX; ++spin) {
      if (__all(gr[0][1] == etag && gr[0][3] == etag && gr[1][1] == etag && gr[1][3] == etag)) break;
      __builtin_amdgcn_s_sleep(2);
      GRL();
      asm volatile("s_waitcnt vmcnt(0)" : "+v"(gr[0]), "+v"(gr[1])::"memory");
    }
#pragma unroll
    for (int it = 0; it < 2; ++it)
#pragma unroll
      for (int r = 0; r < 2; ++r)
        if (gr[it][2 * r + 1] != etag) gr[it][2 * r] = ln1_row(x, 2 * (tid + 512 * it) + r);
  }
  const float dk[4] = {bf_lo(dkv.x), bf_hi(dkv.x), bf_lo(dkv.y), bf_hi(dkv.y)};
  float z[2][8];
#pragma unroll
  for (int it = 0; it < 2; ++it) {
    if (it == 1) asm volatile("s_waitcnt vmcnt(0)" : "+v"(xg[2]), "+v"(xg[3])::"memory");
    const int j = tid + 512 * it;
    const unsigned s0 = gr[it][0], s1 = gr[it][2];
    const float m0 = bf_lo(s0), r0 = __builtin_amdgcn_rcpf(bf_hi(s0)), m1 = bf_lo(s1), r1 = __builtin_amdgcn_rcpf(bf_hi(s1));
    const u2_t xa = xg[2 * it], xc = xg[2 * it + 1];
    const float x0[4] = {bf_lo(xa.x), bf_hi(xa.x), bf_lo(xa.y), bf_hi(xa.y)};
    const float x1[4] = {bf_lo(xc.x), bf_hi(xc.x), bf_lo(xc.y), bf_hi(xc.y)};
#pragma unroll
    for (int cc = 0; cc < 4; ++cc) {
      const float u0 = (x0[cc] - m0) * r0, u1 = (x1[cc] - m1) * r1;
      *(unsigned*)(smem + B_UOFF + cc * B_UCH + 4 * (1023 - j)) = pk_f16(u1, u0);
      z[it][cc] = x0[cc] + u0 * dk[cc];
      z[it][4 + cc] = x1[cc] + u1 * dk[cc];
    }
  }
  __syncthreads();
  if (w >= 4 && w < 7 && cb * 3 + (w - 4) < 432) pack_wd(wd, ws, smem + B_PACK + (w - 4) * 5120, cb * 3 + (w - 4), lane);
  f4_t acc[8];
  if (w < 4) {
    const char* ub = smem + B_UOFF + w * B_UCH + 4112 - 32 * c - 16 * kg - 512 * 7;
    const char* kb = smem + B_KOFF + w * KDS + ((c & 1) ? 0 : KD_O) + 4 * (12 + ((c + 1) >> 1) - 4 * kg);
    const unsigned* dr = (const unsigned*)(smem + B_KOFF + w * KDS + KD_DR) + (127 - c + 4 * kg);
    const char* uw = smem + B_UOFF + w * B_UCH + 4112 - 32 * c - 16 * kg;
    auto rdB = [&](BFrag& F, int i0, int p) { F.B[p] = *(const h8_t*)(ub + 512 * (7 - p) + 64 * i0); };
    auto rdA = [&](BFrag& F, int i0, int i1, int half) {
      const unsigned* ap = (const unsigned*)(kb + 64 * i0 + 512 * i1) + 2 * half;
      F.A[i1][half] = u2_t{ap[0], ap[1]};
    };
    auto frag = [&](const u2_t (&a)[2]) { return u4_t{a[0].x, a[0].y, a[1].x, a[1].y}; };
    auto rdnext = [&](BFrag& F, int i0, int m) {
      if (m < 8) rdB(F, i0, m); else if (m < 16) rdA(F, i0, (m - 8) >> 1, (m - 8) & 1);
    };
    auto step = [&](auto i0c, const BFrag& F, BFrag& N, auto nextc) {
      constexpr int I0 = decltype(i0c)::value, NX = decltype(nextc)::value;
      int m = 0;
#pragma unroll
      for (int i1 = 0; i1 < 5; ++i1) {
        if (i1 == 4 && I0 != 0) continue;
        const u4_t a = frag(F.A[i1]);
#pragma unroll
        for (int t = i1; t < 8; ++t) {
          if (I0 == 0 && i1 == 0) mfma_f16_0(acc[t], a, F.B[t]); else mfma_f16(acc[t], a, F.B[t - i1]);
          if (NX >= 0) rdnext(N, NX, m);
          SB;
          ++m;
        }
      }
    };
    BFrag F0, F1;
    h8_t U9[4], U7[4];
#pragma unroll
    for (int bt = 0; bt < 4; ++bt) { U9[bt] = *(const h8_t*)(uw - 512 * bt + 16); U7[bt] = *(const h8_t*)(uw - 512 * bt); }
#pragma unroll
    for (int m = 0; m < 16; ++m) rdnext(F0, 0, m);
    rdA(F0, 0, 4, 0); rdA(F0, 0, 4, 1);
    SB;
    f4_t X9[4], X7[4];
#pragma unroll
    for (int bt = 0; bt < 4; ++bt) { mfma_f16_0(X9[bt], __builtin_bit_cast(u4_t, U9[bt]), band9); mfma_f16_0(X7[bt], __builtin_bit_cast(u4_t, U7[bt]), band7); }
    SB;
    step(IC(0), F0, F1, IC(1));
    h8_t Vf[4]; u4_t Tf[4];
    {
      int m = 0;
#pragma unroll
      for (int i1 = 0; i1 < 4; ++i1) {
        const u4_t a = frag(F1.A[i1]);
#pragma unroll
        for (int t = i1; t < 8; ++t) {
          mfma_f16(acc[t], a, F1.B[t - i1]);
          rdnext(F0, 2, m);
          if (m < 4) {
            const u4_t vu = {pk_f16(X7[m][0], X9[m][0]), pk_f16(X7[m][1], X9[m][1]), pk_f16(X7[m][2], X9[m][2]), pk_f16(X7[m][3], X9[m][3])};
            Vf[m] = __builtin_bit_cast(h8_t, vu);
          }
          if (m >= 16 && m < 20) { const unsigned* tp = dr - 16 * (m - 16 + 4); Tf[m - 16] = u4_t{tp[0], tp[1], tp[2], tp[3]}; }
          SB;
          ++m;
        }
      }
    }
    step(IC(2), F0, F1, IC(3));
#pragma unroll
    for (int bt = 0; bt < 4; ++bt)
#pragma unroll
      for (int at = 4 + bt; at < 8; ++at) mfma_f16(acc[at], __builtin_bit_cast(u4_t, Vf[bt]), __builtin_bit_cast(h8_t, Tf[at - bt - 4]));
    SB;
    step(IC(3), F1, F0, IC(4));
    step(IC(4), F0, F1, IC(5));
    step(IC(5), F1, F0, IC(6));
    step(IC(6), F0, F1, IC(7));
    step(IC(7), F1, F0, IC(-1));
    asm volatile("s_nop 7\n\ts_nop 1" : "+v"(acc[0]), "+v"(acc[1]), "+v"(acc[2]), "+v"(acc[3]), "+v"(acc[4]), "+v"(acc[5]), "+v"(acc[6]), "+v"(acc[7]));
  }
  bar();
  if (w < 4) {
#pragma unroll
    for (int t = 0; t < 8; ++t) *(f4_t*)(smem + w * B_YCH + t * 1280 + kg * 320 + c * 16) = acc[t] * invs;
  }
  asm volatile("s_waitcnt lgkmcnt(0)" ::: "memory");
  bar();
#pragma unroll
  for (int it = 0; it < 2; ++it) {
    const int j = tid + 512 * it, t = 2 * j;
    const int yo = (t >> 8) * 1280 + ((t >> 2) & 3) * 320 + ((t >> 4) & 15) * 16 + (t & 3) * 4;
    float h0[4], h1[4];
#pragma unroll
    for (int cc = 0; cc < 4; ++cc) {
      f2_t y = *(const f2_t*)(smem + cc * B_YCH + yo);
      h0[cc] = z[it][cc] + y.x;
      h1[cc] = z[it][4 + cc] + y.y;
    }
    char* dst = ws + OFF_H8 + ((size_t)(q >> 1) * SL + t) * 16 + (q & 1) * 8;
    *(u2_t*)(dst) = u2_t{pk_bf16(h0[0], h0[1]), pk_bf16(h0[2], h0[3])};
    *(u2_t*)(dst + 16) = u2_t{pk_bf16(h1[0], h1[1]), pk_bf16(h1[2], h1[3])};
  }
}

constexpr int C_T = 240;
constexpr int C_SLOT = 23552, C_NSLOT = 6, C_GOFF = 15360;
constexpr int C_STAT = C_NSLOT * C_SLOT;
constexpr int C_CS = C_STAT + 960;
constexpr int C_LDS = C_CS + 512;
constexpr size_t OFF_DUMMY = 0x1140000;

__device__ __forceinline__ unsigned ln_pack(float sum, float sq) {
  const float mean = sum * (1.0f / DM);
  return ln_fin(mean, fmaxf(sq * (1.0f / DM) - mean * mean, 0.f));
}
__device__ __forceinline__ unsigned ln_row(const char* h8, int t) {
  float s = 0.f, q = 0.f;
  for (int pl = 0; pl < 80; ++pl) {
    const u4_t v = *(const u4_t*)(h8 + (size_t)pl * 32768 + t * 16);
    for (int j = 0; j < 4; ++j) { const float a = bf_lo(v[j]), b = bf_hi(v[j]); s += a + b; q += a * a + b * b; }
  }
  return ln_pack(s, q);
}

template <int NDMA>
__device__ __forceinline__ void c_loader(char* smem, char* ws, const char* wbase, int w4, int lane, int t0, int jline, int xcd) {
  const unsigned lds0 = (unsigned)(size_t)(LAS char*)smem;
  const char* h8 = ws + OFF_H8;
  unsigned voffH[4], voffW[2];
#pragma unroll
  for (int k = 0; k < 2; ++k) {
    const int r = 8 * (2 * (w4 & 1) + k) + (lane >> 3), c = lane & 7;
    const int f = ((r >> 1) & 1) | ((r >> 2) & 2);
    voffW[k] = r * (FF * 2) + 128 * jline + (((c >> 1) ^ f) * 32) + (c & 1) * 16;
  }
#pragma unroll
  for (int k = 0; k < 4; ++k) {
    const int idx = 64 * (w4 + 4 * k) + lane, pl = idx / C_T, tt = idx % C_T;
    const int t = min(t0 + tt, SL - 1);
    voffH[k] = pl * 32768 + t * 16;
  }
  const unsigned ldsW = C_GOFF + (w4 >> 1) * 4096 + (w4 & 1) * 2048;
  auto issue = [&](int step, unsigned so) {
#pragma unroll
    for (int k = 0; k < 2; ++k) glds16(wbase + (size_t)step * (32 * FF * 2), voffW[k], lds0 + so + ldsW + k * 1024);
#pragma unroll
    for (int k = 0; k < NDMA - 2; ++k) glds16(h8 + (size_t)step * (4 * 32768), voffH[k], lds0 + so + (w4 + 4 * k) * 1024);
  };
  constexpr unsigned S = C_SLOT;
  issue(0, 0); issue(1, S); issue(2, 2 * S); issue(3, 3 * S);
  wait_vm<3 * NDMA>(); bar();
  issue(4, 4 * S);
  wait_vm<2 * NDMA>(); bar();
  issue(5, 5 * S); issue(6, 0);
  unsigned sw = S;
#pragma unroll 1
  for (int j = 1; j < 7; ++j) {
    wait_vm<2 * NDMA>();
    bar();
    issue(2 * j + 5, sw);
    sw += S; if (sw == C_NSLOT * S) sw = 0;
    issue(2 * j + 6, sw);
    sw += S; if (sw == C_NSLOT * S) sw = 0;
  }
  wait_vm<2 * NDMA>(); bar();
  issue(19, sw);
  const int tl = min(60 * w4 + lane, C_T - 1), tg = min(t0 + tl, SL - 1);
  const gu64* gp = (const gu64*)(ws + OFF_G2) + tg;
  const float* csp = (const float*)(ws + OFF_CS) + ((lane >> 4) & 1) * FF + 64 * jline + 16 * w4 + (lane & 15);
  const char* tb = ws + OFF_WP2 + (size_t)(4 * (xcd & 1)) * (54 * 5120);
  const int tl0 = ((int)(blockIdx.x >> 3) * 4 + w4) * 68;
  u2_t gv; float csv; unsigned tv0, tv1;
  asm volatile("global_load_dwordx2 %0, %1, off sc1" : "=v"(gv) : "v"(gp) : "memory");
  asm volatile("global_load_dword %0, %1, off" : "=v"(csv) : "v"(csp) : "memory");
  asm volatile("global_load_dword %0, %1, off" : "=v"(tv0) : "v"(tb + (size_t)min(tl0 + lane, 8639) * 128) : "memory");
  asm volatile("global_load_dword %0, %1, off" : "=v"(tv1) : "v"(tb + (size_t)min(tl0 + 64 + (lane & 3), 8639) * 128) : "memory");
  wait_vm<NDMA + 4>(); bar();
  asm volatile("s_waitcnt vmcnt(0)" : "+v"(gv), "+v"(csv), "+v"(tv0), "+v"(tv1)::"memory");
  bar();
  {
    bool ok = gv.y == 1u;
    for (int spin = 0; spin < 4096 && !__all(ok); ++spin) {
      const unsigned long long g = __hip_atomic_load(gp, __ATOMIC_RELAXED, __HIP_MEMORY_SCOPE_AGENT);
      gv.x = (unsigned)g; ok = (unsigned)(g >> 32) == 1u;
      __builtin_amdgcn_s_sleep(2);
    }
    if (!__all(ok)) gv.x = ln_row(h8, tg);
    if (lane < 60) *(float*)(smem + C_STAT + (60 * w4 + lane) * 4) = bf_lo(gv.x);
    if (lane < 32) *(float*)(smem + C_CS + (lane >> 4) * 256 + (16 * w4 + (lane & 15)) * 4) = csv;
  }
  asm volatile("s_waitcnt lgkmcnt(0)" ::: "memory");
  bar();
}

struct CFrag { b8_t A[2][2]; b8_t B[8]; };

extern "C" __global__ __launch_bounds__(512, 2) void kC(const u16* __restrict__ wg, const u16* __restrict__ wu, char* __restrict__ ws) {
  extern __shared__ __attribute__((aligned(16))) char smem[];
  const int tid = threadIdx.x, lane = tid & 63;
  const int w = __builtin_amdgcn_readfirstlane(tid >> 6);
  int panel, jline;
  const int xcd = blockIdx.x & 7, rank = blockIdx.x >> 3;
  {
    const int g4 = xcd >> 1, a = xcd & 1, nl = (g4 < 3) ? 7 : 6, total = 9 * nl, n0 = (total + 1) >> 1;
    if (a == 0 ? rank >= n0 : rank >= total - n0) {
      const int sp = (xcd < 6) ? (xcd >> 1) : 3 + 5 * (xcd - 6) + (rank - 27);
      const int wv = sp * 8 + w;
      for (int tile = wv; tile < 128; tile += 104) {
        const int t = 16 * tile + (lane & 15);
        const char* hp = ws + OFF_H8 + (size_t)(lane >> 4) * 32768 + t * 16;
        u4_t v[20];
#pragma unroll
        for (int i = 0; i < 20; ++i) v[i] = *(const u4_t*)(hp + (size_t)i * (4 * 32768));
        float s0 = 0.f, s1 = 0.f, q0 = 0.f, q1 = 0.f;
        unsigned k_both = 0x3f803f80u;
        asm volatile("" : "+s"(k_both));
#pragma unroll
        for (int i = 0; i < 20; ++i) {
          s0 = dot2bf(v[i][0], k_both, s0); q0 = dot2bf(v[i][0], v[i][0], q0);
          s1 = dot2bf(v[i][1], k_both, s1); q1 = dot2bf(v[i][1], v[i][1], q1);
          s0 = dot2bf(v[i][2], k_both, s0); q0 = dot2bf(v[i][2], v[i][2], q0);
          s1 = dot2bf(v[i][3], k_both, s1); q1 = dot2bf(v[i][3], v[i][3], q1);
        }
        float s = s0 + s1, q = q0 + q1;
        s += __shfl_xor(s, 16); q += __shfl_xor(q, 16);
        s += __shfl_xor(s, 32); q += __shfl_xor(q, 32);
        if (lane < 16)
          __hip_atomic_store((gu64*)(ws + OFF_G2) + t, (1ull << 32) | ln_pack(s, q), __ATOMIC_RELAXED, __HIP_MEMORY_SCOPE_AGENT);
      }
      return;
    }
    const int idx = rank + a * n0;
    panel = idx / nl; jline = 7 * g4 + idx % nl;
  }
  const int t0 = panel * C_T;
  if (w >= 4) {
    const int w4 = w - 4;
    const char* wbase = (const char*)((w4 >> 1) ? wu : wg);
    if (w4 < 3) c_loader<6>(smem, ws, wbase, w4, lane, t0, jline, xcd);
    else c_loader<5>(smem, ws, wbase, w4, lane, t0, jline, xcd);
    return;
  }
  const int s = w >> 1, th = w & 1;
  const int fr = lane & 15, kg = lane >> 4, q = (lane & 15) >> 2, p = lane & 3;
  const unsigned hb0 = kg * 3840 + fr * 16 + 256 * 7;
  const unsigned hb1 = kg * 3840 + fr * 16 + 256 * (th ? 8 : 0);
  unsigned wa[2];
#pragma unroll
  for (int v = 0; v < 2; ++v) {
    const int sub = 2 * s + (v ? 1 - th : th);
    const int f = (q >> 1) | (2 * (kg & 1));
    wa[v] = C_GOFF + (8 * kg + q) * 128 + ((sub ^ f) * 32) + 8 * p;
  }
  f4_t ag[8][2], au[8][2];
#pragma unroll
  for (int u = 0; u < 8; ++u)
#pragma unroll
    for (int v = 0; v < 2; ++v) { ag[u][v] = f4_t{0.f, 0.f, 0.f, 0.f}; au[u][v] = f4_t{0.f, 0.f, 0.f, 0.f}; }
  auto load = [&](CFrag& F, unsigned so) {
    const char* sp = smem + so;
#pragma unroll
    for (int v = 0; v < 2; ++v)
#pragma unroll
      for (int m = 0; m < 2; ++m) {
        s4_t lo = tr_read(sp + wa[v] + m * 4096), hi = tr_read(sp + wa[v] + m * 4096 + 512);
        u2_t a = __builtin_bit_cast(u2_t, lo), b = __builtin_bit_cast(u2_t, hi);
        F.A[v][m] = __builtin_bit_cast(b8_t, (u4_t{a.x, a.y, b.x, b.y}));
      }
    F.B[0] = *(const b8_t*)(sp + hb0);
#pragma unroll
    for (int u = 1; u < 8; ++u) F.B[u] = *(const b8_t*)(sp + hb1 + 256 * (u - 1));
  };
  auto mma = [&](const CFrag& F) {
    ag[0][0] = __builtin_amdgcn_mfma_f32_16x16x32_bf16(F.A[0][0], F.B[0], ag[0][0], 0, 0, 0);
    au[0][0] = __builtin_amdgcn_mfma_f32_16x16x32_bf16(F.A[0][1], F.B[0], au[0][0], 0, 0, 0);
#pragma unroll
    for (int u = 1; u < 8; ++u)
#pragma unroll
      for (int v = 0; v < 2; ++v) {
        ag[u][v] = __builtin_amdgcn_mfma_f32_16x16x32_bf16(F.A[v][0], F.B[u], ag[u][v], 0, 0, 0);
        au[u][v] = __builtin_amdgcn_mfma_f32_16x16x32_bf16(F.A[v][1], F.B[u], au[u][v], 0, 0, 0);
      }
  };
  auto interleave = [&]() {
#pragma unroll
    for (int i = 0; i < 16; ++i) {
      __builtin_amdgcn_sched_group_barrier(0x008, 1, 0);
      __builtin_amdgcn_sched_group_barrier(0x100, 1, 0);
    }
    __builtin_amdgcn_sched_group_barrier(0x008, 14, 0);
  };
  CFrag F0, F1;
  bar();
  load(F0, 0);
  unsigned sn = C_SLOT;
  SB;
#pragma unroll 1
  for (int j = 0; j < 9; ++j) {
    asm volatile("s_waitcnt lgkmcnt(0)" ::: "memory");
    bar(); SB;
    load(F1, sn);
    mma(F0);
    interleave();
    SB;
    sn += C_SLOT; if (sn == C_NSLOT * C_SLOT) sn = 0;
    load(F0, sn);
    mma(F1);
    interleave();
    SB;
    sn += C_SLOT; if (sn == C_NSLOT * C_SLOT) sn = 0;
  }
  asm volatile("s_waitcnt lgkmcnt(0)" ::: "memory");
  bar(); SB;
  load(F1, sn);
  mma(F0);
  interleave();
  SB;
  bar(); SB;
  f4_t csg[2], csu[2];
  float mu[8];
#pragma unroll
  for (int v = 0; v < 2; ++v) {
    const int sub = 2 * s + (v ? 1 - th : th);
    csg[v] = *(const f4_t*)(smem + C_CS + (16 * sub + 4 * kg) * 4);
    csu[v] = *(const f4_t*)(smem + C_CS + 256 + (16 * sub + 4 * kg) * 4);
  }
#pragma unroll
  for (int u = 0; u < 8; ++u) {
    const int tile = (u == 0) ? 7 : (th ? 7 + u : u - 1);
    mu[u] = *(const float*)(smem + C_STAT + (16 * tile + fr) * 4);
  }
  mma(F1);
  char* hh = ws + OFF_HH + (size_t)(2 * jline + s) * 1024 + lane * 16;
  char* sink = ws + OFF_DUMMY + lane * 16;
  auto epi = [&](auto thc) {
    constexpr int TH = decltype(thc)::value;
#pragma unroll
    for (int u = 0; u < 8; ++u) {
      const int tile = (u == 0) ? 7 : (TH ? 7 + u : u - 1);
      const int t16 = 15 * panel + tile;
      char* dst = (t16 < 128) ? hh + (size_t)t16 * (54 * 1024) : sink;
      u2_t o[2];
#pragma unroll
      for (int v = 0; v < 2; ++v) {
        if (u == 0 && v == 1) continue;
        const f4_t r = (ag[u][v] - mu[u] * csg[v]) * (au[u][v] - mu[u] * csu[v]);
        o[v] = u2_t{pk_bf16(r[0], r[1]), pk_bf16(r[2], r[3])};
      }
      if (u == 0) *(u2_t*)(dst + 8 * TH) = o[0];
      else *(u4_t*)(dst) = u4_t{o[TH].x, o[TH].y, o[1 - TH].x, o[1 - TH].y};
    }
  };
  if (th) epi(IC(1)); else epi(IC(0));
}

constexpr int D_SLOT = 15360, D_NSLOT = 9;
constexpr int D_LDS = D_NSLOT * D_SLOT;

template <int NDMA>
__device__ __forceinline__ void d_loader(char* smem, const char* wp, int w4, int lane) {
  const unsigned lds0 = (unsigned)(size_t)(LAS char*)smem;
  const unsigned voff = lane * 16;
  auto issue = [&](int step, unsigned so) {
#pragma unroll
    for (int k = 0; k < NDMA; ++k) {
      const int i = w4 + 4 * k;
      glds16(wp + (size_t)step * D_SLOT + i * 1024, voff, lds0 + so + i * 1024);
    }
  };
  constexpr unsigned S = D_SLOT;
  issue(0, 0); issue(1, S); issue(2, 2 * S);
  wait_vm<2 * NDMA>(); bar();
  issue(3, 3 * S); issue(4, 4 * S); issue(5, 5 * S);
  wait_vm<3 * NDMA>(); bar();
  issue(6, 6 * S); issue(7, 7 * S); issue(8, 8 * S);
  unsigned sw = 0;
#pragma unroll 1
  for (int j = 1; j < 5; ++j) {
    wait_vm<4 * NDMA>();
    bar();
    issue(2 * j + 7, sw);
    sw += S; if (sw == D_NSLOT * S) sw = 0;
    issue(2 * j + 8, sw);
    sw += S; if (sw == D_NSLOT * S) sw = 0;
  }
  wait_vm<4 * NDMA>(); bar();
  issue(17, sw);
  wait_vm<3 * NDMA>(); bar();
  wait_vm<1 * NDMA>(); bar();
  wait_vm<0>(); bar();
}

extern "C" __global__ __launch_bounds__(512, 2) void kD(char* __restrict__ ws, u16* __restrict__ out) {
  extern __shared__ __attribute__((aligned(16))) char smem[];
  const int tid = threadIdx.x, lane = tid & 63;
  const int w = __builtin_amdgcn_readfirstlane(tid >> 6);
  const int xcd = blockIdx.x & 7, rank = blockIdx.x >> 3;
  const int it = 8 * (xcd >> 1) + (rank & 7), dg = 4 * (xcd & 1) + (rank >> 3);
  if (w >= 4) {
    const int w4 = w - 4;
    const char* wp = ws + OFF_WP2 + (size_t)dg * (54 * 5120);
    if (w4 < 3) d_loader<4>(smem, wp, w4, lane);
    else d_loader<3>(smem, wp, w4, lane);
    return;
  }
  const int fr = lane & 15, g = lane >> 4;
  const int t = 64 * it + 16 * w + fr;
  const char* hp = ws + OFF_HH + (size_t)(4 * it + w) * (54 * 1024);
  const unsigned voff = lane * 16;
  u4_t R[18];
#pragma unroll
  for (int i = 0; i < 9; ++i)
    asm volatile("global_load_dwordx4 %0, %1, %2" : "=v"(R[i]) : "v"(voff), "s"(hp + (size_t)i * 1024) : "memory");
  u2_t res[5]; unsigned st;
  f4_t acc[5];
#pragma unroll
  for (int dt = 0; dt < 5; ++dt) acc[dt] = f4_t{0.f, 0.f, 0.f, 0.f};
  b8_t A0[5], A1[5];
  auto rdA = [&](b8_t (&A)[5], unsigned off) {
    const char* sp = smem + off + lane * 16;
#pragma unroll
    for (int dt = 0; dt < 5; ++dt) A[dt] = *(const b8_t*)(sp + dt * 1024);
  };
  bar();
  rdA(A0, 0);
  unsigned so = 0;
  auto kstep = [&](auto mode, auto idx, b8_t (&Ac)[5], b8_t (&An)[5], const char* hnext) {
    constexpr int MODE = decltype(mode)::value, i = decltype(idx)::value, kk = i % 3;
    if (i % 6 == 0) bar();
    unsigned nx = so + (kk + 1) * 5120;
    if (kk == 2) { so += D_SLOT; if (so == D_NSLOT * D_SLOT) so = 0; nx = so; }
    SB;
    if (!(MODE == 2 && i == 17)) rdA(An, nx);
    constexpr int NV = (MODE == 0) ? (i < 9 ? 8 + i : 26 - i) : (MODE == 1) ? (i < 9 ? 25 - i : 17) : (i == 0 ? 17 : 23 - i);
    asm volatile("s_waitcnt vmcnt(%1)" : "+v"(R[i]) : "i"(NV) : "memory");
    const b8_t B = __builtin_bit_cast(b8_t, R[i]);
#pragma unroll
    for (int dt = 0; dt < 5; ++dt) acc[dt] = __builtin_amdgcn_mfma_f32_16x16x32_bf16(Ac[dt], B, acc[dt], 0, 0, 0);
    SB;
    if (MODE == 0 && i < 9)
      asm volatile("global_load_dwordx4 %0, %1, %2" : "=v"(R[i < 9 ? 9 + i : 0]) : "v"(voff), "s"(hp + (size_t)(9 + i) * 1024) : "memory");
    if (MODE != 2) asm volatile("global_load_dwordx4 %0, %1, %2" : "+v"(R[i]) : "v"(voff), "s"(hnext + (size_t)i * 1024) : "memory");
    if (MODE == 2 && i == 0) {
#pragma unroll
      for (int dt = 0; dt < 5; ++dt) {
        const int d = 80 * dg + 16 * dt + 4 * g;
        const char* rp = ws + OFF_H8 + ((size_t)(d >> 3) * SL + t) * 16 + (d & 4) * 2;
        asm volatile("global_load_dwordx2 %0, %1, off" : "=v"(res[dt]) : "v"(rp) : "memory");
      }
      asm volatile("global_load_dword %0, %1, off" : "=v"(st) : "v"(ws + OFF_G2 + (size_t)t * 8) : "memory");
    }
    SB;
  };
  auto round = [&](auto mode, const char* hnext) {
    kstep(mode, IC(0), A0, A1, hnext); kstep(mode, IC(1), A1, A0, hnext); kstep(mode, IC(2), A0, A1, hnext);
    kstep(mode, IC(3), A1, A0, hnext); kstep(mode, IC(4), A0, A1, hnext); kstep(mode, IC(5), A1, A0, hnext);
    kstep(mode, IC(6), A0, A1, hnext); kstep(mode, IC(7), A1, A0, hnext); kstep(mode, IC(8), A0, A1, hnext);
    kstep(mode, IC(9), A1, A0, hnext); kstep(mode, IC(10), A0, A1, hnext); kstep(mode, IC(11), A1, A0, hnext);
    kstep(mode, IC(12), A0, A1, hnext); kstep(mode, IC(13), A1, A0, hnext); kstep(mode, IC(14), A0, A1, hnext);
    kstep(mode, IC(15), A1, A0, hnext); kstep(mode, IC(16), A0, A1, hnext); kstep(mode, IC(17), A1, A0, hnext);
  };
  round(IC(0), hp + 18 * 1024);
  round(IC(1), hp + 36 * 1024);
  round(IC(2), hp);
  asm volatile("s_waitcnt vmcnt(0)" : "+v"(res[0]), "+v"(res[1]), "+v"(res[2]), "+v"(res[3]), "+v"(res[4]), "+v"(st)::"memory");
  const float sd = bf_hi(st), r2 = 1.0f / (sd * sd);
#pragma unroll
  for (int dt = 0; dt < 5; ++dt) {
    const int d = 80 * dg + 16 * dt + 4 * g;
    const float h0 = bf_lo(res[dt].x), h1 = bf_hi(res[dt].x), h2 = bf_lo(res[dt].y), h3 = bf_hi(res[dt].y);
    *(u2_t*)(out + (size_t)t * DM + d) = u2_t{pk_bf16(h0 + r2 * acc[dt][0], h1 + r2 * acc[dt][1]), pk_bf16(h2 + r2 * acc[dt][2], h3 + r2 * acc[dt][3])};
  }
}

extern "C" void kernel_launch(void* const* d_in, const int* in_sizes, int n_in, void* d_out, int out_size, void* d_ws,
                              size_t ws_size, hipStream_t stream) {
  char* ws = (char*)d_ws;
  (void)hipFuncSetAttribute((const void*)kB, hipFuncAttributeMaxDynamicSharedMemorySize, B_LDS);
  (void)hipFuncSetAttribute((const void*)kC, hipFuncAttributeMaxDynamicSharedMemorySize, C_LDS);
  (void)hipFuncSetAttribute((const void*)kD, hipFuncAttributeMaxDynamicSharedMemorySize, D_LDS);
  kB<<<256, 512, B_LDS, stream>>>((const u16*)d_in[0], (const u16*)d_in[1], (const u16*)d_in[2], (const u16*)d_in[3], (const u16*)d_in[4],
                                  (const u16*)d_in[5], (const u16*)d_in[6], (const u16*)d_in[7], (const u16*)d_in[8], (const u16*)d_in[10],
                                  (const u16*)d_in[11], (const u16*)d_in[12], ws);
  kC<<<256, 512, C_LDS, stream>>>((const u16*)d_in[10], (const u16*)d_in[11], ws);
  kD<<<256, 512, D_LDS, stream>>>(ws, (u16*)d_out);
}
```

```cpp
#define STAMPS
#define PROBE 9
#define PROBE_M 4
#include <hip/hip_runtime.h>
#include <hip/hip_bf16.h>
#include <stdint.h>
#include <type_traits>

typedef unsigned short u16;
typedef _Float16 h2_t __attribute__((ext_vector_type(2)));
typedef _Float16 h8_t __attribute__((ext_vector_type(8)));
typedef __bf16 b2_t __attribute__((ext_vector_type(2)));
typedef __bf16 b8_t __attribute__((ext_vector_type(8)));
typedef float f2_t __attribute__((ext_vector_type(2)));
typedef float f4_t __attribute__((ext_vector_type(4)));
typedef unsigned u2_t __attribute__((ext_vector_type(2)));
typedef unsigned u4_t __attribute__((ext_vector_type(4)));
typedef short s4_t __attribute__((ext_vector_type(4)));
typedef __attribute__((address_space(1))) unsigned long long gu64;
#define LAS __attribute__((address_space(3)))
#define SB __builtin_amdgcn_sched_barrier(0)
#define IC(n) std::integral_constant<int, n>{}

constexpr int SL = 2048;
constexpr int DM = 640;
constexpr int FF = 1728;

constexpr size_t OFF_G1 = 0;
constexpr size_t OFF_EP = 0x10000;
constexpr size_t OFF_RD = 0x10100;
constexpr size_t OFF_H8 = 0x5A0000;
constexpr size_t OFF_HH = 0x820000;
constexpr size_t OFF_WP2 = 0xF00000;
constexpr size_t OFF_CS = 0x1120000;
constexpr size_t OFF_G2 = 0x1130000;
constexpr int KRS = 1344;
constexpr int KDS = 4864;
constexpr int KD_O = 2240;
constexpr int KD_DR = 4384;

constexpr size_t OFF_DBG = 0x2000000;
#define STAMP(k, i) do { if ((threadIdx.x & 63) == 0) ((unsigned long long*)(ws + OFF_DBG))[((k) * 512 + blockIdx.x) * 16 + (i)] = __builtin_amdgcn_s_memrealtime(); } while (0)

__device__ __forceinline__ float bf_lo(unsigned d) { return __uint_as_float(d << 16); }
__device__ __forceinline__ float bf_hi(unsigned d) { return __uint_as_float(d & 0xffff0000u); }
__device__ __forceinline__ float bf2f(u16 v) { return __uint_as_float((unsigned)v << 16); }
__device__ __forceinline__ unsigned pk_bf16(float a, float b) {
  f2_t v = {a, b};
  return __builtin_bit_cast(unsigned, __builtin_convertvector(v, b2_t));
}
__device__ __forceinline__ unsigned pk_f16(float a, float b) {
  f2_t v = {a, b};
  return __builtin_bit_cast(unsigned, __builtin_convertvector(v, h2_t));
}
__device__ __forceinline__ float rbf(float a) { return bf_lo(pk_bf16(a, 0.f)); }
template <int N> __device__ __forceinline__ float dpp_ror_add(float v) {
  int r = __builtin_amdgcn_update_dpp(0, __float_as_int(v), 0x120 + N, 0xf, 0xf, false);
  return v + __int_as_float(r);
}
__device__ __forceinline__ float row16_sum(float v) {
  v = dpp_ror_add<8>(v); v = dpp_ror_add<4>(v); v = dpp_ror_add<2>(v); v = dpp_ror_add<1>(v);
  return v;
}
__device__ __forceinline__ float wave_sum(float v) {
  v = row16_sum(v);
  const int b = __float_as_int(v);
  return (__int_as_float(__builtin_amdgcn_readlane(b, 0)) + __int_as_float(__builtin_amdgcn_readlane(b, 16))) +
         (__int_as_float(__builtin_amdgcn_readlane(b, 32)) + __int_as_float(__builtin_amdgcn_readlane(b, 48)));
}
template <typename T> __device__ __forceinline__ T ldnt(const T* p) { return __builtin_nontemporal_load(p); }
__device__ __forceinline__ void wave_fence() {
  __builtin_amdgcn_fence(__ATOMIC_RELEASE, "wavefront");
  __builtin_amdgcn_wave_barrier();
  __builtin_amdgcn_fence(__ATOMIC_ACQUIRE, "wavefront");
}
__device__ __forceinline__ s4_t tr_read(const char* p) {
  return __builtin_amdgcn_ds_read_tr16_b64_v4i16((LAS s4_t*)p);
}
__device__ __forceinline__ void glds16(const void* sbase, unsigned voff, unsigned lds_addr) {
  asm volatile("s_mov_b32 m0, %2\n\ts_nop 0\n\tglobal_load_lds_dwordx4 %0, %1" ::"v"(voff), "s"(sbase), "s"(lds_addr) : "memory");
}
template <int N> __device__ __forceinline__ void wait_vm() { asm volatile("s_waitcnt vmcnt(%0)" ::"i"(N) : "memory"); }
__device__ __forceinline__ void bar() { asm volatile("s_barrier" ::: "memory"); }
__device__ __forceinline__ float dot2bf(unsigned a, unsigned b, float c) {
  return __builtin_amdgcn_fdot2_f32_bf16(__builtin_bit_cast(b2_t, a), __builtin_bit_cast(b2_t, b), c, false);
}

#define ST_MODE 0
__device__ __forceinline__ void st16(void* p, u4_t v) {
  *(u4_t*)p = v;
}
__device__ __forceinline__ void st8(void* p, u2_t v) {
  *(u2_t*)p = v;
}

struct Cw { f2_t w, ws; };
__device__ __forceinline__ Cw mkw(f2_t w) { Cw r; r.w = w; r.ws = f2_t{-w.y, w.x}; return r; }
__device__ __forceinline__ f2_t cmulw(f2_t a, const Cw& W) {
  return __builtin_elementwise_fma(f2_t{a.y, a.y}, W.ws, f2_t{a.x, a.x} * W.w);
}
__device__ __forceinline__ f2_t cexp_l(float er, float wr, float l) {
  float mag = __builtin_amdgcn_exp2f(er * l);
  float rev = __builtin_amdgcn_fractf(wr * l);
  return f2_t{mag * __builtin_amdgcn_cosf(rev), mag * __builtin_amdgcn_sinf(rev)};
}
__device__ __forceinline__ float pow2_scale(float bound, int target) {
  unsigned e = (__float_as_uint(bound) >> 23) & 0xff;
  int k = target - ((int)e - 126);
  k = k > 126 ? 126 : (k < -126 ? -126 : k);
  return (bound > 0.f) ? __uint_as_float((unsigned)(k + 127) << 23) : 1.0f;
}
__device__ __forceinline__ float pow2_inv(float p) { return __uint_as_float(0x7f000000u - __float_as_uint(p)); }

constexpr int B_SIDE = 96;
constexpr int KG_R = 0, KG_L0 = 4352, KG_L1 = 8704, KG_W = 8704 + 25 * 272;
constexpr int B_BND = 8 * KG_W;
constexpr int B_KC = B_BND + 64;
constexpr int B_LDS = B_KC + 4 * KRS;
constexpr int B_KOFF = 0;
constexpr int B_UOFF = 4 * KDS;
constexpr int B_UCH = 4608;
constexpr int B_YCH = 10240;
constexpr int B_PACK = 40960;

__device__ __forceinline__ void pack_wd(const u16* __restrict__ wd, char* __restrict__ ws, char* buf, int u, int lane) {
  const int ks = u >> 3, dg = u & 7;
#pragma unroll
  for (int i = 0; i < 5; ++i) {
    const int idx = i * 64 + lane, row = idx / 10, c = idx % 10;
    __builtin_amdgcn_global_load_lds((const unsigned*)((const char*)wd + (size_t)(32 * ks + row) * 1280 + 160 * dg + 16 * c),
                                     (LAS unsigned*)(buf + i * 1024), 16, 0, 2);
  }
  asm volatile("s_waitcnt vmcnt(0)" ::: "memory");
  wave_fence();
  const int g = lane >> 4, q = (lane & 15) >> 2, p = lane & 3;
  char* dst = ws + OFF_WP2 + ((size_t)(dg * 54 + ks) * 5) * 1024 + lane * 16;
#pragma unroll
  for (int dt = 0; dt < 5; ++dt) {
    s4_t aa = tr_read(buf + (4 * g + q) * 160 + 32 * dt + 8 * p);
    s4_t bb = tr_read(buf + (16 + 4 * g + q) * 160 + 32 * dt + 8 * p);
    u2_t ua = __builtin_bit_cast(u2_t, aa), ub = __builtin_bit_cast(u2_t, bb);
    const u4_t pv = {ua.x, ua.y, ub.x, ub.y};
    asm volatile("global_store_dwordx4 %0, %1, off sc1\n\ts_nop 1" ::"v"(dst + dt * 1024), "v"(pv) : "memory");
  }
}

struct BFrag { h8_t B[8]; u2_t A[5][2]; };
__device__ __forceinline__ void mfma_f16(f4_t& d, u4_t a, h8_t b) {
  asm volatile("v_mfma_f32_16x16x32_f16 %0, %1, %2, %0" : "+v"(d) : "v"(a), "v"(b));
}
__device__ __forceinline__ void mfma_f16_0(f4_t& d, u4_t a, h8_t b) {
  asm volatile("v_mfma_f32_16x16x32_f16 %0, %1, %2, 0" : "=&v"(d) : "v"(a), "v"(b));
}
__device__ __forceinline__ unsigned ln_fin(float mean, float var) {
  const float mb = rbf(mean), sd = rbf(sqrtf(rbf(rbf(var) + 1e-5f)));
  return (__float_as_uint(mb) >> 16) | (__float_as_uint(sd) & 0xffff0000u);
}
__device__ __forceinline__ unsigned ln1_row(const u16* __restrict__ x, int t) {
  const u4_t* p = (const u4_t*)(x + (size_t)t * DM);
  float s = 0.f, q = 0.f;
  for (int i = 0; i < 80; ++i) { const u4_t v = p[i]; for (int j = 0; j < 4; ++j) s += bf_lo(v[j]) + bf_hi(v[j]); }
  const float mean = s * (1.0f / DM);
  for (int i = 0; i < 80; ++i) {
    const u4_t v = p[i];
    for (int j = 0; j < 4; ++j) { const float a = bf_lo(v[j]) - mean, b = bf_hi(v[j]) - mean; q += a * a; q += b * b; }
  }
  return ln_fin(mean, q * (1.0f / DM));
}
constexpr int SPIN_MAX = 1 << 14;

extern "C" __global__ __launch_bounds__(512) void kB(const u16* __restrict__ x, const u16* __restrict__ Are, const u16* __restrict__ Aim,
                                                     const u16* __restrict__ Bre, const u16* __restrict__ Bim, const u16* __restrict__ Cre,
                                                     const u16* __restrict__ Cim, const u16* __restrict__ logdt, const u16* __restrict__ dskip,
                                                     const u16* __restrict__ wg, const u16* __restrict__ wu, const u16* __restrict__ wd,
                                                     const u16* __restrict__ outp, char* __restrict__ ws) {
  extern __shared__ __attribute__((aligned(16))) char smem[];
  const int tid = threadIdx.x, lane = tid & 63;
  const int w = __builtin_amdgcn_readfirstlane(tid >> 6);
  const int xcd = blockIdx.x & 7;
#define EPLOAD(v) asm volatile("global_load_dwordx2 %0, %1, %2 sc1" : "=v"(v) : "v"(0u), "s"(ws + OFF_EP) : "memory")
  if (w == 0) STAMP(1, 0);
  auto post_epoch = [&](u2_t ep, int off) {
    if (tid == 0) {
      const unsigned long long e = (((unsigned long long)ep.y << 32) | ep.x) + 1ull;
      *(unsigned long long*)(smem + off) = e;
      *(unsigned*)(smem + off + 8) = (unsigned)((e * 0x9E3779B97F4A7C15ull) >> 32);
      __hip_atomic_store((gu64*)(ws + OFF_RD) + blockIdx.x, e, __ATOMIC_RELAXED, __HIP_MEMORY_SCOPE_AGENT);
    }
  };
  if (blockIdx.x < B_SIDE) {
    const int rk = blockIdx.x >> 3;
    const int g4 = xcd >> 1, a = xcd & 1, nl = (g4 < 3) ? 7 : 6;
    constexpr int S_EP = 17408;
    unsigned pf[2] = {0u, 0u};
    if (rk >= 8) {
#pragma unroll
      for (int k = 0; k < 2; ++k) {
        const int id = (((rk - 8) * 8 + w) * 2 + k) * 64 + lane;
        asm volatile("global_load_dword %0, %1, off" : "=v"(pf[k]) : "v"((const char*)x + (size_t)(id >> 1) * (DM * 2) + (((160 * xcd) >> 7) + (id & 1)) * 128) : "memory");
      }
    }
    if (rk < 8) {
      const int r = lane >> 4, c = lane & 15, t = (blockIdx.x * 8 + w) * 4 + r;
      const u4_t* p = (const u4_t*)(x + (size_t)t * DM);
      u2_t ep;
      EPLOAD(ep);
      u4_t v[5];
#pragma unroll
      for (int i = 0; i < 5; ++i) v[i] = ldnt(p + c + 16 * i);
      float s = 0.f;
#pragma unroll
      for (int i = 0; i < 5; ++i)
#pragma unroll
        for (int j = 0; j < 4; ++j) s += bf_lo(v[i][j]) + bf_hi(v[i][j]);
      asm volatile("" : "+v"(ep) : "v"(s));
      post_epoch(ep, S_EP);
      const float mean = row16_sum(s) * (1.0f / DM);
      float q = 0.f;
#pragma unroll
      for (int i = 0; i < 5; ++i)
#pragma unroll
        for (int j = 0; j < 4; ++j) {
          const float aa = bf_lo(v[i][j]) - mean, bb = bf_hi(v[i][j]) - mean;
          q += aa * aa; q += bb * bb;
        }
      const unsigned sv = ln_fin(mean, row16_sum(q) * (1.0f / DM));
      __syncthreads();
      const u2_t e = *(const u2_t*)(smem + S_EP);
      if (c == 0) {
        __hip_atomic_store((gu64*)(ws + OFF_G1) + t, ((unsigned long long)*(const unsigned*)(smem + S_EP + 8) << 32) | sv, __ATOMIC_RELAXED, __HIP_MEMORY_SCOPE_AGENT);
        ((unsigned long long*)(ws + OFF_G2))[t] = 0ull;
      }
      if (w == 0) STAMP(1, 1);
      if (rk == 4 && w == 0) {
        unsigned long long a0, a1;
        asm volatile("s_getpc_b64 s[20:21]\n\ts_add_u32 s20, s20, kB@rel32@lo+4\n\ts_addc_u32 s21, s21, kB@rel32@hi+12\n\ts_mov_b64 %0, s[20:21]" : "=s"(a0) :: "s20", "s21", "scc");
        asm volatile("s_getpc_b64 s[20:21]\n\ts_add_u32 s20, s20, kD@rel32@lo+4\n\ts_addc_u32 s21, s21, kD@rel32@hi+12\n\ts_mov_b64 %0, s[20:21]" : "=s"(a1) :: "s20", "s21", "scc");
        const unsigned long long lo = a0 < a1 ? a0 : a1, last = ((a0 < a1 ? a1 - a0 : a0 - a1) + 8192) & ~127ull;
        unsigned cf[8];
#pragma unroll
        for (int i = 0; i < 8; ++i) {
          const unsigned long long o = (unsigned long long)(lane + 64 * i) * 128;
          asm volatile("global_load_dword %0, %1, off" : "=v"(cf[i]) : "v"(lo + (o < last ? o : last)) : "memory");
        }
        asm volatile("s_waitcnt vmcnt(0)" ::"v"(cf[0]), "v"(cf[1]), "v"(cf[2]), "v"(cf[3]), "v"(cf[4]), "v"(cf[5]), "v"(cf[6]), "v"(cf[7]) : "memory");
      }
      if (blockIdx.x == 63 && w == 7) {
        const unsigned long long e64 = ((unsigned long long)e.y << 32) | e.x;
        for (int spin = 0; spin < SPIN_MAX; ++spin) {
          bool ok = true;
#pragma unroll
          for (int i = 0; i < 4; ++i) ok &= __hip_atomic_load((const gu64*)(ws + OFF_RD) + lane + 64 * i, __ATOMIC_RELAXED, __HIP_MEMORY_SCOPE_AGENT) == e64;
          if (__all(ok)) break;
          __builtin_amdgcn_s_sleep(4);
        }
        if (lane == 0) __hip_atomic_store((gu64*)(ws + OFF_EP), e64, __ATOMIC_RELAXED, __HIP_MEMORY_SCOPE_AGENT);
      }
    }
    const int strip = (rk >= 8) ? rk - 8 : 4 + rk;
    if ((rk >= 8 || rk < 3) && strip < nl) {
      const int jl = 7 * g4 + strip;
      const char* pm = (const char*)(a ? wu : wg) + (size_t)(80 * w + (lane >> 3)) * (FF * 2) + 128 * jl + (lane & 7) * 16;
      u2_t ep;
      EPLOAD(ep);
      u4_t v[10];
#pragma unroll
      for (int i = 0; i < 10; ++i) v[i] = ldnt((const u4_t*)(pm + (size_t)i * 8 * (FF * 2)));
      const char* po = (const char*)(a ? wg : wu) + (size_t)(80 * w) * (FF * 2) + 128 * jl;
      unsigned t0v, t1v;
      asm volatile("global_load_dword %0, %1, off nt" : "=v"(t0v) : "v"(po + (size_t)lane * (FF * 2)) : "memory");
      asm volatile("global_load_dword %0, %1, off nt" : "=v"(t1v) : "v"(po + (size_t)(64 + (lane & 15)) * (FF * 2)) : "memory");
      f2_t s[4];
#pragma unroll
      for (int e = 0; e < 4; ++e) s[e] = f2_t{0.f, 0.f};
#pragma unroll
      for (int i = 0; i < 10; ++i)
#pragma unroll
        for (int jj = 0; jj < 4; ++jj) s[jj] += f2_t{bf_lo(v[i][jj]), bf_hi(v[i][jj])};
      asm volatile("" : "+v"(ep), "+v"(pf[0]), "+v"(pf[1]) : "v"(s[0]));
      if (rk >= 8) post_epoch(ep, S_EP);
      float* pp = (float*)smem;
      *(f4_t*)(pp + (w * 8 + (lane >> 3)) * 68 + (lane & 7) * 8) = f4_t{s[0].x, s[0].y, s[1].x, s[1].y};
      *(f4_t*)(pp + (w * 8 + (lane >> 3)) * 68 + (lane & 7) * 8 + 4) = f4_t{s[2].x, s[2].y, s[3].x, s[3].y};
      __syncthreads();
      float acc = 0.f;
#pragma unroll
      for (int i = 0; i < 8; ++i) acc += pp[(w * 8 + i) * 68 + lane];
      __syncthreads();
      pp[w * 64 + lane] = acc;
      __syncthreads();
      if (w == 0) {
        float t = 0.f;
#pragma unroll
        for (int i = 0; i < 8; ++i) t += pp[i * 64 + lane];
        ((float*)(ws + OFF_CS))[a * FF + 64 * jl + lane] = t;
      }
      asm volatile("s_waitcnt vmcnt(0)" ::"v"(t0v), "v"(t1v) : "memory");
    }
    if (w == 0) STAMP(1, 7);
    return;
  }
  const int cb = blockIdx.x - B_SIDE;
  const int q = 20 * xcd + (cb >> 3);
  const int cl = w >> 1, hw = w & 1, ch = 4 * q + cl;
  const int n = ch * 128 + hw * 64 + lane;
  u2_t ep;
  EPLOAD(ep);
  const u16 i_are = ldnt(Are + n), i_aim = ldnt(Aim + n), i_bre = ldnt(Bre + n), i_bim = ldnt(Bim + n), i_cre = ldnt(Cre + n), i_cim = ldnt(Cim + n);
  const u16 i_dt = logdt[ch];
  u2_t xg[4];
  const unsigned xoff = tid * (2 * DM * 2);
  const char* xb = (const char*)(x + 4 * q);
#define XG(i) asm volatile("global_load_dwordx2 %0, %1, %2 offset:%3" : "=v"(xg[i]) : "v"(xoff), "s"(xb + ((i) >> 1) * (size_t)(1024 * DM * 2)), "i"(((i) & 1) * DM * 2) : "memory")
  const u2_t dkv = *(const u2_t*)(dskip + 4 * q);
  const int c = lane & 15, kg = lane >> 4;
  h8_t band9, band7;
#pragma unroll
  for (int j = 0; j < 8; ++j) {
    const int k = 8 * kg + 7 - j;
    band9[j] = (k >= c + 4 && k <= c + 12) ? (_Float16)1.0f : (_Float16)0.0f;
    band7[j] = (k >= c + 5 && k <= c + 11) ? (_Float16)1.0f : (_Float16)0.0f;
  }
  asm volatile("" : "+v"(band9), "+v"(band7));
  char* hb = smem + w * KG_W;
  float Sw, bsum;
  {
    const float dt = rbf(expf(bf2f(i_dt)));
    const float are = dt * bf2f(i_are), aim = dt * bf2f(i_aim);
    const float bre = dt * bf2f(i_bre), bim = dt * bf2f(i_bim);
    const float cre = bf2f(i_cre), cim = bf2f(i_cim);
    f2_t c0 = {cre * bre - cim * bim, cre * bim + cim * bre};
    const float er = are * 1.44269504088896341f, wr = aim * 0.15915494309189535f;
    const float bnd = (fabsf(c0.x) + fabsf(c0.y)) * fmaxf(1.0f, __builtin_amdgcn_exp2f(er * 2048.f));
    bsum = wave_sum(bnd);
    asm volatile("" : "+v"(ep) : "v"(bnd));
    post_epoch(ep, B_BND + 48);
    if (lane == 0) ((float*)(smem + B_BND))[w] = bsum;
    if (w == 0) STAMP(1, 1);
    Sw = pow2_scale(bsum, 15);
    c0 *= Sw;
    const Cw z1 = mkw(cexp_l(er, wr, 1.f)), w8 = mkw(cexp_l(er, -wr, 8.f)), w128 = mkw(cexp_l(er, wr, 128.f));
    const Cw z2 = mkw(cmulw(z1.w, z1));
    const Cw z4 = mkw(cmulw(z2.w, z2));
    auto put = [&](int off, int row, f2_t v) { *(unsigned*)(hb + off + row * 272 + 4 * lane) = pk_f16(v.x, v.y); };
    f2_t R = w8.w, v0 = c0, v1 = cmulw(c0, w128), T = v1;
    *(unsigned*)(hb + KG_R + 4 * lane) = pk_f16(1.f, 0.f);
#pragma unroll
    for (int i = 0; i < 8; ++i) {
      SB;
      if ((w & 3) == (i & 3)) { if (i < 4) XG(0); else XG(1); }
      SB;
      *(unsigned*)(hb + KG_R + (2 * i + 1) * 272 + 4 * lane) = pk_f16(R.x, R.y);
      if (i < 7) {
        R = cmulw(R, w8);
        *(unsigned*)(hb + KG_R + (2 * i + 2) * 272 + 4 * lane) = pk_f16(R.x, R.y);
        R = cmulw(R, w8);
      }
      put(KG_L0, i, v0); put(KG_L0, 8 + i, v1);
      if (i < 7) { v0 = cmulw(v0, z1); v1 = cmulw(v1, z1); }
#pragma unroll
      for (int k = 0; k < 2; ++k) {
        const int i1 = 2 + 2 * i + k;
        if (i1 <= 16) {
          T = cmulw(T, w128);
          if (i1 < 4) {
            f2_t v = T;
#pragma unroll
            for (int i0 = 0; i0 < 4; ++i0) { put(KG_L1, 4 * (i1 - 2) + i0, v); if (i0 < 3) v = cmulw(v, z2); }
          } else if (i1 < 8) {
            put(KG_L1, 8 + 2 * (i1 - 4), T); put(KG_L1, 8 + 2 * (i1 - 4) + 1, cmulw(T, z4));
          } else {
            put(KG_L1, 16 + (i1 - 8), T);
          }
        }
      }
    }
  }
  SB;
  if (w == 0) STAMP(1, 4);
  u4_t gr[2];
  const unsigned goff = tid * 16;
#define GRL() do { asm volatile("global_load_dwordx4 %0, %1, %2 sc1" : "=v"(gr[0]) : "v"(goff), "s"(ws + OFF_G1) : "memory"); \
                   asm volatile("global_load_dwordx4 %0, %1, %2 sc1" : "=v"(gr[1]) : "v"(goff), "s"(ws + OFF_G1 + 8192) : "memory"); } while (0)
  GRL();
  if (w < 4) XG(2);
  wave_fence();
  f4_t ka[3];
  {
    h8_t Bf[4];
#pragma unroll
    for (int ks = 0; ks < 4; ++ks) Bf[ks] = *(const h8_t*)(hb + KG_R + c * 272 + 64 * ks + 16 * kg);
#pragma unroll
    for (int t = 0; t < 3; ++t) ka[t] = f4_t{0.f, 0.f, 0.f, 0.f};
#pragma unroll
    for (int ks = 0; ks < 4; ++ks)
#pragma unroll
      for (int t = 0; t < 3; ++t) {
        const int off = (t == 0) ? KG_L0 + c * 272 : (t == 1) ? KG_L1 + c * 272 : KG_L1 + (16 + min(c, 8)) * 272;
        const h8_t Af = *(const h8_t*)(hb + off + 64 * ks + 16 * kg);
        ka[t] = __builtin_amdgcn_mfma_f32_16x16x32_f16(Af, Bf[ks], ka[t], 0, 0, 0);
      }
  }
  SB;
  if (w >= 4) XG(2); else XG(3);
  SB;
  const float iSw = pow2_inv(Sw);
  wave_fence();
  if (hw == 0) *(f4_t*)(hb + lane * 16) = ka[1] * iSw;
  else { *(f4_t*)(hb + lane * 16) = ka[0] * iSw; *(f4_t*)(hb + 1024 + lane * 16) = ka[2] * iSw; }
  __syncthreads();
  if (w == 0) STAMP(1, 5);
  const unsigned etag = *(const unsigned*)(smem + B_BND + 48 + 8);
  if (w >= 4) XG(3);
  {
    const char* pb = smem + (w ^ 1) * KG_W;
    const float Sc = pow2_scale(((const float*)(smem + B_BND))[2 * cl] + ((const float*)(smem + B_BND))[2 * cl + 1], 15);
    char* kr = smem + B_KC + cl * KRS;
    if (hw == 0) {
      const f4_t k0 = (ka[0] * iSw + *(const f4_t*)(pb + lane * 16)) * Sc, k2 = (ka[2] * iSw + *(const f4_t*)(pb + 1024 + lane * 16)) * Sc;
      *(u2_t*)(kr + 2 * (128 * (kg >> 1) + 4 * (kg & 1) + 8 * c)) = u2_t{pk_f16(k0[0], k0[1]), pk_f16(k0[2], k0[3])};
      const unsigned p01 = pk_f16(k2[0], k2[1]), p23 = pk_f16(k2[2], k2[3]);
      u16* k3 = (u16*)kr + 512 + 64 * kg + c;
      if (kg < 2) { k3[0] = (u16)p01; k3[16] = (u16)(p01 >> 16); k3[32] = (u16)p23; k3[48] = (u16)(p23 >> 16); }
      else if (lane == 32) k3[0] = (u16)p01;
      if (lane == 0) ((float*)(smem + B_BND + 32))[cl] = pow2_inv(Sc);
    } else {
      const f4_t k1 = (ka[1] * iSw + *(const f4_t*)(pb + lane * 16)) * Sc;
      const unsigned p01 = pk_f16(k1[0], k1[1]), p23 = pk_f16(k1[2], k1[3]);
      if (kg < 2) {
        *(u2_t*)(kr + 2 * (256 + 64 * kg + 4 * c)) = u2_t{p01, p23};
      } else {
        *(unsigned*)(kr + 2 * (384 + 64 * (kg - 2) + 2 * c)) = p01;
        *(unsigned*)(kr + 2 * (384 + 64 * (kg - 2) + 32 + 2 * c)) = p23;
      }
    }
  }
  __syncthreads();
  const float invs = ((const float*)(smem + B_BND + 32))[w & 3];
  {
    const int kch = tid >> 7, kc = tid & 127;
    const char* kr = smem + B_KC + kch * KRS;
    const int e0 = (kc < 32) ? 8 * kc : (kc < 64) ? 256 + 4 * (kc - 32) : 384 + 2 * (kc - 64);
    const unsigned* kp = (const unsigned*)(kr + 2 * e0);
    const unsigned d0 = kp[0], d1 = kp[1], d2 = kp[2], d3 = kp[3], d4 = kp[4];
    const unsigned kfirst = *(const u16*)kr, k1024 = *(const u16*)(kr + 2 * 512);
    const int dl = 127 - kc;
    const unsigned kfa = *(const u16*)(kr + 2 * (512 + max(2 * dl - 127, 0))), kfb = *(const u16*)(kr + 2 * (512 + max(2 * dl - 126, 0)));
    char* ki = smem + B_KOFF + kch * KDS;
    const unsigned k0 = d0 & 0xffffu;
    unsigned o0, o1, o2, o3, nx;
    if (kc < 32) { o0 = d0; o1 = d1; o2 = d2; o3 = d3; nx = d4; }
    else if (kc < 64) {
      const unsigned k2 = d0 >> 16, k4 = d1 & 0xffffu, k6 = d1 >> 16, k8 = d2 & 0xffffu;
      o0 = k0 * 0x10001u; o1 = k2 | (k4 << 16); o2 = k4 * 0x10001u; o3 = k6 | (k8 << 16); nx = k8;
    } else {
      const unsigned k4 = d0 >> 16, k8 = d1 & 0xffffu;
      o0 = k0 * 0x10001u; o1 = k0 | (k4 << 16); o2 = k4 * 0x10001u; o3 = k8 * 0x10001u; nx = k8;
    }
    *(u4_t*)(ki + 16 * (kc + 2)) = u4_t{o0, o1, o2, o3};
    *(u4_t*)(ki + KD_O + 16 * (kc + 2)) = u4_t{__builtin_amdgcn_alignbit(o1, o0, 16), __builtin_amdgcn_alignbit(o2, o1, 16),
                                              __builtin_amdgcn_alignbit(o3, o2, 16), __builtin_amdgcn_alignbit(nx, o3, 16)};
    if (kc < 6) {
      const int cp = (kc < 2) ? kc : 128 + kc;
      const unsigned kk = k1024 * 0x10001u;
      const u4_t e = (kc == 2) ? u4_t{kk, kk, k1024, 0u} : u4_t{0u, 0u, 0u, 0u};
      const u4_t o = (kc == 2) ? u4_t{kk, kk, 0u, 0u} : u4_t{0u, 0u, 0u, (kc == 1) ? (kfirst << 16) : 0u};
      *(u4_t*)(ki + 16 * cp) = e;
      *(u4_t*)(ki + KD_O + 16 * cp) = o;
    }
    if (kc < 80) *(unsigned*)(ki + KD_DR + 4 * kc) = (dl >= 64) ? (kfa | (kfb << 16)) : 0u;
  }
  *(unsigned*)(smem + B_UOFF + (tid >> 7) * B_UCH + 4096 + 4 * (tid & 127)) = 0u;
  asm volatile("s_waitcnt vmcnt(2)" : "+v"(gr[0]), "+v"(gr[1]), "+v"(xg[0]), "+v"(xg[1])::"memory");
  {
    for (int spin = 0; spin < SPIN_MAX; ++spin) {
      if (__all(gr[0][1] == etag && gr[0][3] == etag && gr[1][1] == etag && gr[1][3] == etag)) break;
      __builtin_amdgcn_s_sleep(2);
      GRL();
      asm volatile("s_waitcnt vmcnt(0)" : "+v"(gr[0]), "+v"(gr[1])::"memory");
    }
#pragma unroll
    for (int it = 0; it < 2; ++it)
#pragma unroll
      for (int r = 0; r < 2; ++r)
        if (gr[it][2 * r + 1] != etag) gr[it][2 * r] = ln1_row(x, 2 * (tid + 512 * it) + r);
  }
  if (w == 0) STAMP(1, 6);
  const float dk[4] = {bf_lo(dkv.x), bf_hi(dkv.x), bf_lo(dkv.y), bf_hi(dkv.y)};
  float z[2][8];
#pragma unroll
  for (int it = 0; it < 2; ++it) {
    if (it == 1) asm volatile("s_waitcnt vmcnt(0)" : "+v"(xg[2]), "+v"(xg[3])::"memory");
    const int j = tid + 512 * it;
    const unsigned s0 = gr[it][0], s1 = gr[it][2];
    const float m0 = bf_lo(s0), r0 = __builtin_amdgcn_rcpf(bf_hi(s0)), m1 = bf_lo(s1), r1 = __builtin_amdgcn_rcpf(bf_hi(s1));
    const u2_t xa = xg[2 * it], xc = xg[2 * it + 1];
    const float x0[4] = {bf_lo(xa.x), bf_hi(xa.x), bf_lo(xa.y), bf_hi(xa.y)};
    const float x1[4] = {bf_lo(xc.x), bf_hi(xc.x), bf_lo(xc.y), bf_hi(xc.y)};
#pragma unroll
    for (int cc = 0; cc < 4; ++cc) {
      const float u0 = (x0[cc] - m0) * r0, u1 = (x1[cc] - m1) * r1;
      *(unsigned*)(smem + B_UOFF + cc * B_UCH + 4 * (1023 - j)) = pk_f16(u1, u0);
      z[it][cc] = x0[cc] + u0 * dk[cc];
      z[it][4 + cc] = x1[cc] + u1 * dk[cc];
    }
  }
  __syncthreads();
  if (w == 0) STAMP(1, 2);
  if (w >= 4 && w < 7 && cb * 3 + (w - 4) < 432) pack_wd(wd, ws, smem + B_PACK + (w - 4) * 5120, cb * 3 + (w - 4), lane);
  f4_t acc[8];
  if (w < 4) {
    const char* ub = smem + B_UOFF + w * B_UCH + 4112 - 32 * c - 16 * kg - 512 * 7;
    const char* kb = smem + B_KOFF + w * KDS + ((c & 1) ? 0 : KD_O) + 4 * (12 + ((c + 1) >> 1) - 4 * kg);
    const unsigned* dr = (const unsigned*)(smem + B_KOFF + w * KDS + KD_DR) + (127 - c + 4 * kg);
    const char* uw = smem + B_UOFF + w * B_UCH + 4112 - 32 * c - 16 * kg;
    auto rdB = [&](BFrag& F, int i0, int p) { F.B[p] = *(const h8_t*)(ub + 512 * (7 - p) + 64 * i0); };
    auto rdA = [&](BFrag& F, int i0, int i1, int half) {
      const unsigned* ap = (const unsigned*)(kb + 64 * i0 + 512 * i1) + 2 * half;
      F.A[i1][half] = u2_t{ap[0], ap[1]};
    };
    auto frag = [&](const u2_t (&a)[2]) { return u4_t{a[0].x, a[0].y, a[1].x, a[1].y}; };
    auto rdnext = [&](BFrag& F, int i0, int m) {
      if (m < 8) rdB(F, i0, m); else if (m < 16) rdA(F, i0, (m - 8) >> 1, (m - 8) & 1);
    };
    auto step = [&](auto i0c, const BFrag& F, BFrag& N, auto nextc) {
      constexpr int I0 = decltype(i0c)::value, NX = decltype(nextc)::value;
      int m = 0;
#pragma unroll
      for (int i1 = 0; i1 < 5; ++i1) {
        if (i1 == 4 && I0 != 0) continue;
        const u4_t a = frag(F.A[i1]);
#pragma unroll
        for (int t = i1; t < 8; ++t) {
          if (I0 == 0 && i1 == 0) mfma_f16_0(acc[t], a, F.B[t]); else mfma_f16(acc[t], a, F.B[t - i1]);
          if (NX >= 0) rdnext(N, NX, m);
          SB;
          ++m;
        }
      }
    };
    BFrag F0, F1;
    h8_t U9[4], U7[4];
#pragma unroll
    for (int bt = 0; bt < 4; ++bt) { U9[bt] = *(const h8_t*)(uw - 512 * bt + 16); U7[bt] = *(const h8_t*)(uw - 512 * bt); }
#pragma unroll
    for (int m = 0; m < 16; ++m) rdnext(F0, 0, m);
    rdA(F0, 0, 4, 0); rdA(F0, 0, 4, 1);
    SB;
    f4_t X9[4], X7[4];
#pragma unroll
    for (int bt = 0; bt < 4; ++bt) { mfma_f16_0(X9[bt], __builtin_bit_cast(u4_t, U9[bt]), band9); mfma_f16_0(X7[bt], __builtin_bit_cast(u4_t, U7[bt]), band7); }
    SB;
    step(IC(0), F0, F1, IC(1));
    h8_t Vf[4]; u4_t Tf[4];
    {
      int m = 0;
#pragma unroll
      for (int i1 = 0; i1 < 4; ++i1) {
        const u4_t a = frag(F1.A[i1]);
#pragma unroll
        for (int t = i1; t < 8; ++t) {
          mfma_f16(acc[t], a, F1.B[t - i1]);
          rdnext(F0, 2, m);
          if (m < 4) {
            const u4_t vu = {pk_f16(X7[m][0], X9[m][0]), pk_f16(X7[m][1], X9[m][1]), pk_f16(X7[m][2], X9[m][2]), pk_f16(X7[m][3], X9[m][3])};
            Vf[m] = __builtin_bit_cast(h8_t, vu);
          }
          if (m >= 16 && m < 20) { const unsigned* tp = dr - 16 * (m - 16 + 4); Tf[m - 16] = u4_t{tp[0], tp[1], tp[2], tp[3]}; }
          SB;
          ++m;
        }
      }
    }
    step(IC(2), F0, F1, IC(3));
#pragma unroll
    for (int bt = 0; bt < 4; ++bt)
#pragma unroll
      for (int at = 4 + bt; at < 8; ++at) mfma_f16(acc[at], __builtin_bit_cast(u4_t, Vf[bt]), __builtin_bit_cast(h8_t, Tf[at - bt - 4]));
    SB;
    step(IC(3), F1, F0, IC(4));
    step(IC(4), F0, F1, IC(5));
    step(IC(5), F1, F0, IC(6));
    step(IC(6), F0, F1, IC(7));
    step(IC(7), F1, F0, IC(-1));
    asm volatile("s_nop 7\n\ts_nop 1" : "+v"(acc[0]), "+v"(acc[1]), "+v"(acc[2]), "+v"(acc[3]), "+v"(acc[4]), "+v"(acc[5]), "+v"(acc[6]), "+v"(acc[7]));
  }
  if (w == 0) STAMP(1, 3);
  bar();
  if (w < 4) {
#pragma unroll
    for (int t = 0; t < 8; ++t) *(f4_t*)(smem + w * B_YCH + t * 1280 + kg * 320 + c * 16) = acc[t] * invs;
  }
  asm volatile("s_waitcnt lgkmcnt(0)" ::: "memory");
  bar();
#pragma unroll
  for (int it = 0; it < 2; ++it) {
    const int j = tid + 512 * it, t = 2 * j;
    const int yo = (t >> 8) * 1280 + ((t >> 2) & 3) * 320 + ((t >> 4) & 15) * 16 + (t & 3) * 4;
    float h0[4], h1[4];
#pragma unroll
    for (int cc = 0; cc < 4; ++cc) {
      f2_t y = *(const f2_t*)(smem + cc * B_YCH + yo);
      h0[cc] = z[it][cc] + y.x;
      h1[cc] = z[it][4 + cc] + y.y;
    }
    char* dst = ws + OFF_H8 + ((size_t)(q >> 1) * SL + t) * 16 + (q & 1) * 8;
    st8(dst, u2_t{pk_bf16(h0[0], h0[1]), pk_bf16(h0[2], h0[3])});
    st8(dst + 16, u2_t{pk_bf16(h1[0], h1[1]), pk_bf16(h1[2], h1[3])});
  }
  if (w == 0) STAMP(1, 7);
}

constexpr int C_T = 240;
constexpr int C_SLOT = 23552, C_NSLOT = 6, C_GOFF = 15360;
constexpr int C_STAT = C_NSLOT * C_SLOT;
constexpr int C_CS = C_STAT + 960;
constexpr int C_LDS = C_CS + 512;
constexpr int C_STEPS = 20;
constexpr size_t OFF_DUMMY = 0x1140000;

__device__ __forceinline__ unsigned ln_pack(float sum, float sq) {
  const float mean = sum * (1.0f / DM);
  return ln_fin(mean, fmaxf(sq * (1.0f / DM) - mean * mean, 0.f));
}
__device__ __forceinline__ unsigned ln_row(const char* h8, int t) {
  float s = 0.f, q = 0.f;
  for (int pl = 0; pl < 80; ++pl) {
    const u4_t v = *(const u4_t*)(h8 + (size_t)pl * 32768 + t * 16);
    for (int j = 0; j < 4; ++j) { const float a = bf_lo(v[j]), b = bf_hi(v[j]); s += a + b; q += a * a + b * b; }
  }
  return ln_pack(s, q);
}

template <int NDMA>
__device__ __forceinline__ void c_loader(char* smem, char* ws, const char* wbase, int w4, int lane, int t0, int jline, int xcd) {
  const unsigned lds0 = (unsigned)(size_t)(LAS char*)smem;
  const char* h8 = ws + OFF_H8;
  unsigned voffH[4], voffW[2];
#pragma unroll
  for (int k = 0; k < 2; ++k) {
    const int r = 8 * (2 * (w4 & 1) + k) + (lane >> 3), c = lane & 7;
    const int f = ((r >> 1) & 1) | ((r >> 2) & 2);
    voffW[k] = r * (FF * 2) + 128 * jline + (((c >> 1) ^ f) * 32) + (c & 1) * 16;
  }
#pragma unroll
  for (int k = 0; k < 4; ++k) {
    const int idx = 64 * (w4 + 4 * k) + lane, pl = idx / C_T, tt = idx % C_T;
    const int t = min(t0 + tt, SL - 1);
    voffH[k] = pl * 32768 + t * 16;
  }
  const unsigned ldsW = C_GOFF + (w4 >> 1) * 4096 + (w4 & 1) * 2048;
  auto issue = [&](int step, unsigned so) {
#pragma unroll
    for (int k = 0; k < 2; ++k) glds16(wbase + (size_t)step * (32 * FF * 2), voffW[k], lds0 + so + ldsW + k * 1024);
#pragma unroll
    for (int k = 0; k < NDMA - 2; ++k) glds16(h8 + (size_t)step * (4 * 32768), voffH[k], lds0 + so + (w4 + 4 * k) * 1024);
  };
  constexpr unsigned S = C_SLOT;
  issue(0, 0); issue(1, S); issue(2, 2 * S); issue(3, 3 * S);
  wait_vm<3 * NDMA>(); bar();
  issue(4, 4 * S);
  wait_vm<2 * NDMA>(); bar();
  issue(5, 5 * S); issue(6, 0);
  unsigned sw = S;
#pragma unroll 1
  for (int j = 1; j < 7; ++j) {
    wait_vm<2 * NDMA>();
    bar();
    issue(2 * j + 5, sw);
    sw += S; if (sw == C_NSLOT * S) sw = 0;
    issue(2 * j + 6, sw);
    sw += S; if (sw == C_NSLOT * S) sw = 0;
  }
  wait_vm<2 * NDMA>(); bar();
  issue(19, sw);
  const int tl = min(60 * w4 + lane, C_T - 1), tg = min(t0 + tl, SL - 1);
  const gu64* gp = (const gu64*)(ws + OFF_G2) + tg;
  const float* csp = (const float*)(ws + OFF_CS) + ((lane >> 4) & 1) * FF + 64 * jline + 16 * w4 + (lane & 15);
  const char* tb = ws + OFF_WP2 + (size_t)(4 * (xcd & 1)) * (54 * 5120);
  const int tl0 = ((int)(blockIdx.x >> 3) * 4 + w4) * 68;
  u2_t gv; float csv; unsigned tv0, tv1;
  asm volatile("global_load_dwordx2 %0, %1, off sc1" : "=v"(gv) : "v"(gp) : "memory");
  asm volatile("global_load_dword %0, %1, off" : "=v"(csv) : "v"(csp) : "memory");
  asm volatile("global_load_dword %0, %1, off" : "=v"(tv0) : "v"(tb + (size_t)min(tl0 + lane, 8639) * 128) : "memory");
  asm volatile("global_load_dword %0, %1, off" : "=v"(tv1) : "v"(tb + (size_t)min(tl0 + 64 + (lane & 3), 8639) * 128) : "memory");
  wait_vm<NDMA + 4>(); bar();
  asm volatile("s_waitcnt vmcnt(0)" : "+v"(gv), "+v"(csv), "+v"(tv0), "+v"(tv1)::"memory");
  bar();
  {
    bool ok = gv.y == 1u;
    for (int spin = 0; spin < 4096 && !__all(ok); ++spin) {
      const unsigned long long g = __hip_atomic_load(gp, __ATOMIC_RELAXED, __HIP_MEMORY_SCOPE_AGENT);
      gv.x = (unsigned)g; ok = (unsigned)(g >> 32) == 1u;
      __builtin_amdgcn_s_sleep(2);
    }
    if (!__all(ok)) gv.x = ln_row(h8, tg);
    if (lane < 60) *(float*)(smem + C_STAT + (60 * w4 + lane) * 4) = bf_lo(gv.x);
    if (lane < 32) *(float*)(smem + C_CS + (lane >> 4) * 256 + (16 * w4 + (lane & 15)) * 4) = csv;
  }
  asm volatile("s_waitcnt lgkmcnt(0)" ::: "memory");
  bar();
}

struct CFrag { b8_t A[2][2]; b8_t B[8]; };

extern "C" __global__ __launch_bounds__(512, 2) void kC(const u16* __restrict__ wg, const u16* __restrict__ wu, char* __restrict__ ws) {
  extern __shared__ __attribute__((aligned(16))) char smem[];
  const int tid = threadIdx.x, lane = tid & 63;
  const int w = __builtin_amdgcn_readfirstlane(tid >> 6);
  int panel, jline;
  const int xcd = blockIdx.x & 7, rank = blockIdx.x >> 3;
  {
    const int g4 = xcd >> 1, a = xcd & 1, nl = (g4 < 3) ? 7 : 6, total = 9 * nl, n0 = (total + 1) >> 1;
    if (a == 0 ? rank >= n0 : rank >= total - n0) {
      const int sp = (xcd < 6) ? (xcd >> 1) : 3 + 5 * (xcd - 6) + (rank - 27);
      const int wv = sp * 8 + w;
      if (w == 0) STAMP(2, 0);
      for (int tile = wv; tile < 128; tile += 104) {
        const int t = 16 * tile + (lane & 15);
        const char* hp = ws + OFF_H8 + (size_t)(lane >> 4) * 32768 + t * 16;
        u4_t v[20];
#pragma unroll
        for (int i = 0; i < 20; ++i) v[i] = *(const u4_t*)(hp + (size_t)i * (4 * 32768));
        float s0 = 0.f, s1 = 0.f, q0 = 0.f, q1 = 0.f;
        unsigned k_both = 0x3f803f80u;
        asm volatile("" : "+s"(k_both));
#pragma unroll
        for (int i = 0; i < 20; ++i) {
          s0 = dot2bf(v[i][0], k_both, s0); q0 = dot2bf(v[i][0], v[i][0], q0);
          s1 = dot2bf(v[i][1], k_both, s1); q1 = dot2bf(v[i][1], v[i][1], q1);
          s0 = dot2bf(v[i][2], k_both, s0); q0 = dot2bf(v[i][2], v[i][2], q0);
          s1 = dot2bf(v[i][3], k_both, s1); q1 = dot2bf(v[i][3], v[i][3], q1);
        }
        float s = s0 + s1, q = q0 + q1;
        s += __shfl_xor(s, 16); q += __shfl_xor(q, 16);
        s += __shfl_xor(s, 32); q += __shfl_xor(q, 32);
        if (lane < 16)
          __hip_atomic_store((gu64*)(ws + OFF_G2) + t, (1ull << 32) | ln_pack(s, q), __ATOMIC_RELAXED, __HIP_MEMORY_SCOPE_AGENT);
      }
      if (w == 0) STAMP(2, 7);
      return;
    }
    const int idx = rank + a * n0;
    panel = idx / nl; jline = 7 * g4 + idx % nl;
  }
  const int t0 = panel * C_T;
  if (w == 0) STAMP(2, 0);
  if (w >= 4) {
    const int w4 = w - 4;
    const char* wbase = (const char*)((w4 >> 1) ? wu : wg);
    if (w4 < 3) c_loader<6>(smem, ws, wbase, w4, lane, t0, jline, xcd);
    else c_loader<5>(smem, ws, wbase, w4, lane, t0, jline, xcd);
    return;
  }
  const int s = w >> 1, th = w & 1;
  const int fr = lane & 15, kg = lane >> 4, q = (lane & 15) >> 2, p = lane & 3;
  const unsigned hb0 = kg * 3840 + fr * 16 + 256 * 7;
  const unsigned hb1 = kg * 3840 + fr * 16 + 256 * (th ? 8 : 0);
  unsigned wa[2];
#pragma unroll
  for (int v = 0; v < 2; ++v) {
    const int sub = 2 * s + (v ? 1 - th : th);
    const int f = (q >> 1) | (2 * (kg & 1));
    wa[v] = C_GOFF + (8 * kg + q) * 128 + ((sub ^ f) * 32) + 8 * p;
  }
  f4_t ag[8][2], au[8][2];
#pragma unroll
  for (int u = 0; u < 8; ++u)
#pragma unroll
    for (int v = 0; v < 2; ++v) { ag[u][v] = f4_t{0.f, 0.f, 0.f, 0.f}; au[u][v] = f4_t{0.f, 0.f, 0.f, 0.f}; }
  auto load = [&](CFrag& F, unsigned so) {
    const char* sp = smem + so;
#pragma unroll
    for (int v = 0; v < 2; ++v)
#pragma unroll
      for (int m = 0; m < 2; ++m) {
        s4_t lo = tr_read(sp + wa[v] + m * 4096), hi = tr_read(sp + wa[v] + m * 4096 + 512);
        u2_t a = __builtin_bit_cast(u2_t, lo), b = __builtin_bit_cast(u2_t, hi);
        F.A[v][m] = __builtin_bit_cast(b8_t, (u4_t{a.x, a.y, b.x, b.y}));
      }
    F.B[0] = *(const b8_t*)(sp + hb0);
#pragma unroll
    for (int u = 1; u < 8; ++u) F.B[u] = *(const b8_t*)(sp + hb1 + 256 * (u - 1));
  };
  auto mma = [&](const CFrag& F) {
    ag[0][0] = __builtin_amdgcn_mfma_f32_16x16x32_bf16(F.A[0][0], F.B[0], ag[0][0], 0, 0, 0);
    au[0][0] = __builtin_amdgcn_mfma_f32_16x16x32_bf16(F.A[0][1], F.B[0], au[0][0], 0, 0, 0);
#pragma unroll
    for (int u = 1; u < 8; ++u)
#pragma unroll
      for (int v = 0; v < 2; ++v) {
        ag[u][v] = __builtin_amdgcn_mfma_f32_16x16x32_bf16(F.A[v][0], F.B[u], ag[u][v], 0, 0, 0);
        au[u][v] = __builtin_amdgcn_mfma_f32_16x16x32_bf16(F.A[v][1], F.B[u], au[u][v], 0, 0, 0);
      }
  };
  auto interleave = [&]() {
#pragma unroll
    for (int i = 0; i < 16; ++i) {
      __builtin_amdgcn_sched_group_barrier(0x008, 1, 0);
      __builtin_amdgcn_sched_group_barrier(0x100, 1, 0);
    }
    __builtin_amdgcn_sched_group_barrier(0x008, 14, 0);
  };
  CFrag F0, F1;
  bar();
  if (w == 0) STAMP(2, 1);
  load(F0, 0);
  unsigned sn = C_SLOT;
  SB;
#pragma unroll 1
  for (int j = 0; j < 9; ++j) {
    asm volatile("s_waitcnt lgkmcnt(0)" ::: "memory");
    bar(); SB;
    load(F1, sn);
    mma(F0);
    interleave();
    SB;
    sn += C_SLOT; if (sn == C_NSLOT * C_SLOT) sn = 0;
    load(F0, sn);
    mma(F1);
    interleave();
    SB;
    sn += C_SLOT; if (sn == C_NSLOT * C_SLOT) sn = 0;
  }
  asm volatile("s_waitcnt lgkmcnt(0)" ::: "memory");
  bar(); SB;
  load(F1, sn);
  mma(F0);
  interleave();
  SB;
  bar(); SB;
  f4_t csg[2], csu[2];
  float mu[8];
#pragma unroll
  for (int v = 0; v < 2; ++v) {
    const int sub = 2 * s + (v ? 1 - th : th);
    csg[v] = *(const f4_t*)(smem + C_CS + (16 * sub + 4 * kg) * 4);
    csu[v] = *(const f4_t*)(smem + C_CS + 256 + (16 * sub + 4 * kg) * 4);
  }
#pragma unroll
  for (int u = 0; u < 8; ++u) {
    const int tile = (u == 0) ? 7 : (th ? 7 + u : u - 1);
    mu[u] = *(const float*)(smem + C_STAT + (16 * tile + fr) * 4);
  }
  mma(F1);
  if (w == 0) STAMP(2, 2);
  char* hh = ws + OFF_HH + (size_t)(2 * jline + s) * 1024 + lane * 16;
  char* sink = ws + OFF_DUMMY + lane * 16;
  auto epi = [&](auto thc) {
    constexpr int TH = decltype(thc)::value;
#pragma unroll
    for (int u = 0; u < 8; ++u) {
      const int tile = (u == 0) ? 7 : (TH ? 7 + u : u - 1);
      const int t16 = 15 * panel + tile;
      char* dst = (t16 < 128) ? hh + (size_t)t16 * (54 * 1024) : sink;
      u2_t o[2];
#pragma unroll
      for (int v = 0; v < 2; ++v) {
        if (u == 0 && v == 1) continue;
        const f4_t r = (ag[u][v] - mu[u] * csg[v]) * (au[u][v] - mu[u] * csu[v]);
        o[v] = u2_t{pk_bf16(r[0], r[1]), pk_bf16(r[2], r[3])};
      }
      if (u == 0) st8(dst + 8 * TH, o[0]);
      else st16(dst, u4_t{o[TH].x, o[TH].y, o[1 - TH].x, o[1 - TH].y});
    }
  };
  if (th) epi(IC(1)); else epi(IC(0));
  if (w == 0) STAMP(2, 7);
}

constexpr int D_SLOT = 15360, D_NSLOT = 9, D_STEPS = 18;
constexpr int D_LDS = D_NSLOT * D_SLOT;

template <int NDMA>
__device__ __forceinline__ void d_loader(char* smem, const char* wp, int w4, int lane) {
  const unsigned lds0 = (unsigned)(size_t)(LAS char*)smem;
  const unsigned voff = lane * 16;
  auto issue = [&](int step, unsigned so) {
#pragma unroll
    for (int k = 0; k < NDMA; ++k) {
      const int i = w4 + 4 * k;
      glds16(wp + (size_t)step * D_SLOT + i * 1024, voff, lds0 + so + i * 1024);
    }
  };
  constexpr unsigned S = D_SLOT;
  issue(0, 0); issue(1, S); issue(2, 2 * S);
  wait_vm<2 * NDMA>(); bar();
  issue(3, 3 * S); issue(4, 4 * S); issue(5, 5 * S);
  wait_vm<3 * NDMA>(); bar();
  issue(6, 6 * S); issue(7, 7 * S); issue(8, 8 * S);
  unsigned sw = 0;
#pragma unroll 1
  for (int j = 1; j < 5; ++j) {
    wait_vm<4 * NDMA>();
    bar();
    issue(2 * j + 7, sw);
    sw += S; if (sw == D_NSLOT * S) sw = 0;
    issue(2 * j + 8, sw);
    sw += S; if (sw == D_NSLOT * S) sw = 0;
  }
  wait_vm<4 * NDMA>(); bar();
  issue(17, sw);
  wait_vm<3 * NDMA>(); bar();
  wait_vm<1 * NDMA>(); bar();
  wait_vm<0>(); bar();
}

extern "C" __global__ __launch_bounds__(512, 2) void kD(char* __restrict__ ws, u16* __restrict__ out) {
  extern __shared__ __attribute__((aligned(16))) char smem[];
  const int tid = threadIdx.x, lane = tid & 63;
  const int w = __builtin_amdgcn_readfirstlane(tid >> 6);
  const int xcd = blockIdx.x & 7, rank = blockIdx.x >> 3;
  const int it = 8 * (xcd >> 1) + (rank & 7), dg = 4 * (xcd & 1) + (rank >> 3);
  if (w == 0) STAMP(3, 0);
  if (w >= 4) {
    const int w4 = w - 4;
    const char* wp = ws + OFF_WP2 + (size_t)dg * (54 * 5120);
    if (w4 < 3) d_loader<4>(smem, wp, w4, lane);
    else d_loader<3>(smem, wp, w4, lane);
    return;
  }
  const int fr = lane & 15, g = lane >> 4;
  const int t = 64 * it + 16 * w + fr;
  const char* hp = ws + OFF_HH + (size_t)(4 * it + w) * (54 * 1024);
  const unsigned voff = lane * 16;
  u4_t R[18];
#pragma unroll
  for (int i = 0; i < 9; ++i)
    asm volatile("global_load_dwordx4 %0, %1, %2" : "=v"(R[i]) : "v"(voff), "s"(hp + (size_t)i * 1024) : "memory");
  u2_t res[5]; unsigned st;
  f4_t acc[5];
#pragma unroll
  for (int dt = 0; dt < 5; ++dt) acc[dt] = f4_t{0.f, 0.f, 0.f, 0.f};
  b8_t A0[5], A1[5];
  auto rdA = [&](b8_t (&A)[5], unsigned off) {
    const char* sp = smem + off + lane * 16;
#pragma unroll
    for (int dt = 0; dt < 5; ++dt) A[dt] = *(const b8_t*)(sp + dt * 1024);
  };
  bar();
  if (w == 0) STAMP(3, 1);
  rdA(A0, 0);
  unsigned so = 0;
  auto kstep = [&](auto mode, auto idx, b8_t (&Ac)[5], b8_t (&An)[5], const char* hnext) {
    constexpr int MODE = decltype(mode)::value, i = decltype(idx)::value, kk = i % 3;
    if (i % 6 == 0) bar();
    unsigned nx = so + (kk + 1) * 5120;
    if (kk == 2) { so += D_SLOT; if (so == D_NSLOT * D_SLOT) so = 0; nx = so; }
    SB;
    if (!(MODE == 2 && i == 17)) rdA(An, nx);
    constexpr int NV = (MODE == 0) ? (i < 9 ? 8 + i : 26 - i) : (MODE == 1) ? (i < 9 ? 25 - i : 17) : (i == 0 ? 17 : 23 - i);
    asm volatile("s_waitcnt vmcnt(%1)" : "+v"(R[i]) : "i"(NV) : "memory");
    const b8_t B = __builtin_bit_cast(b8_t, R[i]);
#pragma unroll
    for (int dt = 0; dt < 5; ++dt) acc[dt] = __builtin_amdgcn_mfma_f32_16x16x32_bf16(Ac[dt], B, acc[dt], 0, 0, 0);
    SB;
    if (MODE == 0 && i < 9)
      asm volatile("global_load_dwordx4 %0, %1, %2" : "=v"(R[i < 9 ? 9 + i : 0]) : "v"(voff), "s"(hp + (size_t)(9 + i) * 1024) : "memory");
    if (MODE != 2) asm volatile("global_load_dwordx4 %0, %1, %2" : "+v"(R[i]) : "v"(voff), "s"(hnext + (size_t)i * 1024) : "memory");
    if (MODE == 2 && i == 0) {
#pragma unroll
      for (int dt = 0; dt < 5; ++dt) {
        const int d = 80 * dg + 16 * dt + 4 * g;
        const char* rp = ws + OFF_H8 + ((size_t)(d >> 3) * SL + t) * 16 + (d & 4) * 2;
        asm volatile("global_load_dwordx2 %0, %1, off" : "=v"(res[dt]) : "v"(rp) : "memory");
      }
      asm volatile("global_load_dword %0, %1, off" : "=v"(st) : "v"(ws + OFF_G2 + (size_t)t * 8) : "memory");
    }
    SB;
  };
  auto round = [&](auto mode, const char* hnext) {
    kstep(mode, IC(0), A0, A1, hnext); kstep(mode, IC(1), A1, A0, hnext); kstep(mode, IC(2), A0, A1, hnext);
    kstep(mode, IC(3), A1, A0, hnext); kstep(mode, IC(4), A0, A1, hnext); kstep(mode, IC(5), A1, A0, hnext);
    kstep(mode, IC(6), A0, A1, hnext); kstep(mode, IC(7), A1, A0, hnext); kstep(mode, IC(8), A0, A1, hnext);
    kstep(mode, IC(9), A1, A0, hnext); kstep(mode, IC(10), A0, A1, hnext); kstep(mode, IC(11), A1, A0, hnext);
    kstep(mode, IC(12), A0, A1, hnext); kstep(mode, IC(13), A1, A0, hnext); kstep(mode, IC(14), A0, A1, hnext);
    kstep(mode, IC(15), A1, A0, hnext); kstep(mode, IC(16), A0, A1, hnext); kstep(mode, IC(17), A1, A0, hnext);
  };
  round(IC(0), hp + 18 * 1024);
  round(IC(1), hp + 36 * 1024);
  round(IC(2), hp);
  if (w == 0) STAMP(3, 2);
  asm volatile("s_waitcnt vmcnt(0)" : "+v"(res[0]), "+v"(res[1]), "+v"(res[2]), "+v"(res[3]), "+v"(res[4]), "+v"(st)::"memory");
  const float sd = bf_hi(st), r2 = 1.0f / (sd * sd);
#pragma unroll
  for (int dt = 0; dt < 5; ++dt) {
    const int d = 80 * dg + 16 * dt + 4 * g;
    const float h0 = bf_lo(res[dt].x), h1 = bf_hi(res[dt].x), h2 = bf_lo(res[dt].y), h3 = bf_hi(res[dt].y);
    st8(out + (size_t)t * DM + d, u2_t{pk_bf16(h0 + r2 * acc[dt][0], h1 + r2 * acc[dt][1]), pk_bf16(h2 + r2 * acc[dt][2], h3 + r2 * acc[dt][3])});
  }
  if (w == 0) STAMP(3, 7);
}

extern "C" __global__ void kE(char* ws) {
  const unsigned long long t = __builtin_amdgcn_s_memrealtime();
  if (threadIdx.x == 0) ((unsigned long long*)(ws + OFF_DBG))[0] = t;
}
extern "C" __global__ void kS(char* ws) {
  const unsigned long long* h = (const unsigned long long*)(ws + OFF_DBG);
  const int lane = threadIdx.x;
  unsigned long long a = ~0ull, a255 = 0;
  for (int blk = lane; blk < 256; blk += 64) { const unsigned long long s0 = h[(1 * 512 + blk) * 16]; a = s0 < a ? s0 : a; a255 = s0 > a255 ? s0 : a255; }
  for (int o = 1; o < 64; o <<= 1) { unsigned long long a2 = __shfl_xor(a, o), b2 = __shfl_xor(a255, o); a = a2 < a ? a2 : a; a255 = b2 > a255 ? b2 : a255; }
  const unsigned long long e0 = h[0];
  long long q = ((a ^ e0 ^ a255) == 0x1234567ull) ? 1 : 0;
  if (PROBE == 20) q += a - e0;
  if (PROBE == 21) q += a255 - a;
  if (q < 0 || q > 5000) q = 5000;
  const unsigned long long t0 = __builtin_amdgcn_s_memrealtime();
  while ((long long)(__builtin_amdgcn_s_memrealtime() - t0) < q * PROBE_M) __builtin_amdgcn_s_sleep(1);
}

extern "C" void kernel_launch(void* const* d_in, const int* in_sizes, int n_in, void* d_out, int out_size, void* d_ws,
                              size_t ws_size, hipStream_t stream) {
  char* ws = (char*)d_ws;
  (void)hipFuncSetAttribute((const void*)kB, hipFuncAttributeMaxDynamicSharedMemorySize, B_LDS);
  (void)hipFuncSetAttribute((const void*)kC, hipFuncAttributeMaxDynamicSharedMemorySize, C_LDS);
  (void)hipFuncSetAttribute((const void*)kD, hipFuncAttributeMaxDynamicSharedMemorySize, D_LDS);
  kE<<<1, 64, 0, stream>>>(ws);
  kB<<<256, 512, B_LDS, stream>>>((const u16*)d_in[0], (const u16*)d_in[1], (const u16*)d_in[2], (const u16*)d_in[3], (const u16*)d_in[4],
                                  (const u16*)d_in[5], (const u16*)d_in[6], (const u16*)d_in[7], (const u16*)d_in[8], (const u16*)d_in[10],
                                  (const u16*)d_in[11], (const u16*)d_in[12], (const u16*)d_out, ws);
  kC<<<256, 512, C_LDS, stream>>>((const u16*)d_in[10], (const u16*)d_in[11], ws);
  kD<<<256, 512, D_LDS, stream>>>(ws, (u16*)d_out);
  kS<<<1, 64, 0, stream>>>(ws);
}
```

```cpp
#include <hip/hip_runtime.h>
#include <hip/hip_bf16.h>
#include <stdint.h>
#include <type_traits>

typedef unsigned short u16;
typedef _Float16 h2_t __attribute__((ext_vector_type(2)));
typedef _Float16 h8_t __attribute__((ext_vector_type(8)));
typedef __bf16 b2_t __attribute__((ext_vector_type(2)));
typedef __bf16 b8_t __attribute__((ext_vector_type(8)));
typedef float f2_t __attribute__((ext_vector_type(2)));
typedef float f4_t __attribute__((ext_vector_type(4)));
typedef unsigned u2_t __attribute__((ext_vector_type(2)));
typedef unsigned u4_t __attribute__((ext_vector_type(4)));
typedef short s4_t __attribute__((ext_vector_type(4)));
typedef __attribute__((address_space(1))) unsigned long long gu64;
#define LAS __attribute__((address_space(3)))
#define SB __builtin_amdgcn_sched_barrier(0)
#define IC(n) std::integral_constant<int, n>{}

constexpr int SL = 2048;
constexpr int DM = 640;
constexpr int FF = 1728;

constexpr size_t OFF_G1 = 0;
constexpr size_t OFF_EP = 0x10000;
constexpr size_t OFF_RD = 0x10100;
constexpr size_t OFF_H8 = 0x5A0000;
constexpr size_t OFF_HH = 0x820000;
constexpr size_t OFF_WP2 = 0xF00000;
constexpr size_t OFF_CS = 0x1120000;
constexpr size_t OFF_G2 = 0x1130000;
constexpr int KRS = 1344;
constexpr int KDS = 4864;
constexpr int KD_O = 2240;
constexpr int KD_DR = 4384;

#define STAMP(k, i)

__device__ __forceinline__ float bf_lo(unsigned d) { return __uint_as_float(d << 16); }
__device__ __forceinline__ float bf_hi(unsigned d) { return __uint_as_float(d & 0xffff0000u); }
__device__ __forceinline__ float bf2f(u16 v) { return __uint_as_float((unsigned)v << 16); }
__device__ __forceinline__ unsigned pk_bf16(float a, float b) {
  f2_t v = {a, b};
  return __builtin_bit_cast(unsigned, __builtin_convertvector(v, b2_t));
}
__device__ __forceinline__ unsigned pk_f16(float a, float b) {
  f2_t v = {a, b};
  return __builtin_bit_cast(unsigned, __builtin_convertvector(v, h2_t));
}
__device__ __forceinline__ float rbf(float a) { return bf_lo(pk_bf16(a, 0.f)); }
template <int N> __device__ __forceinline__ float dpp_ror_add(float v) {
  int r = __builtin_amdgcn_update_dpp(0, __float_as_int(v), 0x120 + N, 0xf, 0xf, false);
  return v + __int_as_float(r);
}
__device__ __forceinline__ float row16_sum(float v) {
  v = dpp_ror_add<8>(v); v = dpp_ror_add<4>(v); v = dpp_ror_add<2>(v); v = dpp_ror_add<1>(v);
  return v;
}
__device__ __forceinline__ float wave_sum(float v) {
  v = row16_sum(v);
  const int b = __float_as_int(v);
  return (__int_as_float(__builtin_amdgcn_readlane(b, 0)) + __int_as_float(__builtin_amdgcn_readlane(b, 16))) +
         (__int_as_float(__builtin_amdgcn_readlane(b, 32)) + __int_as_float(__builtin_amdgcn_readlane(b, 48)));
}
template <typename T> __device__ __forceinline__ T ldnt(const T* p) { return __builtin_nontemporal_load(p); }
__device__ __forceinline__ void wave_fence() {
  __builtin_amdgcn_fence(__ATOMIC_RELEASE, "wavefront");
  __builtin_amdgcn_wave_barrier();
  __builtin_amdgcn_fence(__ATOMIC_ACQUIRE, "wavefront");
}
__device__ __forceinline__ s4_t tr_read(const char* p) {
  return __builtin_amdgcn_ds_read_tr16_b64_v4i16((LAS s4_t*)p);
}
__device__ __forceinline__ void glds16(const void* sbase, unsigned voff, unsigned lds_addr) {
  asm volatile("s_mov_b32 m0, %2\n\ts_nop 0\n\tglobal_load_lds_dwordx4 %0, %1" ::"v"(voff), "s"(sbase), "s"(lds_addr) : "memory");
}
template <int N> __device__ __forceinline__ void wait_vm() { asm volatile("s_waitcnt vmcnt(%0)" ::"i"(N) : "memory"); }
__device__ __forceinline__ void bar() { asm volatile("s_barrier" ::: "memory"); }
__device__ __forceinline__ float dot2bf(unsigned a, unsigned b, float c) {
  return __builtin_amdgcn_fdot2_f32_bf16(__builtin_bit_cast(b2_t, a), __builtin_bit_cast(b2_t, b), c, false);
}

#define ST_MODE 0
__device__ __forceinline__ void st16(void* p, u4_t v) {
  *(u4_t*)p = v;
}
__device__ __forceinline__ void st8(void* p, u2_t v) {
  *(u2_t*)p = v;
}

struct __attribute__((aligned(128))) Ptrs { char* ws; const u16* wg; const u16* wu; u16* out; void* pad[12]; };
__device__ Ptrs g_ptrs[8];
typedef const __attribute__((address_space(4))) Ptrs* cptrs_t;
__device__ __forceinline__ cptrs_t ptrs() { return (cptrs_t)(unsigned long long)&g_ptrs[blockIdx.x & 7]; }

struct Cw { f2_t w, ws; };
__device__ __forceinline__ Cw mkw(f2_t w) { Cw r; r.w = w; r.ws = f2_t{-w.y, w.x}; return r; }
__device__ __forceinline__ f2_t cmulw(f2_t a, const Cw& W) {
  return __builtin_elementwise_fma(f2_t{a.y, a.y}, W.ws, f2_t{a.x, a.x} * W.w);
}
__device__ __forceinline__ f2_t cexp_l(float er, float wr, float l) {
  float mag = __builtin_amdgcn_exp2f(er * l);
  float rev = __builtin_amdgcn_fractf(wr * l);
  return f2_t{mag * __builtin_amdgcn_cosf(rev), mag * __builtin_amdgcn_sinf(rev)};
}
__device__ __forceinline__ float pow2_scale(float bound, int target) {
  unsigned e = (__float_as_uint(bound) >> 23) & 0xff;
  int k = target - ((int)e - 126);
  k = k > 126 ? 126 : (k < -126 ? -126 : k);
  return (bound > 0.f) ? __uint_as_float((unsigned)(k + 127) << 23) : 1.0f;
}
__device__ __forceinline__ float pow2_inv(float p) { return __uint_as_float(0x7f000000u - __float_as_uint(p)); }

constexpr int B_SIDE = 96;
constexpr int KG_R = 0, KG_L0 = 4352, KG_L1 = 8704, KG_W = 8704 + 25 * 272;
constexpr int B_BND = 8 * KG_W;
constexpr int B_KC = B_BND + 64;
constexpr int B_LDS = B_KC + 4 * KRS;
constexpr int B_KOFF = 0;
constexpr int B_UOFF = 4 * KDS;
constexpr int B_UCH = 4608;
constexpr int B_YCH = 10240;
constexpr int B_PACK = 40960;

__device__ __forceinline__ void pack_wd(const u16* __restrict__ wd, char* __restrict__ ws, char* buf, int u, int lane) {
  const int ks = u >> 3, dg = u & 7;
#pragma unroll
  for (int i = 0; i < 5; ++i) {
    const int idx = i * 64 + lane, row = idx / 10, c = idx % 10;
    __builtin_amdgcn_global_load_lds((const unsigned*)((const char*)wd + (size_t)(32 * ks + row) * 1280 + 160 * dg + 16 * c),
                                     (LAS unsigned*)(buf + i * 1024), 16, 0, 2);
  }
  asm volatile("s_waitcnt vmcnt(0)" ::: "memory");
  wave_fence();
  const int g = lane >> 4, q = (lane & 15) >> 2, p = lane & 3;
  char* dst = ws + OFF_WP2 + ((size_t)(dg * 54 + ks) * 5) * 1024 + lane * 16;
#pragma unroll
  for (int dt = 0; dt < 5; ++dt) {
    s4_t aa = tr_read(buf + (4 * g + q) * 160 + 32 * dt + 8 * p);
    s4_t bb = tr_read(buf + (16 + 4 * g + q) * 160 + 32 * dt + 8 * p);
    u2_t ua = __builtin_bit_cast(u2_t, aa), ub = __builtin_bit_cast(u2_t, bb);
    const u4_t pv = {ua.x, ua.y, ub.x, ub.y};
    asm volatile("global_store_dwordx4 %0, %1, off sc1\n\ts_nop 1" ::"v"(dst + dt * 1024), "v"(pv) : "memory");
  }
}

struct BFrag { h8_t B[8]; u2_t A[5][2]; };
__device__ __forceinline__ void mfma_f16(f4_t& d, u4_t a, h8_t b) {
  asm volatile("v_mfma_f32_16x16x32_f16 %0, %1, %2, %0" : "+v"(d) : "v"(a), "v"(b));
}
__device__ __forceinline__ void mfma_f16_0(f4_t& d, u4_t a, h8_t b) {
  asm volatile("v_mfma_f32_16x16x32_f16 %0, %1, %2, 0" : "=&v"(d) : "v"(a), "v"(b));
}
__device__ __forceinline__ unsigned ln_fin(float mean, float var) {
  const float mb = rbf(mean), sd = rbf(sqrtf(rbf(rbf(var) + 1e-5f)));
  return (__float_as_uint(mb) >> 16) | (__float_as_uint(sd) & 0xffff0000u);
}
__device__ __forceinline__ unsigned ln1_row(const u16* __restrict__ x, int t) {
  const u4_t* p = (const u4_t*)(x + (size_t)t * DM);
  float s = 0.f, q = 0.f;
  for (int i = 0; i < 80; ++i) { const u4_t v = p[i]; for (int j = 0; j < 4; ++j) s += bf_lo(v[j]) + bf_hi(v[j]); }
  const float mean = s * (1.0f / DM);
  for (int i = 0; i < 80; ++i) {
    const u4_t v = p[i];
    for (int j = 0; j < 4; ++j) { const float a = bf_lo(v[j]) - mean, b = bf_hi(v[j]) - mean; q += a * a; q += b * b; }
  }
  return ln_fin(mean, q * (1.0f / DM));
}
constexpr int SPIN_MAX = 1 << 14;

extern "C" __global__ __launch_bounds__(512) void kB(const u16* __restrict__ x, const u16* __restrict__ Are, const u16* __restrict__ Aim,
                                                     const u16* __restrict__ Bre, const u16* __restrict__ Bim, const u16* __restrict__ Cre,
                                                     const u16* __restrict__ Cim, const u16* __restrict__ logdt, const u16* __restrict__ dskip,
                                                     const u16* __restrict__ wg, const u16* __restrict__ wu, const u16* __restrict__ wd,
                                                     const u16* __restrict__ outp, char* __restrict__ ws) {
  extern __shared__ __attribute__((aligned(16))) char smem[];
  const int tid = threadIdx.x, lane = tid & 63;
  const int w = __builtin_amdgcn_readfirstlane(tid >> 6);
  const int xcd = blockIdx.x & 7;
#define EPLOAD(v) asm volatile("global_load_dwordx2 %0, %1, %2 sc1" : "=v"(v) : "v"(0u), "s"(ws + OFF_EP) : "memory")
  if (w == 0) STAMP(1, 0);
  auto post_epoch = [&](u2_t ep, int off) {
    if (tid == 0) {
      const unsigned long long e = (((unsigned long long)ep.y << 32) | ep.x) + 1ull;
      *(unsigned long long*)(smem + off) = e;
      *(unsigned*)(smem + off + 8) = (unsigned)((e * 0x9E3779B97F4A7C15ull) >> 32);
      __hip_atomic_store((gu64*)(ws + OFF_RD) + blockIdx.x, e, __ATOMIC_RELAXED, __HIP_MEMORY_SCOPE_AGENT);
    }
  };
  if (blockIdx.x < B_SIDE) {
    const int rk = blockIdx.x >> 3;
    const int g4 = xcd >> 1, a = xcd & 1, nl = (g4 < 3) ? 7 : 6;
    constexpr int S_EP = 17408;
    unsigned pf[2] = {0u, 0u};
    if (rk >= 8) {
#pragma unroll
      for (int k = 0; k < 2; ++k) {
        const int id = (((rk - 8) * 8 + w) * 2 + k) * 64 + lane;
        asm volatile("global_load_dword %0, %1, off" : "=v"(pf[k]) : "v"((const char*)x + (size_t)(id >> 1) * (DM * 2) + (((160 * xcd) >> 7) + (id & 1)) * 128) : "memory");
      }
    }
    if (rk < 8) {
      const int r = lane >> 4, c = lane & 15, t = (blockIdx.x * 8 + w) * 4 + r;
      const u4_t* p = (const u4_t*)(x + (size_t)t * DM);
      u2_t ep;
      EPLOAD(ep);
      u4_t v[5];
#pragma unroll
      for (int i = 0; i < 5; ++i) v[i] = ldnt(p + c + 16 * i);
      float s = 0.f;
#pragma unroll
      for (int i = 0; i < 5; ++i)
#pragma unroll
        for (int j = 0; j < 4; ++j) s += bf_lo(v[i][j]) + bf_hi(v[i][j]);
      asm volatile("" : "+v"(ep) : "v"(s));
      post_epoch(ep, S_EP);
      const float mean = row16_sum(s) * (1.0f / DM);
      float q = 0.f;
#pragma unroll
      for (int i = 0; i < 5; ++i)
#pragma unroll
        for (int j = 0; j < 4; ++j) {
          const float aa = bf_lo(v[i][j]) - mean, bb = bf_hi(v[i][j]) - mean;
          q += aa * aa; q += bb * bb;
        }
      const unsigned sv = ln_fin(mean, row16_sum(q) * (1.0f / DM));
      __syncthreads();
      const u2_t e = *(const u2_t*)(smem + S_EP);
      if (c == 0) {
        __hip_atomic_store((gu64*)(ws + OFF_G1) + t, ((unsigned long long)*(const unsigned*)(smem + S_EP + 8) << 32) | sv, __ATOMIC_RELAXED, __HIP_MEMORY_SCOPE_AGENT);
        ((unsigned long long*)(ws + OFF_G2))[t] = 0ull;
      }
      if (w == 0) STAMP(1, 1);
      if (rk == 0 && w == 7) {
        gu64* gp = (gu64*)(unsigned long long)&g_ptrs[xcd];
        if (lane < 4) {
          const unsigned long long v = lane == 0 ? (unsigned long long)ws : lane == 1 ? (unsigned long long)wg : lane == 2 ? (unsigned long long)wu : (unsigned long long)outp;
          __hip_atomic_store(gp + lane, v, __ATOMIC_RELAXED, __HIP_MEMORY_SCOPE_AGENT);
        }
        unsigned back;
        asm volatile("s_waitcnt vmcnt(0)\n\tglobal_load_dword %0, %1, off\n\ts_waitcnt vmcnt(0)" : "=&v"(back) : "v"(gp) : "memory");
        asm volatile("" ::"v"(back));
      }
      if (rk == 4 && w == 0) {
        unsigned long long a0, a1;
        asm volatile("s_getpc_b64 s[20:21]\n\ts_add_u32 s20, s20, kB@rel32@lo+4\n\ts_addc_u32 s21, s21, kB@rel32@hi+12\n\ts_mov_b64 %0, s[20:21]" : "=s"(a0) :: "s20", "s21", "scc");
        asm volatile("s_getpc_b64 s[20:21]\n\ts_add_u32 s20, s20, kD@rel32@lo+4\n\ts_addc_u32 s21, s21, kD@rel32@hi+12\n\ts_mov_b64 %0, s[20:21]" : "=s"(a1) :: "s20", "s21", "scc");
        const unsigned long long lo = a0 < a1 ? a0 : a1, last = ((a0 < a1 ? a1 - a0 : a0 - a1) + 8192) & ~127ull;
        unsigned cf[8];
#pragma unroll
        for (int i = 0; i < 8; ++i) {
          const unsigned long long o = (unsigned long long)(lane + 64 * i) * 128;
          asm volatile("global_load_dword %0, %1, off" : "=v"(cf[i]) : "v"(lo + (o < last ? o : last)) : "memory");
        }
        asm volatile("s_waitcnt vmcnt(0)" ::"v"(cf[0]), "v"(cf[1]), "v"(cf[2]), "v"(cf[3]), "v"(cf[4]), "v"(cf[5]), "v"(cf[6]), "v"(cf[7]) : "memory");
      }
      if (blockIdx.x == 63 && w == 7) {
        const unsigned long long e64 = ((unsigned long long)e.y << 32) | e.x;
        for (int spin = 0; spin < SPIN_MAX; ++spin) {
          bool ok = true;
#pragma unroll
          for (int i = 0; i < 4; ++i) ok &= __hip_atomic_load((const gu64*)(ws + OFF_RD) + lane + 64 * i, __ATOMIC_RELAXED, __HIP_MEMORY_SCOPE_AGENT) == e64;
          if (__all(ok)) break;
          __builtin_amdgcn_s_sleep(4);
        }
        if (lane == 0) __hip_atomic_store((gu64*)(ws + OFF_EP), e64, __ATOMIC_RELAXED, __HIP_MEMORY_SCOPE_AGENT);
      }
    }
    const int strip = (rk >= 8) ? rk - 8 : 4 + rk;
    if ((rk >= 8 || rk < 3) && strip < nl) {
      const int jl = 7 * g4 + strip;
      const char* pm = (const char*)(a ? wu : wg) + (size_t)(80 * w + (lane >> 3)) * (FF * 2) + 128 * jl + (lane & 7) * 16;
      u2_t ep;
      EPLOAD(ep);
      u4_t v[10];
#pragma unroll
      for (int i = 0; i < 10; ++i) v[i] = ldnt((const u4_t*)(pm + (size_t)i * 8 * (FF * 2)));
      const char* po = (const char*)(a ? wg : wu) + (size_t)(80 * w) * (FF * 2) + 128 * jl;
      unsigned t0v, t1v;
      asm volatile("global_load_dword %0, %1, off nt" : "=v"(t0v) : "v"(po + (size_t)lane * (FF * 2)) : "memory");
      asm volatile("global_load_dword %0, %1, off nt" : "=v"(t1v) : "v"(po + (size_t)(64 + (lane & 15)) * (FF * 2)) : "memory");
      f2_t s[4];
#pragma unroll
      for (int e = 0; e < 4; ++e) s[e] = f2_t{0.f, 0.f};
#pragma unroll
      for (int i = 0; i < 10; ++i)
#pragma unroll
        for (int jj = 0; jj < 4; ++jj) s[jj] += f2_t{bf_lo(v[i][jj]), bf_hi(v[i][jj])};
      asm volatile("" : "+v"(ep), "+v"(pf[0]), "+v"(pf[1]) : "v"(s[0]));
      if (rk >= 8) post_epoch(ep, S_EP);
      float* pp = (float*)smem;
      *(f4_t*)(pp + (w * 8 + (lane >> 3)) * 68 + (lane & 7) * 8) = f4_t{s[0].x, s[0].y, s[1].x, s[1].y};
      *(f4_t*)(pp + (w * 8 + (lane >> 3)) * 68 + (lane & 7) * 8 + 4) = f4_t{s[2].x, s[2].y, s[3].x, s[3].y};
      __syncthreads();
      float acc = 0.f;
#pragma unroll
      for (int i = 0; i < 8; ++i) acc += pp[(w * 8 + i) * 68 + lane];
      __syncthreads();
      pp[w * 64 + lane] = acc;
      __syncthreads();
      if (w == 0) {
        float t = 0.f;
#pragma unroll
        for (int i = 0; i < 8; ++i) t += pp[i * 64 + lane];
        ((float*)(ws + OFF_CS))[a * FF + 64 * jl + lane] = t;
      }
      asm volatile("s_waitcnt vmcnt(0)" ::"v"(t0v), "v"(t1v) : "memory");
    }
    if (w == 0) STAMP(1, 7);
    return;
  }
  const int cb = blockIdx.x - B_SIDE;
  const int q = 20 * xcd + (cb >> 3);
  const int cl = w >> 1, hw = w & 1, ch = 4 * q + cl;
  const int n = ch * 128 + hw * 64 + lane;
  u2_t ep;
  EPLOAD(ep);
  const u16 i_are = ldnt(Are + n), i_aim = ldnt(Aim + n), i_bre = ldnt(Bre + n), i_bim = ldnt(Bim + n), i_cre = ldnt(Cre + n), i_cim = ldnt(Cim + n);
  const u16 i_dt = logdt[ch];
  u2_t xg[4];
  const unsigned xoff = tid * (2 * DM * 2);
  const char* xb = (const char*)(x + 4 * q);
#define XG(i) asm volatile("global_load_dwordx2 %0, %1, %2 offset:%3" : "=v"(xg[i]) : "v"(xoff), "s"(xb + ((i) >> 1) * (size_t)(1024 * DM * 2)), "i"(((i) & 1) * DM * 2) : "memory")
  const u2_t dkv = *(const u2_t*)(dskip + 4 * q);
  const int c = lane & 15, kg = lane >> 4;
  h8_t band9, band7;
#pragma unroll
  for (int j = 0; j < 8; ++j) {
    const int k = 8 * kg + 7 - j;
    band9[j] = (k >= c + 4 && k <= c + 12) ? (_Float16)1.0f : (_Float16)0.0f;
    band7[j] = (k >= c + 5 && k <= c + 11) ? (_Float16)1.0f : (_Float16)0.0f;
  }
  asm volatile("" : "+v"(band9), "+v"(band7));
  char* hb = smem + w * KG_W;
  float Sw, bsum;
  {
    const float dt = rbf(expf(bf2f(i_dt)));
    const float are = dt * bf2f(i_are), aim = dt * bf2f(i_aim);
    const float bre = dt * bf2f(i_bre), bim = dt * bf2f(i_bim);
    const float cre = bf2f(i_cre), cim = bf2f(i_cim);
    f2_t c0 = {cre * bre - cim * bim, cre * bim + cim * bre};
    const float er = are * 1.44269504088896341f, wr = aim * 0.15915494309189535f;
    const float bnd = (fabsf(c0.x) + fabsf(c0.y)) * fmaxf(1.0f, __builtin_amdgcn_exp2f(er * 2048.f));
    bsum = wave_sum(bnd);
    asm volatile("" : "+v"(ep) : "v"(bnd));
    post_epoch(ep, B_BND + 48);
    if (lane == 0) ((float*)(smem + B_BND))[w] = bsum;
    if (w == 0) STAMP(1, 1);
    Sw = pow2_scale(bsum, 15);
    c0 *= Sw;
    const Cw z1 = mkw(cexp_l(er, wr, 1.f)), w8 = mkw(cexp_l(er, -wr, 8.f)), w128 = mkw(cexp_l(er, wr, 128.f));
    const Cw z2 = mkw(cmulw(z1.w, z1));
    const Cw z4 = mkw(cmulw(z2.w, z2));
    auto put = [&](int off, int row, f2_t v) { *(unsigned*)(hb + off + row * 272 + 4 * lane) = pk_f16(v.x, v.y); };
    f2_t R = w8.w, v0 = c0, v1 = cmulw(c0, w128), T = v1;
    *(unsigned*)(hb + KG_R + 4 * lane) = pk_f16(1.f, 0.f);
#pragma unroll
    for (int i = 0; i < 8; ++i) {
      SB;
      if ((w & 3) == (i & 3)) { if (i < 4) XG(0); else XG(1); }
      SB;
      *(unsigned*)(hb + KG_R + (2 * i + 1) * 272 + 4 * lane) = pk_f16(R.x, R.y);
      if (i < 7) {
        R = cmulw(R, w8);
        *(unsigned*)(hb + KG_R + (2 * i + 2) * 272 + 4 * lane) = pk_f16(R.x, R.y);
        R = cmulw(R, w8);
      }
      put(KG_L0, i, v0); put(KG_L0, 8 + i, v1);
      if (i < 7) { v0 = cmulw(v0, z1); v1 = cmulw(v1, z1); }
#pragma unroll
      for (int k = 0; k < 2; ++k) {
        const int i1 = 2 + 2 * i + k;
        if (i1 <= 16) {
          T = cmulw(T, w128);
          if (i1 < 4) {
            f2_t v = T;
#pragma unroll
            for (int i0 = 0; i0 < 4; ++i0) { put(KG_L1, 4 * (i1 - 2) + i0, v); if (i0 < 3) v = cmulw(v, z2); }
          } else if (i1 < 8) {
            put(KG_L1, 8 + 2 * (i1 - 4), T); put(KG_L1, 8 + 2 * (i1 - 4) + 1, cmulw(T, z4));
          } else {
            put(KG_L1, 16 + (i1 - 8), T);
          }
        }
      }
    }
  }
  SB;
  if (w == 0) STAMP(1, 4);
  u4_t gr[2];
  const unsigned goff = tid * 16;
#define GRL() do { asm volatile("global_load_dwordx4 %0, %1, %2 sc1" : "=v"(gr[0]) : "v"(goff), "s"(ws + OFF_G1) : "memory"); \
                   asm volatile("global_load_dwordx4 %0, %1, %2 sc1" : "=v"(gr[1]) : "v"(goff), "s"(ws + OFF_G1 + 8192) : "memory"); } while (0)
  GRL();
  if (w < 4) XG(2);
  wave_fence();
  f4_t ka[3];
  {
    h8_t Bf[4];
#pragma unroll
    for (int ks = 0; ks < 4; ++ks) Bf[ks] = *(const h8_t*)(hb + KG_R + c * 272 + 64 * ks + 16 * kg);
#pragma unroll
    for (int t = 0; t < 3; ++t) ka[t] = f4_t{0.f, 0.f, 0.f, 0.f};
#pragma unroll
    for (int ks = 0; ks < 4; ++ks)
#pragma unroll
      for (int t = 0; t < 3; ++t) {
        const int off = (t == 0) ? KG_L0 + c * 272 : (t == 1) ? KG_L1 + c * 272 : KG_L1 + (16 + min(c, 8)) * 272;
        const h8_t Af = *(const h8_t*)(hb + off + 64 * ks + 16 * kg);
        ka[t] = __builtin_amdgcn_mfma_f32_16x16x32_f16(Af, Bf[ks], ka[t], 0, 0, 0);
      }
  }
  SB;
  if (w >= 4) XG(2); else XG(3);
  SB;
  const float iSw = pow2_inv(Sw);
  wave_fence();
  if (hw == 0) *(f4_t*)(hb + lane * 16) = ka[1] * iSw;
  else { *(f4_t*)(hb + lane * 16) = ka[0] * iSw; *(f4_t*)(hb + 1024 + lane * 16) = ka[2] * iSw; }
  __syncthreads();
  if (w == 0) STAMP(1, 5);
  const unsigned etag = *(const unsigned*)(smem + B_BND + 48 + 8);
  if (w >= 4) XG(3);
  {
    const char* pb = smem + (w ^ 1) * KG_W;
    const float Sc = pow2_scale(((const float*)(smem + B_BND))[2 * cl] + ((const float*)(smem + B_BND))[2 * cl + 1], 15);
    char* kr = smem + B_KC + cl * KRS;
    if (hw == 0) {
      const f4_t k0 = (ka[0] * iSw + *(const f4_t*)(pb + lane * 16)) * Sc, k2 = (ka[2] * iSw + *(const f4_t*)(pb + 1024 + lane * 16)) * Sc;
      *(u2_t*)(kr + 2 * (128 * (kg >> 1) + 4 * (kg & 1) + 8 * c)) = u2_t{pk_f16(k0[0], k0[1]), pk_f16(k0[2], k0[3])};
      const unsigned p01 = pk_f16(k2[0], k2[1]), p23 = pk_f16(k2[2], k2[3]);
      u16* k3 = (u16*)kr + 512 + 64 * kg + c;
      if (kg < 2) { k3[0] = (u16)p01; k3[16] = (u16)(p01 >> 16); k3[32] = (u16)p23; k3[48] = (u16)(p23 >> 16); }
      else if (lane == 32) k3[0] = (u16)p01;
      if (lane == 0) ((float*)(smem + B_BND + 32))[cl] = pow2_inv(Sc);
    } else {
      const f4_t k1 = (ka[1] * iSw + *(const f4_t*)(pb + lane * 16)) * Sc;
      const unsigned p01 = pk_f16(k1[0], k1[1]), p23 = pk_f16(k1[2], k1[3]);
      if (kg < 2) {
        *(u2_t*)(kr + 2 * (256 + 64 * kg + 4 * c)) = u2_t{p01, p23};
      } else {
        *(unsigned*)(kr + 2 * (384 + 64 * (kg - 2) + 2 * c)) = p01;
        *(unsigned*)(kr + 2 * (384 + 64 * (kg - 2) + 32 + 2 * c)) = p23;
      }
    }
  }
  __syncthreads();
  const float invs = ((const float*)(smem + B_BND + 32))[w & 3];
  {
    const int kch = tid >> 7, kc = tid & 127;
    const char* kr = smem + B_KC + kch * KRS;
    const int e0 = (kc < 32) ? 8 * kc : (kc < 64) ? 256 + 4 * (kc - 32) : 384 + 2 * (kc - 64);
    const unsigned* kp = (const unsigned*)(kr + 2 * e0);
    const unsigned d0 = kp[0], d1 = kp[1], d2 = kp[2], d3 = kp[3], d4 = kp[4];
    const unsigned kfirst = *(const u16*)kr, k1024 = *(const u16*)(kr + 2 * 512);
    const int dl = 127 - kc;
    const unsigned kfa = *(const u16*)(kr + 2 * (512 + max(2 * dl - 127, 0))), kfb = *(const u16*)(kr + 2 * (512 + max(2 * dl - 126, 0)));
    char* ki = smem + B_KOFF + kch * KDS;
    const unsigned k0 = d0 & 0xffffu;
    unsigned o0, o1, o2, o3, nx;
    if (kc < 32) { o0 = d0; o1 = d1; o2 = d2; o3 = d3; nx = d4; }
    else if (kc < 64) {
      const unsigned k2 = d0 >> 16, k4 = d1 & 0xffffu, k6 = d1 >> 16, k8 = d2 & 0xffffu;
      o0 = k0 * 0x10001u; o1 = k2 | (k4 << 16); o2 = k4 * 0x10001u; o3 = k6 | (k8 << 16); nx = k8;
    } else {
      const unsigned k4 = d0 >> 16, k8 = d1 & 0xffffu;
      o0 = k0 * 0x10001u; o1 = k0 | (k4 << 16); o2 = k4 * 0x10001u; o3 = k8 * 0x10001u; nx = k8;
    }
    *(u4_t*)(ki + 16 * (kc + 2)) = u4_t{o0, o1, o2, o3};
    *(u4_t*)(ki + KD_O + 16 * (kc + 2)) = u4_t{__builtin_amdgcn_alignbit(o1, o0, 16), __builtin_amdgcn_alignbit(o2, o1, 16),
                                              __builtin_amdgcn_alignbit(o3, o2, 16), __builtin_amdgcn_alignbit(nx, o3, 16)};
    if (kc < 6) {
      const int cp = (kc < 2) ? kc : 128 + kc;
      const unsigned kk = k1024 * 0x10001u;
      const u4_t e = (kc == 2) ? u4_t{kk, kk, k1024, 0u} : u4_t{0u, 0u, 0u, 0u};
      const u4_t o = (kc == 2) ? u4_t{kk, kk, 0u, 0u} : u4_t{0u, 0u, 0u, (kc == 1) ? (kfirst << 16) : 0u};
      *(u4_t*)(ki + 16 * cp) = e;
      *(u4_t*)(ki + KD_O + 16 * cp) = o;
    }
    if (kc < 80) *(unsigned*)(ki + KD_DR + 4 * kc) = (dl >= 64) ? (kfa | (kfb << 16)) : 0u;
  }
  *(unsigned*)(smem + B_UOFF + (tid >> 7) * B_UCH + 4096 + 4 * (tid & 127)) = 0u;
  asm volatile("s_waitcnt vmcnt(2)" : "+v"(gr[0]), "+v"(gr[1]), "+v"(xg[0]), "+v"(xg[1])::"memory");
  {
    for (int spin = 0; spin < SPIN_MAX; ++spin) {
      if (__all(gr[0][1] == etag && gr[0][3] == etag && gr[1][1] == etag && gr[1][3] == etag)) break;
      __builtin_amdgcn_s_sleep(2);
      GRL();
      asm volatile("s_waitcnt vmcnt(0)" : "+v"(gr[0]), "+v"(gr[1])::"memory");
    }
#pragma unroll
    for (int it = 0; it < 2; ++it)
#pragma unroll
      for (int r = 0; r < 2; ++r)
        if (gr[it][2 * r + 1] != etag) gr[it][2 * r] = ln1_row(x, 2 * (tid + 512 * it) + r);
  }
  if (w == 0) STAMP(1, 6);
  const float dk[4] = {bf_lo(dkv.x), bf_hi(dkv.x), bf_lo(dkv.y), bf_hi(dkv.y)};
  float z[2][8];
#pragma unroll
  for (int it = 0; it < 2; ++it) {
    if (it == 1) asm volatile("s_waitcnt vmcnt(0)" : "+v"(xg[2]), "+v"(xg[3])::"memory");
    const int j = tid + 512 * it;
    const unsigned s0 = gr[it][0], s1 = gr[it][2];
    const float m0 = bf_lo(s0), r0 = __builtin_amdgcn_rcpf(bf_hi(s0)), m1 = bf_lo(s1), r1 = __builtin_amdgcn_rcpf(bf_hi(s1));
    const u2_t xa = xg[2 * it], xc = xg[2 * it + 1];
    const float x0[4] = {bf_lo(xa.x), bf_hi(xa.x), bf_lo(xa.y), bf_hi(xa.y)};
    const float x1[4] = {bf_lo(xc.x), bf_hi(xc.x), bf_lo(xc.y), bf_hi(xc.y)};
#pragma unroll
    for (int cc = 0; cc < 4; ++cc) {
      const float u0 = (x0[cc] - m0) * r0, u1 = (x1[cc] - m1) * r1;
      *(unsigned*)(smem + B_UOFF + cc * B_UCH + 4 * (1023 - j)) = pk_f16(u1, u0);
      z[it][cc] = x0[cc] + u0 * dk[cc];
      z[it][4 + cc] = x1[cc] + u1 * dk[cc];
    }
  }
  __syncthreads();
  if (w == 0) STAMP(1, 2);
  if (w >= 4 && w < 7 && cb * 3 + (w - 4) < 432) pack_wd(wd, ws, smem + B_PACK + (w - 4) * 5120, cb * 3 + (w - 4), lane);
  f4_t acc[8];
  if (w < 4) {
    const char* ub = smem + B_UOFF + w * B_UCH + 4112 - 32 * c - 16 * kg - 512 * 7;
    const char* kb = smem + B_KOFF + w * KDS + ((c & 1) ? 0 : KD_O) + 4 * (12 + ((c + 1) >> 1) - 4 * kg);
    const unsigned* dr = (const unsigned*)(smem + B_KOFF + w * KDS + KD_DR) + (127 - c + 4 * kg);
    const char* uw = smem + B_UOFF + w * B_UCH + 4112 - 32 * c - 16 * kg;
    auto rdB = [&](BFrag& F, int i0, int p) { F.B[p] = *(const h8_t*)(ub + 512 * (7 - p) + 64 * i0); };
    auto rdA = [&](BFrag& F, int i0, int i1, int half) {
      const unsigned* ap = (const unsigned*)(kb + 64 * i0 + 512 * i1) + 2 * half;
      F.A[i1][half] = u2_t{ap[0], ap[1]};
    };
    auto frag = [&](const u2_t (&a)[2]) { return u4_t{a[0].x, a[0].y, a[1].x, a[1].y}; };
    auto rdnext = [&](BFrag& F, int i0, int m) {
      if (m < 8) rdB(F, i0, m); else if (m < 16) rdA(F, i0, (m - 8) >> 1, (m - 8) & 1);
    };
    auto step = [&](auto i0c, const BFrag& F, BFrag& N, auto nextc) {
      constexpr int I0 = decltype(i0c)::value, NX = decltype(nextc)::value;
      int m = 0;
#pragma unroll
      for (int i1 = 0; i1 < 5; ++i1) {
        if (i1 == 4 && I0 != 0) continue;
        const u4_t a = frag(F.A[i1]);
#pragma unroll
        for (int t = i1; t < 8; ++t) {
          if (I0 == 0 && i1 == 0) mfma_f16_0(acc[t], a, F.B[t]); else mfma_f16(acc[t], a, F.B[t - i1]);
          if (NX >= 0) rdnext(N, NX, m);
          SB;
          ++m;
        }
      }
    };
    BFrag F0, F1;
    h8_t U9[4], U7[4];
#pragma unroll
    for (int bt = 0; bt < 4; ++bt) { U9[bt] = *(const h8_t*)(uw - 512 * bt + 16); U7[bt] = *(const h8_t*)(uw - 512 * bt); }
#pragma unroll
    for (int m = 0; m < 16; ++m) rdnext(F0, 0, m);
    rdA(F0, 0, 4, 0); rdA(F0, 0, 4, 1);
    SB;
    f4_t X9[4], X7[4];
#pragma unroll
    for (int bt = 0; bt < 4; ++bt) { mfma_f16_0(X9[bt], __builtin_bit_cast(u4_t, U9[bt]), band9); mfma_f16_0(X7[bt], __builtin_bit_cast(u4_t, U7[bt]), band7); }
    SB;
    step(IC(0), F0, F1, IC(1));
    h8_t Vf[4]; u4_t Tf[4];
    {
      int m = 0;
#pragma unroll
      for (int i1 = 0; i1 < 4; ++i1) {
        const u4_t a = frag(F1.A[i1]);
#pragma unroll
        for (int t = i1; t < 8; ++t) {
          mfma_f16(acc[t], a, F1.B[t - i1]);
          rdnext(F0, 2, m);
          if (m < 4) {
            const u4_t vu = {pk_f16(X7[m][0], X9[m][0]), pk_f16(X7[m][1], X9[m][1]), pk_f16(X7[m][2], X9[m][2]), pk_f16(X7[m][3], X9[m][3])};
            Vf[m] = __builtin_bit_cast(h8_t, vu);
          }
          if (m >= 16 && m < 20) { const unsigned* tp = dr - 16 * (m - 16 + 4); Tf[m - 16] = u4_t{tp[0], tp[1], tp[2], tp[3]}; }
          SB;
          ++m;
        }
      }
    }
    step(IC(2), F0, F1, IC(3));
#pragma unroll
    for (int bt = 0; bt < 4; ++bt)
#pragma unroll
      for (int at = 4 + bt; at < 8; ++at) mfma_f16(acc[at], __builtin_bit_cast(u4_t, Vf[bt]), __builtin_bit_cast(h8_t, Tf[at - bt - 4]));
    SB;
    step(IC(3), F1, F0, IC(4));
    step(IC(4), F0, F1, IC(5));
    step(IC(5), F1, F0, IC(6));
    step(IC(6), F0, F1, IC(7));
    step(IC(7), F1, F0, IC(-1));
    asm volatile("s_nop 7\n\ts_nop 1" : "+v"(acc[0]), "+v"(acc[1]), "+v"(acc[2]), "+v"(acc[3]), "+v"(acc[4]), "+v"(acc[5]), "+v"(acc[6]), "+v"(acc[7]));
  }
  if (w == 0) STAMP(1, 3);
  bar();
  if (w < 4) {
#pragma unroll
    for (int t = 0; t < 8; ++t) *(f4_t*)(smem + w * B_YCH + t * 1280 + kg * 320 + c * 16) = acc[t] * invs;
  }
  asm volatile("s_waitcnt lgkmcnt(0)" ::: "memory");
  bar();
#pragma unroll
  for (int it = 0; it < 2; ++it) {
    const int j = tid + 512 * it, t = 2 * j;
    const int yo = (t >> 8) * 1280 + ((t >> 2) & 3) * 320 + ((t >> 4) & 15) * 16 + (t & 3) * 4;
    float h0[4], h1[4];
#pragma unroll
    for (int cc = 0; cc < 4; ++cc) {
      f2_t y = *(const f2_t*)(smem + cc * B_YCH + yo);
      h0[cc] = z[it][cc] + y.x;
      h1[cc] = z[it][4 + cc] + y.y;
    }
    char* dst = ws + OFF_H8 + ((size_t)(q >> 1) * SL + t) * 16 + (q & 1) * 8;
    st8(dst, u2_t{pk_bf16(h0[0], h0[1]), pk_bf16(h0[2], h0[3])});
    st8(dst + 16, u2_t{pk_bf16(h1[0], h1[1]), pk_bf16(h1[2], h1[3])});
  }
  if (w == 0) STAMP(1, 7);
}

constexpr int C_T = 240;
constexpr int C_SLOT = 23552, C_NSLOT = 6, C_GOFF = 15360;
constexpr int C_STAT = C_NSLOT * C_SLOT;
constexpr int C_CS = C_STAT + 960;
constexpr int C_LDS = C_CS + 512;
constexpr int C_STEPS = 20;
constexpr size_t OFF_DUMMY = 0x1140000;

__device__ __forceinline__ unsigned ln_pack(float sum, float sq) {
  const float mean = sum * (1.0f / DM);
  return ln_fin(mean, fmaxf(sq * (1.0f / DM) - mean * mean, 0.f));
}
__device__ __forceinline__ unsigned ln_row(const char* h8, int t) {
  float s = 0.f, q = 0.f;
  for (int pl = 0; pl < 80; ++pl) {
    const u4_t v = *(const u4_t*)(h8 + (size_t)pl * 32768 + t * 16);
    for (int j = 0; j < 4; ++j) { const float a = bf_lo(v[j]), b = bf_hi(v[j]); s += a + b; q += a * a + b * b; }
  }
  return ln_pack(s, q);
}

template <int NDMA>
__device__ __forceinline__ void c_loader(char* smem, char* ws, const char* wbase, int w4, int lane, int t0, int jline, int xcd) {
  const unsigned lds0 = (unsigned)(size_t)(LAS char*)smem;
  const char* h8 = ws + OFF_H8;
  unsigned voffH[4], voffW[2];
#pragma unroll
  for (int k = 0; k < 2; ++k) {
    const int r = 8 * (2 * (w4 & 1) + k) + (lane >> 3), c = lane & 7;
    const int f = ((r >> 1) & 1) | ((r >> 2) & 2);
    voffW[k] = r * (FF * 2) + 128 * jline + (((c >> 1) ^ f) * 32) + (c & 1) * 16;
  }
#pragma unroll
  for (int k = 0; k < 4; ++k) {
    const int idx = 64 * (w4 + 4 * k) + lane, pl = idx / C_T, tt = idx % C_T;
    const int t = min(t0 + tt, SL - 1);
    voffH[k] = pl * 32768 + t * 16;
  }
  const unsigned ldsW = C_GOFF + (w4 >> 1) * 4096 + (w4 & 1) * 2048;
  auto issue = [&](int step, unsigned so) {
#pragma unroll
    for (int k = 0; k < 2; ++k) glds16(wbase + (size_t)step * (32 * FF * 2), voffW[k], lds0 + so + ldsW + k * 1024);
#pragma unroll
    for (int k = 0; k < NDMA - 2; ++k) glds16(h8 + (size_t)step * (4 * 32768), voffH[k], lds0 + so + (w4 + 4 * k) * 1024);
  };
  constexpr unsigned S = C_SLOT;
  issue(0, 0); issue(1, S); issue(2, 2 * S); issue(3, 3 * S);
  wait_vm<3 * NDMA>(); bar();
  issue(4, 4 * S);
  wait_vm<2 * NDMA>(); bar();
  issue(5, 5 * S); issue(6, 0);
  unsigned sw = S;
#pragma unroll 1
  for (int j = 1; j < 7; ++j) {
    wait_vm<2 * NDMA>();
    bar();
    issue(2 * j + 5, sw);
    sw += S; if (sw == C_NSLOT * S) sw = 0;
    issue(2 * j + 6, sw);
    sw += S; if (sw == C_NSLOT * S) sw = 0;
  }
  wait_vm<2 * NDMA>(); bar();
  issue(19, sw);
  const int tl = min(60 * w4 + lane, C_T - 1), tg = min(t0 + tl, SL - 1);
  const gu64* gp = (const gu64*)(ws + OFF_G2) + tg;
  const float* csp = (const float*)(ws + OFF_CS) + ((lane >> 4) & 1) * FF + 64 * jline + 16 * w4 + (lane & 15);
  const char* tb = ws + OFF_WP2 + (size_t)(4 * (xcd & 1)) * (54 * 5120);
  const int tl0 = ((int)(blockIdx.x >> 3) * 4 + w4) * 68;
  u2_t gv; float csv; unsigned tv0, tv1, tv2;
  asm volatile("global_load_dwordx2 %0, %1, off sc1" : "=v"(gv) : "v"(gp) : "memory");
  asm volatile("global_load_dword %0, %1, off" : "=v"(csv) : "v"(csp) : "memory");
  asm volatile("global_load_dword %0, %1, off" : "=v"(tv0) : "v"(tb + (size_t)min(tl0 + lane, 8639) * 128) : "memory");
  asm volatile("global_load_dword %0, %1, off" : "=v"(tv1) : "v"(tb + (size_t)min(tl0 + 64 + (lane & 3), 8639) * 128) : "memory");
  asm volatile("global_load_dword %0, %1, off" : "=v"(tv2) : "v"((const char*)&g_ptrs[xcd]) : "memory");
  wait_vm<NDMA + 5>(); bar();
  asm volatile("s_waitcnt vmcnt(0)" : "+v"(gv), "+v"(csv), "+v"(tv0), "+v"(tv1), "+v"(tv2)::"memory");
  bar();
  {
    bool ok = gv.y == 1u;
    for (int spin = 0; spin < 4096 && !__all(ok); ++spin) {
      const unsigned long long g = __hip_atomic_load(gp, __ATOMIC_RELAXED, __HIP_MEMORY_SCOPE_AGENT);
      gv.x = (unsigned)g; ok = (unsigned)(g >> 32) == 1u;
      __builtin_amdgcn_s_sleep(2);
    }
    if (!__all(ok)) gv.x = ln_row(h8, tg);
    if (lane < 60) *(float*)(smem + C_STAT + (60 * w4 + lane) * 4) = bf_lo(gv.x);
    if (lane < 32) *(float*)(smem + C_CS + (lane >> 4) * 256 + (16 * w4 + (lane & 15)) * 4) = csv;
  }
  asm volatile("s_waitcnt lgkmcnt(0)" ::: "memory");
  bar();
}

struct CFrag { b8_t A[2][2]; b8_t B[8]; };

extern "C" __global__ __launch_bounds__(512, 2) void kC() {
  extern __shared__ __attribute__((aligned(16))) char smem[];
  char* __restrict__ ws = ptrs()->ws;
  const u16* __restrict__ wg = ptrs()->wg;
  const u16* __restrict__ wu = ptrs()->wu;
  const int tid = threadIdx.x, lane = tid & 63;
  const int w = __builtin_amdgcn_readfirstlane(tid >> 6);
  int panel, jline;
  const int xcd = blockIdx.x & 7, rank = blockIdx.x >> 3;
  {
    const int g4 = xcd >> 1, a = xcd & 1, nl = (g4 < 3) ? 7 : 6, total = 9 * nl, n0 = (total + 1) >> 1;
    if (a == 0 ? rank >= n0 : rank >= total - n0) {
      const int sp = (xcd < 6) ? (xcd >> 1) : 3 + 5 * (xcd - 6) + (rank - 27);
      const int wv = sp * 8 + w;
      if (w == 0) STAMP(2, 0);
      for (int tile = wv; tile < 128; tile += 104) {
        const int t = 16 * tile + (lane & 15);
        const char* hp = ws + OFF_H8 + (size_t)(lane >> 4) * 32768 + t * 16;
        u4_t v[20];
#pragma unroll
        for (int i = 0; i < 20; ++i) v[i] = *(const u4_t*)(hp + (size_t)i * (4 * 32768));
        float s0 = 0.f, s1 = 0.f, q0 = 0.f, q1 = 0.f;
        unsigned k_both = 0x3f803f80u;
        asm volatile("" : "+s"(k_both));
#pragma unroll
        for (int i = 0; i < 20; ++i) {
          s0 = dot2bf(v[i][0], k_both, s0); q0 = dot2bf(v[i][0], v[i][0], q0);
          s1 = dot2bf(v[i][1], k_both, s1); q1 = dot2bf(v[i][1], v[i][1], q1);
          s0 = dot2bf(v[i][2], k_both, s0); q0 = dot2bf(v[i][2], v[i][2], q0);
          s1 = dot2bf(v[i][3], k_both, s1); q1 = dot2bf(v[i][3], v[i][3], q1);
        }
        float s = s0 + s1, q = q0 + q1;
        s += __shfl_xor(s, 16); q += __shfl_xor(q, 16);
        s += __shfl_xor(s, 32); q += __shfl_xor(q, 32);
        if (lane < 16)
          __hip_atomic_store((gu64*)(ws + OFF_G2) + t, (1ull << 32) | ln_pack(s, q), __ATOMIC_RELAXED, __HIP_MEMORY_SCOPE_AGENT);
      }
      if (w == 0) STAMP(2, 7);
      return;
    }
    const int idx = rank + a * n0;
    panel = idx / nl; jline = 7 * g4 + idx % nl;
  }
  const int t0 = panel * C_T;
  if (w == 0) STAMP(2, 0);
  if (w >= 4) {
    const int w4 = w - 4;
    const char* wbase = (const char*)((w4 >> 1) ? wu : wg);
    if (w4 < 3) c_loader<6>(smem, ws, wbase, w4, lane, t0, jline, xcd);
    else c_loader<5>(smem, ws, wbase, w4, lane, t0, jline, xcd);
    return;
  }
  const int s = w >> 1, th = w & 1;
  const int fr = lane & 15, kg = lane >> 4, q = (lane & 15) >> 2, p = lane & 3;
  const unsigned hb0 = kg * 3840 + fr * 16 + 256 * 7;
  const unsigned hb1 = kg * 3840 + fr * 16 + 256 * (th ? 8 : 0);
  unsigned wa[2];
#pragma unroll
  for (int v = 0; v < 2; ++v) {
    const int sub = 2 * s + (v ? 1 - th : th);
    const int f = (q >> 1) | (2 * (kg & 1));
    wa[v] = C_GOFF + (8 * kg + q) * 128 + ((sub ^ f) * 32) + 8 * p;
  }
  f4_t ag[8][2], au[8][2];
#pragma unroll
  for (int u = 0; u < 8; ++u)
#pragma unroll
    for (int v = 0; v < 2; ++v) { ag[u][v] = f4_t{0.f, 0.f, 0.f, 0.f}; au[u][v] = f4_t{0.f, 0.f, 0.f, 0.f}; }
  auto load = [&](CFrag& F, unsigned so) {
    const char* sp = smem + so;
#pragma unroll
    for (int v = 0; v < 2; ++v)
#pragma unroll
      for (int m = 0; m < 2; ++m) {
        s4_t lo = tr_read(sp + wa[v] + m * 4096), hi = tr_read(sp + wa[v] + m * 4096 + 512);
        u2_t a = __builtin_bit_cast(u2_t, lo), b = __builtin_bit_cast(u2_t, hi);
        F.A[v][m] = __builtin_bit_cast(b8_t, (u4_t{a.x, a.y, b.x, b.y}));
      }
    F.B[0] = *(const b8_t*)(sp + hb0);
#pragma unroll
    for (int u = 1; u < 8; ++u) F.B[u] = *(const b8_t*)(sp + hb1 + 256 * (u - 1));
  };
  auto mma = [&](const CFrag& F) {
    ag[0][0] = __builtin_amdgcn_mfma_f32_16x16x32_bf16(F.A[0][0], F.B[0], ag[0][0], 0, 0, 0);
    au[0][0] = __builtin_amdgcn_mfma_f32_16x16x32_bf16(F.A[0][1], F.B[0], au[0][0], 0, 0, 0);
#pragma unroll
    for (int u = 1; u < 8; ++u)
#pragma unroll
      for (int v = 0; v < 2; ++v) {
        ag[u][v] = __builtin_amdgcn_mfma_f32_16x16x32_bf16(F.A[v][0], F.B[u], ag[u][v], 0, 0, 0);
        au[u][v] = __builtin_amdgcn_mfma_f32_16x16x32_bf16(F.A[v][1], F.B[u], au[u][v], 0, 0, 0);
      }
  };
  auto interleave = [&]() {
#pragma unroll
    for (int i = 0; i < 16; ++i) {
      __builtin_amdgcn_sched_group_barrier(0x008, 1, 0);
      __builtin_amdgcn_sched_group_barrier(0x100, 1, 0);
    }
    __builtin_amdgcn_sched_group_barrier(0x008, 14, 0);
  };
  CFrag F0, F1;
  bar();
  if (w == 0) STAMP(2, 1);
  load(F0, 0);
  unsigned sn = C_SLOT;
  SB;
#pragma unroll 1
  for (int j = 0; j < 9; ++j) {
    asm volatile("s_waitcnt lgkmcnt(0)" ::: "memory");
    bar(); SB;
    load(F1, sn);
    mma(F0);
    interleave();
    SB;
    sn += C_SLOT; if (sn == C_NSLOT * C_SLOT) sn = 0;
    load(F0, sn);
    mma(F1);
    interleave();
    SB;
    sn += C_SLOT; if (sn == C_NSLOT * C_SLOT) sn = 0;
  }
  asm volatile("s_waitcnt lgkmcnt(0)" ::: "memory");
  bar(); SB;
  load(F1, sn);
  mma(F0);
  interleave();
  SB;
  bar(); SB;
  f4_t csg[2], csu[2];
  float mu[8];
#pragma unroll
  for (int v = 0; v < 2; ++v) {
    const int sub = 2 * s + (v ? 1 - th : th);
    csg[v] = *(const f4_t*)(smem + C_CS + (16 * sub + 4 * kg) * 4);
    csu[v] = *(const f4_t*)(smem + C_CS + 256 + (16 * sub + 4 * kg) * 4);
  }
#pragma unroll
  for (int u = 0; u < 8; ++u) {
    const int tile = (u == 0) ? 7 : (th ? 7 + u : u - 1);
    mu[u] = *(const float*)(smem + C_STAT + (16 * tile + fr) * 4);
  }
  mma(F1);
  if (w == 0) STAMP(2, 2);
  char* hh = ws + OFF_HH + (size_t)(2 * jline + s) * 1024 + lane * 16;
  char* sink = ws + OFF_DUMMY + lane * 16;
  auto epi = [&](auto thc) {
    constexpr int TH = decltype(thc)::value;
#pragma unroll
    for (int u = 0; u < 8; ++u) {
      const int tile = (u == 0) ? 7 : (TH ? 7 + u : u - 1);
      const int t16 = 15 * panel + tile;
      char* dst = (t16 < 128) ? hh + (size_t)t16 * (54 * 1024) : sink;
      u2_t o[2];
#pragma unroll
      for (int v = 0; v < 2; ++v) {
        if (u == 0 && v == 1) continue;
        const f4_t r = (ag[u][v] - mu[u] * csg[v]) * (au[u][v] - mu[u] * csu[v]);
        o[v] = u2_t{pk_bf16(r[0], r[1]), pk_bf16(r[2], r[3])};
      }
      if (u == 0) st8(dst + 8 * TH, o[0]);
      else st16(dst, u4_t{o[TH].x, o[TH].y, o[1 - TH].x, o[1 - TH].y});
    }
  };
  if (th) epi(IC(1)); else epi(IC(0));
  if (w == 0) STAMP(2, 7);
}

constexpr int D_SLOT = 15360, D_NSLOT = 9, D_STEPS = 18;
constexpr int D_LDS = D_NSLOT * D_SLOT;

template <int NDMA>
__device__ __forceinline__ void d_loader(char* smem, const char* wp, int w4, int lane) {
  const unsigned lds0 = (unsigned)(size_t)(LAS char*)smem;
  const unsigned voff = lane * 16;
  auto issue = [&](int step, unsigned so) {
#pragma unroll
    for (int k = 0; k < NDMA; ++k) {
      const int i = w4 + 4 * k;
      glds16(wp + (size_t)step * D_SLOT + i * 1024, voff, lds0 + so + i * 1024);
    }
  };
  constexpr unsigned S = D_SLOT;
  issue(0, 0); issue(1, S); issue(2, 2 * S);
  wait_vm<2 * NDMA>(); bar();
  issue(3, 3 * S); issue(4, 4 * S); issue(5, 5 * S);
  wait_vm<3 * NDMA>(); bar();
  issue(6, 6 * S); issue(7, 7 * S); issue(8, 8 * S);
  unsigned sw = 0;
#pragma unroll 1
  for (int j = 1; j < 5; ++j) {
    wait_vm<4 * NDMA>();
    bar();
    issue(2 * j + 7, sw);
    sw += S; if (sw == D_NSLOT * S) sw = 0;
    issue(2 * j + 8, sw);
    sw += S; if (sw == D_NSLOT * S) sw = 0;
  }
  wait_vm<4 * NDMA>(); bar();
  issue(17, sw);
  wait_vm<3 * NDMA>(); bar();
  wait_vm<1 * NDMA>(); bar();
  wait_vm<0>(); bar();
}

extern "C" __global__ __launch_bounds__(512, 2) void kD() {
  extern __shared__ __attribute__((aligned(16))) char smem[];
  char* __restrict__ ws = ptrs()->ws;
  u16* __restrict__ out = ptrs()->out;
  const int tid = threadIdx.x, lane = tid & 63;
  const int w = __builtin_amdgcn_readfirstlane(tid >> 6);
  const int xcd = blockIdx.x & 7, rank = blockIdx.x >> 3;
  const int it = 8 * (xcd >> 1) + (rank & 7), dg = 4 * (xcd & 1) + (rank >> 3);
  if (w == 0) STAMP(3, 0);
  if (w >= 4) {
    const int w4 = w - 4;
    const char* wp = ws + OFF_WP2 + (size_t)dg * (54 * 5120);
    if (w4 < 3) d_loader<4>(smem, wp, w4, lane);
    else d_loader<3>(smem, wp, w4, lane);
    return;
  }
  const int fr = lane & 15, g = lane >> 4;
  const int t = 64 * it + 16 * w + fr;
  const char* hp = ws + OFF_HH + (size_t)(4 * it + w) * (54 * 1024);
  const unsigned voff = lane * 16;
  u4_t R[18];
#pragma unroll
  for (int i = 0; i < 9; ++i)
    asm volatile("global_load_dwordx4 %0, %1, %2" : "=v"(R[i]) : "v"(voff), "s"(hp + (size_t)i * 1024) : "memory");
  u2_t res[5]; unsigned st;
  f4_t acc[5];
#pragma unroll
  for (int dt = 0; dt < 5; ++dt) acc[dt] = f4_t{0.f, 0.f, 0.f, 0.f};
  b8_t A0[5], A1[5];
  auto rdA = [&](b8_t (&A)[5], unsigned off) {
    const char* sp = smem + off + lane * 16;
#pragma unroll
    for (int dt = 0; dt < 5; ++dt) A[dt] = *(const b8_t*)(sp + dt * 1024);
  };
  bar();
  if (w == 0) STAMP(3, 1);
  rdA(A0, 0);
  unsigned so = 0;
  auto kstep = [&](auto mode, auto idx, b8_t (&Ac)[5], b8_t (&An)[5], const char* hnext) {
    constexpr int MODE = decltype(mode)::value, i = decltype(idx)::value, kk = i % 3;
    if (i % 6 == 0) bar();
    unsigned nx = so + (kk + 1) * 5120;
    if (kk == 2) { so += D_SLOT; if (so == D_NSLOT * D_SLOT) so = 0; nx = so; }
    SB;
    if (!(MODE == 2 && i == 17)) rdA(An, nx);
    constexpr int NV = (MODE == 0) ? (i < 9 ? 8 + i : 26 - i) : (MODE == 1) ? (i < 9 ? 25 - i : 17) : (i == 0 ? 17 : 23 - i);
    asm volatile("s_waitcnt vmcnt(%1)" : "+v"(R[i]) : "i"(NV) : "memory");
    const b8_t B = __builtin_bit_cast(b8_t, R[i]);
#pragma unroll
    for (int dt = 0; dt < 5; ++dt) acc[dt] = __builtin_amdgcn_mfma_f32_16x16x32_bf16(Ac[dt], B, acc[dt], 0, 0, 0);
    SB;
    if (MODE == 0 && i < 9)
      asm volatile("global_load_dwordx4 %0, %1, %2" : "=v"(R[i < 9 ? 9 + i : 0]) : "v"(voff), "s"(hp + (size_t)(9 + i) * 1024) : "memory");
    if (MODE != 2) asm volatile("global_load_dwordx4 %0, %1, %2" : "+v"(R[i]) : "v"(voff), "s"(hnext + (size_t)i * 1024) : "memory");
    if (MODE == 2 && i == 0) {
#pragma unroll
      for (int dt = 0; dt < 5; ++dt) {
        const int d = 80 * dg + 16 * dt + 4 * g;
        const char* rp = ws + OFF_H8 + ((size_t)(d >> 3) * SL + t) * 16 + (d & 4) * 2;
        asm volatile("global_load_dwordx2 %0, %1, off" : "=v"(res[dt]) : "v"(rp) : "memory");
      }
      asm volatile("global_load_dword %0, %1, off" : "=v"(st) : "v"(ws + OFF_G2 + (size_t)t * 8) : "memory");
    }
    SB;
  };
  auto round = [&](auto mode, const char* hnext) {
    kstep(mode, IC(0), A0, A1, hnext); kstep(mode, IC(1), A1, A0, hnext); kstep(mode, IC(2), A0, A1, hnext);
    kstep(mode, IC(3), A1, A0, hnext); kstep(mode, IC(4), A0, A1, hnext); kstep(mode, IC(5), A1, A0, hnext);
    kstep(mode, IC(6), A0, A1, hnext); kstep(mode, IC(7), A1, A0, hnext); kstep(mode, IC(8), A0, A1, hnext);
    kstep(mode, IC(9), A1, A0, hnext); kstep(mode, IC(10), A0, A1, hnext); kstep(mode, IC(11), A1, A0, hnext);
    kstep(mode, IC(12), A0, A1, hnext); kstep(mode, IC(13), A1, A0, hnext); kstep(mode, IC(14), A0, A1, hnext);
    kstep(mode, IC(15), A1, A0, hnext); kstep(mode, IC(16), A0, A1, hnext); kstep(mode, IC(17), A1, A0, hnext);
  };
  round(IC(0), hp + 18 * 1024);
  round(IC(1), hp + 36 * 1024);
  round(IC(2), hp);
  if (w == 0) STAMP(3, 2);
  asm volatile("s_waitcnt vmcnt(0)" : "+v"(res[0]), "+v"(res[1]), "+v"(res[2]), "+v"(res[3]), "+v"(res[4]), "+v"(st)::"memory");
  const float sd = bf_hi(st), r2 = 1.0f / (sd * sd);
#pragma unroll
  for (int dt = 0; dt < 5; ++dt) {
    const int d = 80 * dg + 16 * dt + 4 * g;
    const float h0 = bf_lo(res[dt].x), h1 = bf_hi(res[dt].x), h2 = bf_lo(res[dt].y), h3 = bf_hi(res[dt].y);
    st8(out + (size_t)t * DM + d, u2_t{pk_bf16(h0 + r2 * acc[dt][0], h1 + r2 * acc[dt][1]), pk_bf16(h2 + r2 * acc[dt][2], h3 + r2 * acc[dt][3])});
  }
  if (w == 0) STAMP(3, 7);
}

extern "C" void kernel_launch(void* const* d_in, const int* in_sizes, int n_in, void* d_out, int out_size, void* d_ws,
                              size_t ws_size, hipStream_t stream) {
  char* ws = (char*)d_ws;
  (void)hipFuncSetAttribute((const void*)kB, hipFuncAttributeMaxDynamicSharedMemorySize, B_LDS);
  (void)hipFuncSetAttribute((const void*)kC, hipFuncAttributeMaxDynamicSharedMemorySize, C_LDS);
  (void)hipFuncSetAttribute((const void*)kD, hipFuncAttributeMaxDynamicSharedMemorySize, D_LDS);
  kB<<<256, 512, B_LDS, stream>>>((const u16*)d_in[0], (const u16*)d_in[1], (const u16*)d_in[2], (const u16*)d_in[3], (const u16*)d_in[4],
                                  (const u16*)d_in[5], (const u16*)d_in[6], (const u16*)d_in[7], (const u16*)d_in[8], (const u16*)d_in[10],
                                  (const u16*)d_in[11], (const u16*)d_in[12], (const u16*)d_out, ws);
  kC<<<256, 512, C_LDS, stream>>>();
  kD<<<256, 512, D_LDS, stream>>>();
}
```

```cpp
#include <hip/hip_runtime.h>
#include <hip/hip_bf16.h>
#include <stdint.h>
#include <type_traits>

typedef unsigned short u16;
typedef _Float16 h2_t __attribute__((ext_vector_type(2)));
typedef _Float16 h8_t __attribute__((ext_vector_type(8)));
typedef __bf16 b2_t __attribute__((ext_vector_type(2)));
typedef __bf16 b8_t __attribute__((ext_vector_type(8)));
typedef float f2_t __attribute__((ext_vector_type(2)));
typedef float f4_t __attribute__((ext_vector_type(4)));
typedef unsigned u2_t __attribute__((ext_vector_type(2)));
typedef unsigned u4_t __attribute__((ext_vector_type(4)));
typedef short s4_t __attribute__((ext_vector_type(4)));
typedef __attribute__((address_space(1))) unsigned long long gu64;
#define LAS __attribute__((address_space(3)))
#define SB __builtin_amdgcn_sched_barrier(0)
#define IC(n) std::integral_constant<int, n>{}

constexpr int SL = 2048;
constexpr int DM = 640;
constexpr int FF = 1728;

constexpr size_t OFF_G1 = 0;
constexpr size_t OFF_EP = 0x10000;
constexpr size_t OFF_RD = 0x10100;
constexpr size_t OFF_H8 = 0x5A0000;
constexpr size_t OFF_HH = 0x820000;
constexpr size_t OFF_WP2 = 0xF00000;
constexpr size_t OFF_CS = 0x1120000;
constexpr size_t OFF_G2 = 0x1130000;
constexpr int KRS = 1344;
constexpr int KDS = 4864;
constexpr int KD_O = 2240;
constexpr int KD_DR = 4384;

#define STAMP(k, i)

__device__ __forceinline__ float bf_lo(unsigned d) { return __uint_as_float(d << 16); }
__device__ __forceinline__ float bf_hi(unsigned d) { return __uint_as_float(d & 0xffff0000u); }
__device__ __forceinline__ float bf2f(u16 v) { return __uint_as_float((unsigned)v << 16); }
__device__ __forceinline__ unsigned pk_bf16(float a, float b) {
  f2_t v = {a, b};
  return __builtin_bit_cast(unsigned, __builtin_convertvector(v, b2_t));
}
__device__ __forceinline__ unsigned pk_f16(float a, float b) {
  f2_t v = {a, b};
  return __builtin_bit_cast(unsigned, __builtin_convertvector(v, h2_t));
}
__device__ __forceinline__ float rbf(float a) { return bf_lo(pk_bf16(a, 0.f)); }
template <int N> __device__ __forceinline__ float dpp_ror_add(float v) {
  int r = __builtin_amdgcn_update_dpp(0, __float_as_int(v), 0x120 + N, 0xf, 0xf, false);
  return v + __int_as_float(r);
}
__device__ __forceinline__ float row16_sum(float v) {
  v = dpp_ror_add<8>(v); v = dpp_ror_add<4>(v); v = dpp_ror_add<2>(v); v = dpp_ror_add<1>(v);
  return v;
}
__device__ __forceinline__ float wave_sum(float v) {
  v = row16_sum(v);
  const int b = __float_as_int(v);
  return (__int_as_float(__builtin_amdgcn_readlane(b, 0)) + __int_as_float(__builtin_amdgcn_readlane(b, 16))) +
         (__int_as_float(__builtin_amdgcn_readlane(b, 32)) + __int_as_float(__builtin_amdgcn_readlane(b, 48)));
}
template <typename T> __device__ __forceinline__ T ldnt(const T* p) { return __builtin_nontemporal_load(p); }
__device__ __forceinline__ void wave_fence() {
  __builtin_amdgcn_fence(__ATOMIC_RELEASE, "wavefront");
  __builtin_amdgcn_wave_barrier();
  __builtin_amdgcn_fence(__ATOMIC_ACQUIRE, "wavefront");
}
__device__ __forceinline__ s4_t tr_read(const char* p) {
  return __builtin_amdgcn_ds_read_tr16_b64_v4i16((LAS s4_t*)p);
}
__device__ __forceinline__ void glds16(const void* sbase, unsigned voff, unsigned lds_addr) {
  asm volatile("s_mov_b32 m0, %2\n\ts_nop 0\n\tglobal_load_lds_dwordx4 %0, %1" ::"v"(voff), "s"(sbase), "s"(lds_addr) : "memory");
}
template <int N> __device__ __forceinline__ void wait_vm() { asm volatile("s_waitcnt vmcnt(%0)" ::"i"(N) : "memory"); }
__device__ __forceinline__ void bar() { asm volatile("s_barrier" ::: "memory"); }
__device__ __forceinline__ float dot2bf(unsigned a, unsigned b, float c) {
  return __builtin_amdgcn_fdot2_f32_bf16(__builtin_bit_cast(b2_t, a), __builtin_bit_cast(b2_t, b), c, false);
}

#define ST_MODE 0
__device__ __forceinline__ void st16(void* p, u4_t v) {
  *(u4_t*)p = v;
}
__device__ __forceinline__ void st8(void* p, u2_t v) {
  *(u2_t*)p = v;
}

struct __attribute__((aligned(128))) Ptrs { char* ws; const u16* wg; const u16* wu; u16* out; void* pad[12]; };
__device__ Ptrs g_ptrs[8];
typedef const __attribute__((address_space(4))) Ptrs* cptrs_t;
__device__ __forceinline__ cptrs_t ptrs() { return (cptrs_t)(unsigned long long)&g_ptrs[blockIdx.x & 7]; }

struct Cw { f2_t w, ws; };
__device__ __forceinline__ Cw mkw(f2_t w) { Cw r; r.w = w; r.ws = f2_t{-w.y, w.x}; return r; }
__device__ __forceinline__ f2_t cmulw(f2_t a, const Cw& W) {
  return __builtin_elementwise_fma(f2_t{a.y, a.y}, W.ws, f2_t{a.x, a.x} * W.w);
}
__device__ __forceinline__ f2_t cexp_l(float er, float wr, float l) {
  float mag = __builtin_amdgcn_exp2f(er * l);
  float rev = __builtin_amdgcn_fractf(wr * l);
  return f2_t{mag * __builtin_amdgcn_cosf(rev), mag * __builtin_amdgcn_sinf(rev)};
}
__device__ __forceinline__ float pow2_scale(float bound, int target) {
  unsigned e = (__float_as_uint(bound) >> 23) & 0xff;
  int k = target - ((int)e - 126);
  k = k > 126 ? 126 : (k < -126 ? -126 : k);
  return (bound > 0.f) ? __uint_as_float((unsigned)(k + 127) << 23) : 1.0f;
}
__device__ __forceinline__ float pow2_inv(float p) { return __uint_as_float(0x7f000000u - __float_as_uint(p)); }

constexpr int B_SIDE = 96;
constexpr int KG_R = 0, KG_L0 = 4352, KG_L1 = 8704, KG_W = 8704 + 25 * 272;
constexpr int B_BND = 8 * KG_W;
constexpr int B_KC = B_BND + 64;
constexpr int B_LDS = B_KC + 4 * KRS;
constexpr int B_KOFF = 0;
constexpr int B_UOFF = 4 * KDS;
constexpr int B_UCH = 4608;
constexpr int B_YCH = 10240;
constexpr int B_PACK = 40960;

__device__ __forceinline__ void pack_wd(const u16* __restrict__ wd, char* __restrict__ ws, char* buf, int u, int lane) {
  const int ks = u >> 3, dg = u & 7;
#pragma unroll
  for (int i = 0; i < 5; ++i) {
    const int idx = i * 64 + lane, row = idx / 10, c = idx % 10;
    __builtin_amdgcn_global_load_lds((const unsigned*)((const char*)wd + (size_t)(32 * ks + row) * 1280 + 160 * dg + 16 * c),
                                     (LAS unsigned*)(buf + i * 1024), 16, 0, 2);
  }
  asm volatile("s_waitcnt vmcnt(0)" ::: "memory");
  wave_fence();
  const int g = lane >> 4, q = (lane & 15) >> 2, p = lane & 3;
  char* dst = ws + OFF_WP2 + ((size_t)(dg * 54 + ks) * 5) * 1024 + lane * 16;
#pragma unroll
  for (int dt = 0; dt < 5; ++dt) {
    s4_t aa = tr_read(buf + (4 * g + q) * 160 + 32 * dt + 8 * p);
    s4_t bb = tr_read(buf + (16 + 4 * g + q) * 160 + 32 * dt + 8 * p);
    u2_t ua = __builtin_bit_cast(u2_t, aa), ub = __builtin_bit_cast(u2_t, bb);
    const u4_t pv = {ua.x, ua.y, ub.x, ub.y};
    asm volatile("global_store_dwordx4 %0, %1, off sc1\n\ts_nop 1" ::"v"(dst + dt * 1024), "v"(pv) : "memory");
  }
}

struct BFrag { h8_t B[8]; u2_t A[5][2]; };
__device__ __forceinline__ void mfma_f16(f4_t& d, u4_t a, h8_t b) {
  asm volatile("v_mfma_f32_16x16x32_f16 %0, %1, %2, %0" : "+v"(d) : "v"(a), "v"(b));
}
__device__ __forceinline__ void mfma_f16_0(f4_t& d, u4_t a, h8_t b) {
  asm volatile("v_mfma_f32_16x16x32_f16 %0, %1, %2, 0" : "=&v"(d) : "v"(a), "v"(b));
}
__device__ __forceinline__ unsigned ln_fin(float mean, float var) {
  const float mb = rbf(mean), sd = rbf(sqrtf(rbf(rbf(var) + 1e-5f)));
  return (__float_as_uint(mb) >> 16) | (__float_as_uint(sd) & 0xffff0000u);
}
__device__ __forceinline__ unsigned ln1_row(const u16* __restrict__ x, int t) {
  const u4_t* p = (const u4_t*)(x + (size_t)t * DM);
  float s = 0.f, q = 0.f;
  for (int i = 0; i < 80; ++i) { const u4_t v = p[i]; for (int j = 0; j < 4; ++j) s += bf_lo(v[j]) + bf_hi(v[j]); }
  const float mean = s * (1.0f / DM);
  for (int i = 0; i < 80; ++i) {
    const u4_t v = p[i];
    for (int j = 0; j < 4; ++j) { const float a = bf_lo(v[j]) - mean, b = bf_hi(v[j]) - mean; q += a * a; q += b * b; }
  }
  return ln_fin(mean, q * (1.0f / DM));
}
constexpr int SPIN_MAX = 1 << 14;

extern "C" __global__ __launch_bounds__(512) void kB(const u16* __restrict__ x, const u16* __restrict__ Are, const u16* __restrict__ Aim,
                                                     const u16* __restrict__ Bre, const u16* __restrict__ Bim, const u16* __restrict__ Cre,
                                                     const u16* __restrict__ Cim, const u16* __restrict__ logdt, const u16* __restrict__ dskip,
                                                     const u16* __restrict__ wg, const u16* __restrict__ wu, const u16* __restrict__ wd,
                                                     const u16* __restrict__ outp, char* __restrict__ ws) {
  extern __shared__ __attribute__((aligned(16))) char smem[];
  const int tid = threadIdx.x, lane = tid & 63;
  const int w = __builtin_amdgcn_readfirstlane(tid >> 6);
  const int xcd = blockIdx.x & 7;
#define EPLOAD(v) asm volatile("global_load_dwordx2 %0, %1, %2 sc1" : "=v"(v) : "v"(0u), "s"(ws + OFF_EP) : "memory")
  if (w == 0) STAMP(1, 0);
  auto post_epoch = [&](u2_t ep, int off) {
    if (tid == 0) {
      const unsigned long long e = (((unsigned long long)ep.y << 32) | ep.x) + 1ull;
      *(unsigned long long*)(smem + off) = e;
      *(unsigned*)(smem + off + 8) = (unsigned)((e * 0x9E3779B97F4A7C15ull) >> 32);
      __hip_atomic_store((gu64*)(ws + OFF_RD) + blockIdx.x, e, __ATOMIC_RELAXED, __HIP_MEMORY_SCOPE_AGENT);
    }
  };
  if (blockIdx.x < B_SIDE) {
    const int rk = blockIdx.x >> 3;
    const int g4 = xcd >> 1, a = xcd & 1, nl = (g4 < 3) ? 7 : 6;
    constexpr int S_EP = 17408;
    unsigned pf[2] = {0u, 0u};
    if (rk >= 8) {
#pragma unroll
      for (int k = 0; k < 2; ++k) {
        const int id = (((rk - 8) * 8 + w) * 2 + k) * 64 + lane;
        asm volatile("global_load_dword %0, %1, off" : "=v"(pf[k]) : "v"((const char*)x + (size_t)(id >> 1) * (DM * 2) + (((160 * xcd) >> 7) + (id & 1)) * 128) : "memory");
      }
    }
    if (rk < 8) {
      const int r = lane >> 4, c = lane & 15, t = (blockIdx.x * 8 + w) * 4 + r;
      const u4_t* p = (const u4_t*)(x + (size_t)t * DM);
      u2_t ep;
      EPLOAD(ep);
      u4_t v[5];
#pragma unroll
      for (int i = 0; i < 5; ++i) v[i] = ldnt(p + c + 16 * i);
      float s = 0.f;
#pragma unroll
      for (int i = 0; i < 5; ++i)
#pragma unroll
        for (int j = 0; j < 4; ++j) s += bf_lo(v[i][j]) + bf_hi(v[i][j]);
      asm volatile("" : "+v"(ep) : "v"(s));
      post_epoch(ep, S_EP);
      const float mean = row16_sum(s) * (1.0f / DM);
      float q = 0.f;
#pragma unroll
      for (int i = 0; i < 5; ++i)
#pragma unroll
        for (int j = 0; j < 4; ++j) {
          const float aa = bf_lo(v[i][j]) - mean, bb = bf_hi(v[i][j]) - mean;
          q += aa * aa; q += bb * bb;
        }
      const unsigned sv = ln_fin(mean, row16_sum(q) * (1.0f / DM));
      __syncthreads();
      const u2_t e = *(const u2_t*)(smem + S_EP);
      if (c == 0) {
        __hip_atomic_store((gu64*)(ws + OFF_G1) + t, ((unsigned long long)*(const unsigned*)(smem + S_EP + 8) << 32) | sv, __ATOMIC_RELAXED, __HIP_MEMORY_SCOPE_AGENT);
        ((unsigned long long*)(ws + OFF_G2))[t] = 0ull;
      }
      if (w == 0) STAMP(1, 1);
      if (rk == 0 && w == 7) {
        gu64* gp = (gu64*)(unsigned long long)&g_ptrs[xcd];
        if (lane < 4) {
          const unsigned long long v = lane == 0 ? (unsigned long long)ws : lane == 1 ? (unsigned long long)wg : lane == 2 ? (unsigned long long)wu : (unsigned long long)outp;
          __hip_atomic_store(gp + lane, v, __ATOMIC_RELAXED, __HIP_MEMORY_SCOPE_AGENT);
        }
        unsigned back;
        asm volatile("s_waitcnt vmcnt(0)\n\tglobal_load_dword %0, %1, off\n\ts_waitcnt vmcnt(0)" : "=&v"(back) : "v"(gp) : "memory");
        asm volatile("" ::"v"(back));
      }
      if (rk == 4 && w == 0) {
        unsigned long long a0, a1;
        asm volatile("s_getpc_b64 s[20:21]\n\ts_add_u32 s20, s20, kB@rel32@lo+4\n\ts_addc_u32 s21, s21, kB@rel32@hi+12\n\ts_mov_b64 %0, s[20:21]" : "=s"(a0) :: "s20", "s21", "scc");
        asm volatile("s_getpc_b64 s[20:21]\n\ts_add_u32 s20, s20, kD@rel32@lo+4\n\ts_addc_u32 s21, s21, kD@rel32@hi+12\n\ts_mov_b64 %0, s[20:21]" : "=s"(a1) :: "s20", "s21", "scc");
        const unsigned long long lo = a0 < a1 ? a0 : a1, last = ((a0 < a1 ? a1 - a0 : a0 - a1) + 8192) & ~127ull;
        unsigned cf[8];
#pragma unroll
        for (int i = 0; i < 8; ++i) {
          const unsigned long long o = (unsigned long long)(lane + 64 * i) * 128;
          asm volatile("global_load_dword %0, %1, off" : "=v"(cf[i]) : "v"(lo + (o < last ? o : last)) : "memory");
        }
        asm volatile("s_waitcnt vmcnt(0)" ::"v"(cf[0]), "v"(cf[1]), "v"(cf[2]), "v"(cf[3]), "v"(cf[4]), "v"(cf[5]), "v"(cf[6]), "v"(cf[7]) : "memory");
      }
      if (blockIdx.x == 63 && w == 7) {
        const unsigned long long e64 = ((unsigned long long)e.y << 32) | e.x;
        for (int spin = 0; spin < SPIN_MAX; ++spin) {
          bool ok = true;
#pragma unroll
          for (int i = 0; i < 4; ++i) ok &= __hip_atomic_load((const gu64*)(ws + OFF_RD) + lane + 64 * i, __ATOMIC_RELAXED, __HIP_MEMORY_SCOPE_AGENT) == e64;
          if (__all(ok)) break;
          __builtin_amdgcn_s_sleep(4);
        }
        if (lane == 0) __hip_atomic_store((gu64*)(ws + OFF_EP), e64, __ATOMIC_RELAXED, __HIP_MEMORY_SCOPE_AGENT);
      }
    }
    const int strip = (rk >= 8) ? rk - 8 : 4 + rk;
    if ((rk >= 8 || rk < 3) && strip < nl) {
      const int jl = 7 * g4 + strip;
      const char* pm = (const char*)(a ? wu : wg) + (size_t)(80 * w + (lane >> 3)) * (FF * 2) + 128 * jl + (lane & 7) * 16;
      u2_t ep;
      EPLOAD(ep);
      u4_t v[10];
#pragma unroll
      for (int i = 0; i < 10; ++i) v[i] = ldnt((const u4_t*)(pm + (size_t)i * 8 * (FF * 2)));
      const char* po = (const char*)(a ? wg : wu) + (size_t)(80 * w) * (FF * 2) + 128 * jl;
      unsigned t0v, t1v;
      asm volatile("global_load_dword %0, %1, off nt" : "=v"(t0v) : "v"(po + (size_t)lane * (FF * 2)) : "memory");
      asm volatile("global_load_dword %0, %1, off nt" : "=v"(t1v) : "v"(po + (size_t)(64 + (lane & 15)) * (FF * 2)) : "memory");
      f2_t s[4];
#pragma unroll
      for (int e = 0; e < 4; ++e) s[e] = f2_t{0.f, 0.f};
#pragma unroll
      for (int i = 0; i < 10; ++i)
#pragma unroll
        for (int jj = 0; jj < 4; ++jj) s[jj] += f2_t{bf_lo(v[i][jj]), bf_hi(v[i][jj])};
      asm volatile("" : "+v"(ep), "+v"(pf[0]), "+v"(pf[1]) : "v"(s[0]));
      if (rk >= 8) post_epoch(ep, S_EP);
      float* pp = (float*)smem;
      *(f4_t*)(pp + (w * 8 + (lane >> 3)) * 68 + (lane & 7) * 8) = f4_t{s[0].x, s[0].y, s[1].x, s[1].y};
      *(f4_t*)(pp + (w * 8 + (lane >> 3)) * 68 + (lane & 7) * 8 + 4) = f4_t{s[2].x, s[2].y, s[3].x, s[3].y};
      __syncthreads();
      float acc = 0.f;
#pragma unroll
      for (int i = 0; i < 8; ++i) acc += pp[(w * 8 + i) * 68 + lane];
      __syncthreads();
      pp[w * 64 + lane] = acc;
      __syncthreads();
      if (w == 0) {
        float t = 0.f;
#pragma unroll
        for (int i = 0; i < 8; ++i) t += pp[i * 64 + lane];
        ((float*)(ws + OFF_CS))[a * FF + 64 * jl + lane] = t;
      }
      asm volatile("s_waitcnt vmcnt(0)" ::"v"(t0v), "v"(t1v) : "memory");
    }
    if (w == 0) STAMP(1, 7);
    return;
  }
  const int cb = blockIdx.x - B_SIDE;
  const int q = 20 * xcd + (cb >> 3);
  const int cl = w >> 1, hw = w & 1, ch = 4 * q + cl;
  const int n = ch * 128 + hw * 64 + lane;
  u2_t ep;
  EPLOAD(ep);
  const u16 i_are = ldnt(Are + n), i_aim = ldnt(Aim + n), i_bre = ldnt(Bre + n), i_bim = ldnt(Bim + n), i_cre = ldnt(Cre + n), i_cim = ldnt(Cim + n);
  const u16 i_dt = logdt[ch];
  u2_t xg[4];
  const unsigned xoff = tid * (2 * DM * 2);
  const char* xb = (const char*)(x + 4 * q);
#define XG(i) asm volatile("global_load_dwordx2 %0, %1, %2 offset:%3" : "=v"(xg[i]) : "v"(xoff), "s"(xb + ((i) >> 1) * (size_t)(1024 * DM * 2)), "i"(((i) & 1) * DM * 2) : "memory")
  const u2_t dkv = *(const u2_t*)(dskip + 4 * q);
  const int c = lane & 15, kg = lane >> 4;
  h8_t band9, band7;
#pragma unroll
  for (int j = 0; j < 8; ++j) {
    const int k = 8 * kg + 7 - j;
    band9[j] = (k >= c + 4 && k <= c + 12) ? (_Float16)1.0f : (_Float16)0.0f;
    band7[j] = (k >= c + 5 && k <= c + 11) ? (_Float16)1.0f : (_Float16)0.0f;
  }
  asm volatile("" : "+v"(band9), "+v"(band7));
  char* hb = smem + w * KG_W;
  float Sw, bsum;
  {
    const float dt = rbf(expf(bf2f(i_dt)));
    const float are = dt * bf2f(i_are), aim = dt * bf2f(i_aim);
    const float bre = dt * bf2f(i_bre), bim = dt * bf2f(i_bim);
    const float cre = bf2f(i_cre), cim = bf2f(i_cim);
    f2_t c0 = {cre * bre - cim * bim, cre * bim + cim * bre};
    const float er = are * 1.44269504088896341f, wr = aim * 0.15915494309189535f;
    const float bnd = (fabsf(c0.x) + fabsf(c0.y)) * fmaxf(1.0f, __builtin_amdgcn_exp2f(er * 2048.f));
    bsum = wave_sum(bnd);
    asm volatile("" : "+v"(ep) : "v"(bnd));
    post_epoch(ep, B_BND + 48);
    if (lane == 0) ((float*)(smem + B_BND))[w] = bsum;
    if (w == 0) STAMP(1, 1);
    Sw = pow2_scale(bsum, 15);
    c0 *= Sw;
    const Cw z1 = mkw(cexp_l(er, wr, 1.f)), w8 = mkw(cexp_l(er, -wr, 8.f)), w128 = mkw(cexp_l(er, wr, 128.f));
    const Cw z2 = mkw(cmulw(z1.w, z1));
    const Cw z4 = mkw(cmulw(z2.w, z2));
    auto put = [&](int off, int row, f2_t v) { *(unsigned*)(hb + off + row * 272 + 4 * lane) = pk_f16(v.x, v.y); };
    f2_t R = w8.w, v0 = c0, v1 = cmulw(c0, w128), T = v1;
    *(unsigned*)(hb + KG_R + 4 * lane) = pk_f16(1.f, 0.f);
#pragma unroll
    for (int i = 0; i < 8; ++i) {
      SB;
      if ((w & 3) == (i & 3)) { if (i < 4) XG(0); else XG(1); }
      SB;
      *(unsigned*)(hb + KG_R + (2 * i + 1) * 272 + 4 * lane) = pk_f16(R.x, R.y);
      if (i < 7) {
        R = cmulw(R, w8);
        *(unsigned*)(hb + KG_R + (2 * i + 2) * 272 + 4 * lane) = pk_f16(R.x, R.y);
        R = cmulw(R, w8);
      }
      put(KG_L0, i, v0); put(KG_L0, 8 + i, v1);
      if (i < 7) { v0 = cmulw(v0, z1); v1 = cmulw(v1, z1); }
#pragma unroll
      for (int k = 0; k < 2; ++k) {
        const int i1 = 2 + 2 * i + k;
        if (i1 <= 16) {
          T = cmulw(T, w128);
          if (i1 < 4) {
            f2_t v = T;
#pragma unroll
            for (int i0 = 0; i0 < 4; ++i0) { put(KG_L1, 4 * (i1 - 2) + i0, v); if (i0 < 3) v = cmulw(v, z2); }
          } else if (i1 < 8) {
            put(KG_L1, 8 + 2 * (i1 - 4), T); put(KG_L1, 8 + 2 * (i1 - 4) + 1, cmulw(T, z4));
          } else {
            put(KG_L1, 16 + (i1 - 8), T);
          }
        }
      }
    }
  }
  SB;
  if (w == 0) STAMP(1, 4);
  u4_t gr[2];
  const unsigned goff = tid * 16;
#define GRL() do { asm volatile("global_load_dwordx4 %0, %1, %2 sc1" : "=v"(gr[0]) : "v"(goff), "s"(ws + OFF_G1) : "memory"); \
                   asm volatile("global_load_dwordx4 %0, %1, %2 sc1" : "=v"(gr[1]) : "v"(goff), "s"(ws + OFF_G1 + 8192) : "memory"); } while (0)
  GRL();
  if (w < 4) XG(2);
  wave_fence();
  f4_t ka[3];
  {
    h8_t Bf[4];
#pragma unroll
    for (int ks = 0; ks < 4; ++ks) Bf[ks] = *(const h8_t*)(hb + KG_R + c * 272 + 64 * ks + 16 * kg);
#pragma unroll
    for (int t = 0; t < 3; ++t) ka[t] = f4_t{0.f, 0.f, 0.f, 0.f};
#pragma unroll
    for (int ks = 0; ks < 4; ++ks)
#pragma unroll
      for (int t = 0; t < 3; ++t) {
        const int off = (t == 0) ? KG_L0 + c * 272 : (t == 1) ? KG_L1 + c * 272 : KG_L1 + (16 + min(c, 8)) * 272;
        const h8_t Af = *(const h8_t*)(hb + off + 64 * ks + 16 * kg);
        ka[t] = __builtin_amdgcn_mfma_f32_16x16x32_f16(Af, Bf[ks], ka[t], 0, 0, 0);
      }
  }
  SB;
  if (w >= 4) XG(2); else XG(3);
  SB;
  const float iSw = pow2_inv(Sw);
  wave_fence();
  if (hw == 0) *(f4_t*)(hb + lane * 16) = ka[1] * iSw;
  else { *(f4_t*)(hb + lane * 16) = ka[0] * iSw; *(f4_t*)(hb + 1024 + lane * 16) = ka[2] * iSw; }
  __syncthreads();
  if (w == 0) STAMP(1, 5);
  const unsigned etag = *(const unsigned*)(smem + B_BND + 48 + 8);
  if (w >= 4) XG(3);
  {
    const char* pb = smem + (w ^ 1) * KG_W;
    const float Sc = pow2_scale(((const float*)(smem + B_BND))[2 * cl] + ((const float*)(smem + B_BND))[2 * cl + 1], 15);
    char* kr = smem + B_KC + cl * KRS;
    if (hw == 0) {
      const f4_t k0 = (ka[0] * iSw + *(const f4_t*)(pb + lane * 16)) * Sc, k2 = (ka[2] * iSw + *(const f4_t*)(pb + 1024 + lane * 16)) * Sc;
      *(u2_t*)(kr + 2 * (128 * (kg >> 1) + 4 * (kg & 1) + 8 * c)) = u2_t{pk_f16(k0[0], k0[1]), pk_f16(k0[2], k0[3])};
      const unsigned p01 = pk_f16(k2[0], k2[1]), p23 = pk_f16(k2[2], k2[3]);
      u16* k3 = (u16*)kr + 512 + 64 * kg + c;
      if (kg < 2) { k3[0] = (u16)p01; k3[16] = (u16)(p01 >> 16); k3[32] = (u16)p23; k3[48] = (u16)(p23 >> 16); }
      else if (lane == 32) k3[0] = (u16)p01;
      if (lane == 0) ((float*)(smem + B_BND + 32))[cl] = pow2_inv(Sc);
    } else {
      const f4_t k1 = (ka[1] * iSw + *(const f4_t*)(pb + lane * 16)) * Sc;
      const unsigned p01 = pk_f16(k1[0], k1[1]), p23 = pk_f16(k1[2], k1[3]);
      if (kg < 2) {
        *(u2_t*)(kr + 2 * (256 + 64 * kg + 4 * c)) = u2_t{p01, p23};
      } else {
        *(unsigned*)(kr + 2 * (384 + 64 * (kg - 2) + 2 * c)) = p01;
        *(unsigned*)(kr + 2 * (384 + 64 * (kg - 2) + 32 + 2 * c)) = p23;
      }
    }
  }
  __syncthreads();
  const float invs = ((const float*)(smem + B_BND + 32))[w & 3];
  {
    const int kch = tid >> 7, kc = tid & 127;
    const char* kr = smem + B_KC + kch * KRS;
    const int e0 = (kc < 32) ? 8 * kc : (kc < 64) ? 256 + 4 * (kc - 32) : 384 + 2 * (kc - 64);
    const unsigned* kp = (const unsigned*)(kr + 2 * e0);
    const unsigned d0 = kp[0], d1 = kp[1], d2 = kp[2], d3 = kp[3], d4 = kp[4];
    const unsigned kfirst = *(const u16*)kr, k1024 = *(const u16*)(kr + 2 * 512);
    const int dl = 127 - kc;
    const unsigned kfa = *(const u16*)(kr + 2 * (512 + max(2 * dl - 127, 0))), kfb = *(const u16*)(kr + 2 * (512 + max(2 * dl - 126, 0)));
    char* ki = smem + B_KOFF + kch * KDS;
    const unsigned k0 = d0 & 0xffffu;
    unsigned o0, o1, o2, o3, nx;
    if (kc < 32) { o0 = d0; o1 = d1; o2 = d2; o3 = d3; nx = d4; }
    else if (kc < 64) {
      const unsigned k2 = d0 >> 16, k4 = d1 & 0xffffu, k6 = d1 >> 16, k8 = d2 & 0xffffu;
      o0 = k0 * 0x10001u; o1 = k2 | (k4 << 16); o2 = k4 * 0x10001u; o3 = k6 | (k8 << 16); nx = k8;
    } else {
      const unsigned k4 = d0 >> 16, k8 = d1 & 0xffffu;
      o0 = k0 * 0x10001u; o1 = k0 | (k4 << 16); o2 = k4 * 0x10001u; o3 = k8 * 0x10001u; nx = k8;
    }
    *(u4_t*)(ki + 16 * (kc + 2)) = u4_t{o0, o1, o2, o3};
    *(u4_t*)(ki + KD_O + 16 * (kc + 2)) = u4_t{__builtin_amdgcn_alignbit(o1, o0, 16), __builtin_amdgcn_alignbit(o2, o1, 16),
                                              __builtin_amdgcn_alignbit(o3, o2, 16), __builtin_amdgcn_alignbit(nx, o3, 16)};
    if (kc < 6) {
      const int cp = (kc < 2) ? kc : 128 + kc;
      const unsigned kk = k1024 * 0x10001u;
      const u4_t e = (kc == 2) ? u4_t{kk, kk, k1024, 0u} : u4_t{0u, 0u, 0u, 0u};
      const u4_t o = (kc == 2) ? u4_t{kk, kk, 0u, 0u} : u4_t{0u, 0u, 0u, (kc == 1) ? (kfirst << 16) : 0u};
      *(u4_t*)(ki + 16 * cp) = e;
      *(u4_t*)(ki + KD_O + 16 * cp) = o;
    }
    if (kc < 80) *(unsigned*)(ki + KD_DR + 4 * kc) = (dl >= 64) ? (kfa | (kfb << 16)) : 0u;
  }
  *(unsigned*)(smem + B_UOFF + (tid >> 7) * B_UCH + 4096 + 4 * (tid & 127)) = 0u;
  asm volatile("s_waitcnt vmcnt(2)" : "+v"(gr[0]), "+v"(gr[1]), "+v"(xg[0]), "+v"(xg[1])::"memory");
  {
    for (int spin = 0; spin < SPIN_MAX; ++spin) {
      if (__all(gr[0][1] == etag && gr[0][3] == etag && gr[1][1] == etag && gr[1][3] == etag)) break;
      __builtin_amdgcn_s_sleep(2);
      GRL();
      asm volatile("s_waitcnt vmcnt(0)" : "+v"(gr[0]), "+v"(gr[1])::"memory");
    }
#pragma unroll
    for (int it = 0; it < 2; ++it)
#pragma unroll
      for (int r = 0; r < 2; ++r)
        if (gr[it][2 * r + 1] != etag) gr[it][2 * r] = ln1_row(x, 2 * (tid + 512 * it) + r);
  }
  if (w == 0) STAMP(1, 6);
  const float dk[4] = {bf_lo(dkv.x), bf_hi(dkv.x), bf_lo(dkv.y), bf_hi(dkv.y)};
  float z[2][8];
#pragma unroll
  for (int it = 0; it < 2; ++it) {
    if (it == 1) asm volatile("s_waitcnt vmcnt(0)" : "+v"(xg[2]), "+v"(xg[3])::"memory");
    const int j = tid + 512 * it;
    const unsigned s0 = gr[it][0], s1 = gr[it][2];
    const float m0 = bf_lo(s0), r0 = __builtin_amdgcn_rcpf(bf_hi(s0)), m1 = bf_lo(s1), r1 = __builtin_amdgcn_rcpf(bf_hi(s1));
    const u2_t xa = xg[2 * it], xc = xg[2 * it + 1];
    const float x0[4] = {bf_lo(xa.x), bf_hi(xa.x), bf_lo(xa.y), bf_hi(xa.y)};
    const float x1[4] = {bf_lo(xc.x), bf_hi(xc.x), bf_lo(xc.y), bf_hi(xc.y)};
#pragma unroll
    for (int cc = 0; cc < 4; ++cc) {
      const float u0 = (x0[cc] - m0) * r0, u1 = (x1[cc] - m1) * r1;
      *(unsigned*)(smem + B_UOFF + cc * B_UCH + 4 * (1023 - j)) = pk_f16(u1, u0);
      z[it][cc] = x0[cc] + u0 * dk[cc];
      z[it][4 + cc] = x1[cc] + u1 * dk[cc];
    }
  }
  __syncthreads();
  if (w == 0) STAMP(1, 2);
  if (w >= 4 && w < 7 && cb * 3 + (w - 4) < 432) pack_wd(wd, ws, smem + B_PACK + (w - 4) * 5120, cb * 3 + (w - 4), lane);
  f4_t acc[8];
  if (w < 4) {
    const char* ub = smem + B_UOFF + w * B_UCH + 4112 - 32 * c - 16 * kg - 512 * 7;
    const char* kb = smem + B_KOFF + w * KDS + ((c & 1) ? 0 : KD_O) + 4 * (12 + ((c + 1) >> 1) - 4 * kg);
    const unsigned* dr = (const unsigned*)(smem + B_KOFF + w * KDS + KD_DR) + (127 - c + 4 * kg);
    const char* uw = smem + B_UOFF + w * B_UCH + 4112 - 32 * c - 16 * kg;
    auto rdB = [&](BFrag& F, int i0, int p) { F.B[p] = *(const h8_t*)(ub + 512 * (7 - p) + 64 * i0); };
    auto rdA = [&](BFrag& F, int i0, int i1, int half) {
      const unsigned* ap = (const unsigned*)(kb + 64 * i0 + 512 * i1) + 2 * half;
      F.A[i1][half] = u2_t{ap[0], ap[1]};
    };
    auto frag = [&](const u2_t (&a)[2]) { return u4_t{a[0].x, a[0].y, a[1].x, a[1].y}; };
    auto rdnext = [&](BFrag& F, int i0, int m) {
      if (m < 8) rdB(F, i0, m); else if (m < 16) rdA(F, i0, (m - 8) >> 1, (m - 8) & 1);
    };
    auto step = [&](auto i0c, const BFrag& F, BFrag& N, auto nextc) {
      constexpr int I0 = decltype(i0c)::value, NX = decltype(nextc)::value;
      int m = 0;
#pragma unroll
      for (int i1 = 0; i1 < 5; ++i1) {
        if (i1 == 4 && I0 != 0) continue;
        const u4_t a = frag(F.A[i1]);
#pragma unroll
        for (int t = i1; t < 8; ++t) {
          if (I0 == 0 && i1 == 0) mfma_f16_0(acc[t], a, F.B[t]); else mfma_f16(acc[t], a, F.B[t - i1]);
          if (NX >= 0) rdnext(N, NX, m);
          SB;
          ++m;
        }
      }
    };
    BFrag F0, F1;
    h8_t U9[4], U7[4];
#pragma unroll
    for (int bt = 0; bt < 4; ++bt) { U9[bt] = *(const h8_t*)(uw - 512 * bt + 16); U7[bt] = *(const h8_t*)(uw - 512 * bt); }
#pragma unroll
    for (int m = 0; m < 16; ++m) rdnext(F0, 0, m);
    rdA(F0, 0, 4, 0); rdA(F0, 0, 4, 1);
    SB;
    f4_t X9[4], X7[4];
#pragma unroll
    for (int bt = 0; bt < 4; ++bt) { mfma_f16_0(X9[bt], __builtin_bit_cast(u4_t, U9[bt]), band9); mfma_f16_0(X7[bt], __builtin_bit_cast(u4_t, U7[bt]), band7); }
    SB;
    step(IC(0), F0, F1, IC(1));
    h8_t Vf[4]; u4_t Tf[4];
    {
      int m = 0;
#pragma unroll
      for (int i1 = 0; i1 < 4; ++i1) {
        const u4_t a = frag(F1.A[i1]);
#pragma unroll
        for (int t = i1; t < 8; ++t) {
          mfma_f16(acc[t], a, F1.B[t - i1]);
          rdnext(F0, 2, m);
          if (m < 4) {
            const u4_t vu = {pk_f16(X7[m][0], X9[m][0]), pk_f16(X7[m][1], X9[m][1]), pk_f16(X7[m][2], X9[m][2]), pk_f16(X7[m][3], X9[m][3])};
            Vf[m] = __builtin_bit_cast(h8_t, vu);
          }
          if (m >= 16 && m < 20) { const unsigned* tp = dr - 16 * (m - 16 + 4); Tf[m - 16] = u4_t{tp[0], tp[1], tp[2], tp[3]}; }
          SB;
          ++m;
        }
      }
    }
    step(IC(2), F0, F1, IC(3));
#pragma unroll
    for (int bt = 0; bt < 4; ++bt)
#pragma unroll
      for (int at = 4 + bt; at < 8; ++at) mfma_f16(acc[at], __builtin_bit_cast(u4_t, Vf[bt]), __builtin_bit_cast(h8_t, Tf[at - bt - 4]));
    SB;
    step(IC(3), F1, F0, IC(4));
    step(IC(4), F0, F1, IC(5));
    step(IC(5), F1, F0, IC(6));
    step(IC(6), F0, F1, IC(7));
    step(IC(7), F1, F0, IC(-1));
    asm volatile("s_nop 7\n\ts_nop 1" : "+v"(acc[0]), "+v"(acc[1]), "+v"(acc[2]), "+v"(acc[3]), "+v"(acc[4]), "+v"(acc[5]), "+v"(acc[6]), "+v"(acc[7]));
  }
  if (w == 0) STAMP(1, 3);
  bar();
  if (w < 4) {
#pragma unroll
    for (int t = 0; t < 8; ++t) *(f4_t*)(smem + w * B_YCH + t * 1280 + kg * 320 + c * 16) = acc[t] * invs;
  }
  asm volatile("s_waitcnt lgkmcnt(0)" ::: "memory");
  bar();
#pragma unroll
  for (int it = 0; it < 2; ++it) {
    const int j = tid + 512 * it, t = 2 * j;
    const int yo = (t >> 8) * 1280 + ((t >> 2) & 3) * 320 + ((t >> 4) & 15) * 16 + (t & 3) * 4;
    float h0[4], h1[4];
#pragma unroll
    for (int cc = 0; cc < 4; ++cc) {
      f2_t y = *(const f2_t*)(smem + cc * B_YCH + yo);
      h0[cc] = z[it][cc] + y.x;
      h1[cc] = z[it][4 + cc] + y.y;
    }
    char* dst = ws + OFF_H8 + ((size_t)(q >> 1) * SL + t) * 16 + (q & 1) * 8;
    st8(dst, u2_t{pk_bf16(h0[0], h0[1]), pk_bf16(h0[2], h0[3])});
    st8(dst + 16, u2_t{pk_bf16(h1[0], h1[1]), pk_bf16(h1[2], h1[3])});
  }
  if (w == 0) STAMP(1, 7);
}

constexpr int C_T = 240;
constexpr int C_SLOT = 23552, C_NSLOT = 6, C_GOFF = 15360;
constexpr int C_STAT = C_NSLOT * C_SLOT;
constexpr int C_CS = C_STAT + 960;
constexpr int C_LDS = C_CS + 512;
constexpr int C_STEPS = 20;
#define KD_LINES 80
constexpr size_t OFF_DUMMY = 0x1140000;

__device__ __forceinline__ unsigned ln_pack(float sum, float sq) {
  const float mean = sum * (1.0f / DM);
  return ln_fin(mean, fmaxf(sq * (1.0f / DM) - mean * mean, 0.f));
}
__device__ __forceinline__ unsigned ln_row(const char* h8, int t) {
  float s = 0.f, q = 0.f;
  for (int pl = 0; pl < 80; ++pl) {
    const u4_t v = *(const u4_t*)(h8 + (size_t)pl * 32768 + t * 16);
    for (int j = 0; j < 4; ++j) { const float a = bf_lo(v[j]), b = bf_hi(v[j]); s += a + b; q += a * a + b * b; }
  }
  return ln_pack(s, q);
}

template <int NDMA>
__device__ __forceinline__ void c_loader(char* smem, char* ws, const char* wbase, int w4, int lane, int t0, int jline, int xcd) {
  const unsigned lds0 = (unsigned)(size_t)(LAS char*)smem;
  const char* h8 = ws + OFF_H8;
  unsigned voffH[4], voffW[2];
#pragma unroll
  for (int k = 0; k < 2; ++k) {
    const int r = 8 * (2 * (w4 & 1) + k) + (lane >> 3), c = lane & 7;
    const int f = ((r >> 1) & 1) | ((r >> 2) & 2);
    voffW[k] = r * (FF * 2) + 128 * jline + (((c >> 1) ^ f) * 32) + (c & 1) * 16;
  }
#pragma unroll
  for (int k = 0; k < 4; ++k) {
    const int idx = 64 * (w4 + 4 * k) + lane, pl = idx / C_T, tt = idx % C_T;
    const int t = min(t0 + tt, SL - 1);
    voffH[k] = pl * 32768 + t * 16;
  }
  const unsigned ldsW = C_GOFF + (w4 >> 1) * 4096 + (w4 & 1) * 2048;
  auto issue = [&](int step, unsigned so) {
#pragma unroll
    for (int k = 0; k < 2; ++k) glds16(wbase + (size_t)step * (32 * FF * 2), voffW[k], lds0 + so + ldsW + k * 1024);
#pragma unroll
    for (int k = 0; k < NDMA - 2; ++k) glds16(h8 + (size_t)step * (4 * 32768), voffH[k], lds0 + so + (w4 + 4 * k) * 1024);
  };
  constexpr unsigned S = C_SLOT;
  issue(0, 0); issue(1, S); issue(2, 2 * S); issue(3, 3 * S);
  wait_vm<3 * NDMA>(); bar();
  issue(4, 4 * S);
  wait_vm<2 * NDMA>(); bar();
  issue(5, 5 * S); issue(6, 0);
  unsigned sw = S;
#pragma unroll 1
  for (int j = 1; j < 7; ++j) {
    wait_vm<2 * NDMA>();
    bar();
    issue(2 * j + 5, sw);
    sw += S; if (sw == C_NSLOT * S) sw = 0;
    issue(2 * j + 6, sw);
    sw += S; if (sw == C_NSLOT * S) sw = 0;
  }
  wait_vm<2 * NDMA>(); bar();
  issue(19, sw);
  const int tl = min(60 * w4 + lane, C_T - 1), tg = min(t0 + tl, SL - 1);
  const gu64* gp = (const gu64*)(ws + OFF_G2) + tg;
  const float* csp = (const float*)(ws + OFF_CS) + ((lane >> 4) & 1) * FF + 64 * jline + 16 * w4 + (lane & 15);
  const char* tb = ws + OFF_WP2 + (size_t)(4 * (xcd & 1)) * (54 * 5120);
  const int tl0 = ((int)(blockIdx.x >> 3) * 4 + w4) * 68;
  u2_t gv; float csv; unsigned tv0, tv1, tv2;
  asm volatile("global_load_dwordx2 %0, %1, off sc1" : "=v"(gv) : "v"(gp) : "memory");
  asm volatile("global_load_dword %0, %1, off" : "=v"(csv) : "v"(csp) : "memory");
  asm volatile("global_load_dword %0, %1, off" : "=v"(tv0) : "v"(tb + (size_t)min(tl0 + lane, 8639) * 128) : "memory");
  asm volatile("global_load_dword %0, %1, off" : "=v"(tv1) : "v"(tb + (size_t)min(tl0 + 64 + (lane & 3), 8639) * 128) : "memory");
  {
    unsigned long long kd;
    asm volatile("s_getpc_b64 s[20:21]\n\ts_add_u32 s20, s20, kD@rel32@lo+4\n\ts_addc_u32 s21, s21, kD@rel32@hi+12\n\ts_mov_b64 %0, s[20:21]" : "=s"(kd) :: "s20", "s21", "scc");
    const char* tp = (lane == 0) ? (const char*)&g_ptrs[xcd] : (const char*)kd + min((int)(blockIdx.x >> 3) * 4 + w4, KD_LINES - 1) * 128;
    asm volatile("global_load_dword %0, %1, off" : "=v"(tv2) : "v"(tp) : "memory");
  }
  wait_vm<NDMA + 5>(); bar();
  asm volatile("s_waitcnt vmcnt(0)" : "+v"(gv), "+v"(csv), "+v"(tv0), "+v"(tv1), "+v"(tv2)::"memory");
  bar();
  {
    bool ok = gv.y == 1u;
    for (int spin = 0; spin < 4096 && !__all(ok); ++spin) {
      const unsigned long long g = __hip_atomic_load(gp, __ATOMIC_RELAXED, __HIP_MEMORY_SCOPE_AGENT);
      gv.x = (unsigned)g; ok = (unsigned)(g >> 32) == 1u;
      __builtin_amdgcn_s_sleep(2);
    }
    if (!__all(ok)) gv.x = ln_row(h8, tg);
    if (lane < 60) *(float*)(smem + C_STAT + (60 * w4 + lane) * 4) = bf_lo(gv.x);
    if (lane < 32) *(float*)(smem + C_CS + (lane >> 4) * 256 + (16 * w4 + (lane & 15)) * 4) = csv;
  }
  asm volatile("s_waitcnt lgkmcnt(0)" ::: "memory");
  bar();
}

struct CFrag { b8_t A[2][2]; b8_t B[8]; };

extern "C" __global__ __launch_bounds__(512, 2) void kC() {
  extern __shared__ __attribute__((aligned(16))) char smem[];
  char* __restrict__ ws = ptrs()->ws;
  const u16* __restrict__ wg = ptrs()->wg;
  const u16* __restrict__ wu = ptrs()->wu;
  const int tid = threadIdx.x, lane = tid & 63;
  const int w = __builtin_amdgcn_readfirstlane(tid >> 6);
  int panel, jline;
  const int xcd = blockIdx.x & 7, rank = blockIdx.x >> 3;
  {
    const int g4 = xcd >> 1, a = xcd & 1, nl = (g4 < 3) ? 7 : 6, total = 9 * nl, n0 = (total + 1) >> 1;
    if (a == 0 ? rank >= n0 : rank >= total - n0) {
      const int sp = (xcd < 6) ? (xcd >> 1) : 3 + 5 * (xcd - 6) + (rank - 27);
      const int wv = sp * 8 + w;
      if (w == 0) STAMP(2, 0);
      for (int tile = wv; tile < 128; tile += 104) {
        const int t = 16 * tile + (lane & 15);
        const char* hp = ws + OFF_H8 + (size_t)(lane >> 4) * 32768 + t * 16;
        u4_t v[20];
#pragma unroll
        for (int i = 0; i < 20; ++i) v[i] = *(const u4_t*)(hp + (size_t)i * (4 * 32768));
        float s0 = 0.f, s1 = 0.f, q0 = 0.f, q1 = 0.f;
        unsigned k_both = 0x3f803f80u;
        asm volatile("" : "+s"(k_both));
#pragma unroll
        for (int i = 0; i < 20; ++i) {
          s0 = dot2bf(v[i][0], k_both, s0); q0 = dot2bf(v[i][0], v[i][0], q0);
          s1 = dot2bf(v[i][1], k_both, s1); q1 = dot2bf(v[i][1], v[i][1], q1);
          s0 = dot2bf(v[i][2], k_both, s0); q0 = dot2bf(v[i][2], v[i][2], q0);
          s1 = dot2bf(v[i][3], k_both, s1); q1 = dot2bf(v[i][3], v[i][3], q1);
        }
        float s = s0 + s1, q = q0 + q1;
        s += __shfl_xor(s, 16); q += __shfl_xor(q, 16);
        s += __shfl_xor(s, 32); q += __shfl_xor(q, 32);
        if (lane < 16)
          __hip_atomic_store((gu64*)(ws + OFF_G2) + t, (1ull << 32) | ln_pack(s, q), __ATOMIC_RELAXED, __HIP_MEMORY_SCOPE_AGENT);
      }
      if (w == 0) STAMP(2, 7);
      return;
    }
    const int idx = rank + a * n0;
    panel = idx / nl; jline = 7 * g4 + idx % nl;
  }
  const int t0 = panel * C_T;
  if (w == 0) STAMP(2, 0);
  if (w >= 4) {
    const int w4 = w - 4;
    const char* wbase = (const char*)((w4 >> 1) ? wu : wg);
    if (w4 < 3) c_loader<6>(smem, ws, wbase, w4, lane, t0, jline, xcd);
    else c_loader<5>(smem, ws, wbase, w4, lane, t0, jline, xcd);
    return;
  }
  const int s = w >> 1, th = w & 1;
  const int fr = lane & 15, kg = lane >> 4, q = (lane & 15) >> 2, p = lane & 3;
  const unsigned hb0 = kg * 3840 + fr * 16 + 256 * 7;
  const unsigned hb1 = kg * 3840 + fr * 16 + 256 * (th ? 8 : 0);
  unsigned wa[2];
#pragma unroll
  for (int v = 0; v < 2; ++v) {
    const int sub = 2 * s + (v ? 1 - th : th);
    const int f = (q >> 1) | (2 * (kg & 1));
    wa[v] = C_GOFF + (8 * kg + q) * 128 + ((sub ^ f) * 32) + 8 * p;
  }
  f4_t ag[8][2], au[8][2];
#pragma unroll
  for (int u = 0; u < 8; ++u)
#pragma unroll
    for (int v = 0; v < 2; ++v) { ag[u][v] = f4_t{0.f, 0.f, 0.f, 0.f}; au[u][v] = f4_t{0.f, 0.f, 0.f, 0.f}; }
  auto load = [&](CFrag& F, unsigned so) {
    const char* sp = smem + so;
#pragma unroll
    for (int v = 0; v < 2; ++v)
#pragma unroll
      for (int m = 0; m < 2; ++m) {
        s4_t lo = tr_read(sp + wa[v] + m * 4096), hi = tr_read(sp + wa[v] + m * 4096 + 512);
        u2_t a = __builtin_bit_cast(u2_t, lo), b = __builtin_bit_cast(u2_t, hi);
        F.A[v][m] = __builtin_bit_cast(b8_t, (u4_t{a.x, a.y, b.x, b.y}));
      }
    F.B[0] = *(const b8_t*)(sp + hb0);
#pragma unroll
    for (int u = 1; u < 8; ++u) F.B[u] = *(const b8_t*)(sp + hb1 + 256 * (u - 1));
  };
  auto mma = [&](const CFrag& F) {
    ag[0][0] = __builtin_amdgcn_mfma_f32_16x16x32_bf16(F.A[0][0], F.B[0], ag[0][0], 0, 0, 0);
    au[0][0] = __builtin_amdgcn_mfma_f32_16x16x32_bf16(F.A[0][1], F.B[0], au[0][0], 0, 0, 0);
#pragma unroll
    for (int u = 1; u < 8; ++u)
#pragma unroll
      for (int v = 0; v < 2; ++v) {
        ag[u][v] = __builtin_amdgcn_mfma_f32_16x16x32_bf16(F.A[v][0], F.B[u], ag[u][v], 0, 0, 0);
        au[u][v] = __builtin_amdgcn_mfma_f32_16x16x32_bf16(F.A[v][1], F.B[u], au[u][v], 0, 0, 0);
      }
  };
  auto interleave = [&]() {
#pragma unroll
    for (int i = 0; i < 16; ++i) {
      __builtin_amdgcn_sched_group_barrier(0x008, 1, 0);
      __builtin_amdgcn_sched_group_barrier(0x100, 1, 0);
    }
    __builtin_amdgcn_sched_group_barrier(0x008, 14, 0);
  };
  CFrag F0, F1;
  bar();
  if (w == 0) STAMP(2, 1);
  load(F0, 0);
  unsigned sn = C_SLOT;
  SB;
#pragma unroll 1
  for (int j = 0; j < 9; ++j) {
    asm volatile("s_waitcnt lgkmcnt(0)" ::: "memory");
    bar(); SB;
    load(F1, sn);
    mma(F0);
    interleave();
    SB;
    sn += C_SLOT; if (sn == C_NSLOT * C_SLOT) sn = 0;
    load(F0, sn);
    mma(F1);
    interleave();
    SB;
    sn += C_SLOT; if (sn == C_NSLOT * C_SLOT) sn = 0;
  }
  asm volatile("s_waitcnt lgkmcnt(0)" ::: "memory");
  bar(); SB;
  load(F1, sn);
  mma(F0);
  interleave();
  SB;
  bar(); SB;
  f4_t csg[2], csu[2];
  float mu[8];
#pragma unroll
  for (int v = 0; v < 2; ++v) {
    const int sub = 2 * s + (v ? 1 - th : th);
    csg[v] = *(const f4_t*)(smem + C_CS + (16 * sub + 4 * kg) * 4);
    csu[v] = *(const f4_t*)(smem + C_CS + 256 + (16 * sub + 4 * kg) * 4);
  }
#pragma unroll
  for (int u = 0; u < 8; ++u) {
    const int tile = (u == 0) ? 7 : (th ? 7 + u : u - 1);
    mu[u] = *(const float*)(smem + C_STAT + (16 * tile + fr) * 4);
  }
  mma(F1);
  if (w == 0) STAMP(2, 2);
  char* hh = ws + OFF_HH + (size_t)(2 * jline + s) * 1024 + lane * 16;
  char* sink = ws + OFF_DUMMY + lane * 16;
  auto epi = [&](auto thc) {
    constexpr int TH = decltype(thc)::value;
#pragma unroll
    for (int u = 0; u < 8; ++u) {
      const int tile = (u == 0) ? 7 : (TH ? 7 + u : u - 1);
      const int t16 = 15 * panel + tile;
      char* dst = (t16 < 128) ? hh + (size_t)t16 * (54 * 1024) : sink;
      u2_t o[2];
#pragma unroll
      for (int v = 0; v < 2; ++v) {
        if (u == 0 && v == 1) continue;
        const f4_t r = (ag[u][v] - mu[u] * csg[v]) * (au[u][v] - mu[u] * csu[v]);
        o[v] = u2_t{pk_bf16(r[0], r[1]), pk_bf16(r[2], r[3])};
      }
      if (u == 0) st8(dst + 8 * TH, o[0]);
      else st16(dst, u4_t{o[TH].x, o[TH].y, o[1 - TH].x, o[1 - TH].y});
    }
  };
  if (th) epi(IC(1)); else epi(IC(0));
  if (w == 0) STAMP(2, 7);
}

constexpr int D_SLOT = 15360, D_NSLOT = 9, D_STEPS = 18;
constexpr int D_LDS = D_NSLOT * D_SLOT;

template <int NDMA>
__device__ __forceinline__ void d_loader(char* smem, const char* wp, int w4, int lane) {
  const unsigned lds0 = (unsigned)(size_t)(LAS char*)smem;
  const unsigned voff = lane * 16;
  auto issue = [&](int step, unsigned so) {
#pragma unroll
    for (int k = 0; k < NDMA; ++k) {
      const int i = w4 + 4 * k;
      glds16(wp + (size_t)step * D_SLOT + i * 1024, voff, lds0 + so + i * 1024);
    }
  };
  constexpr unsigned S = D_SLOT;
  issue(0, 0); issue(1, S); issue(2, 2 * S);
  wait_vm<2 * NDMA>(); bar();
  issue(3, 3 * S); issue(4, 4 * S); issue(5, 5 * S);
  wait_vm<3 * NDMA>(); bar();
  issue(6, 6 * S); issue(7, 7 * S); issue(8, 8 * S);
  unsigned sw = 0;
#pragma unroll 1
  for (int j = 1; j < 5; ++j) {
    wait_vm<4 * NDMA>();
    bar();
    issue(2 * j + 7, sw);
    sw += S; if (sw == D_NSLOT * S) sw = 0;
    issue(2 * j + 8, sw);
    sw += S; if (sw == D_NSLOT * S) sw = 0;
  }
  wait_vm<4 * NDMA>(); bar();
  issue(17, sw);
  wait_vm<3 * NDMA>(); bar();
  wait_vm<1 * NDMA>(); bar();
  wait_vm<0>(); bar();
}

extern "C" __global__ __launch_bounds__(512, 2) void kD() {
  extern __shared__ __attribute__((aligned(16))) char smem[];
  char* __restrict__ ws = ptrs()->ws;
  u16* __restrict__ out = ptrs()->out;
  const int tid = threadIdx.x, lane = tid & 63;
  const int w = __builtin_amdgcn_readfirstlane(tid >> 6);
  const int xcd = blockIdx.x & 7, rank = blockIdx.x >> 3;
  const int it = 8 * (xcd >> 1) + (rank & 7), dg = 4 * (xcd & 1) + (rank >> 3);
  if (w == 0) STAMP(3, 0);
  if (w >= 4) {
    const int w4 = w - 4;
    const char* wp = ws + OFF_WP2 + (size_t)dg * (54 * 5120);
    if (w4 < 3) d_loader<4>(smem, wp, w4, lane);
    else d_loader<3>(smem, wp, w4, lane);
    return;
  }
  const int fr = lane & 15, g = lane >> 4;
  const int t = 64 * it + 16 * w + fr;
  const char* hp = ws + OFF_HH + (size_t)(4 * it + w) * (54 * 1024);
  const unsigned voff = lane * 16;
  u4_t R[18];
#pragma unroll
  for (int i = 0; i < 9; ++i)
    asm volatile("global_load_dwordx4 %0, %1, %2" : "=v"(R[i]) : "v"(voff), "s"(hp + (size_t)i * 1024) : "memory");
  u2_t res[5]; unsigned st;
  f4_t acc[5];
#pragma unroll
  for (int dt = 0; dt < 5; ++dt) acc[dt] = f4_t{0.f, 0.f, 0.f, 0.f};
  b8_t A0[5], A1[5];
  auto rdA = [&](b8_t (&A)[5], unsigned off) {
    const char* sp = smem + off + lane * 16;
#pragma unroll
    for (int dt = 0; dt < 5; ++dt) A[dt] = *(const b8_t*)(sp + dt * 1024);
  };
  bar();
  if (w == 0) STAMP(3, 1);
  rdA(A0, 0);
  unsigned so = 0;
  auto kstep = [&](auto mode, auto idx, b8_t (&Ac)[5], b8_t (&An)[5], const char* hnext) {
    constexpr int MODE = decltype(mode)::value, i = decltype(idx)::value, kk = i % 3;
    if (i % 6 == 0) bar();
    unsigned nx = so + (kk + 1) * 5120;
    if (kk == 2) { so += D_SLOT; if (so == D_NSLOT * D_SLOT) so = 0; nx = so; }
    SB;
    if (!(MODE == 2 && i == 17)) rdA(An, nx);
    constexpr int NV = (MODE == 0) ? (i < 9 ? 8 + i : 26 - i) : (MODE == 1) ? (i < 9 ? 25 - i : 17) : (i == 0 ? 17 : 23 - i);
    asm volatile("s_waitcnt vmcnt(%1)" : "+v"(R[i]) : "i"(NV) : "memory");
    const b8_t B = __builtin_bit_cast(b8_t, R[i]);
#pragma unroll
    for (int dt = 0; dt < 5; ++dt) acc[dt] = __builtin_amdgcn_mfma_f32_16x16x32_bf16(Ac[dt], B, acc[dt], 0, 0, 0);
    SB;
    if (MODE == 0 && i < 9)
      asm volatile("global_load_dwordx4 %0, %1, %2" : "=v"(R[i < 9 ? 9 + i : 0]) : "v"(voff), "s"(hp + (size_t)(9 + i) * 1024) : "memory");
    if (MODE != 2) asm volatile("global_load_dwordx4 %0, %1, %2" : "+v"(R[i]) : "v"(voff), "s"(hnext + (size_t)i * 1024) : "memory");
    if (MODE == 2 && i == 0) {
#pragma unroll
      for (int dt = 0; dt < 5; ++dt) {
        const int d = 80 * dg + 16 * dt + 4 * g;
        const char* rp = ws + OFF_H8 + ((size_t)(d >> 3) * SL + t) * 16 + (d & 4) * 2;
        asm volatile("global_load_dwordx2 %0, %1, off" : "=v"(res[dt]) : "v"(rp) : "memory");
      }
      asm volatile("global_load_dword %0, %1, off" : "=v"(st) : "v"(ws + OFF_G2 + (size_t)t * 8) : "memory");
    }
    SB;
  };
  auto round = [&](auto mode, const char* hnext) {
    kstep(mode, IC(0), A0, A1, hnext); kstep(mode, IC(1), A1, A0, hnext); kstep(mode, IC(2), A0, A1, hnext);
    kstep(mode, IC(3), A1, A0, hnext); kstep(mode, IC(4), A0, A1, hnext); kstep(mode, IC(5), A1, A0, hnext);
    kstep(mode, IC(6), A0, A1, hnext); kstep(mode, IC(7), A1, A0, hnext); kstep(mode, IC(8), A0, A1, hnext);
    kstep(mode, IC(9), A1, A0, hnext); kstep(mode, IC(10), A0, A1, hnext); kstep(mode, IC(11), A1, A0, hnext);
    kstep(mode, IC(12), A0, A1, hnext); kstep(mode, IC(13), A1, A0, hnext); kstep(mode, IC(14), A0, A1, hnext);
    kstep(mode, IC(15), A1, A0, hnext); kstep(mode, IC(16), A0, A1, hnext); kstep(mode, IC(17), A1, A0, hnext);
  };
  round(IC(0), hp + 18 * 1024);
  round(IC(1), hp + 36 * 1024);
  round(IC(2), hp);
  if (w == 0) STAMP(3, 2);
  asm volatile("s_waitcnt vmcnt(0)" : "+v"(res[0]), "+v"(res[1]), "+v"(res[2]), "+v"(res[3]), "+v"(res[4]), "+v"(st)::"memory");
  const float sd = bf_hi(st), r2 = 1.0f / (sd * sd);
#pragma unroll
  for (int dt = 0; dt < 5; ++dt) {
    const int d = 80 * dg + 16 * dt + 4 * g;
    const float h0 = bf_lo(res[dt].x), h1 = bf_hi(res[dt].x), h2 = bf_lo(res[dt].y), h3 = bf_hi(res[dt].y);
    st8(out + (size_t)t * DM + d, u2_t{pk_bf16(h0 + r2 * acc[dt][0], h1 + r2 * acc[dt][1]), pk_bf16(h2 + r2 * acc[dt][2], h3 + r2 * acc[dt][3])});
  }
  if (w == 0) STAMP(3, 7);
}

extern "C" void kernel_launch(void* const* d_in, const int* in_sizes, int n_in, void* d_out, int out_size, void* d_ws,
                              size_t ws_size, hipStream_t stream) {
  char* ws = (char*)d_ws;
  (void)hipFuncSetAttribute((const void*)kB, hipFuncAttributeMaxDynamicSharedMemorySize, B_LDS);
  (void)hipFuncSetAttribute((const void*)kC, hipFuncAttributeMaxDynamicSharedMemorySize, C_LDS);
  (void)hipFuncSetAttribute((const void*)kD, hipFuncAttributeMaxDynamicSharedMemorySize, D_LDS);
  kB<<<256, 512, B_LDS, stream>>>((const u16*)d_in[0], (const u16*)d_in[1], (const u16*)d_in[2], (const u16*)d_in[3], (const u16*)d_in[4],
                                  (const u16*)d_in[5], (const u16*)d_in[6], (const u16*)d_in[7], (const u16*)d_in[8], (const u16*)d_in[10],
                                  (const u16*)d_in[11], (const u16*)d_in[12], (const u16*)d_out, ws);
  kC<<<256, 512, C_LDS, stream>>>();
  kD<<<256, 512, D_LDS, stream>>>();
}
```

```cpp
#define SELFPF
#include <hip/hip_runtime.h>
#include <hip/hip_bf16.h>
#include <stdint.h>
#include <type_traits>

typedef unsigned short u16;
typedef _Float16 h2_t __attribute__((ext_vector_type(2)));
typedef _Float16 h8_t __attribute__((ext_vector_type(8)));
typedef __bf16 b2_t __attribute__((ext_vector_type(2)));
typedef __bf16 b8_t __attribute__((ext_vector_type(8)));
typedef float f2_t __attribute__((ext_vector_type(2)));
typedef float f4_t __attribute__((ext_vector_type(4)));
typedef unsigned u2_t __attribute__((ext_vector_type(2)));
typedef unsigned u4_t __attribute__((ext_vector_type(4)));
typedef short s4_t __attribute__((ext_vector_type(4)));
typedef __attribute__((address_space(1))) unsigned long long gu64;
#define LAS __attribute__((address_space(3)))
#define SB __builtin_amdgcn_sched_barrier(0)
#define IC(n) std::integral_constant<int, n>{}

constexpr int SL = 2048;
constexpr int DM = 640;
constexpr int FF = 1728;

constexpr size_t OFF_G1 = 0;
constexpr size_t OFF_EP = 0x10000;
constexpr size_t OFF_RD = 0x10100;
constexpr size_t OFF_H8 = 0x5A0000;
constexpr size_t OFF_HH = 0x820000;
constexpr size_t OFF_WP2 = 0xF00000;
constexpr size_t OFF_CS = 0x1120000;
constexpr size_t OFF_G2 = 0x1130000;
constexpr int KRS = 1344;
constexpr int KDS = 4864;
constexpr int KD_O = 2240;
constexpr int KD_DR = 4384;

#define STAMP(k, i)

__device__ __forceinline__ float bf_lo(unsigned d) { return __uint_as_float(d << 16); }
__device__ __forceinline__ float bf_hi(unsigned d) { return __uint_as_float(d & 0xffff0000u); }
__device__ __forceinline__ float bf2f(u16 v) { return __uint_as_float((unsigned)v << 16); }
__device__ __forceinline__ unsigned pk_bf16(float a, float b) {
  f2_t v = {a, b};
  return __builtin_bit_cast(unsigned, __builtin_convertvector(v, b2_t));
}
__device__ __forceinline__ unsigned pk_f16(float a, float b) {
  f2_t v = {a, b};
  return __builtin_bit_cast(unsigned, __builtin_convertvector(v, h2_t));
}
__device__ __forceinline__ float rbf(float a) { return bf_lo(pk_bf16(a, 0.f)); }
template <int N> __device__ __forceinline__ float dpp_ror_add(float v) {
  int r = __builtin_amdgcn_update_dpp(0, __float_as_int(v), 0x120 + N, 0xf, 0xf, false);
  return v + __int_as_float(r);
}
__device__ __forceinline__ float row16_sum(float v) {
  v = dpp_ror_add<8>(v); v = dpp_ror_add<4>(v); v = dpp_ror_add<2>(v); v = dpp_ror_add<1>(v);
  return v;
}
__device__ __forceinline__ float wave_sum(float v) {
  v = row16_sum(v);
  const int b = __float_as_int(v);
  return (__int_as_float(__builtin_amdgcn_readlane(b, 0)) + __int_as_float(__builtin_amdgcn_readlane(b, 16))) +
         (__int_as_float(__builtin_amdgcn_readlane(b, 32)) + __int_as_float(__builtin_amdgcn_readlane(b, 48)));
}
template <typename T> __device__ __forceinline__ T ldnt(const T* p) { return __builtin_nontemporal_load(p); }
__device__ __forceinline__ void wave_fence() {
  __builtin_amdgcn_fence(__ATOMIC_RELEASE, "wavefront");
  __builtin_amdgcn_wave_barrier();
  __builtin_amdgcn_fence(__ATOMIC_ACQUIRE, "wavefront");
}
__device__ __forceinline__ s4_t tr_read(const char* p) {
  return __builtin_amdgcn_ds_read_tr16_b64_v4i16((LAS s4_t*)p);
}
__device__ __forceinline__ void glds16(const void* sbase, unsigned voff, unsigned lds_addr) {
  asm volatile("s_mov_b32 m0, %2\n\ts_nop 0\n\tglobal_load_lds_dwordx4 %0, %1" ::"v"(voff), "s"(sbase), "s"(lds_addr) : "memory");
}
template <int N> __device__ __forceinline__ void wait_vm() { asm volatile("s_waitcnt vmcnt(%0)" ::"i"(N) : "memory"); }
__device__ __forceinline__ void bar() { asm volatile("s_barrier" ::: "memory"); }
__device__ __forceinline__ float dot2bf(unsigned a, unsigned b, float c) {
  return __builtin_amdgcn_fdot2_f32_bf16(__builtin_bit_cast(b2_t, a), __builtin_bit_cast(b2_t, b), c, false);
}

#define ST_MODE 0
__device__ __forceinline__ void st16(void* p, u4_t v) {
  *(u4_t*)p = v;
}
__device__ __forceinline__ void st8(void* p, u2_t v) {
  *(u2_t*)p = v;
}

struct __attribute__((aligned(128))) Ptrs { char* ws; const u16* wg; const u16* wu; u16* out; void* pad[12]; };
__device__ Ptrs g_ptrs[8];
typedef const __attribute__((address_space(4))) Ptrs* cptrs_t;
__device__ __forceinline__ cptrs_t ptrs() { return (cptrs_t)(unsigned long long)&g_ptrs[blockIdx.x & 7]; }

struct Cw { f2_t w, ws; };
__device__ __forceinline__ Cw mkw(f2_t w) { Cw r; r.w = w; r.ws = f2_t{-w.y, w.x}; return r; }
__device__ __forceinline__ f2_t cmulw(f2_t a, const Cw& W) {
  return __builtin_elementwise_fma(f2_t{a.y, a.y}, W.ws, f2_t{a.x, a.x} * W.w);
}
__device__ __forceinline__ f2_t cexp_l(float er, float wr, float l) {
  float mag = __builtin_amdgcn_exp2f(er * l);
  float rev = __builtin_amdgcn_fractf(wr * l);
  return f2_t{mag * __builtin_amdgcn_cosf(rev), mag * __builtin_amdgcn_sinf(rev)};
}
__device__ __forceinline__ float pow2_scale(float bound, int target) {
  unsigned e = (__float_as_uint(bound) >> 23) & 0xff;
  int k = target - ((int)e - 126);
  k = k > 126 ? 126 : (k < -126 ? -126 : k);
  return (bound > 0.f) ? __uint_as_float((unsigned)(k + 127) << 23) : 1.0f;
}
__device__ __forceinline__ float pow2_inv(float p) { return __uint_as_float(0x7f000000u - __float_as_uint(p)); }

constexpr int B_SIDE = 96;
constexpr int KG_R = 0, KG_L0 = 4352, KG_L1 = 8704, KG_W = 8704 + 25 * 272;
constexpr int B_BND = 8 * KG_W;
constexpr int B_KC = B_BND + 64;
constexpr int B_LDS = B_KC + 4 * KRS;
constexpr int B_KOFF = 0;
constexpr int B_UOFF = 4 * KDS;
constexpr int B_UCH = 4608;
constexpr int B_YCH = 10240;
constexpr int B_PACK = 40960;

__device__ __forceinline__ void pack_wd(const u16* __restrict__ wd, char* __restrict__ ws, char* buf, int u, int lane) {
  const int ks = u >> 3, dg = u & 7;
#pragma unroll
  for (int i = 0; i < 5; ++i) {
    const int idx = i * 64 + lane, row = idx / 10, c = idx % 10;
    __builtin_amdgcn_global_load_lds((const unsigned*)((const char*)wd + (size_t)(32 * ks + row) * 1280 + 160 * dg + 16 * c),
                                     (LAS unsigned*)(buf + i * 1024), 16, 0, 2);
  }
  asm volatile("s_waitcnt vmcnt(0)" ::: "memory");
  wave_fence();
  const int g = lane >> 4, q = (lane & 15) >> 2, p = lane & 3;
  char* dst = ws + OFF_WP2 + ((size_t)(dg * 54 + ks) * 5) * 1024 + lane * 16;
#pragma unroll
  for (int dt = 0; dt < 5; ++dt) {
    s4_t aa = tr_read(buf + (4 * g + q) * 160 + 32 * dt + 8 * p);
    s4_t bb = tr_read(buf + (16 + 4 * g + q) * 160 + 32 * dt + 8 * p);
    u2_t ua = __builtin_bit_cast(u2_t, aa), ub = __builtin_bit_cast(u2_t, bb);
    const u4_t pv = {ua.x, ua.y, ub.x, ub.y};
    asm volatile("global_store_dwordx4 %0, %1, off sc1\n\ts_nop 1" ::"v"(dst + dt * 1024), "v"(pv) : "memory");
  }
}

struct BFrag { h8_t B[8]; u2_t A[5][2]; };
__device__ __forceinline__ void mfma_f16(f4_t& d, u4_t a, h8_t b) {
  asm volatile("v_mfma_f32_16x16x32_f16 %0, %1, %2, %0" : "+v"(d) : "v"(a), "v"(b));
}
__device__ __forceinline__ void mfma_f16_0(f4_t& d, u4_t a, h8_t b) {
  asm volatile("v_mfma_f32_16x16x32_f16 %0, %1, %2, 0" : "=&v"(d) : "v"(a), "v"(b));
}
__device__ __forceinline__ unsigned ln_fin(float mean, float var) {
  const float mb = rbf(mean), sd = rbf(sqrtf(rbf(rbf(var) + 1e-5f)));
  return (__float_as_uint(mb) >> 16) | (__float_as_uint(sd) & 0xffff0000u);
}
__device__ __forceinline__ unsigned ln1_row(const u16* __restrict__ x, int t) {
  const u4_t* p = (const u4_t*)(x + (size_t)t * DM);
  float s = 0.f, q = 0.f;
  for (int i = 0; i < 80; ++i) { const u4_t v = p[i]; for (int j = 0; j < 4; ++j) s += bf_lo(v[j]) + bf_hi(v[j]); }
  const float mean = s * (1.0f / DM);
  for (int i = 0; i < 80; ++i) {
    const u4_t v = p[i];
    for (int j = 0; j < 4; ++j) { const float a = bf_lo(v[j]) - mean, b = bf_hi(v[j]) - mean; q += a * a; q += b * b; }
  }
  return ln_fin(mean, q * (1.0f / DM));
}
constexpr int SPIN_MAX = 1 << 14;

extern "C" __global__ __launch_bounds__(512) void kB(const u16* __restrict__ x, const u16* __restrict__ Are, const u16* __restrict__ Aim,
                                                     const u16* __restrict__ Bre, const u16* __restrict__ Bim, const u16* __restrict__ Cre,
                                                     const u16* __restrict__ Cim, const u16* __restrict__ logdt, const u16* __restrict__ dskip,
                                                     const u16* __restrict__ wg, const u16* __restrict__ wu, const u16* __restrict__ wd,
                                                     const u16* __restrict__ outp, char* __restrict__ ws) {
  extern __shared__ __attribute__((aligned(16))) char smem[];
  const int tid = threadIdx.x, lane = tid & 63;
  const int w = __builtin_amdgcn_readfirstlane(tid >> 6);
  const int xcd = blockIdx.x & 7;
#define EPLOAD(v) asm volatile("global_load_dwordx2 %0, %1, %2 sc1" : "=v"(v) : "v"(0u), "s"(ws + OFF_EP) : "memory")
  if (w == 0) STAMP(1, 0);
  auto post_epoch = [&](u2_t ep, int off) {
    if (tid == 0) {
      const unsigned long long e = (((unsigned long long)ep.y << 32) | ep.x) + 1ull;
      *(unsigned long long*)(smem + off) = e;
      *(unsigned*)(smem + off + 8) = (unsigned)((e * 0x9E3779B97F4A7C15ull) >> 32);
      __hip_atomic_store((gu64*)(ws + OFF_RD) + blockIdx.x, e, __ATOMIC_RELAXED, __HIP_MEMORY_SCOPE_AGENT);
    }
  };
  if (blockIdx.x < B_SIDE) {
    const int rk = blockIdx.x >> 3;
    const int g4 = xcd >> 1, a = xcd & 1, nl = (g4 < 3) ? 7 : 6;
    constexpr int S_EP = 17408;
    unsigned pf[2] = {0u, 0u};
    if (rk >= 8) {
#pragma unroll
      for (int k = 0; k < 2; ++k) {
        const int id = (((rk - 8) * 8 + w) * 2 + k) * 64 + lane;
        asm volatile("global_load_dword %0, %1, off" : "=v"(pf[k]) : "v"((const char*)x + (size_t)(id >> 1) * (DM * 2) + (((160 * xcd) >> 7) + (id & 1)) * 128) : "memory");
      }
    }
    if (rk < 8) {
      const int r = lane >> 4, c = lane & 15, t = (blockIdx.x * 8 + w) * 4 + r;
      const u4_t* p = (const u4_t*)(x + (size_t)t * DM);
      u2_t ep;
      EPLOAD(ep);
      u4_t v[5];
#pragma unroll
      for (int i = 0; i < 5; ++i) v[i] = ldnt(p + c + 16 * i);
      float s = 0.f;
#pragma unroll
      for (int i = 0; i < 5; ++i)
#pragma unroll
        for (int j = 0; j < 4; ++j) s += bf_lo(v[i][j]) + bf_hi(v[i][j]);
      asm volatile("" : "+v"(ep) : "v"(s));
      post_epoch(ep, S_EP);
      const float mean = row16_sum(s) * (1.0f / DM);
      float q = 0.f;
#pragma unroll
      for (int i = 0; i < 5; ++i)
#pragma unroll
        for (int j = 0; j < 4; ++j) {
          const float aa = bf_lo(v[i][j]) - mean, bb = bf_hi(v[i][j]) - mean;
          q += aa * aa; q += bb * bb;
        }
      const unsigned sv = ln_fin(mean, row16_sum(q) * (1.0f / DM));
      __syncthreads();
      const u2_t e = *(const u2_t*)(smem + S_EP);
      if (c == 0) {
        __hip_atomic_store((gu64*)(ws + OFF_G1) + t, ((unsigned long long)*(const unsigned*)(smem + S_EP + 8) << 32) | sv, __ATOMIC_RELAXED, __HIP_MEMORY_SCOPE_AGENT);
        ((unsigned long long*)(ws + OFF_G2))[t] = 0ull;
      }
      if (w == 0) STAMP(1, 1);
      if (rk == 0 && w == 7) {
        gu64* gp = (gu64*)(unsigned long long)&g_ptrs[xcd];
        if (lane < 4) {
          const unsigned long long v = lane == 0 ? (unsigned long long)ws : lane == 1 ? (unsigned long long)wg : lane == 2 ? (unsigned long long)wu : (unsigned long long)outp;
          __hip_atomic_store(gp + lane, v, __ATOMIC_RELAXED, __HIP_MEMORY_SCOPE_AGENT);
        }
        unsigned back;
        asm volatile("s_waitcnt vmcnt(0)\n\tglobal_load_dword %0, %1, off\n\ts_waitcnt vmcnt(0)" : "=&v"(back) : "v"(gp) : "memory");
        asm volatile("" ::"v"(back));
      }
      if (rk == 4 && w == 0) {
        unsigned long long a0, a1;
        asm volatile("s_getpc_b64 s[20:21]\n\ts_add_u32 s20, s20, kB@rel32@lo+4\n\ts_addc_u32 s21, s21, kB@rel32@hi+12\n\ts_mov_b64 %0, s[20:21]" : "=s"(a0) :: "s20", "s21", "scc");
        asm volatile("s_getpc_b64 s[20:21]\n\ts_add_u32 s20, s20, kD@rel32@lo+4\n\ts_addc_u32 s21, s21, kD@rel32@hi+12\n\ts_mov_b64 %0, s[20:21]" : "=s"(a1) :: "s20", "s21", "scc");
        const unsigned long long lo = a0 < a1 ? a0 : a1, last = ((a0 < a1 ? a1 - a0 : a0 - a1) + 8192) & ~127ull;
        unsigned cf[8];
#pragma unroll
        for (int i = 0; i < 8; ++i) {
          const unsigned long long o = (unsigned long long)(lane + 64 * i) * 128;
          asm volatile("global_load_dword %0, %1, off" : "=v"(cf[i]) : "v"(lo + (o < last ? o : last)) : "memory");
        }
        asm volatile("s_waitcnt vmcnt(0)" ::"v"(cf[0]), "v"(cf[1]), "v"(cf[2]), "v"(cf[3]), "v"(cf[4]), "v"(cf[5]), "v"(cf[6]), "v"(cf[7]) : "memory");
      }
      if (blockIdx.x == 63 && w == 7) {
        const unsigned long long e64 = ((unsigned long long)e.y << 32) | e.x;
        for (int spin = 0; spin < SPIN_MAX; ++spin) {
          bool ok = true;
#pragma unroll
          for (int i = 0; i < 4; ++i) ok &= __hip_atomic_load((const gu64*)(ws + OFF_RD) + lane + 64 * i, __ATOMIC_RELAXED, __HIP_MEMORY_SCOPE_AGENT) == e64;
          if (__all(ok)) break;
          __builtin_amdgcn_s_sleep(4);
        }
        if (lane == 0) __hip_atomic_store((gu64*)(ws + OFF_EP), e64, __ATOMIC_RELAXED, __HIP_MEMORY_SCOPE_AGENT);
      }
    }
    const int strip = (rk >= 8) ? rk - 8 : 4 + rk;
    if ((rk >= 8 || rk < 3) && strip < nl) {
      const int jl = 7 * g4 + strip;
      const char* pm = (const char*)(a ? wu : wg) + (size_t)(80 * w + (lane >> 3)) * (FF * 2) + 128 * jl + (lane & 7) * 16;
      u2_t ep;
      EPLOAD(ep);
      u4_t v[10];
#pragma unroll
      for (int i = 0; i < 10; ++i) v[i] = ldnt((const u4_t*)(pm + (size_t)i * 8 * (FF * 2)));
      const char* po = (const char*)(a ? wg : wu) + (size_t)(80 * w) * (FF * 2) + 128 * jl;
      unsigned t0v, t1v;
      asm volatile("global_load_dword %0, %1, off nt" : "=v"(t0v) : "v"(po + (size_t)lane * (FF * 2)) : "memory");
      asm volatile("global_load_dword %0, %1, off nt" : "=v"(t1v) : "v"(po + (size_t)(64 + (lane & 15)) * (FF * 2)) : "memory");
      f2_t s[4];
#pragma unroll
      for (int e = 0; e < 4; ++e) s[e] = f2_t{0.f, 0.f};
#pragma unroll
      for (int i = 0; i < 10; ++i)
#pragma unroll
        for (int jj = 0; jj < 4; ++jj) s[jj] += f2_t{bf_lo(v[i][jj]), bf_hi(v[i][jj])};
      asm volatile("" : "+v"(ep), "+v"(pf[0]), "+v"(pf[1]) : "v"(s[0]));
      if (rk >= 8) post_epoch(ep, S_EP);
      float* pp = (float*)smem;
      *(f4_t*)(pp + (w * 8 + (lane >> 3)) * 68 + (lane & 7) * 8) = f4_t{s[0].x, s[0].y, s[1].x, s[1].y};
      *(f4_t*)(pp + (w * 8 + (lane >> 3)) * 68 + (lane & 7) * 8 + 4) = f4_t{s[2].x, s[2].y, s[3].x, s[3].y};
      __syncthreads();
      float acc = 0.f;
#pragma unroll
      for (int i = 0; i < 8; ++i) acc += pp[(w * 8 + i) * 68 + lane];
      __syncthreads();
      pp[w * 64 + lane] = acc;
      __syncthreads();
      if (w == 0) {
        float t = 0.f;
#pragma unroll
        for (int i = 0; i < 8; ++i) t += pp[i * 64 + lane];
        ((float*)(ws + OFF_CS))[a * FF + 64 * jl + lane] = t;
      }
      asm volatile("s_waitcnt vmcnt(0)" ::"v"(t0v), "v"(t1v) : "memory");
    }
    if (w == 0) STAMP(1, 7);
    return;
  }
  const int cb = blockIdx.x - B_SIDE;
  const int q = 20 * xcd + (cb >> 3);
  const int cl = w >> 1, hw = w & 1, ch = 4 * q + cl;
  const int n = ch * 128 + hw * 64 + lane;
  unsigned spf[3] = {0u, 0u, 0u};
  if (w == 7) {
    unsigned long long kb;
    asm volatile("s_getpc_b64 s[20:21]\n\ts_add_u32 s20, s20, kB@rel32@lo+4\n\ts_addc_u32 s21, s21, kB@rel32@hi+12\n\ts_mov_b64 %0, s[20:21]" : "=s"(kb) :: "s20", "s21", "scc");
#pragma unroll
    for (int i = 0; i < 3; ++i) asm volatile("global_load_dword %0, %1, off" : "=v"(spf[i]) : "v"(kb + (unsigned long long)min(lane + 64 * i, 159) * 128) : "memory");
  }
  u2_t ep;
  EPLOAD(ep);
  const u16 i_are = ldnt(Are + n), i_aim = ldnt(Aim + n), i_bre = ldnt(Bre + n), i_bim = ldnt(Bim + n), i_cre = ldnt(Cre + n), i_cim = ldnt(Cim + n);
  const u16 i_dt = logdt[ch];
  u2_t xg[4];
  const unsigned xoff = tid * (2 * DM * 2);
  const char* xb = (const char*)(x + 4 * q);
#define XG(i) asm volatile("global_load_dwordx2 %0, %1, %2 offset:%3" : "=v"(xg[i]) : "v"(xoff), "s"(xb + ((i) >> 1) * (size_t)(1024 * DM * 2)), "i"(((i) & 1) * DM * 2) : "memory")
  const u2_t dkv = *(const u2_t*)(dskip + 4 * q);
  const int c = lane & 15, kg = lane >> 4;
  h8_t band9, band7;
#pragma unroll
  for (int j = 0; j < 8; ++j) {
    const int k = 8 * kg + 7 - j;
    band9[j] = (k >= c + 4 && k <= c + 12) ? (_Float16)1.0f : (_Float16)0.0f;
    band7[j] = (k >= c + 5 && k <= c + 11) ? (_Float16)1.0f : (_Float16)0.0f;
  }
  asm volatile("" : "+v"(band9), "+v"(band7));
  char* hb = smem + w * KG_W;
  float Sw, bsum;
  {
    const float dt = rbf(expf(bf2f(i_dt)));
    const float are = dt * bf2f(i_are), aim = dt * bf2f(i_aim);
    const float bre = dt * bf2f(i_bre), bim = dt * bf2f(i_bim);
    const float cre = bf2f(i_cre), cim = bf2f(i_cim);
    f2_t c0 = {cre * bre - cim * bim, cre * bim + cim * bre};
    const float er = are * 1.44269504088896341f, wr = aim * 0.15915494309189535f;
    const float bnd = (fabsf(c0.x) + fabsf(c0.y)) * fmaxf(1.0f, __builtin_amdgcn_exp2f(er * 2048.f));
    bsum = wave_sum(bnd);
    asm volatile("" : "+v"(ep) : "v"(bnd));
    asm volatile("" : "+v"(spf[0]), "+v"(spf[1]), "+v"(spf[2]) : "v"(bnd));
    post_epoch(ep, B_BND + 48);
    if (lane == 0) ((float*)(smem + B_BND))[w] = bsum;
    if (w == 0) STAMP(1, 1);
    Sw = pow2_scale(bsum, 15);
    c0 *= Sw;
    const Cw z1 = mkw(cexp_l(er, wr, 1.f)), w8 = mkw(cexp_l(er, -wr, 8.f)), w128 = mkw(cexp_l(er, wr, 128.f));
    const Cw z2 = mkw(cmulw(z1.w, z1));
    const Cw z4 = mkw(cmulw(z2.w, z2));
    auto put = [&](int off, int row, f2_t v) { *(unsigned*)(hb + off + row * 272 + 4 * lane) = pk_f16(v.x, v.y); };
    f2_t R = w8.w, v0 = c0, v1 = cmulw(c0, w128), T = v1;
    *(unsigned*)(hb + KG_R + 4 * lane) = pk_f16(1.f, 0.f);
#pragma unroll
    for (int i = 0; i < 8; ++i) {
      SB;
      if ((w & 3) == (i & 3)) { if (i < 4) XG(0); else XG(1); }
      SB;
      *(unsigned*)(hb + KG_R + (2 * i + 1) * 272 + 4 * lane) = pk_f16(R.x, R.y);
      if (i < 7) {
        R = cmulw(R, w8);
        *(unsigned*)(hb + KG_R + (2 * i + 2) * 272 + 4 * lane) = pk_f16(R.x, R.y);
        R = cmulw(R, w8);
      }
      put(KG_L0, i, v0); put(KG_L0, 8 + i, v1);
      if (i < 7) { v0 = cmulw(v0, z1); v1 = cmulw(v1, z1); }
#pragma unroll
      for (int k = 0; k < 2; ++k) {
        const int i1 = 2 + 2 * i + k;
        if (i1 <= 16) {
          T = cmulw(T, w128);
          if (i1 < 4) {
            f2_t v = T;
#pragma unroll
            for (int i0 = 0; i0 < 4; ++i0) { put(KG_L1, 4 * (i1 - 2) + i0, v); if (i0 < 3) v = cmulw(v, z2); }
          } else if (i1 < 8) {
            put(KG_L1, 8 + 2 * (i1 - 4), T); put(KG_L1, 8 + 2 * (i1 - 4) + 1, cmulw(T, z4));
          } else {
            put(KG_L1, 16 + (i1 - 8), T);
          }
        }
      }
    }
  }
  SB;
  if (w == 0) STAMP(1, 4);
  u4_t gr[2];
  const unsigned goff = tid * 16;
#define GRL() do { asm volatile("global_load_dwordx4 %0, %1, %2 sc1" : "=v"(gr[0]) : "v"(goff), "s"(ws + OFF_G1) : "memory"); \
                   asm volatile("global_load_dwordx4 %0, %1, %2 sc1" : "=v"(gr[1]) : "v"(goff), "s"(ws + OFF_G1 + 8192) : "memory"); } while (0)
  GRL();
  if (w < 4) XG(2);
  wave_fence();
  f4_t ka[3];
  {
    h8_t Bf[4];
#pragma unroll
    for (int ks = 0; ks < 4; ++ks) Bf[ks] = *(const h8_t*)(hb + KG_R + c * 272 + 64 * ks + 16 * kg);
#pragma unroll
    for (int t = 0; t < 3; ++t) ka[t] = f4_t{0.f, 0.f, 0.f, 0.f};
#pragma unroll
    for (int ks = 0; ks < 4; ++ks)
#pragma unroll
      for (int t = 0; t < 3; ++t) {
        const int off = (t == 0) ? KG_L0 + c * 272 : (t == 1) ? KG_L1 + c * 272 : KG_L1 + (16 + min(c, 8)) * 272;
        const h8_t Af = *(const h8_t*)(hb + off + 64 * ks + 16 * kg);
        ka[t] = __builtin_amdgcn_mfma_f32_16x16x32_f16(Af, Bf[ks], ka[t], 0, 0, 0);
      }
  }
  SB;
  if (w >= 4) XG(2); else XG(3);
  SB;
  const float iSw = pow2_inv(Sw);
  wave_fence();
  if (hw == 0) *(f4_t*)(hb + lane * 16) = ka[1] * iSw;
  else { *(f4_t*)(hb + lane * 16) = ka[0] * iSw; *(f4_t*)(hb + 1024 + lane * 16) = ka[2] * iSw; }
  __syncthreads();
  if (w == 0) STAMP(1, 5);
  const unsigned etag = *(const unsigned*)(smem + B_BND + 48 + 8);
  if (w >= 4) XG(3);
  {
    const char* pb = smem + (w ^ 1) * KG_W;
    const float Sc = pow2_scale(((const float*)(smem + B_BND))[2 * cl] + ((const float*)(smem + B_BND))[2 * cl + 1], 15);
    char* kr = smem + B_KC + cl * KRS;
    if (hw == 0) {
      const f4_t k0 = (ka[0] * iSw + *(const f4_t*)(pb + lane * 16)) * Sc, k2 = (ka[2] * iSw + *(const f4_t*)(pb + 1024 + lane * 16)) * Sc;
      *(u2_t*)(kr + 2 * (128 * (kg >> 1) + 4 * (kg & 1) + 8 * c)) = u2_t{pk_f16(k0[0], k0[1]), pk_f16(k0[2], k0[3])};
      const unsigned p01 = pk_f16(k2[0], k2[1]), p23 = pk_f16(k2[2], k2[3]);
      u16* k3 = (u16*)kr + 512 + 64 * kg + c;
      if (kg < 2) { k3[0] = (u16)p01; k3[16] = (u16)(p01 >> 16); k3[32] = (u16)p23; k3[48] = (u16)(p23 >> 16); }
      else if (lane == 32) k3[0] = (u16)p01;
      if (lane == 0) ((float*)(smem + B_BND + 32))[cl] = pow2_inv(Sc);
    } else {
      const f4_t k1 = (ka[1] * iSw + *(const f4_t*)(pb + lane * 16)) * Sc;
      const unsigned p01 = pk_f16(k1[0], k1[1]), p23 = pk_f16(k1[2], k1[3]);
      if (kg < 2) {
        *(u2_t*)(kr + 2 * (256 + 64 * kg + 4 * c)) = u2_t{p01, p23};
      } else {
        *(unsigned*)(kr + 2 * (384 + 64 * (kg - 2) + 2 * c)) = p01;
        *(unsigned*)(kr + 2 * (384 + 64 * (kg - 2) + 32 + 2 * c)) = p23;
      }
    }
  }
  __syncthreads();
  const float invs = ((const float*)(smem + B_BND + 32))[w & 3];
  {
    const int kch = tid >> 7, kc = tid & 127;
    const char* kr = smem + B_KC + kch * KRS;
    const int e0 = (kc < 32) ? 8 * kc : (kc < 64) ? 256 + 4 * (kc - 32) : 384 + 2 * (kc - 64);
    const unsigned* kp = (const unsigned*)(kr + 2 * e0);
    const unsigned d0 = kp[0], d1 = kp[1], d2 = kp[2], d3 = kp[3], d4 = kp[4];
    const unsigned kfirst = *(const u16*)kr, k1024 = *(const u16*)(kr + 2 * 512);
    const int dl = 127 - kc;
    const unsigned kfa = *(const u16*)(kr + 2 * (512 + max(2 * dl - 127, 0))), kfb = *(const u16*)(kr + 2 * (512 + max(2 * dl - 126, 0)));
    char* ki = smem + B_KOFF + kch * KDS;
    const unsigned k0 = d0 & 0xffffu;
    unsigned o0, o1, o2, o3, nx;
    if (kc < 32) { o0 = d0; o1 = d1; o2 = d2; o3 = d3; nx = d4; }
    else if (kc < 64) {
      const unsigned k2 = d0 >> 16, k4 = d1 & 0xffffu, k6 = d1 >> 16, k8 = d2 & 0xffffu;
      o0 = k0 * 0x10001u; o1 = k2 | (k4 << 16); o2 = k4 * 0x10001u; o3 = k6 | (k8 << 16); nx = k8;
    } else {
      const unsigned k4 = d0 >> 16, k8 = d1 & 0xffffu;
      o0 = k0 * 0x10001u; o1 = k0 | (k4 << 16); o2 = k4 * 0x10001u; o3 = k8 * 0x10001u; nx = k8;
    }
    *(u4_t*)(ki + 16 * (kc + 2)) = u4_t{o0, o1, o2, o3};
    *(u4_t*)(ki + KD_O + 16 * (kc + 2)) = u4_t{__builtin_amdgcn_alignbit(o1, o0, 16), __builtin_amdgcn_alignbit(o2, o1, 16),
                                              __builtin_amdgcn_alignbit(o3, o2, 16), __builtin_amdgcn_alignbit(nx, o3, 16)};
    if (kc < 6) {
      const int cp = (kc < 2) ? kc : 128 + kc;
      const unsigned kk = k1024 * 0x10001u;
      const u4_t e = (kc == 2) ? u4_t{kk, kk, k1024, 0u} : u4_t{0u, 0u, 0u, 0u};
      const u4_t o = (kc == 2) ? u4_t{kk, kk, 0u, 0u} : u4_t{0u, 0u, 0u, (kc == 1) ? (kfirst << 16) : 0u};
      *(u4_t*)(ki + 16 * cp) = e;
      *(u4_t*)(ki + KD_O + 16 * cp) = o;
    }
    if (kc < 80) *(unsigned*)(ki + KD_DR + 4 * kc) = (dl >= 64) ? (kfa | (kfb << 16)) : 0u;
  }
  *(unsigned*)(smem + B_UOFF + (tid >> 7) * B_UCH + 4096 + 4 * (tid & 127)) = 0u;
  asm volatile("s_waitcnt vmcnt(2)" : "+v"(gr[0]), "+v"(gr[1]), "+v"(xg[0]), "+v"(xg[1])::"memory");
  {
    for (int spin = 0; spin < SPIN_MAX; ++spin) {
      if (__all(gr[0][1] == etag && gr[0][3] == etag && gr[1][1] == etag && gr[1][3] == etag)) break;
      __builtin_amdgcn_s_sleep(2);
      GRL();
      asm volatile("s_waitcnt vmcnt(0)" : "+v"(gr[0]), "+v"(gr[1])::"memory");
    }
#pragma unroll
    for (int it = 0; it < 2; ++it)
#pragma unroll
      for (int r = 0; r < 2; ++r)
        if (gr[it][2 * r + 1] != etag) gr[it][2 * r] = ln1_row(x, 2 * (tid + 512 * it) + r);
  }
  if (w == 0) STAMP(1, 6);
  const float dk[4] = {bf_lo(dkv.x), bf_hi(dkv.x), bf_lo(dkv.y), bf_hi(dkv.y)};
  float z[2][8];
#pragma unroll
  for (int it = 0; it < 2; ++it) {
    if (it == 1) asm volatile("s_waitcnt vmcnt(0)" : "+v"(xg[2]), "+v"(xg[3])::"memory");
    const int j = tid + 512 * it;
    const unsigned s0 = gr[it][0], s1 = gr[it][2];
    const float m0 = bf_lo(s0), r0 = __builtin_amdgcn_rcpf(bf_hi(s0)), m1 = bf_lo(s1), r1 = __builtin_amdgcn_rcpf(bf_hi(s1));
    const u2_t xa = xg[2 * it], xc = xg[2 * it + 1];
    const float x0[4] = {bf_lo(xa.x), bf_hi(xa.x), bf_lo(xa.y), bf_hi(xa.y)};
    const float x1[4] = {bf_lo(xc.x), bf_hi(xc.x), bf_lo(xc.y), bf_hi(xc.y)};
#pragma unroll
    for (int cc = 0; cc < 4; ++cc) {
      const float u0 = (x0[cc] - m0) * r0, u1 = (x1[cc] - m1) * r1;
      *(unsigned*)(smem + B_UOFF + cc * B_UCH + 4 * (1023 - j)) = pk_f16(u1, u0);
      z[it][cc] = x0[cc] + u0 * dk[cc];
      z[it][4 + cc] = x1[cc] + u1 * dk[cc];
    }
  }
  __syncthreads();
  if (w == 0) STAMP(1, 2);
  if (w >= 4 && w < 7 && cb * 3 + (w - 4) < 432) pack_wd(wd, ws, smem + B_PACK + (w - 4) * 5120, cb * 3 + (w - 4), lane);
  f4_t acc[8];
  if (w < 4) {
    const char* ub = smem + B_UOFF + w * B_UCH + 4112 - 32 * c - 16 * kg - 512 * 7;
    const char* kb = smem + B_KOFF + w * KDS + ((c & 1) ? 0 : KD_O) + 4 * (12 + ((c + 1) >> 1) - 4 * kg);
    const unsigned* dr = (const unsigned*)(smem + B_KOFF + w * KDS + KD_DR) + (127 - c + 4 * kg);
    const char* uw = smem + B_UOFF + w * B_UCH + 4112 - 32 * c - 16 * kg;
    auto rdB = [&](BFrag& F, int i0, int p) { F.B[p] = *(const h8_t*)(ub + 512 * (7 - p) + 64 * i0); };
    auto rdA = [&](BFrag& F, int i0, int i1, int half) {
      const unsigned* ap = (const unsigned*)(kb + 64 * i0 + 512 * i1) + 2 * half;
      F.A[i1][half] = u2_t{ap[0], ap[1]};
    };
    auto frag = [&](const u2_t (&a)[2]) { return u4_t{a[0].x, a[0].y, a[1].x, a[1].y}; };
    auto rdnext = [&](BFrag& F, int i0, int m) {
      if (m < 8) rdB(F, i0, m); else if (m < 16) rdA(F, i0, (m - 8) >> 1, (m - 8) & 1);
    };
    auto step = [&](auto i0c, const BFrag& F, BFrag& N, auto nextc) {
      constexpr int I0 = decltype(i0c)::value, NX = decltype(nextc)::value;
      int m = 0;
#pragma unroll
      for (int i1 = 0; i1 < 5; ++i1) {
        if (i1 == 4 && I0 != 0) continue;
        const u4_t a = frag(F.A[i1]);
#pragma unroll
        for (int t = i1; t < 8; ++t) {
          if (I0 == 0 && i1 == 0) mfma_f16_0(acc[t], a, F.B[t]); else mfma_f16(acc[t], a, F.B[t - i1]);
          if (NX >= 0) rdnext(N, NX, m);
          SB;
          ++m;
        }
      }
    };
    BFrag F0, F1;
    h8_t U9[4], U7[4];
#pragma unroll
    for (int bt = 0; bt < 4; ++bt) { U9[bt] = *(const h8_t*)(uw - 512 * bt + 16); U7[bt] = *(const h8_t*)(uw - 512 * bt); }
#pragma unroll
    for (int m = 0; m < 16; ++m) rdnext(F0, 0, m);
    rdA(F0, 0, 4, 0); rdA(F0, 0, 4, 1);
    SB;
    f4_t X9[4], X7[4];
#pragma unroll
    for (int bt = 0; bt < 4; ++bt) { mfma_f16_0(X9[bt], __builtin_bit_cast(u4_t, U9[bt]), band9); mfma_f16_0(X7[bt], __builtin_bit_cast(u4_t, U7[bt]), band7); }
    SB;
    step(IC(0), F0, F1, IC(1));
    h8_t Vf[4]; u4_t Tf[4];
    {
      int m = 0;
#pragma unroll
      for (int i1 = 0; i1 < 4; ++i1) {
        const u4_t a = frag(F1.A[i1]);
#pragma unroll
        for (int t = i1; t < 8; ++t) {
          mfma_f16(acc[t], a, F1.B[t - i1]);
          rdnext(F0, 2, m);
          if (m < 4) {
            const u4_t vu = {pk_f16(X7[m][0], X9[m][0]), pk_f16(X7[m][1], X9[m][1]), pk_f16(X7[m][2], X9[m][2]), pk_f16(X7[m][3], X9[m][3])};
            Vf[m] = __builtin_bit_cast(h8_t, vu);
          }
          if (m >= 16 && m < 20) { const unsigned* tp = dr - 16 * (m - 16 + 4); Tf[m - 16] = u4_t{tp[0], tp[1], tp[2], tp[3]}; }
          SB;
          ++m;
        }
      }
    }
    step(IC(2), F0, F1, IC(3));
#pragma unroll
    for (int bt = 0; bt < 4; ++bt)
#pragma unroll
      for (int at = 4 + bt; at < 8; ++at) mfma_f16(acc[at], __builtin_bit_cast(u4_t, Vf[bt]), __builtin_bit_cast(h8_t, Tf[at - bt - 4]));
    SB;
    step(IC(3), F1, F0, IC(4));
    step(IC(4), F0, F1, IC(5));
    step(IC(5), F1, F0, IC(6));
    step(IC(6), F0, F1, IC(7));
    step(IC(7), F1, F0, IC(-1));
    asm volatile("s_nop 7\n\ts_nop 1" : "+v"(acc[0]), "+v"(acc[1]), "+v"(acc[2]), "+v"(acc[3]), "+v"(acc[4]), "+v"(acc[5]), "+v"(acc[6]), "+v"(acc[7]));
  }
  if (w == 0) STAMP(1, 3);
  bar();
  if (w < 4) {
#pragma unroll
    for (int t = 0; t < 8; ++t) *(f4_t*)(smem + w * B_YCH + t * 1280 + kg * 320 + c * 16) = acc[t] * invs;
  }
  asm volatile("s_waitcnt lgkmcnt(0)" ::: "memory");
  bar();
#pragma unroll
  for (int it = 0; it < 2; ++it) {
    const int j = tid + 512 * it, t = 2 * j;
    const int yo = (t >> 8) * 1280 + ((t >> 2) & 3) * 320 + ((t >> 4) & 15) * 16 + (t & 3) * 4;
    float h0[4], h1[4];
#pragma unroll
    for (int cc = 0; cc < 4; ++cc) {
      f2_t y = *(const f2_t*)(smem + cc * B_YCH + yo);
      h0[cc] = z[it][cc] + y.x;
      h1[cc] = z[it][4 + cc] + y.y;
    }
    char* dst = ws + OFF_H8 + ((size_t)(q >> 1) * SL + t) * 16 + (q & 1) * 8;
    st8(dst, u2_t{pk_bf16(h0[0], h0[1]), pk_bf16(h0[2], h0[3])});
    st8(dst + 16, u2_t{pk_bf16(h1[0], h1[1]), pk_bf16(h1[2], h1[3])});
  }
  if (w == 0) STAMP(1, 7);
}

constexpr int C_T = 240;
constexpr int C_SLOT = 23552, C_NSLOT = 6, C_GOFF = 15360;
constexpr int C_STAT = C_NSLOT * C_SLOT;
constexpr int C_CS = C_STAT + 960;
constexpr int C_LDS = C_CS + 512;
constexpr int C_STEPS = 20;
#define KD_LINES 80
constexpr size_t OFF_DUMMY = 0x1140000;

__device__ __forceinline__ unsigned ln_pack(float sum, float sq) {
  const float mean = sum * (1.0f / DM);
  return ln_fin(mean, fmaxf(sq * (1.0f / DM) - mean * mean, 0.f));
}
__device__ __forceinline__ unsigned ln_row(const char* h8, int t) {
  float s = 0.f, q = 0.f;
  for (int pl = 0; pl < 80; ++pl) {
    const u4_t v = *(const u4_t*)(h8 + (size_t)pl * 32768 + t * 16);
    for (int j = 0; j < 4; ++j) { const float a = bf_lo(v[j]), b = bf_hi(v[j]); s += a + b; q += a * a + b * b; }
  }
  return ln_pack(s, q);
}

template <int NDMA>
__device__ __forceinline__ void c_loader(char* smem, char* ws, const char* wbase, int w4, int lane, int t0, int jline, int xcd) {
  const unsigned lds0 = (unsigned)(size_t)(LAS char*)smem;
  const char* h8 = ws + OFF_H8;
  unsigned voffH[4], voffW[2];
#pragma unroll
  for (int k = 0; k < 2; ++k) {
    const int r = 8 * (2 * (w4 & 1) + k) + (lane >> 3), c = lane & 7;
    const int f = ((r >> 1) & 1) | ((r >> 2) & 2);
    voffW[k] = r * (FF * 2) + 128 * jline + (((c >> 1) ^ f) * 32) + (c & 1) * 16;
  }
#pragma unroll
  for (int k = 0; k < 4; ++k) {
    const int idx = 64 * (w4 + 4 * k) + lane, pl = idx / C_T, tt = idx % C_T;
    const int t = min(t0 + tt, SL - 1);
    voffH[k] = pl * 32768 + t * 16;
  }
  const unsigned ldsW = C_GOFF + (w4 >> 1) * 4096 + (w4 & 1) * 2048;
  auto issue = [&](int step, unsigned so) {
#pragma unroll
    for (int k = 0; k < 2; ++k) glds16(wbase + (size_t)step * (32 * FF * 2), voffW[k], lds0 + so + ldsW + k * 1024);
#pragma unroll
    for (int k = 0; k < NDMA - 2; ++k) glds16(h8 + (size_t)step * (4 * 32768), voffH[k], lds0 + so + (w4 + 4 * k) * 1024);
  };
  constexpr unsigned S = C_SLOT;
  issue(0, 0); issue(1, S); issue(2, 2 * S); issue(3, 3 * S);
  wait_vm<3 * NDMA>(); bar();
  issue(4, 4 * S);
  wait_vm<2 * NDMA>(); bar();
  issue(5, 5 * S); issue(6, 0);
  unsigned sw = S;
#pragma unroll 1
  for (int j = 1; j < 7; ++j) {
    wait_vm<2 * NDMA>();
    bar();
    issue(2 * j + 5, sw);
    sw += S; if (sw == C_NSLOT * S) sw = 0;
    issue(2 * j + 6, sw);
    sw += S; if (sw == C_NSLOT * S) sw = 0;
  }
  wait_vm<2 * NDMA>(); bar();
  issue(19, sw);
  const int tl = min(60 * w4 + lane, C_T - 1), tg = min(t0 + tl, SL - 1);
  const gu64* gp = (const gu64*)(ws + OFF_G2) + tg;
  const float* csp = (const float*)(ws + OFF_CS) + ((lane >> 4) & 1) * FF + 64 * jline + 16 * w4 + (lane & 15);
  const char* tb = ws + OFF_WP2 + (size_t)(4 * (xcd & 1)) * (54 * 5120);
  const int tl0 = ((int)(blockIdx.x >> 3) * 4 + w4) * 68;
  u2_t gv; float csv; unsigned tv0, tv1, tv2;
  asm volatile("global_load_dwordx2 %0, %1, off sc1" : "=v"(gv) : "v"(gp) : "memory");
  asm volatile("global_load_dword %0, %1, off" : "=v"(csv) : "v"(csp) : "memory");
  asm volatile("global_load_dword %0, %1, off" : "=v"(tv0) : "v"(tb + (size_t)min(tl0 + lane, 8639) * 128) : "memory");
  asm volatile("global_load_dword %0, %1, off" : "=v"(tv1) : "v"(tb + (size_t)min(tl0 + 64 + (lane & 3), 8639) * 128) : "memory");
  {
    unsigned long long kd;
    asm volatile("s_getpc_b64 s[20:21]\n\ts_add_u32 s20, s20, kD@rel32@lo+4\n\ts_addc_u32 s21, s21, kD@rel32@hi+12\n\ts_mov_b64 %0, s[20:21]" : "=s"(kd) :: "s20", "s21", "scc");
    const char* tp = (lane == 0) ? (const char*)&g_ptrs[xcd] : (const char*)kd + min((int)(blockIdx.x >> 3) * 4 + w4, KD_LINES - 1) * 128;
    asm volatile("global_load_dword %0, %1, off" : "=v"(tv2) : "v"(tp) : "memory");
  }
  wait_vm<NDMA + 5>(); bar();
  asm volatile("s_waitcnt vmcnt(0)" : "+v"(gv), "+v"(csv), "+v"(tv0), "+v"(tv1), "+v"(tv2)::"memory");
  bar();
  {
    bool ok = gv.y == 1u;
    for (int spin = 0; spin < 4096 && !__all(ok); ++spin) {
      const unsigned long long g = __hip_atomic_load(gp, __ATOMIC_RELAXED, __HIP_MEMORY_SCOPE_AGENT);
      gv.x = (unsigned)g; ok = (unsigned)(g >> 32) == 1u;
      __builtin_amdgcn_s_sleep(2);
    }
    if (!__all(ok)) gv.x = ln_row(h8, tg);
    if (lane < 60) *(float*)(smem + C_STAT + (60 * w4 + lane) * 4) = bf_lo(gv.x);
    if (lane < 32) *(float*)(smem + C_CS + (lane >> 4) * 256 + (16 * w4 + (lane & 15)) * 4) = csv;
  }
  asm volatile("s_waitcnt lgkmcnt(0)" ::: "memory");
  bar();
}

struct CFrag { b8_t A[2][2]; b8_t B[8]; };

extern "C" __global__ __launch_bounds__(512, 2) void kC() {
  extern __shared__ __attribute__((aligned(16))) char smem[];
  char* __restrict__ ws = ptrs()->ws;
  const u16* __restrict__ wg = ptrs()->wg;
  const u16* __restrict__ wu = ptrs()->wu;
  const int tid = threadIdx.x, lane = tid & 63;
  const int w = __builtin_amdgcn_readfirstlane(tid >> 6);
  int panel, jline;
  const int xcd = blockIdx.x & 7, rank = blockIdx.x >> 3;
  {
    const int g4 = xcd >> 1, a = xcd & 1, nl = (g4 < 3) ? 7 : 6, total = 9 * nl, n0 = (total + 1) >> 1;
    if (a == 0 ? rank >= n0 : rank >= total - n0) {
      const int sp = (xcd < 6) ? (xcd >> 1) : 3 + 5 * (xcd - 6) + (rank - 27);
      const int wv = sp * 8 + w;
      if (w == 0) STAMP(2, 0);
      for (int tile = wv; tile < 128; tile += 104) {
        const int t = 16 * tile + (lane & 15);
        const char* hp = ws + OFF_H8 + (size_t)(lane >> 4) * 32768 + t * 16;
        u4_t v[20];
#pragma unroll
        for (int i = 0; i < 20; ++i) v[i] = *(const u4_t*)(hp + (size_t)i * (4 * 32768));
        float s0 = 0.f, s1 = 0.f, q0 = 0.f, q1 = 0.f;
        unsigned k_both = 0x3f803f80u;
        asm volatile("" : "+s"(k_both));
#pragma unroll
        for (int i = 0; i < 20; ++i) {
          s0 = dot2bf(v[i][0], k_both, s0); q0 = dot2bf(v[i][0], v[i][0], q0);
          s1 = dot2bf(v[i][1], k_both, s1); q1 = dot2bf(v[i][1], v[i][1], q1);
          s0 = dot2bf(v[i][2], k_both, s0); q0 = dot2bf(v[i][2], v[i][2], q0);
          s1 = dot2bf(v[i][3], k_both, s1); q1 = dot2bf(v[i][3], v[i][3], q1);
        }
        float s = s0 + s1, q = q0 + q1;
        s += __shfl_xor(s, 16); q += __shfl_xor(q, 16);
        s += __shfl_xor(s, 32); q += __shfl_xor(q, 32);
        if (lane < 16)
          __hip_atomic_store((gu64*)(ws + OFF_G2) + t, (1ull << 32) | ln_pack(s, q), __ATOMIC_RELAXED, __HIP_MEMORY_SCOPE_AGENT);
      }
      if (w == 0) STAMP(2, 7);
      return;
    }
    const int idx = rank + a * n0;
    panel = idx / nl; jline = 7 * g4 + idx % nl;
  }
  const int t0 = panel * C_T;
  if (w == 0) STAMP(2, 0);
  if (w >= 4) {
    const int w4 = w - 4;
    const char* wbase = (const char*)((w4 >> 1) ? wu : wg);
    if (w4 < 3) c_loader<6>(smem, ws, wbase, w4, lane, t0, jline, xcd);
    else c_loader<5>(smem, ws, wbase, w4, lane, t0, jline, xcd);
    return;
  }
  const int s = w >> 1, th = w & 1;
  const int fr = lane & 15, kg = lane >> 4, q = (lane & 15) >> 2, p = lane & 3;
  const unsigned hb0 = kg * 3840 + fr * 16 + 256 * 7;
  const unsigned hb1 = kg * 3840 + fr * 16 + 256 * (th ? 8 : 0);
  unsigned wa[2];
#pragma unroll
  for (int v = 0; v < 2; ++v) {
    const int sub = 2 * s + (v ? 1 - th : th);
    const int f = (q >> 1) | (2 * (kg & 1));
    wa[v] = C_GOFF + (8 * kg + q) * 128 + ((sub ^ f) * 32) + 8 * p;
  }
  f4_t ag[8][2], au[8][2];
#pragma unroll
  for (int u = 0; u < 8; ++u)
#pragma unroll
    for (int v = 0; v < 2; ++v) { ag[u][v] = f4_t{0.f, 0.f, 0.f, 0.f}; au[u][v] = f4_t{0.f, 0.f, 0.f, 0.f}; }
  auto load = [&](CFrag& F, unsigned so) {
    const char* sp = smem + so;
#pragma unroll
    for (int v = 0; v < 2; ++v)
#pragma unroll
      for (int m = 0; m < 2; ++m) {
        s4_t lo = tr_read(sp + wa[v] + m * 4096), hi = tr_read(sp + wa[v] + m * 4096 + 512);
        u2_t a = __builtin_bit_cast(u2_t, lo), b = __builtin_bit_cast(u2_t, hi);
        F.A[v][m] = __builtin_bit_cast(b8_t, (u4_t{a.x, a.y, b.x, b.y}));
      }
    F.B[0] = *(const b8_t*)(sp + hb0);
#pragma unroll
    for (int u = 1; u < 8; ++u) F.B[u] = *(const b8_t*)(sp + hb1 + 256 * (u - 1));
  };
  auto mma = [&](const CFrag& F) {
    ag[0][0] = __builtin_amdgcn_mfma_f32_16x16x32_bf16(F.A[0][0], F.B[0], ag[0][0], 0, 0, 0);
    au[0][0] = __builtin_amdgcn_mfma_f32_16x16x32_bf16(F.A[0][1], F.B[0], au[0][0], 0, 0, 0);
#pragma unroll
    for (int u = 1; u < 8; ++u)
#pragma unroll
      for (int v = 0; v < 2; ++v) {
        ag[u][v] = __builtin_amdgcn_mfma_f32_16x16x32_bf16(F.A[v][0], F.B[u], ag[u][v], 0, 0, 0);
        au[u][v] = __builtin_amdgcn_mfma_f32_16x16x32_bf16(F.A[v][1], F.B[u], au[u][v], 0, 0, 0);
      }
  };
  auto interleave = [&]() {
#pragma unroll
    for (int i = 0; i < 16; ++i) {
      __builtin_amdgcn_sched_group_barrier(0x008, 1, 0);
      __builtin_amdgcn_sched_group_barrier(0x100, 1, 0);
    }
    __builtin_amdgcn_sched_group_barrier(0x008, 14, 0);
  };
  CFrag F0, F1;
  bar();
  if (w == 0) STAMP(2, 1);
  load(F0, 0);
  unsigned sn = C_SLOT;
  SB;
#pragma unroll 1
  for (int j = 0; j < 9; ++j) {
    asm volatile("s_waitcnt lgkmcnt(0)" ::: "memory");
    bar(); SB;
    load(F1, sn);
    mma(F0);
    interleave();
    SB;
    sn += C_SLOT; if (sn == C_NSLOT * C_SLOT) sn = 0;
    load(F0, sn);
    mma(F1);
    interleave();
    SB;
    sn += C_SLOT; if (sn == C_NSLOT * C_SLOT) sn = 0;
  }
  asm volatile("s_waitcnt lgkmcnt(0)" ::: "memory");
  bar(); SB;
  load(F1, sn);
  mma(F0);
  interleave();
  SB;
  bar(); SB;
  f4_t csg[2], csu[2];
  float mu[8];
#pragma unroll
  for (int v = 0; v < 2; ++v) {
    const int sub = 2 * s + (v ? 1 - th : th);
    csg[v] = *(const f4_t*)(smem + C_CS + (16 * sub + 4 * kg) * 4);
    csu[v] = *(const f4_t*)(smem + C_CS + 256 + (16 * sub + 4 * kg) * 4);
  }
#pragma unroll
  for (int u = 0; u < 8; ++u) {
    const int tile = (u == 0) ? 7 : (th ? 7 + u : u - 1);
    mu[u] = *(const float*)(smem + C_STAT + (16 * tile + fr) * 4);
  }
  mma(F1);
  if (w == 0) STAMP(2, 2);
  char* hh = ws + OFF_HH + (size_t)(2 * jline + s) * 1024 + lane * 16;
  char* sink = ws + OFF_DUMMY + lane * 16;
  auto epi = [&](auto thc) {
    constexpr int TH = decltype(thc)::value;
#pragma unroll
    for (int u = 0; u < 8; ++u) {
      const int tile = (u == 0) ? 7 : (TH ? 7 + u : u - 1);
      const int t16 = 15 * panel + tile;
      char* dst = (t16 < 128) ? hh + (size_t)t16 * (54 * 1024) : sink;
      u2_t o[2];
#pragma unroll
      for (int v = 0; v < 2; ++v) {
        if (u == 0 && v == 1) continue;
        const f4_t r = (ag[u][v] - mu[u] * csg[v]) * (au[u][v] - mu[u] * csu[v]);
        o[v] = u2_t{pk_bf16(r[0], r[1]), pk_bf16(r[2], r[3])};
      }
      if (u == 0) st8(dst + 8 * TH, o[0]);
      else st16(dst, u4_t{o[TH].x, o[TH].y, o[1 - TH].x, o[1 - TH].y});
    }
  };
  if (th) epi(IC(1)); else epi(IC(0));
  if (w == 0) STAMP(2, 7);
}

constexpr int D_SLOT = 15360, D_NSLOT = 9, D_STEPS = 18;
constexpr int D_LDS = D_NSLOT * D_SLOT;

template <int NDMA>
__device__ __forceinline__ void d_loader(char* smem, const char* wp, int w4, int lane) {
  const unsigned lds0 = (unsigned)(size_t)(LAS char*)smem;
  const unsigned voff = lane * 16;
  auto issue = [&](int step, unsigned so) {
#pragma unroll
    for (int k = 0; k < NDMA; ++k) {
      const int i = w4 + 4 * k;
      glds16(wp + (size_t)step * D_SLOT + i * 1024, voff, lds0 + so + i * 1024);
    }
  };
  constexpr unsigned S = D_SLOT;
  issue(0, 0); issue(1, S); issue(2, 2 * S);
  wait_vm<2 * NDMA>(); bar();
  issue(3, 3 * S); issue(4, 4 * S); issue(5, 5 * S);
  wait_vm<3 * NDMA>(); bar();
  issue(6, 6 * S); issue(7, 7 * S); issue(8, 8 * S);
  unsigned sw = 0;
#pragma unroll 1
  for (int j = 1; j < 5; ++j) {
    wait_vm<4 * NDMA>();
    bar();
    issue(2 * j + 7, sw);
    sw += S; if (sw == D_NSLOT * S) sw = 0;
    issue(2 * j + 8, sw);
    sw += S; if (sw == D_NSLOT * S) sw = 0;
  }
  wait_vm<4 * NDMA>(); bar();
  issue(17, sw);
  wait_vm<3 * NDMA>(); bar();
  wait_vm<1 * NDMA>(); bar();
  wait_vm<0>(); bar();
}

extern "C" __global__ __launch_bounds__(512, 2) void kD() {
  extern __shared__ __attribute__((aligned(16))) char smem[];
  char* __restrict__ ws = ptrs()->ws;
  u16* __restrict__ out = ptrs()->out;
  const int tid = threadIdx.x, lane = tid & 63;
  const int w = __builtin_amdgcn_readfirstlane(tid >> 6);
  const int xcd = blockIdx.x & 7, rank = blockIdx.x >> 3;
  const int it = 8 * (xcd >> 1) + (rank & 7), dg = 4 * (xcd & 1) + (rank >> 3);
  if (w == 0) STAMP(3, 0);
  if (w >= 4) {
    const int w4 = w - 4;
    const char* wp = ws + OFF_WP2 + (size_t)dg * (54 * 5120);
    if (w4 < 3) d_loader<4>(smem, wp, w4, lane);
    else d_loader<3>(smem, wp, w4, lane);
    return;
  }
  const int fr = lane & 15, g = lane >> 4;
  const int t = 64 * it + 16 * w + fr;
  const char* hp = ws + OFF_HH + (size_t)(4 * it + w) * (54 * 1024);
  const unsigned voff = lane * 16;
  u4_t R[18];
#pragma unroll
  for (int i = 0; i < 9; ++i)
    asm volatile("global_load_dwordx4 %0, %1, %2" : "=v"(R[i]) : "v"(voff), "s"(hp + (size_t)i * 1024) : "memory");
  u2_t res[5]; unsigned st;
  f4_t acc[5];
#pragma unroll
  for (int dt = 0; dt < 5; ++dt) acc[dt] = f4_t{0.f, 0.f, 0.f, 0.f};
  b8_t A0[5], A1[5];
  auto rdA = [&](b8_t (&A)[5], unsigned off) {
    const char* sp = smem + off + lane * 16;
#pragma unroll
    for (int dt = 0; dt < 5; ++dt) A[dt] = *(const b8_t*)(sp + dt * 1024);
  };
  bar();
  if (w == 0) STAMP(3, 1);
  rdA(A0, 0);
  unsigned so = 0;
  auto kstep = [&](auto mode, auto idx, b8_t (&Ac)[5], b8_t (&An)[5], const char* hnext) {
    constexpr int MODE = decltype(mode)::value, i = decltype(idx)::value, kk = i % 3;
    if (i % 6 == 0) bar();
    unsigned nx = so + (kk + 1) * 5120;
    if (kk == 2) { so += D_SLOT; if (so == D_NSLOT * D_SLOT) so = 0; nx = so; }
    SB;
    if (!(MODE == 2 && i == 17)) rdA(An, nx);
    constexpr int NV = (MODE == 0) ? (i < 9 ? 8 + i : 26 - i) : (MODE == 1) ? (i < 9 ? 25 - i : 17) : (i == 0 ? 17 : 23 - i);
    asm volatile("s_waitcnt vmcnt(%1)" : "+v"(R[i]) : "i"(NV) : "memory");
    const b8_t B = __builtin_bit_cast(b8_t, R[i]);
#pragma unroll
    for (int dt = 0; dt < 5; ++dt) acc[dt] = __builtin_amdgcn_mfma_f32_16x16x32_bf16(Ac[dt], B, acc[dt], 0, 0, 0);
    SB;
    if (MODE == 0 && i < 9)
      asm volatile("global_load_dwordx4 %0, %1, %2" : "=v"(R[i < 9 ? 9 + i : 0]) : "v"(voff), "s"(hp + (size_t)(9 + i) * 1024) : "memory");
    if (MODE != 2) asm volatile("global_load_dwordx4 %0, %1, %2" : "+v"(R[i]) : "v"(voff), "s"(hnext + (size_t)i * 1024) : "memory");
    if (MODE == 2 && i == 0) {
#pragma unroll
      for (int dt = 0; dt < 5; ++dt) {
        const int d = 80 * dg + 16 * dt + 4 * g;
        const char* rp = ws + OFF_H8 + ((size_t)(d >> 3) * SL + t) * 16 + (d & 4) * 2;
        asm volatile("global_load_dwordx2 %0, %1, off" : "=v"(res[dt]) : "v"(rp) : "memory");
      }
      asm volatile("global_load_dword %0, %1, off" : "=v"(st) : "v"(ws + OFF_G2 + (size_t)t * 8) : "memory");
    }
    SB;
  };
  auto round = [&](auto mode, const char* hnext) {
    kstep(mode, IC(0), A0, A1, hnext); kstep(mode, IC(1), A1, A0, hnext); kstep(mode, IC(2), A0, A1, hnext);
    kstep(mode, IC(3), A1, A0, hnext); kstep(mode, IC(4), A0, A1, hnext); kstep(mode, IC(5), A1, A0, hnext);
    kstep(mode, IC(6), A0, A1, hnext); kstep(mode, IC(7), A1, A0, hnext); kstep(mode, IC(8), A0, A1, hnext);
    kstep(mode, IC(9), A1, A0, hnext); kstep(mode, IC(10), A0, A1, hnext); kstep(mode, IC(11), A1, A0, hnext);
    kstep(mode, IC(12), A0, A1, hnext); kstep(mode, IC(13), A1, A0, hnext); kstep(mode, IC(14), A0, A1, hnext);
    kstep(mode, IC(15), A1, A0, hnext); kstep(mode, IC(16), A0, A1, hnext); kstep(mode, IC(17), A1, A0, hnext);
  };
  round(IC(0), hp + 18 * 1024);
  round(IC(1), hp + 36 * 1024);
  round(IC(2), hp);
  if (w == 0) STAMP(3, 2);
  asm volatile("s_waitcnt vmcnt(0)" : "+v"(res[0]), "+v"(res[1]), "+v"(res[2]), "+v"(res[3]), "+v"(res[4]), "+v"(st)::"memory");
  const float sd = bf_hi(st), r2 = 1.0f / (sd * sd);
#pragma unroll
  for (int dt = 0; dt < 5; ++dt) {
    const int d = 80 * dg + 16 * dt + 4 * g;
    const float h0 = bf_lo(res[dt].x), h1 = bf_hi(res[dt].x), h2 = bf_lo(res[dt].y), h3 = bf_hi(res[dt].y);
    st8(out + (size_t)t * DM + d, u2_t{pk_bf16(h0 + r2 * acc[dt][0], h1 + r2 * acc[dt][1]), pk_bf16(h2 + r2 * acc[dt][2], h3 + r2 * acc[dt][3])});
  }
  if (w == 0) STAMP(3, 7);
}

extern "C" void kernel_launch(void* const* d_in, const int* in_sizes, int n_in, void* d_out, int out_size, void* d_ws,
                              size_t ws_size, hipStream_t stream) {
  char* ws = (char*)d_ws;
  (void)hipFuncSetAttribute((const void*)kB, hipFuncAttributeMaxDynamicSharedMemorySize, B_LDS);
  (void)hipFuncSetAttribute((const void*)kC, hipFuncAttributeMaxDynamicSharedMemorySize, C_LDS);
  (void)hipFuncSetAttribute((const void*)kD, hipFuncAttributeMaxDynamicSharedMemorySize, D_LDS);
  kB<<<256, 512, B_LDS, stream>>>((const u16*)d_in[0], (const u16*)d_in[1], (const u16*)d_in[2], (const u16*)d_in[3], (const u16*)d_in[4],
                                  (const u16*)d_in[5], (const u16*)d_in[6], (const u16*)d_in[7], (const u16*)d_in[8], (const u16*)d_in[10],
                                  (const u16*)d_in[11], (const u16*)d_in[12], (const u16*)d_out, ws);
  kC<<<256, 512, C_LDS, stream>>>();
  kD<<<256, 512, D_LDS, stream>>>();
}
```

```cpp
#include <hip/hip_runtime.h>
#include <hip/hip_bf16.h>
#include <stdint.h>
#include <type_traits>

typedef unsigned short u16;
typedef _Float16 h2_t __attribute__((ext_vector_type(2)));
typedef _Float16 h8_t __attribute__((ext_vector_type(8)));
typedef __bf16 b2_t __attribute__((ext_vector_type(2)));
typedef __bf16 b8_t __attribute__((ext_vector_type(8)));
typedef float f2_t __attribute__((ext_vector_type(2)));
typedef float f4_t __attribute__((ext_vector_type(4)));
typedef unsigned u2_t __attribute__((ext_vector_type(2)));
typedef unsigned u4_t __attribute__((ext_vector_type(4)));
typedef short s4_t __attribute__((ext_vector_type(4)));
typedef __attribute__((address_space(1))) unsigned long long gu64;
#define LAS __attribute__((address_space(3)))
#define SB __builtin_amdgcn_sched_barrier(0)
#define IC(n) std::integral_constant<int, n>{}

constexpr int SL = 2048;
constexpr int DM = 640;
constexpr int FF = 1728;

constexpr size_t OFF_G1 = 0;
constexpr size_t OFF_EP = 0x10000;
constexpr size_t OFF_RD = 0x10100;
constexpr size_t OFF_H8 = 0x5A0000;
constexpr size_t OFF_HH = 0x820000;
constexpr size_t OFF_WP2 = 0xF00000;
constexpr size_t OFF_CS = 0x1120000;
constexpr size_t OFF_G2 = 0x1130000;
constexpr int KRS = 1344;
constexpr int KDS = 4864;
constexpr int KD_O = 2240;
constexpr int KD_DR = 4384;

__device__ __forceinline__ float bf_lo(unsigned d) { return __uint_as_float(d << 16); }
__device__ __forceinline__ float bf_hi(unsigned d) { return __uint_as_float(d & 0xffff0000u); }
__device__ __forceinline__ float bf2f(u16 v) { return __uint_as_float((unsigned)v << 16); }
__device__ __forceinline__ unsigned pk_bf16(float a, float b) {
  f2_t v = {a, b};
  return __builtin_bit_cast(unsigned, __builtin_convertvector(v, b2_t));
}
__device__ __forceinline__ unsigned pk_f16(float a, float b) {
  f2_t v = {a, b};
  return __builtin_bit_cast(unsigned, __builtin_convertvector(v, h2_t));
}
__device__ __forceinline__ float rbf(float a) { return bf_lo(pk_bf16(a, 0.f)); }
template <int N> __device__ __forceinline__ float dpp_ror_add(float v) {
  int r = __builtin_amdgcn_update_dpp(0, __float_as_int(v), 0x120 + N, 0xf, 0xf, false);
  return v + __int_as_float(r);
}
__device__ __forceinline__ float row16_sum(float v) {
  v = dpp_ror_add<8>(v); v = dpp_ror_add<4>(v); v = dpp_ror_add<2>(v); v = dpp_ror_add<1>(v);
  return v;
}
__device__ __forceinline__ float wave_sum(float v) {
  v = row16_sum(v);
  const int b = __float_as_int(v);
  return (__int_as_float(__builtin_amdgcn_readlane(b, 0)) + __int_as_float(__builtin_amdgcn_readlane(b, 16))) +
         (__int_as_float(__builtin_amdgcn_readlane(b, 32)) + __int_as_float(__builtin_amdgcn_readlane(b, 48)));
}
template <typename T> __device__ __forceinline__ T ldnt(const T* p) { return __builtin_nontemporal_load(p); }
__device__ __forceinline__ void wave_fence() {
  __builtin_amdgcn_fence(__ATOMIC_RELEASE, "wavefront");
  __builtin_amdgcn_wave_barrier();
  __builtin_amdgcn_fence(__ATOMIC_ACQUIRE, "wavefront");
}
__device__ __forceinline__ s4_t tr_read(const char* p) {
  return __builtin_amdgcn_ds_read_tr16_b64_v4i16((LAS s4_t*)p);
}
__device__ __forceinline__ void glds16(const void* sbase, unsigned voff, unsigned lds_addr) {
  asm volatile("s_mov_b32 m0, %2\n\ts_nop 0\n\tglobal_load_lds_dwordx4 %0, %1" ::"v"(voff), "s"(sbase), "s"(lds_addr) : "memory");
}
template <int N> __device__ __forceinline__ void wait_vm() { asm volatile("s_waitcnt vmcnt(%0)" ::"i"(N) : "memory"); }
__device__ __forceinline__ void bar() { asm volatile("s_barrier" ::: "memory"); }
__device__ __forceinline__ float dot2bf(unsigned a, unsigned b, float c) {
  return __builtin_amdgcn_fdot2_f32_bf16(__builtin_bit_cast(b2_t, a), __builtin_bit_cast(b2_t, b), c, false);
}

struct __attribute__((aligned(128))) Ptrs { char* ws; const u16* wg; const u16* wu; u16* out; void* pad[12]; };
__device__ Ptrs g_ptrs[8];
typedef const __attribute__((address_space(4))) Ptrs* cptrs_t;
__device__ __forceinline__ cptrs_t ptrs() { return (cptrs_t)(unsigned long long)&g_ptrs[blockIdx.x & 7]; }

struct Cw { f2_t w, ws; };
__device__ __forceinline__ Cw mkw(f2_t w) { Cw r; r.w = w; r.ws = f2_t{-w.y, w.x}; return r; }
__device__ __forceinline__ f2_t cmulw(f2_t a, const Cw& W) {
  return __builtin_elementwise_fma(f2_t{a.y, a.y}, W.ws, f2_t{a.x, a.x} * W.w);
}
__device__ __forceinline__ f2_t cexp_l(float er, float wr, float l) {
  float mag = __builtin_amdgcn_exp2f(er * l);
  float rev = __builtin_amdgcn_fractf(wr * l);
  return f2_t{mag * __builtin_amdgcn_cosf(rev), mag * __builtin_amdgcn_sinf(rev)};
}
__device__ __forceinline__ float pow2_scale(float bound, int target) {
  unsigned e = (__float_as_uint(bound) >> 23) & 0xff;
  int k = target - ((int)e - 126);
  k = k > 126 ? 126 : (k < -126 ? -126 : k);
  return (bound > 0.f) ? __uint_as_float((unsigned)(k + 127) << 23) : 1.0f;
}
__device__ __forceinline__ float pow2_inv(float p) { return __uint_as_float(0x7f000000u - __float_as_uint(p)); }

constexpr int B_SIDE = 96;
constexpr int KG_R = 0, KG_L0 = 4352, KG_L1 = 8704, KG_W = 8704 + 25 * 272;
constexpr int B_BND = 8 * KG_W;
constexpr int B_KC = B_BND + 64;
constexpr int B_LDS = B_KC + 4 * KRS;
constexpr int B_KOFF = 0;
constexpr int B_UOFF = 4 * KDS;
constexpr int B_UCH = 4608;
constexpr int B_YCH = 10240;
constexpr int B_PACK = 40960;

__device__ __forceinline__ void pack_wd(const u16* __restrict__ wd, char* __restrict__ ws, char* buf, int u, int lane) {
  const int ks = u >> 3, dg = u & 7;
#pragma unroll
  for (int i = 0; i < 5; ++i) {
    const int idx = i * 64 + lane, row = idx / 10, c = idx % 10;
    __builtin_amdgcn_global_load_lds((const unsigned*)((const char*)wd + (size_t)(32 * ks + row) * 1280 + 160 * dg + 16 * c),
                                     (LAS unsigned*)(buf + i * 1024), 16, 0, 2);
  }
  asm volatile("s_waitcnt vmcnt(0)" ::: "memory");
  wave_fence();
  const int g = lane >> 4, q = (lane & 15) >> 2, p = lane & 3;
  char* dst = ws + OFF_WP2 + ((size_t)(dg * 54 + ks) * 5) * 1024 + lane * 16;
#pragma unroll
  for (int dt = 0; dt < 5; ++dt) {
    s4_t aa = tr_read(buf + (4 * g + q) * 160 + 32 * dt + 8 * p);
    s4_t bb = tr_read(buf + (16 + 4 * g + q) * 160 + 32 * dt + 8 * p);
    u2_t ua = __builtin_bit_cast(u2_t, aa), ub = __builtin_bit_cast(u2_t, bb);
    const u4_t pv = {ua.x, ua.y, ub.x, ub.y};
    asm volatile("global_store_dwordx4 %0, %1, off sc1\n\ts_nop 1" ::"v"(dst + dt * 1024), "v"(pv) : "memory");
  }
}

struct BFrag { h8_t B[8]; u2_t A[5][2]; };
__device__ __forceinline__ void mfma_f16(f4_t& d, u4_t a, h8_t b) {
  asm volatile("v_mfma_f32_16x16x32_f16 %0, %1, %2, %0" : "+v"(d) : "v"(a), "v"(b));
}
__device__ __forceinline__ void mfma_f16_0(f4_t& d, u4_t a, h8_t b) {
  asm volatile("v_mfma_f32_16x16x32_f16 %0, %1, %2, 0" : "=&v"(d) : "v"(a), "v"(b));
}
__device__ __forceinline__ unsigned ln_fin(float mean, float var) {
  const float mb = rbf(mean), sd = rbf(sqrtf(rbf(rbf(var) + 1e-5f)));
  return (__float_as_uint(mb) >> 16) | (__float_as_uint(sd) & 0xffff0000u);
}
__device__ __forceinline__ unsigned ln1_row(const u16* __restrict__ x, int t) {
  const u4_t* p = (const u4_t*)(x + (size_t)t * DM);
  float s = 0.f, q = 0.f;
  for (int i = 0; i < 80; ++i) { const u4_t v = p[i]; for (int j = 0; j < 4; ++j) s += bf_lo(v[j]) + bf_hi(v[j]); }
  const float mean = s * (1.0f / DM);
  for (int i = 0; i < 80; ++i) {
    const u4_t v = p[i];
    for (int j = 0; j < 4; ++j) { const float a = bf_lo(v[j]) - mean, b = bf_hi(v[j]) - mean; q += a * a; q += b * b; }
  }
  return ln_fin(mean, q * (1.0f / DM));
}
constexpr int SPIN_MAX = 1 << 14;

extern "C" __global__ __launch_bounds__(512, 2) void kB(const u16* __restrict__ x, const u16* __restrict__ Are, const u16* __restrict__ Aim,
                                                     const u16* __restrict__ Bre, const u16* __restrict__ Bim, const u16* __restrict__ Cre,
                                                     const u16* __restrict__ Cim, const u16* __restrict__ logdt, const u16* __restrict__ dskip,
                                                     const u16* __restrict__ wg, const u16* __restrict__ wu, const u16* __restrict__ wd,
                                                     u16* __restrict__ outp, char* __restrict__ ws) {
  extern __shared__ __attribute__((aligned(16))) char smem[];
  const int tid = threadIdx.x, lane = tid & 63;
  const int w = __builtin_amdgcn_readfirstlane(tid >> 6);
  const int xcd = blockIdx.x & 7;
#define EPLOAD(v) asm volatile("global_load_dwordx2 %0, %1, %2 sc1" : "=v"(v) : "v"(0u), "s"(ws + OFF_EP) : "memory")
  auto post_epoch = [&](u2_t ep, int off) {
    if (tid == 0) {
      const unsigned long long e = (((unsigned long long)ep.y << 32) | ep.x) + 1ull;
      *(unsigned long long*)(smem + off) = e;
      *(unsigned*)(smem + off + 8) = (unsigned)((e * 0x9E3779B97F4A7C15ull) >> 32);
      __hip_atomic_store((gu64*)(ws + OFF_RD) + blockIdx.x, e, __ATOMIC_RELAXED, __HIP_MEMORY_SCOPE_AGENT);
    }
  };
  if (blockIdx.x < B_SIDE) {
    const int rk = blockIdx.x >> 3;
    const int g4 = xcd >> 1, a = xcd & 1, nl = (g4 < 3) ? 7 : 6;
    constexpr int S_EP = 17408;
    unsigned pf[2] = {0u, 0u};
    if (rk >= 8) {
#pragma unroll
      for (int k = 0; k < 2; ++k) {
        const int id = (((rk - 8) * 8 + w) * 2 + k) * 64 + lane;
        asm volatile("global_load_dword %0, %1, off" : "=v"(pf[k]) : "v"((const char*)x + (size_t)(id >> 1) * (DM * 2) + (((160 * xcd) >> 7) + (id & 1)) * 128) : "memory");
      }
    }
    if (rk < 8) {
      const int r = lane >> 4, c = lane & 15, t = (blockIdx.x * 8 + w) * 4 + r;
      const u4_t* p = (const u4_t*)(x + (size_t)t * DM);
      u2_t ep;
      EPLOAD(ep);
      u4_t v[5];
#pragma unroll
      for (int i = 0; i < 5; ++i) v[i] = ldnt(p + c + 16 * i);
      float s = 0.f;
#pragma unroll
      for (int i = 0; i < 5; ++i)
#pragma unroll
        for (int j = 0; j < 4; ++j) s += bf_lo(v[i][j]) + bf_hi(v[i][j]);
      asm volatile("" : "+v"(ep) : "v"(s));
      post_epoch(ep, S_EP);
      const float mean = row16_sum(s) * (1.0f / DM);
      float q = 0.f;
#pragma unroll
      for (int i = 0; i < 5; ++i)
#pragma unroll
        for (int j = 0; j < 4; ++j) {
          const float aa = bf_lo(v[i][j]) - mean, bb = bf_hi(v[i][j]) - mean;
          q += aa * aa; q += bb * bb;
        }
      const unsigned sv = ln_fin(mean, row16_sum(q) * (1.0f / DM));
      __syncthreads();
      const u2_t e = *(const u2_t*)(smem + S_EP);
      if (c == 0) {
        __hip_atomic_store((gu64*)(ws + OFF_G1) + t, ((unsigned long long)*(const unsigned*)(smem + S_EP + 8) << 32) | sv, __ATOMIC_RELAXED, __HIP_MEMORY_SCOPE_AGENT);
        ((unsigned long long*)(ws + OFF_G2))[t] = 0ull;
      }
      if (rk == 0 && w == 7) {
        gu64* gp = (gu64*)(unsigned long long)&g_ptrs[xcd];
        if (lane < 4) {
          const unsigned long long v = lane == 0 ? (unsigned long long)ws : lane == 1 ? (unsigned long long)wg : lane == 2 ? (unsigned long long)wu : (unsigned long long)outp;
          __hip_atomic_store(gp + lane, v, __ATOMIC_RELAXED, __HIP_MEMORY_SCOPE_AGENT);
        }
        unsigned back;
        asm volatile("s_waitcnt vmcnt(0)\n\tglobal_load_dword %0, %1, off\n\ts_waitcnt vmcnt(0)" : "=&v"(back) : "v"(gp) : "memory");
        asm volatile("" ::"v"(back));
      }
      if (rk == 4 && w == 0) {
        unsigned long long a0, a1;
        asm volatile("s_getpc_b64 s[20:21]\n\ts_add_u32 s20, s20, kB@rel32@lo+4\n\ts_addc_u32 s21, s21, kB@rel32@hi+12\n\ts_mov_b64 %0, s[20:21]" : "=s"(a0) :: "s20", "s21", "scc");
        asm volatile("s_getpc_b64 s[20:21]\n\ts_add_u32 s20, s20, kD@rel32@lo+4\n\ts_addc_u32 s21, s21, kD@rel32@hi+12\n\ts_mov_b64 %0, s[20:21]" : "=s"(a1) :: "s20", "s21", "scc");
        const unsigned long long lo = a0 < a1 ? a0 : a1, last = ((a0 < a1 ? a1 - a0 : a0 - a1) + 8192) & ~127ull;
        unsigned cf[8];
#pragma unroll
        for (int i = 0; i < 8; ++i) {
          const unsigned long long o = (unsigned long long)(lane + 64 * i) * 128;
          asm volatile("global_load_dword %0, %1, off" : "=v"(cf[i]) : "v"(lo + (o < last ? o : last)) : "memory");
        }
        asm volatile("s_waitcnt vmcnt(0)" ::"v"(cf[0]), "v"(cf[1]), "v"(cf[2]), "v"(cf[3]), "v"(cf[4]), "v"(cf[5]), "v"(cf[6]), "v"(cf[7]) : "memory");
      }
      if (blockIdx.x == 63 && w == 7) {
        const unsigned long long e64 = ((unsigned long long)e.y << 32) | e.x;
        for (int spin = 0; spin < SPIN_MAX; ++spin) {
          bool ok = true;
#pragma unroll
          for (int i = 0; i < 4; ++i) ok &= __hip_atomic_load((const gu64*)(ws + OFF_RD) + lane + 64 * i, __ATOMIC_RELAXED, __HIP_MEMORY_SCOPE_AGENT) == e64;
          if (__all(ok)) break;
          __builtin_amdgcn_s_sleep(4);
        }
        if (lane == 0) __hip_atomic_store((gu64*)(ws + OFF_EP), e64, __ATOMIC_RELAXED, __HIP_MEMORY_SCOPE_AGENT);
      }
    }
    const int strip = (rk >= 8) ? rk - 8 : 4 + rk;
    if ((rk >= 8 || rk < 3) && strip < nl) {
      const int jl = 7 * g4 + strip;
      const char* pm = (const char*)(a ? wu : wg) + (size_t)(80 * w + (lane >> 3)) * (FF * 2) + 128 * jl + (lane & 7) * 16;
      u2_t ep;
      EPLOAD(ep);
      u4_t v[10];
#pragma unroll
      for (int i = 0; i < 10; ++i) v[i] = ldnt((const u4_t*)(pm + (size_t)i * 8 * (FF * 2)));
      const char* po = (const char*)(a ? wg : wu) + (size_t)(80 * w) * (FF * 2) + 128 * jl;
      unsigned t0v, t1v;
      asm volatile("global_load_dword %0, %1, off nt" : "=v"(t0v) : "v"(po + (size_t)lane * (FF * 2)) : "memory");
      asm volatile("global_load_dword %0, %1, off nt" : "=v"(t1v) : "v"(po + (size_t)(64 + (lane & 15)) * (FF * 2)) : "memory");
      f2_t s[4];
#pragma unroll
      for (int e = 0; e < 4; ++e) s[e] = f2_t{0.f, 0.f};
#pragma unroll
      for (int i = 0; i < 10; ++i)
#pragma unroll
        for (int jj = 0; jj < 4; ++jj) s[jj] += f2_t{bf_lo(v[i][jj]), bf_hi(v[i][jj])};
      asm volatile("" : "+v"(ep), "+v"(pf[0]), "+v"(pf[1]) : "v"(s[0]));
      if (rk >= 8) post_epoch(ep, S_EP);
      float* pp = (float*)smem;
      *(f4_t*)(pp + (w * 8 + (lane >> 3)) * 68 + (lane & 7) * 8) = f4_t{s[0].x, s[0].y, s[1].x, s[1].y};
      *(f4_t*)(pp + (w * 8 + (lane >> 3)) * 68 + (lane & 7) * 8 + 4) = f4_t{s[2].x, s[2].y, s[3].x, s[3].y};
      __syncthreads();
      float acc = 0.f;
#pragma unroll
      for (int i = 0; i < 8; ++i) acc += pp[(w * 8 + i) * 68 + lane];
      __syncthreads();
      pp[w * 64 + lane] = acc;
      __syncthreads();
      if (w == 0) {
        float t = 0.f;
#pragma unroll
        for (int i = 0; i < 8; ++i) t += pp[i * 64 + lane];
        ((float*)(ws + OFF_CS))[a * FF + 64 * jl + lane] = t;
      }
      asm volatile("s_waitcnt vmcnt(0)" ::"v"(t0v), "v"(t1v) : "memory");
    }
    return;
  }
  const int cb = blockIdx.x - B_SIDE;
  const int q = 20 * xcd + (cb >> 3);
  const int cl = w >> 1, hw = w & 1, ch = 4 * q + cl;
  const int n = ch * 128 + hw * 64 + lane;
  u2_t ep;
  EPLOAD(ep);
  const u16 i_are = ldnt(Are + n), i_aim = ldnt(Aim + n), i_bre = ldnt(Bre + n), i_bim = ldnt(Bim + n), i_cre = ldnt(Cre + n), i_cim = ldnt(Cim + n);
  const u16 i_dt = logdt[ch];
  u2_t xg[4];
  const unsigned xoff = tid * (2 * DM * 2);
  const char* xb = (const char*)(x + 4 * q);
#define XG(i) asm volatile("global_load_dwordx2 %0, %1, %2 offset:%3" : "=v"(xg[i]) : "v"(xoff), "s"(xb + ((i) >> 1) * (size_t)(1024 * DM * 2)), "i"(((i) & 1) * DM * 2) : "memory")
  const u2_t dkv = *(const u2_t*)(dskip + 4 * q);
  const int c = lane & 15, kg = lane >> 4;
  h8_t band9, band7;
#pragma unroll
  for (int j = 0; j < 8; ++j) {
    const int k = 8 * kg + 7 - j;
    band9[j] = (k >= c + 4 && k <= c + 12) ? (_Float16)1.0f : (_Float16)0.0f;
    band7[j] = (k >= c + 5 && k <= c + 11) ? (_Float16)1.0f : (_Float16)0.0f;
  }
  asm volatile("" : "+v"(band9), "+v"(band7));
  char* hb = smem + w * KG_W;
  float Sw, bsum;
  {
    const float dt = rbf(expf(bf2f(i_dt)));
    const float are = dt * bf2f(i_are), aim = dt * bf2f(i_aim);
    const float bre = dt * bf2f(i_bre), bim = dt * bf2f(i_bim);
    const float cre = bf2f(i_cre), cim = bf2f(i_cim);
    f2_t c0 = {cre * bre - cim * bim, cre * bim + cim * bre};
    const float er = are * 1.44269504088896341f, wr = aim * 0.15915494309189535f;
    const float bnd = (fabsf(c0.x) + fabsf(c0.y)) * fmaxf(1.0f, __builtin_amdgcn_exp2f(er * 2048.f));
    bsum = wave_sum(bnd);
    asm volatile("" : "+v"(ep) : "v"(bnd));
    post_epoch(ep, B_BND + 48);
    if (lane == 0) ((float*)(smem + B_BND))[w] = bsum;
    Sw = pow2_scale(bsum, 15);
    c0 *= Sw;
    const Cw z1 = mkw(cexp_l(er, wr, 1.f)), w8 = mkw(cexp_l(er, -wr, 8.f)), w128 = mkw(cexp_l(er, wr, 128.f));
    const Cw z2 = mkw(cmulw(z1.w, z1));
    const Cw z4 = mkw(cmulw(z2.w, z2));
    auto put = [&](int off, int row, f2_t v) { *(unsigned*)(hb + off + row * 272 + 4 * lane) = pk_f16(v.x, v.y); };
    f2_t R = w8.w, v0 = c0, v1 = cmulw(c0, w128), T = v1;
    *(unsigned*)(hb + KG_R + 4 * lane) = pk_f16(1.f, 0.f);
#pragma unroll
    for (int i = 0; i < 8; ++i) {
      SB;
      if ((w & 3) == (i & 3)) { if (i < 4) XG(0); else XG(1); }
      SB;
      *(unsigned*)(hb + KG_R + (2 * i + 1) * 272 + 4 * lane) = pk_f16(R.x, R.y);
      if (i < 7) {
        R = cmulw(R, w8);
        *(unsigned*)(hb + KG_R + (2 * i + 2) * 272 + 4 * lane) = pk_f16(R.x, R.y);
        R = cmulw(R, w8);
      }
      put(KG_L0, i, v0); put(KG_L0, 8 + i, v1);
      if (i < 7) { v0 = cmulw(v0, z1); v1 = cmulw(v1, z1); }
#pragma unroll
      for (int k = 0; k < 2; ++k) {
        const int i1 = 2 + 2 * i + k;
        if (i1 <= 16) {
          T = cmulw(T, w128);
          if (i1 < 4) {
            f2_t v = T;
#pragma unroll
            for (int i0 = 0; i0 < 4; ++i0) { put(KG_L1, 4 * (i1 - 2) + i0, v); if (i0 < 3) v = cmulw(v, z2); }
          } else if (i1 < 8) {
            put(KG_L1, 8 + 2 * (i1 - 4), T); put(KG_L1, 8 + 2 * (i1 - 4) + 1, cmulw(T, z4));
          } else {
            put(KG_L1, 16 + (i1 - 8), T);
          }
        }
      }
    }
  }
  SB;
  u4_t gr[2];
  const unsigned goff = tid * 16;
#define GRL() do { asm volatile("global_load_dwordx4 %0, %1, %2 sc1" : "=v"(gr[0]) : "v"(goff), "s"(ws + OFF_G1) : "memory"); \
                   asm volatile("global_load_dwordx4 %0, %1, %2 sc1" : "=v"(gr[1]) : "v"(goff), "s"(ws + OFF_G1 + 8192) : "memory"); } while (0)
  GRL();
  if (w < 4) XG(2);
  wave_fence();
  f4_t ka[3];
  {
    h8_t Bf[4];
#pragma unroll
    for (int ks = 0; ks < 4; ++ks) Bf[ks] = *(const h8_t*)(hb + KG_R + c * 272 + 64 * ks + 16 * kg);
#pragma unroll
    for (int t = 0; t < 3; ++t) ka[t] = f4_t{0.f, 0.f, 0.f, 0.f};
#pragma unroll
    for (int ks = 0; ks < 4; ++ks)
#pragma unroll
      for (int t = 0; t < 3; ++t) {
        const int off = (t == 0) ? KG_L0 + c * 272 : (t == 1) ? KG_L1 + c * 272 : KG_L1 + (16 + min(c, 8)) * 272;
        const h8_t Af = *(const h8_t*)(hb + off + 64 * ks + 16 * kg);
        ka[t] = __builtin_amdgcn_mfma_f32_16x16x32_f16(Af, Bf[ks], ka[t], 0, 0, 0);
      }
  }
  SB;
  if (w >= 4) XG(2); else XG(3);
  SB;
  const float iSw = pow2_inv(Sw);
  wave_fence();
  if (hw == 0) *(f4_t*)(hb + lane * 16) = ka[1] * iSw;
  else { *(f4_t*)(hb + lane * 16) = ka[0] * iSw; *(f4_t*)(hb + 1024 + lane * 16) = ka[2] * iSw; }
  __syncthreads();
  const unsigned etag = *(const unsigned*)(smem + B_BND + 48 + 8);
  if (w >= 4) XG(3);
  {
    const char* pb = smem + (w ^ 1) * KG_W;
    const float Sc = pow2_scale(((const float*)(smem + B_BND))[2 * cl] + ((const float*)(smem + B_BND))[2 * cl + 1], 15);
    char* kr = smem + B_KC + cl * KRS;
    if (hw == 0) {
      const f4_t k0 = (ka[0] * iSw + *(const f4_t*)(pb + lane * 16)) * Sc, k2 = (ka[2] * iSw + *(const f4_t*)(pb + 1024 + lane * 16)) * Sc;
      *(u2_t*)(kr + 2 * (128 * (kg >> 1) + 4 * (kg & 1) + 8 * c)) = u2_t{pk_f16(k0[0], k0[1]), pk_f16(k0[2], k0[3])};
      const unsigned p01 = pk_f16(k2[0], k2[1]), p23 = pk_f16(k2[2], k2[3]);
      u16* k3 = (u16*)kr + 512 + 64 * kg + c;
      if (kg < 2) { k3[0] = (u16)p01; k3[16] = (u16)(p01 >> 16); k3[32] = (u16)p23; k3[48] = (u16)(p23 >> 16); }
      else if (lane == 32) k3[0] = (u16)p01;
      if (lane == 0) ((float*)(smem + B_BND + 32))[cl] = pow2_inv(Sc);
    } else {
      const f4_t k1 = (ka[1] * iSw + *(const f4_t*)(pb + lane * 16)) * Sc;
      const unsigned p01 = pk_f16(k1[0], k1[1]), p23 = pk_f16(k1[2], k1[3]);
      if (kg < 2) {
        *(u2_t*)(kr + 2 * (256 + 64 * kg + 4 * c)) = u2_t{p01, p23};
      } else {
        *(unsigned*)(kr + 2 * (384 + 64 * (kg - 2) + 2 * c)) = p01;
        *(unsigned*)(kr + 2 * (384 + 64 * (kg - 2) + 32 + 2 * c)) = p23;
      }
    }
  }
  __syncthreads();
  const float invs = ((const float*)(smem + B_BND + 32))[w & 3];
  {
    const int kch = tid >> 7, kc = tid & 127;
    const char* kr = smem + B_KC + kch * KRS;
    const int e0 = (kc < 32) ? 8 * kc : (kc < 64) ? 256 + 4 * (kc - 32) : 384 + 2 * (kc - 64);
    const unsigned* kp = (const unsigned*)(kr + 2 * e0);
    const unsigned d0 = kp[0], d1 = kp[1], d2 = kp[2], d3 = kp[3], d4 = kp[4];
    const unsigned kfirst = *(const u16*)kr, k1024 = *(const u16*)(kr + 2 * 512);
    const int dl = 127 - kc;
    const unsigned kfa = *(const u16*)(kr + 2 * (512 + max(2 * dl - 127, 0))), kfb = *(const u16*)(kr + 2 * (512 + max(2 * dl - 126, 0)));
    char* ki = smem + B_KOFF + kch * KDS;
    const unsigned k0 = d0 & 0xffffu;
    unsigned o0, o1, o2, o3, nx;
    if (kc < 32) { o0 = d0; o1 = d1; o2 = d2; o3 = d3; nx = d4; }
    else if (kc < 64) {
      const unsigned k2 = d0 >> 16, k4 = d1 & 0xffffu, k6 = d1 >> 16, k8 = d2 & 0xffffu;
      o0 = k0 * 0x10001u; o1 = k2 | (k4 << 16); o2 = k4 * 0x10001u; o3 = k6 | (k8 << 16); nx = k8;
    } else {
      const unsigned k4 = d0 >> 16, k8 = d1 & 0xffffu;
      o0 = k0 * 0x10001u; o1 = k0 | (k4 << 16); o2 = k4 * 0x10001u; o3 = k8 * 0x10001u; nx = k8;
    }
    *(u4_t*)(ki + 16 * (kc + 2)) = u4_t{o0, o1, o2, o3};
    *(u4_t*)(ki + KD_O + 16 * (kc + 2)) = u4_t{__builtin_amdgcn_alignbit(o1, o0, 16), __builtin_amdgcn_alignbit(o2, o1, 16),
                                              __builtin_amdgcn_alignbit(o3, o2, 16), __builtin_amdgcn_alignbit(nx, o3, 16)};
    if (kc < 6) {
      const int cp = (kc < 2) ? kc : 128 + kc;
      const unsigned kk = k1024 * 0x10001u;
      const u4_t e = (kc == 2) ? u4_t{kk, kk, k1024, 0u} : u4_t{0u, 0u, 0u, 0u};
      const u4_t o = (kc == 2) ? u4_t{kk, kk, 0u, 0u} : u4_t{0u, 0u, 0u, (kc == 1) ? (kfirst << 16) : 0u};
      *(u4_t*)(ki + 16 * cp) = e;
      *(u4_t*)(ki + KD_O + 16 * cp) = o;
    }
    if (kc < 80) *(unsigned*)(ki + KD_DR + 4 * kc) = (dl >= 64) ? (kfa | (kfb << 16)) : 0u;
  }
  *(unsigned*)(smem + B_UOFF + (tid >> 7) * B_UCH + 4096 + 4 * (tid & 127)) = 0u;
  asm volatile("s_waitcnt vmcnt(2)" : "+v"(gr[0]), "+v"(gr[1]), "+v"(xg[0]), "+v"(xg[1])::"memory");
  {
    for (int spin = 0; spin < SPIN_MAX; ++spin) {
      if (__all(gr[0][1] == etag && gr[0][3] == etag && gr[1][1] == etag && gr[1][3] == etag)) break;
      __builtin_amdgcn_s_sleep(2);
      GRL();
      asm volatile("s_waitcnt vmcnt(0)" : "+v"(gr[0]), "+v"(gr[1])::"memory");
    }
#pragma unroll
    for (int it = 0; it < 2; ++it)
#pragma unroll
      for (int r = 0; r < 2; ++r)
        if (gr[it][2 * r + 1] != etag) gr[it][2 * r] = ln1_row(x, 2 * (tid + 512 * it) + r);
  }
  const float dk[4] = {bf_lo(dkv.x), bf_hi(dkv.x), bf_lo(dkv.y), bf_hi(dkv.y)};
  float z[2][8];
#pragma unroll
  for (int it = 0; it < 2; ++it) {
    if (it == 1) asm volatile("s_waitcnt vmcnt(0)" : "+v"(xg[2]), "+v"(xg[3])::"memory");
    const int j = tid + 512 * it;
    const unsigned s0 = gr[it][0], s1 = gr[it][2];
    const float m0 = bf_lo(s0), r0 = __builtin_amdgcn_rcpf(bf_hi(s0)), m1 = bf_lo(s1), r1 = __builtin_amdgcn_rcpf(bf_hi(s1));
    const u2_t xa = xg[2 * it], xc = xg[2 * it + 1];
    const float x0[4] = {bf_lo(xa.x), bf_hi(xa.x), bf_lo(xa.y), bf_hi(xa.y)};
    const float x1[4] = {bf_lo(xc.x), bf_hi(xc.x), bf_lo(xc.y), bf_hi(xc.y)};
#pragma unroll
    for (int cc = 0; cc < 4; ++cc) {
      const float u0 = (x0[cc] - m0) * r0, u1 = (x1[cc] - m1) * r1;
      *(unsigned*)(smem + B_UOFF + cc * B_UCH + 4 * (1023 - j)) = pk_f16(u1, u0);
      z[it][cc] = x0[cc] + u0 * dk[cc];
      z[it][4 + cc] = x1[cc] + u1 * dk[cc];
    }
  }
  __syncthreads();
  if (w >= 4 && w < 7 && cb * 3 + (w - 4) < 432) pack_wd(wd, ws, smem + B_PACK + (w - 4) * 5120, cb * 3 + (w - 4), lane);
  f4_t acc[8];
  if (w < 4) {
    const char* ub = smem + B_UOFF + w * B_UCH + 4112 - 32 * c - 16 * kg - 512 * 7;
    const char* kb = smem + B_KOFF + w * KDS + ((c & 1) ? 0 : KD_O) + 4 * (12 + ((c + 1) >> 1) - 4 * kg);
    const unsigned* dr = (const unsigned*)(smem + B_KOFF + w * KDS + KD_DR) + (127 - c + 4 * kg);
    const char* uw = smem + B_UOFF + w * B_UCH + 4112 - 32 * c - 16 * kg;
    auto rdB = [&](BFrag& F, int i0, int p) { F.B[p] = *(const h8_t*)(ub + 512 * (7 - p) + 64 * i0); };
    auto rdA = [&](BFrag& F, int i0, int i1, int half) {
      const unsigned* ap = (const unsigned*)(kb + 64 * i0 + 512 * i1) + 2 * half;
      F.A[i1][half] = u2_t{ap[0], ap[1]};
    };
    auto frag = [&](const u2_t (&a)[2]) { return u4_t{a[0].x, a[0].y, a[1].x, a[1].y}; };
    auto rdnext = [&](BFrag& F, int i0, int m) {
      if (m < 8) rdB(F, i0, m); else if (m < 16) rdA(F, i0, (m - 8) >> 1, (m - 8) & 1);
    };
    auto step = [&](auto i0c, const BFrag& F, BFrag& N, auto nextc) {
      constexpr int I0 = decltype(i0c)::value, NX = decltype(nextc)::value;
      int m = 0;
#pragma unroll
      for (int i1 = 0; i1 < 5; ++i1) {
        if (i1 == 4 && I0 != 0) continue;
        const u4_t a = frag(F.A[i1]);
#pragma unroll
        for (int t = i1; t < 8; ++t) {
          if (I0 == 0 && i1 == 0) mfma_f16_0(acc[t], a, F.B[t]); else mfma_f16(acc[t], a, F.B[t - i1]);
          if (NX >= 0) rdnext(N, NX, m);
          SB;
          ++m;
        }
      }
    };
    BFrag F0, F1;
    h8_t U9[4], U7[4];
#pragma unroll
    for (int bt = 0; bt < 4; ++bt) { U9[bt] = *(const h8_t*)(uw - 512 * bt + 16); U7[bt] = *(const h8_t*)(uw - 512 * bt); }
#pragma unroll
    for (int m = 0; m < 16; ++m) rdnext(F0, 0, m);
    rdA(F0, 0, 4, 0); rdA(F0, 0, 4, 1);
    SB;
    f4_t X9[4], X7[4];
#pragma unroll
    for (int bt = 0; bt < 4; ++bt) { mfma_f16_0(X9[bt], __builtin_bit_cast(u4_t, U9[bt]), band9); mfma_f16_0(X7[bt], __builtin_bit_cast(u4_t, U7[bt]), band7); }
    SB;
    step(IC(0), F0, F1, IC(1));
    h8_t Vf[4]; u4_t Tf[4];
    {
      int m = 0;
#pragma unroll
      for (int i1 = 0; i1 < 4; ++i1) {
        const u4_t a = frag(F1.A[i1]);
#pragma unroll
        for (int t = i1; t < 8; ++t) {
          mfma_f16(acc[t], a, F1.B[t - i1]);
          rdnext(F0, 2, m);
          if (m < 4) {
            const u4_t vu = {pk_f16(X7[m][0], X9[m][0]), pk_f16(X7[m][1], X9[m][1]), pk_f16(X7[m][2], X9[m][2]), pk_f16(X7[m][3], X9[m][3])};
            Vf[m] = __builtin_bit_cast(h8_t, vu);
          }
          if (m >= 16 && m < 20) { const unsigned* tp = dr - 16 * (m - 16 + 4); Tf[m - 16] = u4_t{tp[0], tp[1], tp[2], tp[3]}; }
          SB;
          ++m;
        }
      }
    }
    step(IC(2), F0, F1, IC(3));
#pragma unroll
    for (int bt = 0; bt < 4; ++bt)
#pragma unroll
      for (int at = 4 + bt; at < 8; ++at) mfma_f16(acc[at], __builtin_bit_cast(u4_t, Vf[bt]), __builtin_bit_cast(h8_t, Tf[at - bt - 4]));
    SB;
    step(IC(3), F1, F0, IC(4));
    step(IC(4), F0, F1, IC(5));
    step(IC(5), F1, F0, IC(6));
    step(IC(6), F0, F1, IC(7));
    step(IC(7), F1, F0, IC(-1));
    asm volatile("s_nop 7\n\ts_nop 1" : "+v"(acc[0]), "+v"(acc[1]), "+v"(acc[2]), "+v"(acc[3]), "+v"(acc[4]), "+v"(acc[5]), "+v"(acc[6]), "+v"(acc[7]));
  }
  bar();
  if (w < 4) {
#pragma unroll
    for (int t = 0; t < 8; ++t) *(f4_t*)(smem + w * B_YCH + t * 1280 + kg * 320 + c * 16) = acc[t] * invs;
  }
  asm volatile("s_waitcnt lgkmcnt(0)" ::: "memory");
  bar();
#pragma unroll
  for (int it = 0; it < 2; ++it) {
    const int j = tid + 512 * it, t = 2 * j;
    const int yo = (t >> 8) * 1280 + ((t >> 2) & 3) * 320 + ((t >> 4) & 15) * 16 + (t & 3) * 4;
    float h0[4], h1[4];
#pragma unroll
    for (int cc = 0; cc < 4; ++cc) {
      f2_t y = *(const f2_t*)(smem + cc * B_YCH + yo);
      h0[cc] = z[it][cc] + y.x;
      h1[cc] = z[it][4 + cc] + y.y;
    }
    char* dst = ws + OFF_H8 + ((size_t)(q >> 1) * SL + t) * 16 + (q & 1) * 8;
    *(u2_t*)(dst) = u2_t{pk_bf16(h0[0], h0[1]), pk_bf16(h0[2], h0[3])};
    *(u2_t*)(dst + 16) = u2_t{pk_bf16(h1[0], h1[1]), pk_bf16(h1[2], h1[3])};
  }
}

constexpr int C_T = 240;
constexpr int C_SLOT = 23552, C_NSLOT = 6, C_GOFF = 15360;
constexpr int C_STAT = C_NSLOT * C_SLOT;
constexpr int C_CS = C_STAT + 960;
constexpr int C_LDS = C_CS + 512;
constexpr size_t OFF_DUMMY = 0x1140000;

__device__ __forceinline__ unsigned ln_pack(float sum, float sq) {
  const float mean = sum * (1.0f / DM);
  return ln_fin(mean, fmaxf(sq * (1.0f / DM) - mean * mean, 0.f));
}
__device__ __forceinline__ unsigned ln_row(const char* h8, int t) {
  float s = 0.f, q = 0.f;
  for (int pl = 0; pl < 80; ++pl) {
    const u4_t v = *(const u4_t*)(h8 + (size_t)pl * 32768 + t * 16);
    for (int j = 0; j < 4; ++j) { const float a = bf_lo(v[j]), b = bf_hi(v[j]); s += a + b; q += a * a + b * b; }
  }
  return ln_pack(s, q);
}

template <int NDMA>
__device__ __forceinline__ void c_loader(char* smem, char* ws, const char* wbase, int w4, int lane, int t0, int jline, int xcd) {
  const unsigned lds0 = (unsigned)(size_t)(LAS char*)smem;
  const char* h8 = ws + OFF_H8;
  unsigned voffH[4], voffW[2];
#pragma unroll
  for (int k = 0; k < 2; ++k) {
    const int r = 8 * (2 * (w4 & 1) + k) + (lane >> 3), c = lane & 7;
    const int f = ((r >> 1) & 1) | ((r >> 2) & 2);
    voffW[k] = r * (FF * 2) + 128 * jline + (((c >> 1) ^ f) * 32) + (c & 1) * 16;
  }
#pragma unroll
  for (int k = 0; k < 4; ++k) {
    const int idx = 64 * (w4 + 4 * k) + lane, pl = idx / C_T, tt = idx % C_T;
    const int t = min(t0 + tt, SL - 1);
    voffH[k] = pl * 32768 + t * 16;
  }
  const unsigned ldsW = C_GOFF + (w4 >> 1) * 4096 + (w4 & 1) * 2048;
  auto issue = [&](int step, unsigned so) {
#pragma unroll
    for (int k = 0; k < 2; ++k) glds16(wbase + (size_t)step * (32 * FF * 2), voffW[k], lds0 + so + ldsW + k * 1024);
#pragma unroll
    for (int k = 0; k < NDMA - 2; ++k) glds16(h8 + (size_t)step * (4 * 32768), voffH[k], lds0 + so + (w4 + 4 * k) * 1024);
  };
  constexpr unsigned S = C_SLOT;
  issue(0, 0); issue(1, S); issue(2, 2 * S); issue(3, 3 * S);
  wait_vm<3 * NDMA>(); bar();
  issue(4, 4 * S);
  wait_vm<2 * NDMA>(); bar();
  issue(5, 5 * S); issue(6, 0);
  unsigned sw = S;
#pragma unroll 1
  for (int j = 1; j < 7; ++j) {
    wait_vm<2 * NDMA>();
    bar();
    issue(2 * j + 5, sw);
    sw += S; if (sw == C_NSLOT * S) sw = 0;
    issue(2 * j + 6, sw);
    sw += S; if (sw == C_NSLOT * S) sw = 0;
  }
  wait_vm<2 * NDMA>(); bar();
  issue(19, sw);
  const int tl = min(60 * w4 + lane, C_T - 1), tg = min(t0 + tl, SL - 1);
  const gu64* gp = (const gu64*)(ws + OFF_G2) + tg;
  const float* csp = (const float*)(ws + OFF_CS) + ((lane >> 4) & 1) * FF + 64 * jline + 16 * w4 + (lane & 15);
  const char* tb = ws + OFF_WP2 + (size_t)(4 * (xcd & 1)) * (54 * 5120);
  const int tl0 = ((int)(blockIdx.x >> 3) * 4 + w4) * 68;
  u2_t gv; float csv; unsigned tv0, tv1, tv2;
  asm volatile("global_load_dwordx2 %0, %1, off sc1" : "=v"(gv) : "v"(gp) : "memory");
  asm volatile("global_load_dword %0, %1, off" : "=v"(csv) : "v"(csp) : "memory");
  asm volatile("global_load_dword %0, %1, off" : "=v"(tv0) : "v"(tb + (size_t)min(tl0 + lane, 8639) * 128) : "memory");
  asm volatile("global_load_dword %0, %1, off" : "=v"(tv1) : "v"(tb + (size_t)min(tl0 + 64 + (lane & 3), 8639) * 128) : "memory");
  {
    unsigned long long kd;
    asm volatile("s_getpc_b64 s[20:21]\n\ts_add_u32 s20, s20, kD@rel32@lo+4\n\ts_addc_u32 s21, s21, kD@rel32@hi+12\n\ts_mov_b64 %0, s[20:21]" : "=s"(kd) :: "s20", "s21", "scc");
    const char* tp = (lane == 0) ? (const char*)&g_ptrs[xcd] : (const char*)kd + min((int)(blockIdx.x >> 3) * 4 + w4, 79) * 128;
    asm volatile("global_load_dword %0, %1, off" : "=v"(tv2) : "v"(tp) : "memory");
  }
  wait_vm<NDMA + 5>(); bar();
  asm volatile("s_waitcnt vmcnt(0)" : "+v"(gv), "+v"(csv), "+v"(tv0), "+v"(tv1), "+v"(tv2)::"memory");
  bar();
  {
    bool ok = gv.y == 1u;
    for (int spin = 0; spin < 4096 && !__all(ok); ++spin) {
      const unsigned long long g = __hip_atomic_load(gp, __ATOMIC_RELAXED, __HIP_MEMORY_SCOPE_AGENT);
      gv.x = (unsigned)g; ok = (unsigned)(g >> 32) == 1u;
      __builtin_amdgcn_s_sleep(2);
    }
    if (!__all(ok)) gv.x = ln_row(h8, tg);
    if (lane < 60) *(float*)(smem + C_STAT + (60 * w4 + lane) * 4) = bf_lo(gv.x);
    if (lane < 32) *(float*)(smem + C_CS + (lane >> 4) * 256 + (16 * w4 + (lane & 15)) * 4) = csv;
  }
  asm volatile("s_waitcnt lgkmcnt(0)" ::: "memory");
  bar();
}

struct CFrag { b8_t A[2][2]; b8_t B[8]; };

extern "C" __global__ __launch_bounds__(512, 2) void kC() {
  extern __shared__ __attribute__((aligned(16))) char smem[];
  char* __restrict__ ws = ptrs()->ws;
  const u16* __restrict__ wg = ptrs()->wg;
  const u16* __restrict__ wu = ptrs()->wu;
  const int tid = threadIdx.x, lane = tid & 63;
  const int w = __builtin_amdgcn_readfirstlane(tid >> 6);
  int panel, jline;
  const int xcd = blockIdx.x & 7, rank = blockIdx.x >> 3;
  {
    const int g4 = xcd >> 1, a = xcd & 1, nl = (g4 < 3) ? 7 : 6, total = 9 * nl, n0 = (total + 1) >> 1;
    if (a == 0 ? rank >= n0 : rank >= total - n0) {
      const int sp = (xcd < 6) ? (xcd >> 1) : 3 + 5 * (xcd - 6) + (rank - 27);
      const int wv = sp * 8 + w;
      for (int tile = wv; tile < 128; tile += 104) {
        const int t = 16 * tile + (lane & 15);
        const char* hp = ws + OFF_H8 + (size_t)(lane >> 4) * 32768 + t * 16;
        u4_t v[20];
#pragma unroll
        for (int i = 0; i < 20; ++i) v[i] = *(const u4_t*)(hp + (size_t)i * (4 * 32768));
        float s0 = 0.f, s1 = 0.f, q0 = 0.f, q1 = 0.f;
        unsigned k_both = 0x3f803f80u;
        asm volatile("" : "+s"(k_both));
#pragma unroll
        for (int i = 0; i < 20; ++i) {
          s0 = dot2bf(v[i][0], k_both, s0); q0 = dot2bf(v[i][0], v[i][0], q0);
          s1 = dot2bf(v[i][1], k_both, s1); q1 = dot2bf(v[i][1], v[i][1], q1);
          s0 = dot2bf(v[i][2], k_both, s0); q0 = dot2bf(v[i][2], v[i][2], q0);
          s1 = dot2bf(v[i][3], k_both, s1); q1 = dot2bf(v[i][3], v[i][3], q1);
        }
        float s = s0 + s1, q = q0 + q1;
        s += __shfl_xor(s, 16); q += __shfl_xor(q, 16);
        s += __shfl_xor(s, 32); q += __shfl_xor(q, 32);
        if (lane < 16)
          __hip_atomic_store((gu64*)(ws + OFF_G2) + t, (1ull << 32) | ln_pack(s, q), __ATOMIC_RELAXED, __HIP_MEMORY_SCOPE_AGENT);
      }
      return;
    }
    const int idx = rank + a * n0;
    panel = idx / nl; jline = 7 * g4 + idx % nl;
  }
  const int t0 = panel * C_T;
  if (w >= 4) {
    const int w4 = w - 4;
    const char* wbase = (const char*)((w4 >> 1) ? wu : wg);
    if (w4 < 3) c_loader<6>(smem, ws, wbase, w4, lane, t0, jline, xcd);
    else c_loader<5>(smem, ws, wbase, w4, lane, t0, jline, xcd);
    return;
  }
  const int s = w >> 1, th = w & 1;
  const int fr = lane & 15, kg = lane >> 4, q = (lane & 15) >> 2, p = lane & 3;
  const unsigned hb0 = kg * 3840 + fr * 16 + 256 * 7;
  const unsigned hb1 = kg * 3840 + fr * 16 + 256 * (th ? 8 : 0);
  unsigned wa[2];
#pragma unroll
  for (int v = 0; v < 2; ++v) {
    const int sub = 2 * s + (v ? 1 - th : th);
    const int f = (q >> 1) | (2 * (kg & 1));
    wa[v] = C_GOFF + (8 * kg + q) * 128 + ((sub ^ f) * 32) + 8 * p;
  }
  f4_t ag[8][2], au[8][2];
#pragma unroll
  for (int u = 0; u < 8; ++u)
#pragma unroll
    for (int v = 0; v < 2; ++v) { ag[u][v] = f4_t{0.f, 0.f, 0.f, 0.f}; au[u][v] = f4_t{0.f, 0.f, 0.f, 0.f}; }
  auto load = [&](CFrag& F, unsigned so) {
    const char* sp = smem + so;
#pragma unroll
    for (int v = 0; v < 2; ++v)
#pragma unroll
      for (int m = 0; m < 2; ++m) {
        s4_t lo = tr_read(sp + wa[v] + m * 4096), hi = tr_read(sp + wa[v] + m * 4096 + 512);
        u2_t a = __builtin_bit_cast(u2_t, lo), b = __builtin_bit_cast(u2_t, hi);
        F.A[v][m] = __builtin_bit_cast(b8_t, (u4_t{a.x, a.y, b.x, b.y}));
      }
    F.B[0] = *(const b8_t*)(sp + hb0);
#pragma unroll
    for (int u = 1; u < 8; ++u) F.B[u] = *(const b8_t*)(sp + hb1 + 256 * (u - 1));
  };
  auto mma = [&](const CFrag& F) {
    ag[0][0] = __builtin_amdgcn_mfma_f32_16x16x32_bf16(F.A[0][0], F.B[0], ag[0][0], 0, 0, 0);
    au[0][0] = __builtin_amdgcn_mfma_f32_16x16x32_bf16(F.A[0][1], F.B[0], au[0][0], 0, 0, 0);
#pragma unroll
    for (int u = 1; u < 8; ++u)
#pragma unroll
      for (int v = 0; v < 2; ++v) {
        ag[u][v] = __builtin_amdgcn_mfma_f32_16x16x32_bf16(F.A[v][0], F.B[u], ag[u][v], 0, 0, 0);
        au[u][v] = __builtin_amdgcn_mfma_f32_16x16x32_bf16(F.A[v][1], F.B[u], au[u][v], 0, 0, 0);
      }
  };
  auto interleave = [&]() {
#pragma unroll
    for (int i = 0; i < 16; ++i) {
      __builtin_amdgcn_sched_group_barrier(0x008, 1, 0);
      __builtin_amdgcn_sched_group_barrier(0x100, 1, 0);
    }
    __builtin_amdgcn_sched_group_barrier(0x008, 14, 0);
  };
  CFrag F0, F1;
  bar();
  load(F0, 0);
  unsigned sn = C_SLOT;
  SB;
#pragma unroll 1
  for (int j = 0; j < 9; ++j) {
    asm volatile("s_waitcnt lgkmcnt(0)" ::: "memory");
    bar(); SB;
    load(F1, sn);
    mma(F0);
    interleave();
    SB;
    sn += C_SLOT; if (sn == C_NSLOT * C_SLOT) sn = 0;
    load(F0, sn);
    mma(F1);
    interleave();
    SB;
    sn += C_SLOT; if (sn == C_NSLOT * C_SLOT) sn = 0;
  }
  asm volatile("s_waitcnt lgkmcnt(0)" ::: "memory");
  bar(); SB;
  load(F1, sn);
  mma(F0);
  interleave();
  SB;
  bar(); SB;
  f4_t csg[2], csu[2];
  float mu[8];
#pragma unroll
  for (int v = 0; v < 2; ++v) {
    const int sub = 2 * s + (v ? 1 - th : th);
    csg[v] = *(const f4_t*)(smem + C_CS + (16 * sub + 4 * kg) * 4);
    csu[v] = *(const f4_t*)(smem + C_CS + 256 + (16 * sub + 4 * kg) * 4);
  }
#pragma unroll
  for (int u = 0; u < 8; ++u) {
    const int tile = (u == 0) ? 7 : (th ? 7 + u : u - 1);
    mu[u] = *(const float*)(smem + C_STAT + (16 * tile + fr) * 4);
  }
  mma(F1);
  char* hh = ws + OFF_HH + (size_t)(2 * jline + s) * 1024 + lane * 16;
  char* sink = ws + OFF_DUMMY + lane * 16;
  auto epi = [&](auto thc) {
    constexpr int TH = decltype(thc)::value;
#pragma unroll
    for (int u = 0; u < 8; ++u) {
      const int tile = (u == 0) ? 7 : (TH ? 7 + u : u - 1);
      const int t16 = 15 * panel + tile;
      char* dst = (t16 < 128) ? hh + (size_t)t16 * (54 * 1024) : sink;
      u2_t o[2];
#pragma unroll
      for (int v = 0; v < 2; ++v) {
        if (u == 0 && v == 1) continue;
        const f4_t r = (ag[u][v] - mu[u] * csg[v]) * (au[u][v] - mu[u] * csu[v]);
        o[v] = u2_t{pk_bf16(r[0], r[1]), pk_bf16(r[2], r[3])};
      }
      if (u == 0) *(u2_t*)(dst + 8 * TH) = o[0];
      else *(u4_t*)(dst) = u4_t{o[TH].x, o[TH].y, o[1 - TH].x, o[1 - TH].y};
    }
  };
  if (th) epi(IC(1)); else epi(IC(0));
}

constexpr int D_SLOT = 15360, D_NSLOT = 9;
constexpr int D_LDS = D_NSLOT * D_SLOT;

template <int NDMA>
__device__ __forceinline__ void d_loader(char* smem, const char* wp, int w4, int lane) {
  const unsigned lds0 = (unsigned)(size_t)(LAS char*)smem;
  const unsigned voff = lane * 16;
  auto issue = [&](int step, unsigned so) {
#pragma unroll
    for (int k = 0; k < NDMA; ++k) {
      const int i = w4 + 4 * k;
      glds16(wp + (size_t)step * D_SLOT + i * 1024, voff, lds0 + so + i * 1024);
    }
  };
  constexpr unsigned S = D_SLOT;
  issue(0, 0); issue(1, S); issue(2, 2 * S);
  wait_vm<2 * NDMA>(); bar();
  issue(3, 3 * S); issue(4, 4 * S); issue(5, 5 * S);
  wait_vm<3 * NDMA>(); bar();
  issue(6, 6 * S); issue(7, 7 * S); issue(8, 8 * S);
  unsigned sw = 0;
#pragma unroll 1
  for (int j = 1; j < 5; ++j) {
    wait_vm<4 * NDMA>();
    bar();
    issue(2 * j + 7, sw);
    sw += S; if (sw == D_NSLOT * S) sw = 0;
    issue(2 * j + 8, sw);
    sw += S; if (sw == D_NSLOT * S) sw = 0;
  }
  wait_vm<4 * NDMA>(); bar();
  issue(17, sw);
  wait_vm<3 * NDMA>(); bar();
  wait_vm<1 * NDMA>(); bar();
  wait_vm<0>(); bar();
}

extern "C" __global__ __launch_bounds__(512, 2) void kD() {
  extern __shared__ __attribute__((aligned(16))) char smem[];
  char* __restrict__ ws = ptrs()->ws;
  u16* __restrict__ out = ptrs()->out;
  const int tid = threadIdx.x, lane = tid & 63;
  const int w = __builtin_amdgcn_readfirstlane(tid >> 6);
  const int xcd = blockIdx.x & 7, rank = blockIdx.x >> 3;
  const int it = 8 * (xcd >> 1) + (rank & 7), dg = 4 * (xcd & 1) + (rank >> 3);
  if (w >= 4) {
    const int w4 = w - 4;
    const char* wp = ws + OFF_WP2 + (size_t)dg * (54 * 5120);
    if (w4 < 3) d_loader<4>(smem, wp, w4, lane);
    else d_loader<3>(smem, wp, w4, lane);
    return;
  }
  const int fr = lane & 15, g = lane >> 4;
  const int t = 64 * it + 16 * w + fr;
  const char* hp = ws + OFF_HH + (size_t)(4 * it + w) * (54 * 1024);
  const unsigned voff = lane * 16;
  u4_t R[18];
#pragma unroll
  for (int i = 0; i < 9; ++i)
    asm volatile("global_load_dwordx4 %0, %1, %2" : "=v"(R[i]) : "v"(voff), "s"(hp + (size_t)i * 1024) : "memory");
  u2_t res[5]; unsigned st;
  f4_t acc[5];
#pragma unroll
  for (int dt = 0; dt < 5; ++dt) acc[dt] = f4_t{0.f, 0.f, 0.f, 0.f};
  b8_t A0[5], A1[5];
  auto rdA = [&](b8_t (&A)[5], unsigned off) {
    const char* sp = smem + off + lane * 16;
#pragma unroll
    for (int dt = 0; dt < 5; ++dt) A[dt] = *(const b8_t*)(sp + dt * 1024);
  };
  bar();
  rdA(A0, 0);
  unsigned so = 0;
  auto kstep = [&](auto mode, auto idx, b8_t (&Ac)[5], b8_t (&An)[5], const char* hnext) {
    constexpr int MODE = decltype(mode)::value, i = decltype(idx)::value, kk = i % 3;
    if (i % 6 == 0) bar();
    unsigned nx = so + (kk + 1) * 5120;
    if (kk == 2) { so += D_SLOT; if (so == D_NSLOT * D_SLOT) so = 0; nx = so; }
    SB;
    if (!(MODE == 2 && i == 17)) rdA(An, nx);
    constexpr int NV = (MODE == 0) ? (i < 9 ? 8 + i : 26 - i) : (MODE == 1) ? (i < 9 ? 25 - i : 17) : (i == 0 ? 17 : 23 - i);
    asm volatile("s_waitcnt vmcnt(%1)" : "+v"(R[i]) : "i"(NV) : "memory");
    const b8_t B = __builtin_bit_cast(b8_t, R[i]);
#pragma unroll
    for (int dt = 0; dt < 5; ++dt) acc[dt] = __builtin_amdgcn_mfma_f32_16x16x32_bf16(Ac[dt], B, acc[dt], 0, 0, 0);
    SB;
    if (MODE == 0 && i < 9)
      asm volatile("global_load_dwordx4 %0, %1, %2" : "=v"(R[i < 9 ? 9 + i : 0]) : "v"(voff), "s"(hp + (size_t)(9 + i) * 1024) : "memory");
    if (MODE != 2) asm volatile("global_load_dwordx4 %0, %1, %2" : "+v"(R[i]) : "v"(voff), "s"(hnext + (size_t)i * 1024) : "memory");
    if (MODE == 2 && i == 0) {
#pragma unroll
      for (int dt = 0; dt < 5; ++dt) {
        const int d = 80 * dg + 16 * dt + 4 * g;
        const char* rp = ws + OFF_H8 + ((size_t)(d >> 3) * SL + t) * 16 + (d & 4) * 2;
        asm volatile("global_load_dwordx2 %0, %1, off" : "=v"(res[dt]) : "v"(rp) : "memory");
      }
      asm volatile("global_load_dword %0, %1, off" : "=v"(st) : "v"(ws + OFF_G2 + (size_t)t * 8) : "memory");
    }
    SB;
  };
  auto round = [&](auto mode, const char* hnext) {
    kstep(mode, IC(0), A0, A1, hnext); kstep(mode, IC(1), A1, A0, hnext); kstep(mode, IC(2), A0, A1, hnext);
    kstep(mode, IC(3), A1, A0, hnext); kstep(mode, IC(4), A0, A1, hnext); kstep(mode, IC(5), A1, A0, hnext);
    kstep(mode, IC(6), A0, A1, hnext); kstep(mode, IC(7), A1, A0, hnext); kstep(mode, IC(8), A0, A1, hnext);
    kstep(mode, IC(9), A1, A0, hnext); kstep(mode, IC(10), A0, A1, hnext); kstep(mode, IC(11), A1, A0, hnext);
    kstep(mode, IC(12), A0, A1, hnext); kstep(mode, IC(13), A1, A0, hnext); kstep(mode, IC(14), A0, A1, hnext);
    kstep(mode, IC(15), A1, A0, hnext); kstep(mode, IC(16), A0, A1, hnext); kstep(mode, IC(17), A1, A0, hnext);
  };
  round(IC(0), hp + 18 * 1024);
  round(IC(1), hp + 36 * 1024);
  round(IC(2), hp);
  asm volatile("s_waitcnt vmcnt(0)" : "+v"(res[0]), "+v"(res[1]), "+v"(res[2]), "+v"(res[3]), "+v"(res[4]), "+v"(st)::"memory");
  const float sd = bf_hi(st), r2 = 1.0f / (sd * sd);
#pragma unroll
  for (int dt = 0; dt < 5; ++dt) {
    const int d = 80 * dg + 16 * dt + 4 * g;
    const float h0 = bf_lo(res[dt].x), h1 = bf_hi(res[dt].x), h2 = bf_lo(res[dt].y), h3 = bf_hi(res[dt].y);
    *(u2_t*)(out + (size_t)t * DM + d) = u2_t{pk_bf16(h0 + r2 * acc[dt][0], h1 + r2 * acc[dt][1]), pk_bf16(h2 + r2 * acc[dt][2], h3 + r2 * acc[dt][3])};
  }
}

extern "C" void kernel_launch(void* const* d_in, const int* in_sizes, int n_in, void* d_out, int out_size, void* d_ws,
                              size_t ws_size, hipStream_t stream) {
  char* ws = (char*)d_ws;
  (void)hipFuncSetAttribute((const void*)kB, hipFuncAttributeMaxDynamicSharedMemorySize, B_LDS);
  (void)hipFuncSetAttribute((const void*)kC, hipFuncAttributeMaxDynamicSharedMemorySize, C_LDS);
  (void)hipFuncSetAttribute((const void*)kD, hipFuncAttributeMaxDynamicSharedMemorySize, D_LDS);
  kB<<<256, 512, B_LDS, stream>>>((const u16*)d_in[0], (const u16*)d_in[1], (const u16*)d_in[2], (const u16*)d_in[3], (const u16*)d_in[4],
                                  (const u16*)d_in[5], (const u16*)d_in[6], (const u16*)d_in[7], (const u16*)d_in[8], (const u16*)d_in[10],
                                  (const u16*)d_in[11], (const u16*)d_in[12], (u16*)d_out, ws);
  kC<<<256, 512, C_LDS, stream>>>();
  kD<<<256, 512, D_LDS, stream>>>();
}
```

```cpp
#include <hip/hip_runtime.h>
#include <hip/hip_bf16.h>
#include <stdint.h>
#include <type_traits>

typedef unsigned short u16;
typedef _Float16 h2_t __attribute__((ext_vector_type(2)));
typedef _Float16 h8_t __attribute__((ext_vector_type(8)));
typedef __bf16 b2_t __attribute__((ext_vector_type(2)));
typedef __bf16 b8_t __attribute__((ext_vector_type(8)));
typedef float f2_t __attribute__((ext_vector_type(2)));
typedef float f4_t __attribute__((ext_vector_type(4)));
typedef unsigned u2_t __attribute__((ext_vector_type(2)));
typedef unsigned u4_t __attribute__((ext_vector_type(4)));
typedef short s4_t __attribute__((ext_vector_type(4)));
typedef __attribute__((address_space(1))) unsigned long long gu64;
#define LAS __attribute__((address_space(3)))
#define SB __builtin_amdgcn_sched_barrier(0)
#define IC(n) std::integral_constant<int, n>{}

constexpr int SL = 2048;
constexpr int DM = 640;
constexpr int FF = 1728;

constexpr size_t OFF_G1 = 0;
constexpr size_t OFF_EP = 0x10000;
constexpr size_t OFF_RD = 0x10100;
constexpr size_t OFF_H8 = 0x5A0000;
constexpr size_t OFF_HH = 0x820000;
constexpr size_t OFF_WP2 = 0xF00000;
constexpr size_t OFF_CS = 0x1120000;
constexpr size_t OFF_G2 = 0x1130000;
constexpr int KRS = 1344;
constexpr int KDS = 4864;
constexpr int KD_O = 2240;
constexpr int KD_DR = 4384;

__device__ __forceinline__ float bf_lo(unsigned d) { return __uint_as_float(d << 16); }
__device__ __forceinline__ float bf_hi(unsigned d) { return __uint_as_float(d & 0xffff0000u); }
__device__ __forceinline__ float bf2f(u16 v) { return __uint_as_float((unsigned)v << 16); }
__device__ __forceinline__ unsigned pk_bf16(float a, float b) {
  f2_t v = {a, b};
  return __builtin_bit_cast(unsigned, __builtin_convertvector(v, b2_t));
}
__device__ __forceinline__ unsigned pk_f16(float a, float b) {
  f2_t v = {a, b};
  return __builtin_bit_cast(unsigned, __builtin_convertvector(v, h2_t));
}
__device__ __forceinline__ float rbf(float a) { return bf_lo(pk_bf16(a, 0.f)); }
template <int N> __device__ __forceinline__ float dpp_ror_add(float v) {
  int r = __builtin_amdgcn_update_dpp(0, __float_as_int(v), 0x120 + N, 0xf, 0xf, false);
  return v + __int_as_float(r);
}
__device__ __forceinline__ float row16_sum(float v) {
  v = dpp_ror_add<8>(v); v = dpp_ror_add<4>(v); v = dpp_ror_add<2>(v); v = dpp_ror_add<1>(v);
  return v;
}
__device__ __forceinline__ float wave_sum(float v) {
  v = row16_sum(v);
  const int b = __float_as_int(v);
  return (__int_as_float(__builtin_amdgcn_readlane(b, 0)) + __int_as_float(__builtin_amdgcn_readlane(b, 16))) +
         (__int_as_float(__builtin_amdgcn_readlane(b, 32)) + __int_as_float(__builtin_amdgcn_readlane(b, 48)));
}
template <typename T> __device__ __forceinline__ T ldnt(const T* p) { return __builtin_nontemporal_load(p); }
__device__ __forceinline__ void wave_fence() {
  __builtin_amdgcn_fence(__ATOMIC_RELEASE, "wavefront");
  __builtin_amdgcn_wave_barrier();
  __builtin_amdgcn_fence(__ATOMIC_ACQUIRE, "wavefront");
}
__device__ __forceinline__ s4_t tr_read(const char* p) {
  return __builtin_amdgcn_ds_read_tr16_b64_v4i16((LAS s4_t*)p);
}
__device__ __forceinline__ void glds16(const void* sbase, unsigned voff, unsigned lds_addr) {
  asm volatile("s_mov_b32 m0, %2\n\ts_nop 0\n\tglobal_load_lds_dwordx4 %0, %1" ::"v"(voff), "s"(sbase), "s"(lds_addr) : "memory");
}
template <int N> __device__ __forceinline__ void wait_vm() { asm volatile("s_waitcnt vmcnt(%0)" ::"i"(N) : "memory"); }
__device__ __forceinline__ void bar() { asm volatile("s_barrier" ::: "memory"); }
__device__ __forceinline__ float dot2bf(unsigned a, unsigned b, float c) {
  return __builtin_amdgcn_fdot2_f32_bf16(__builtin_bit_cast(b2_t, a), __builtin_bit_cast(b2_t, b), c, false);
}

struct __attribute__((aligned(128))) Ptrs { char* ws; const u16* wg; const u16* wu; u16* out; void* pad[12]; };
__device__ Ptrs g_ptrs[8];
typedef const __attribute__((address_space(4))) Ptrs* cptrs_t;
__device__ __forceinline__ cptrs_t ptrs() { return (cptrs_t)(unsigned long long)&g_ptrs[blockIdx.x & 7]; }

struct Cw { f2_t w, ws; };
__device__ __forceinline__ Cw mkw(f2_t w) { Cw r; r.w = w; r.ws = f2_t{-w.y, w.x}; return r; }
__device__ __forceinline__ f2_t cmulw(f2_t a, const Cw& W) {
  return __builtin_elementwise_fma(f2_t{a.y, a.y}, W.ws, f2_t{a.x, a.x} * W.w);
}
__device__ __forceinline__ f2_t cexp_l(float er, float wr, float l) {
  float mag = __builtin_amdgcn_exp2f(er * l);
  float rev = __builtin_amdgcn_fractf(wr * l);
  return f2_t{mag * __builtin_amdgcn_cosf(rev), mag * __builtin_amdgcn_sinf(rev)};
}
__device__ __forceinline__ float pow2_scale(float bound, int target) {
  unsigned e = (__float_as_uint(bound) >> 23) & 0xff;
  int k = target - ((int)e - 126);
  k = k > 126 ? 126 : (k < -126 ? -126 : k);
  return (bound > 0.f) ? __uint_as_float((unsigned)(k + 127) << 23) : 1.0f;
}
__device__ __forceinline__ float pow2_inv(float p) { return __uint_as_float(0x7f000000u - __float_as_uint(p)); }

constexpr int B_SIDE = 96;
constexpr int KG_R = 0, KG_L0 = 4352, KG_L1 = 8704, KG_W = 8704 + 25 * 272;
constexpr int B_BND = 8 * KG_W;
constexpr int B_KC = B_BND + 64;
constexpr int B_LDS = B_KC + 4 * KRS;
constexpr int B_KOFF = 0;
constexpr int B_UOFF = 4 * KDS;
constexpr int B_UCH = 4608;
constexpr int B_YCH = 10240;
constexpr int B_PACK = 40960;

__device__ __forceinline__ void pack_wd(const u16* __restrict__ wd, char* __restrict__ ws, char* buf, int u, int lane) {
  const int ks = u >> 3, dg = u & 7;
#pragma unroll
  for (int i = 0; i < 5; ++i) {
    const int idx = i * 64 + lane, row = idx / 10, c = idx % 10;
    __builtin_amdgcn_global_load_lds((const unsigned*)((const char*)wd + (size_t)(32 * ks + row) * 1280 + 160 * dg + 16 * c),
                                     (LAS unsigned*)(buf + i * 1024), 16, 0, 2);
  }
  asm volatile("s_waitcnt vmcnt(0)" ::: "memory");
  wave_fence();
  const int g = lane >> 4, q = (lane & 15) >> 2, p = lane & 3;
  char* dst = ws + OFF_WP2 + ((size_t)(dg * 54 + ks) * 5) * 1024 + lane * 16;
#pragma unroll
  for (int dt = 0; dt < 5; ++dt) {
    s4_t aa = tr_read(buf + (4 * g + q) * 160 + 32 * dt + 8 * p);
    s4_t bb = tr_read(buf + (16 + 4 * g + q) * 160 + 32 * dt + 8 * p);
    u2_t ua = __builtin_bit_cast(u2_t, aa), ub = __builtin_bit_cast(u2_t, bb);
    const u4_t pv = {ua.x, ua.y, ub.x, ub.y};
    asm volatile("global_store_dwordx4 %0, %1, off sc1\n\ts_nop 1" ::"v"(dst + dt * 1024), "v"(pv) : "memory");
  }
}

struct BFrag { h8_t B[8]; u2_t A[5][2]; };
__device__ __forceinline__ void mfma_f16(f4_t& d, u4_t a, h8_t b) {
  asm volatile("v_mfma_f32_16x16x32_f16 %0, %1, %2, %0" : "+v"(d) : "v"(a), "v"(b));
}
__device__ __forceinline__ void mfma_f16_0(f4_t& d, u4_t a, h8_t b) {
  asm volatile("v_mfma_f32_16x16x32_f16 %0, %1, %2, 0" : "=&v"(d) : "v"(a), "v"(b));
}
__device__ __forceinline__ unsigned ln_fin(float mean, float var) {
  const float mb = rbf(mean), sd = rbf(sqrtf(rbf(rbf(var) + 1e-5f)));
  return (__float_as_uint(mb) >> 16) | (__float_as_uint(sd) & 0xffff0000u);
}
__device__ __forceinline__ unsigned ln1_row(const u16* __restrict__ x, int t) {
  const u4_t* p = (const u4_t*)(x + (size_t)t * DM);
  float s = 0.f, q = 0.f;
  for (int i = 0; i < 80; ++i) { const u4_t v = p[i]; for (int j = 0; j < 4; ++j) s += bf_lo(v[j]) + bf_hi(v[j]); }
  const float mean = s * (1.0f / DM);
  for (int i = 0; i < 80; ++i) {
    const u4_t v = p[i];
    for (int j = 0; j < 4; ++j) { const float a = bf_lo(v[j]) - mean, b = bf_hi(v[j]) - mean; q += a * a; q += b * b; }
  }
  return ln_fin(mean, q * (1.0f / DM));
}
constexpr int SPIN_MAX = 1 << 14;

extern "C" __global__ __launch_bounds__(512, 2) void kB(const u16* __restrict__ x, const u16* __restrict__ Are, const u16* __restrict__ Aim,
                                                     const u16* __restrict__ Bre, const u16* __restrict__ Bim, const u16* __restrict__ Cre,
                                                     const u16* __restrict__ Cim, const u16* __restrict__ logdt, const u16* __restrict__ dskip,
                                                     const u16* __restrict__ wg, const u16* __restrict__ wu, const u16* __restrict__ wd,
                                                     u16* __restrict__ outp, char* __restrict__ ws) {
  extern __shared__ __attribute__((aligned(16))) char smem[];
  const int tid = threadIdx.x, lane = tid & 63;
  const int w = __builtin_amdgcn_readfirstlane(tid >> 6);
  const int xcd = blockIdx.x & 7;
#define EPLOAD(v) asm volatile("global_load_dwordx2 %0, %1, %2 sc1" : "=v"(v) : "v"(0u), "s"(ws + OFF_EP) : "memory")
  auto post_epoch = [&](u2_t ep, int off) {
    if (tid == 0) {
      const unsigned long long e = (((unsigned long long)ep.y << 32) | ep.x) + 1ull;
      *(unsigned long long*)(smem + off) = e;
      *(unsigned*)(smem + off + 8) = (unsigned)((e * 0x9E3779B97F4A7C15ull) >> 32);
      __hip_atomic_store((gu64*)(ws + OFF_RD) + blockIdx.x, e, __ATOMIC_RELAXED, __HIP_MEMORY_SCOPE_AGENT);
    }
  };
  if (blockIdx.x < B_SIDE) {
    const int rk = blockIdx.x >> 3;
    const int g4 = xcd >> 1, a = xcd & 1, nl = (g4 < 3) ? 7 : 6;
    constexpr int S_EP = 17408;
    unsigned pf[2] = {0u, 0u};
    if (rk >= 8) {
#pragma unroll
      for (int k = 0; k < 2; ++k) {
        const int id = (((rk - 8) * 8 + w) * 2 + k) * 64 + lane;
        asm volatile("global_load_dword %0, %1, off" : "=v"(pf[k]) : "v"((const char*)x + (size_t)(id >> 1) * (DM * 2) + (((160 * xcd) >> 7) + (id & 1)) * 128) : "memory");
      }
    }
    if (rk < 8) {
      const int r = lane >> 4, c = lane & 15, t = (blockIdx.x * 8 + w) * 4 + r;
      const u4_t* p = (const u4_t*)(x + (size_t)t * DM);
      u2_t ep;
      EPLOAD(ep);
      u4_t v[5];
#pragma unroll
      for (int i = 0; i < 5; ++i) v[i] = ldnt(p + c + 16 * i);
      float s = 0.f;
#pragma unroll
      for (int i = 0; i < 5; ++i)
#pragma unroll
        for (int j = 0; j < 4; ++j) s += bf_lo(v[i][j]) + bf_hi(v[i][j]);
      asm volatile("" : "+v"(ep) : "v"(s));
      post_epoch(ep, S_EP);
      const float mean = row16_sum(s) * (1.0f / DM);
      float q = 0.f;
#pragma unroll
      for (int i = 0; i < 5; ++i)
#pragma unroll
        for (int j = 0; j < 4; ++j) {
          const float aa = bf_lo(v[i][j]) - mean, bb = bf_hi(v[i][j]) - mean;
          q += aa * aa; q += bb * bb;
        }
      const unsigned sv = ln_fin(mean, row16_sum(q) * (1.0f / DM));
      __syncthreads();
      const u2_t e = *(const u2_t*)(smem + S_EP);
      if (c == 0) {
        __hip_atomic_store((gu64*)(ws + OFF_G1) + t, ((unsigned long long)*(const unsigned*)(smem + S_EP + 8) << 32) | sv, __ATOMIC_RELAXED, __HIP_MEMORY_SCOPE_AGENT);
        ((unsigned long long*)(ws + OFF_G2))[t] = 0ull;
      }
      if (rk == 0 && w == 7) {
        gu64* gp = (gu64*)(unsigned long long)&g_ptrs[xcd];
        if (lane < 4) {
          const unsigned long long v = lane == 0 ? (unsigned long long)ws : lane == 1 ? (unsigned long long)wg : lane == 2 ? (unsigned long long)wu : (unsigned long long)outp;
          __hip_atomic_store(gp + lane, v, __ATOMIC_RELAXED, __HIP_MEMORY_SCOPE_AGENT);
        }
        unsigned back;
        asm volatile("s_waitcnt vmcnt(0)\n\tglobal_load_dword %0, %1, off\n\ts_waitcnt vmcnt(0)" : "=&v"(back) : "v"(gp) : "memory");
        asm volatile("" ::"v"(back));
      }
      if (rk == 4 && w == 0) {
        unsigned long long a0, a1;
        asm volatile("s_getpc_b64 s[20:21]\n\ts_add_u32 s20, s20, kB@rel32@lo+4\n\ts_addc_u32 s21, s21, kB@rel32@hi+12\n\ts_mov_b64 %0, s[20:21]" : "=s"(a0) :: "s20", "s21", "scc");
        asm volatile("s_getpc_b64 s[20:21]\n\ts_add_u32 s20, s20, kD@rel32@lo+4\n\ts_addc_u32 s21, s21, kD@rel32@hi+12\n\ts_mov_b64 %0, s[20:21]" : "=s"(a1) :: "s20", "s21", "scc");
        const unsigned long long lo = a0 < a1 ? a0 : a1, last = ((a0 < a1 ? a1 - a0 : a0 - a1) + 8192) & ~127ull;
        unsigned cf[8];
#pragma unroll
        for (int i = 0; i < 8; ++i) {
          const unsigned long long o = (unsigned long long)(lane + 64 * i) * 128;
          asm volatile("global_load_dword %0, %1, off" : "=v"(cf[i]) : "v"(lo + (o < last ? o : last)) : "memory");
        }
        asm volatile("s_waitcnt vmcnt(0)" ::"v"(cf[0]), "v"(cf[1]), "v"(cf[2]), "v"(cf[3]), "v"(cf[4]), "v"(cf[5]), "v"(cf[6]), "v"(cf[7]) : "memory");
      }
      if (blockIdx.x == 63 && w == 7) {
        const unsigned long long e64 = ((unsigned long long)e.y << 32) | e.x;
        for (int spin = 0; spin < SPIN_MAX; ++spin) {
          bool ok = true;
#pragma unroll
          for (int i = 0; i < 4; ++i) ok &= __hip_atomic_load((const gu64*)(ws + OFF_RD) + lane + 64 * i, __ATOMIC_RELAXED, __HIP_MEMORY_SCOPE_AGENT) == e64;
          if (__all(ok)) break;
          __builtin_amdgcn_s_sleep(4);
        }
        if (lane == 0) __hip_atomic_store((gu64*)(ws + OFF_EP), e64, __ATOMIC_RELAXED, __HIP_MEMORY_SCOPE_AGENT);
      }
    }
    const int strip = (rk >= 8) ? rk - 8 : 4 + rk;
    if ((rk >= 8 || rk < 3) && strip < nl) {
      const int jl = 7 * g4 + strip;
      const char* pm = (const char*)(a ? wu : wg) + (size_t)(80 * w + (lane >> 3)) * (FF * 2) + 128 * jl + (lane & 7) * 16;
      u2_t ep;
      EPLOAD(ep);
      u4_t v[10];
#pragma unroll
      for (int i = 0; i < 10; ++i) v[i] = ldnt((const u4_t*)(pm + (size_t)i * 8 * (FF * 2)));
      const char* po = (const char*)(a ? wg : wu) + (size_t)(80 * w) * (FF * 2) + 128 * jl;
      unsigned t0v, t1v;
      asm volatile("global_load_dword %0, %1, off nt" : "=v"(t0v) : "v"(po + (size_t)lane * (FF * 2)) : "memory");
      asm volatile("global_load_dword %0, %1, off nt" : "=v"(t1v) : "v"(po + (size_t)(64 + (lane & 15)) * (FF * 2)) : "memory");
      f2_t s[4];
#pragma unroll
      for (int e = 0; e < 4; ++e) s[e] = f2_t{0.f, 0.f};
#pragma unroll
      for (int i = 0; i < 10; ++i)
#pragma unroll
        for (int jj = 0; jj < 4; ++jj) s[jj] += f2_t{bf_lo(v[i][jj]), bf_hi(v[i][jj])};
      asm volatile("" : "+v"(ep), "+v"(pf[0]), "+v"(pf[1]) : "v"(s[0]));
      if (rk >= 8) post_epoch(ep, S_EP);
      float* pp = (float*)smem;
      *(f4_t*)(pp + (w * 8 + (lane >> 3)) * 68 + (lane & 7) * 8) = f4_t{s[0].x, s[0].y, s[1].x, s[1].y};
      *(f4_t*)(pp + (w * 8 + (lane >> 3)) * 68 + (lane & 7) * 8 + 4) = f4_t{s[2].x, s[2].y, s[3].x, s[3].y};
      __syncthreads();
      float acc = 0.f;
#pragma unroll
      for (int i = 0; i < 8; ++i) acc += pp[(w * 8 + i) * 68 + lane];
      __syncthreads();
      pp[w * 64 + lane] = acc;
      __syncthreads();
      if (w == 0) {
        float t = 0.f;
#pragma unroll
        for (int i = 0; i < 8; ++i) t += pp[i * 64 + lane];
        ((float*)(ws + OFF_CS))[a * FF + 64 * jl + lane] = t;
      }
      asm volatile("s_waitcnt vmcnt(0)" ::"v"(t0v), "v"(t1v) : "memory");
    }
    return;
  }
  const int cb = blockIdx.x - B_SIDE;
  const int q = 20 * xcd + (cb >> 3);
  const int cl = w >> 1, hw = w & 1, ch = 4 * q + cl;
  const int n = ch * 128 + hw * 64 + lane;
  u2_t ep;
  EPLOAD(ep);
  const u16 i_are = ldnt(Are + n), i_aim = ldnt(Aim + n), i_bre = ldnt(Bre + n), i_bim = ldnt(Bim + n), i_cre = ldnt(Cre + n), i_cim = ldnt(Cim + n);
  const u16 i_dt = logdt[ch];
  u2_t xg[4];
  const unsigned xoff = tid * (2 * DM * 2);
  const char* xb = (const char*)(x + 4 * q);
#define XG(i) asm volatile("global_load_dwordx2 %0, %1, %2 offset:%3" : "=v"(xg[i]) : "v"(xoff), "s"(xb + ((i) >> 1) * (size_t)(1024 * DM * 2)), "i"(((i) & 1) * DM * 2) : "memory")
  const u2_t dkv = *(const u2_t*)(dskip + 4 * q);
  const int c = lane & 15, kg = lane >> 4;
  h8_t band9, band7;
#pragma unroll
  for (int j = 0; j < 8; ++j) {
    const int k = 8 * kg + 7 - j;
    band9[j] = (k >= c + 4 && k <= c + 12) ? (_Float16)1.0f : (_Float16)0.0f;
    band7[j] = (k >= c + 5 && k <= c + 11) ? (_Float16)1.0f : (_Float16)0.0f;
  }
  asm volatile("" : "+v"(band9), "+v"(band7));
  char* hb = smem + w * KG_W;
  float Sw, bsum;
  {
    const float dt = rbf(expf(bf2f(i_dt)));
    const float are = dt * bf2f(i_are), aim = dt * bf2f(i_aim);
    const float bre = dt * bf2f(i_bre), bim = dt * bf2f(i_bim);
    const float cre = bf2f(i_cre), cim = bf2f(i_cim);
    f2_t c0 = {cre * bre - cim * bim, cre * bim + cim * bre};
    const float er = are * 1.44269504088896341f, wr = aim * 0.15915494309189535f;
    const float bnd = (fabsf(c0.x) + fabsf(c0.y)) * fmaxf(1.0f, __builtin_amdgcn_exp2f(er * 2048.f));
    bsum = wave_sum(bnd);
    asm volatile("" : "+v"(ep) : "v"(bnd));
    post_epoch(ep, B_BND + 48);
    if (lane == 0) ((float*)(smem + B_BND))[w] = bsum;
    Sw = pow2_scale(bsum, 15);
    c0 *= Sw;
    const Cw z1 = mkw(cexp_l(er, wr, 1.f)), w8 = mkw(cexp_l(er, -wr, 8.f)), w128 = mkw(cexp_l(er, wr, 128.f));
    const Cw z2 = mkw(cmulw(z1.w, z1));
    const Cw z4 = mkw(cmulw(z2.w, z2));
    auto put = [&](int off, int row, f2_t v) { *(unsigned*)(hb + off + row * 272 + 4 * lane) = pk_f16(v.x, v.y); };
    f2_t R = w8.w, v0 = c0, v1 = cmulw(c0, w128), T = v1;
    *(unsigned*)(hb + KG_R + 4 * lane) = pk_f16(1.f, 0.f);
#pragma unroll
    for (int i = 0; i < 8; ++i) {
      SB;
      if ((w & 3) == (i & 3)) { if (i < 4) XG(0); else XG(1); }
      SB;
      *(unsigned*)(hb + KG_R + (2 * i + 1) * 272 + 4 * lane) = pk_f16(R.x, R.y);
      if (i < 7) {
        R = cmulw(R, w8);
        *(unsigned*)(hb + KG_R + (2 * i + 2) * 272 + 4 * lane) = pk_f16(R.x, R.y);
        R = cmulw(R, w8);
      }
      put(KG_L0, i, v0); put(KG_L0, 8 + i, v1);
      if (i < 7) { v0 = cmulw(v0, z1); v1 = cmulw(v1, z1); }
#pragma unroll
      for (int k = 0; k < 2; ++k) {
        const int i1 = 2 + 2 * i + k;
        if (i1 <= 16) {
          T = cmulw(T, w128);
          if (i1 < 4) {
            f2_t v = T;
#pragma unroll
            for (int i0 = 0; i0 < 4; ++i0) { put(KG_L1, 4 * (i1 - 2) + i0, v); if (i0 < 3) v = cmulw(v, z2); }
          } else if (i1 < 8) {
            put(KG_L1, 8 + 2 * (i1 - 4), T); put(KG_L1, 8 + 2 * (i1 - 4) + 1, cmulw(T, z4));
          } else {
            put(KG_L1, 16 + (i1 - 8), T);
          }
        }
      }
    }
  }
  SB;
  u4_t gr[2];
  const unsigned goff = tid * 16;
#define GRL() do { asm volatile("global_load_dwordx4 %0, %1, %2 sc1" : "=v"(gr[0]) : "v"(goff), "s"(ws + OFF_G1) : "memory"); \
                   asm volatile("global_load_dwordx4 %0, %1, %2 sc1" : "=v"(gr[1]) : "v"(goff), "s"(ws + OFF_G1 + 8192) : "memory"); } while (0)
  GRL();
  if (w < 4) XG(2);
  wave_fence();
  f4_t ka[3];
  {
    h8_t Bf[4];
#pragma unroll
    for (int ks = 0; ks < 4; ++ks) Bf[ks] = *(const h8_t*)(hb + KG_R + c * 272 + 64 * ks + 16 * kg);
#pragma unroll
    for (int t = 0; t < 3; ++t) ka[t] = f4_t{0.f, 0.f, 0.f, 0.f};
#pragma unroll
    for (int ks = 0; ks < 4; ++ks)
#pragma unroll
      for (int t = 0; t < 3; ++t) {
        const int off = (t == 0) ? KG_L0 + c * 272 : (t == 1) ? KG_L1 + c * 272 : KG_L1 + (16 + min(c, 8)) * 272;
        const h8_t Af = *(const h8_t*)(hb + off + 64 * ks + 16 * kg);
        ka[t] = __builtin_amdgcn_mfma_f32_16x16x32_f16(Af, Bf[ks], ka[t], 0, 0, 0);
      }
  }
  SB;
  if (w >= 4) XG(2); else XG(3);
  SB;
  const float iSw = pow2_inv(Sw);
  wave_fence();
  if (hw == 0) *(f4_t*)(hb + lane * 16) = ka[1] * iSw;
  else { *(f4_t*)(hb + lane * 16) = ka[0] * iSw; *(f4_t*)(hb + 1024 + lane * 16) = ka[2] * iSw; }
  __syncthreads();
  const unsigned etag = *(const unsigned*)(smem + B_BND + 48 + 8);
  if (w >= 4) XG(3);
  {
    const char* pb = smem + (w ^ 1) * KG_W;
    const float Sc = pow2_scale(((const float*)(smem + B_BND))[2 * cl] + ((const float*)(smem + B_BND))[2 * cl + 1], 15);
    char* kr = smem + B_KC + cl * KRS;
    if (hw == 0) {
      const f4_t k0 = (ka[0] * iSw + *(const f4_t*)(pb + lane * 16)) * Sc, k2 = (ka[2] * iSw + *(const f4_t*)(pb + 1024 + lane * 16)) * Sc;
      *(u2_t*)(kr + 2 * (128 * (kg >> 1) + 4 * (kg & 1) + 8 * c)) = u2_t{pk_f16(k0[0], k0[1]), pk_f16(k0[2], k0[3])};
      const unsigned p01 = pk_f16(k2[0], k2[1]), p23 = pk_f16(k2[2], k2[3]);
      u16* k3 = (u16*)kr + 512 + 64 * kg + c;
      if (kg < 2) { k3[0] = (u16)p01; k3[16] = (u16)(p01 >> 16); k3[32] = (u16)p23; k3[48] = (u16)(p23 >> 16); }
      else if (lane == 32) k3[0] = (u16)p01;
      if (lane == 0) ((float*)(smem + B_BND + 32))[cl] = pow2_inv(Sc);
    } else {
      const f4_t k1 = (ka[1] * iSw + *(const f4_t*)(pb + lane * 16)) * Sc;
      const unsigned p01 = pk_f16(k1[0], k1[1]), p23 = pk_f16(k1[2], k1[3]);
      if (kg < 2) {
        *(u2_t*)(kr + 2 * (256 + 64 * kg + 4 * c)) = u2_t{p01, p23};
      } else {
        *(unsigned*)(kr + 2 * (384 + 64 * (kg - 2) + 2 * c)) = p01;
        *(unsigned*)(kr + 2 * (384 + 64 * (kg - 2) + 32 + 2 * c)) = p23;
      }
    }
  }
  __syncthreads();
  const float invs = ((const float*)(smem + B_BND + 32))[w & 3];
  {
    const int kch = tid >> 7, kc = tid & 127;
    const char* kr = smem + B_KC + kch * KRS;
    const int e0 = (kc < 32) ? 8 * kc : (kc < 64) ? 256 + 4 * (kc - 32) : 384 + 2 * (kc - 64);
    const unsigned* kp = (const unsigned*)(kr + 2 * e0);
    const unsigned d0 = kp[0], d1 = kp[1], d2 = kp[2], d3 = kp[3], d4 = kp[4];
    const unsigned kfirst = *(const u16*)kr, k1024 = *(const u16*)(kr + 2 * 512);
    const int dl = 127 - kc;
    const unsigned kfa = *(const u16*)(kr + 2 * (512 + max(2 * dl - 127, 0))), kfb = *(const u16*)(kr + 2 * (512 + max(2 * dl - 126, 0)));
    char* ki = smem + B_KOFF + kch * KDS;
    const unsigned k0 = d0 & 0xffffu;
    unsigned o0, o1, o2, o3, nx;
    if (kc < 32) { o0 = d0; o1 = d1; o2 = d2; o3 = d3; nx = d4; }
    else if (kc < 64) {
      const unsigned k2 = d0 >> 16, k4 = d1 & 0xffffu, k6 = d1 >> 16, k8 = d2 & 0xffffu;
      o0 = k0 * 0x10001u; o1 = k2 | (k4 << 16); o2 = k4 * 0x10001u; o3 = k6 | (k8 << 16); nx = k8;
    } else {
      const unsigned k4 = d0 >> 16, k8 = d1 & 0xffffu;
      o0 = k0 * 0x10001u; o1 = k0 | (k4 << 16); o2 = k4 * 0x10001u; o3 = k8 * 0x10001u; nx = k8;
    }
    *(u4_t*)(ki + 16 * (kc + 2)) = u4_t{o0, o1, o2, o3};
    *(u4_t*)(ki + KD_O + 16 * (kc + 2)) = u4_t{__builtin_amdgcn_alignbit(o1, o0, 16), __builtin_amdgcn_alignbit(o2, o1, 16),
                                              __builtin_amdgcn_alignbit(o3, o2, 16), __builtin_amdgcn_alignbit(nx, o3, 16)};
    if (kc < 6) {
      const int cp = (kc < 2) ? kc : 128 + kc;
      const unsigned kk = k1024 * 0x10001u;
      const u4_t e = (kc == 2) ? u4_t{kk, kk, k1024, 0u} : u4_t{0u, 0u, 0u, 0u};
      const u4_t o = (kc == 2) ? u4_t{kk, kk, 0u, 0u} : u4_t{0u, 0u, 0u, (kc == 1) ? (kfirst << 16) : 0u};
      *(u4_t*)(ki + 16 * cp) = e;
      *(u4_t*)(ki + KD_O + 16 * cp) = o;
    }
    if (kc < 80) *(unsigned*)(ki + KD_DR + 4 * kc) = (dl >= 64) ? (kfa | (kfb << 16)) : 0u;
  }
  *(unsigned*)(smem + B_UOFF + (tid >> 7) * B_UCH + 4096 + 4 * (tid & 127)) = 0u;
  asm volatile("s_waitcnt vmcnt(2)" : "+v"(gr[0]), "+v"(gr[1]), "+v"(xg[0]), "+v"(xg[1])::"memory");
  {
    for (int spin = 0; spin < SPIN_MAX; ++spin) {
      if (__all(gr[0][1] == etag && gr[0][3] == etag && gr[1][1] == etag && gr[1][3] == etag)) break;
      __builtin_amdgcn_s_sleep(2);
      GRL();
      asm volatile("s_waitcnt vmcnt(0)" : "+v"(gr[0]), "+v"(gr[1])::"memory");
    }
#pragma unroll
    for (int it = 0; it < 2; ++it)
#pragma unroll
      for (int r = 0; r < 2; ++r)
        if (gr[it][2 * r + 1] != etag) gr[it][2 * r] = ln1_row(x, 2 * (tid + 512 * it) + r);
  }
  const float dk[4] = {bf_lo(dkv.x), bf_hi(dkv.x), bf_lo(dkv.y), bf_hi(dkv.y)};
  float z[2][8];
#pragma unroll
  for (int it = 0; it < 2; ++it) {
    if (it == 1) asm volatile("s_waitcnt vmcnt(0)" : "+v"(xg[2]), "+v"(xg[3])::"memory");
    const int j = tid + 512 * it;
    const unsigned s0 = gr[it][0], s1 = gr[it][2];
    const float m0 = bf_lo(s0), r0 = __builtin_amdgcn_rcpf(bf_hi(s0)), m1 = bf_lo(s1), r1 = __builtin_amdgcn_rcpf(bf_hi(s1));
    const u2_t xa = xg[2 * it], xc = xg[2 * it + 1];
    const float x0[4] = {bf_lo(xa.x), bf_hi(xa.x), bf_lo(xa.y), bf_hi(xa.y)};
    const float x1[4] = {bf_lo(xc.x), bf_hi(xc.x), bf_lo(xc.y), bf_hi(xc.y)};
#pragma unroll
    for (int cc = 0; cc < 4; ++cc) {
      const float u0 = (x0[cc] - m0) * r0, u1 = (x1[cc] - m1) * r1;
      *(unsigned*)(smem + B_UOFF + cc * B_UCH + 4 * (1023 - j)) = pk_f16(u1, u0);
      z[it][cc] = x0[cc] + u0 * dk[cc];
      z[it][4 + cc] = x1[cc] + u1 * dk[cc];
    }
  }
  __syncthreads();
  if (w >= 4 && w < 7 && cb * 3 + (w - 4) < 432) pack_wd(wd, ws, smem + B_PACK + (w - 4) * 5120, cb * 3 + (w - 4), lane);
  f4_t acc[8];
  if (w < 4) {
    __builtin_amdgcn_s_setprio(1);
    const char* ub = smem + B_UOFF + w * B_UCH + 4112 - 32 * c - 16 * kg - 512 * 7;
    const char* kb = smem + B_KOFF + w * KDS + ((c & 1) ? 0 : KD_O) + 4 * (12 + ((c + 1) >> 1) - 4 * kg);
    const unsigned* dr = (const unsigned*)(smem + B_KOFF + w * KDS + KD_DR) + (127 - c + 4 * kg);
    const char* uw = smem + B_UOFF + w * B_UCH + 4112 - 32 * c - 16 * kg;
    auto rdB = [&](BFrag& F, int i0, int p) { F.B[p] = *(const h8_t*)(ub + 512 * (7 - p) + 64 * i0); };
    auto rdA = [&](BFrag& F, int i0, int i1, int half) {
      const unsigned* ap = (const unsigned*)(kb + 64 * i0 + 512 * i1) + 2 * half;
      F.A[i1][half] = u2_t{ap[0], ap[1]};
    };
    auto frag = [&](const u2_t (&a)[2]) { return u4_t{a[0].x, a[0].y, a[1].x, a[1].y}; };
    auto rdnext = [&](BFrag& F, int i0, int m) {
      if (m < 8) rdB(F, i0, m); else if (m < 16) rdA(F, i0, (m - 8) >> 1, (m - 8) & 1);
    };
    auto step = [&](auto i0c, const BFrag& F, BFrag& N, auto nextc) {
      constexpr int I0 = decltype(i0c)::value, NX = decltype(nextc)::value;
      int m = 0;
#pragma unroll
      for (int i1 = 0; i1 < 5; ++i1) {
        if (i1 == 4 && I0 != 0) continue;
        const u4_t a = frag(F.A[i1]);
#pragma unroll
        for (int t = i1; t < 8; ++t) {
          if (I0 == 0 && i1 == 0) mfma_f16_0(acc[t], a, F.B[t]); else mfma_f16(acc[t], a, F.B[t - i1]);
          if (NX >= 0) rdnext(N, NX, m);
          SB;
          ++m;
        }
      }
    };
    BFrag F0, F1;
    h8_t U9[4], U7[4];
#pragma unroll
    for (int bt = 0; bt < 4; ++bt) { U9[bt] = *(const h8_t*)(uw - 512 * bt + 16); U7[bt] = *(const h8_t*)(uw - 512 * bt); }
#pragma unroll
    for (int m = 0; m < 16; ++m) rdnext(F0, 0, m);
    rdA(F0, 0, 4, 0); rdA(F0, 0, 4, 1);
    SB;
    f4_t X9[4], X7[4];
#pragma unroll
    for (int bt = 0; bt < 4; ++bt) { mfma_f16_0(X9[bt], __builtin_bit_cast(u4_t, U9[bt]), band9); mfma_f16_0(X7[bt], __builtin_bit_cast(u4_t, U7[bt]), band7); }
    SB;
    step(IC(0), F0, F1, IC(1));
    h8_t Vf[4]; u4_t Tf[4];
    {
      int m = 0;
#pragma unroll
      for (int i1 = 0; i1 < 4; ++i1) {
        const u4_t a = frag(F1.A[i1]);
#pragma unroll
        for (int t = i1; t < 8; ++t) {
          mfma_f16(acc[t], a, F1.B[t - i1]);
          rdnext(F0, 2, m);
          if (m < 4) {
            const u4_t vu = {pk_f16(X7[m][0], X9[m][0]), pk_f16(X7[m][1], X9[m][1]), pk_f16(X7[m][2], X9[m][2]), pk_f16(X7[m][3], X9[m][3])};
            Vf[m] = __builtin_bit_cast(h8_t, vu);
          }
          if (m >= 16 && m < 20) { const unsigned* tp = dr - 16 * (m - 16 + 4); Tf[m - 16] = u4_t{tp[0], tp[1], tp[2], tp[3]}; }
          SB;
          ++m;
        }
      }
    }
    step(IC(2), F0, F1, IC(3));
#pragma unroll
    for (int bt = 0; bt < 4; ++bt)
#pragma unroll
      for (int at = 4 + bt; at < 8; ++at) mfma_f16(acc[at], __builtin_bit_cast(u4_t, Vf[bt]), __builtin_bit_cast(h8_t, Tf[at - bt - 4]));
    SB;
    step(IC(3), F1, F0, IC(4));
    step(IC(4), F0, F1, IC(5));
    step(IC(5), F1, F0, IC(6));
    step(IC(6), F0, F1, IC(7));
    step(IC(7), F1, F0, IC(-1));
    asm volatile("s_nop 7\n\ts_nop 1" : "+v"(acc[0]), "+v"(acc[1]), "+v"(acc[2]), "+v"(acc[3]), "+v"(acc[4]), "+v"(acc[5]), "+v"(acc[6]), "+v"(acc[7]));
  }
  bar();
  if (w < 4) {
#pragma unroll
    for (int t = 0; t < 8; ++t) *(f4_t*)(smem + w * B_YCH + t * 1280 + kg * 320 + c * 16) = acc[t] * invs;
  }
  asm volatile("s_waitcnt lgkmcnt(0)" ::: "memory");
  bar();
#pragma unroll
  for (int it = 0; it < 2; ++it) {
    const int j = tid + 512 * it, t = 2 * j;
    const int yo = (t >> 8) * 1280 + ((t >> 2) & 3) * 320 + ((t >> 4) & 15) * 16 + (t & 3) * 4;
    float h0[4], h1[4];
#pragma unroll
    for (int cc = 0; cc < 4; ++cc) {
      f2_t y = *(const f2_t*)(smem + cc * B_YCH + yo);
      h0[cc] = z[it][cc] + y.x;
      h1[cc] = z[it][4 + cc] + y.y;
    }
    char* dst = ws + OFF_H8 + ((size_t)(q >> 1) * SL + t) * 16 + (q & 1) * 8;
    *(u2_t*)(dst) = u2_t{pk_bf16(h0[0], h0[1]), pk_bf16(h0[2], h0[3])};
    *(u2_t*)(dst + 16) = u2_t{pk_bf16(h1[0], h1[1]), pk_bf16(h1[2], h1[3])};
  }
}

constexpr int C_T = 240;
constexpr int C_SLOT = 23552, C_NSLOT = 6, C_GOFF = 15360;
constexpr int C_STAT = C_NSLOT * C_SLOT;
constexpr int C_CS = C_STAT + 960;
constexpr int C_LDS = C_CS + 512;
constexpr size_t OFF_DUMMY = 0x1140000;

__device__ __forceinline__ unsigned ln_pack(float sum, float sq) {
  const float mean = sum * (1.0f / DM);
  return ln_fin(mean, fmaxf(sq * (1.0f / DM) - mean * mean, 0.f));
}
__device__ __forceinline__ unsigned ln_row(const char* h8, int t) {
  float s = 0.f, q = 0.f;
  for (int pl = 0; pl < 80; ++pl) {
    const u4_t v = *(const u4_t*)(h8 + (size_t)pl * 32768 + t * 16);
    for (int j = 0; j < 4; ++j) { const float a = bf_lo(v[j]), b = bf_hi(v[j]); s += a + b; q += a * a + b * b; }
  }
  return ln_pack(s, q);
}

template <int NDMA>
__device__ __forceinline__ void c_loader(char* smem, char* ws, const char* wbase, int w4, int lane, int t0, int jline, int xcd) {
  const unsigned lds0 = (unsigned)(size_t)(LAS char*)smem;
  const char* h8 = ws + OFF_H8;
  unsigned voffH[4], voffW[2];
#pragma unroll
  for (int k = 0; k < 2; ++k) {
    const int r = 8 * (2 * (w4 & 1) + k) + (lane >> 3), c = lane & 7;
    const int f = ((r >> 1) & 1) | ((r >> 2) & 2);
    voffW[k] = r * (FF * 2) + 128 * jline + (((c >> 1) ^ f) * 32) + (c & 1) * 16;
  }
#pragma unroll
  for (int k = 0; k < 4; ++k) {
    const int idx = 64 * (w4 + 4 * k) + lane, pl = idx / C_T, tt = idx % C_T;
    const int t = min(t0 + tt, SL - 1);
    voffH[k] = pl * 32768 + t * 16;
  }
  const unsigned ldsW = C_GOFF + (w4 >> 1) * 4096 + (w4 & 1) * 2048;
  auto issue = [&](int step, unsigned so) {
#pragma unroll
    for (int k = 0; k < 2; ++k) glds16(wbase + (size_t)step * (32 * FF * 2), voffW[k], lds0 + so + ldsW + k * 1024);
#pragma unroll
    for (int k = 0; k < NDMA - 2; ++k) glds16(h8 + (size_t)step * (4 * 32768), voffH[k], lds0 + so + (w4 + 4 * k) * 1024);
  };
  constexpr unsigned S = C_SLOT;
  issue(0, 0); issue(1, S); issue(2, 2 * S); issue(3, 3 * S);
  wait_vm<3 * NDMA>(); bar();
  issue(4, 4 * S);
  wait_vm<2 * NDMA>(); bar();
  issue(5, 5 * S); issue(6, 0);
  unsigned sw = S;
#pragma unroll 1
  for (int j = 1; j < 7; ++j) {
    wait_vm<2 * NDMA>();
    bar();
    issue(2 * j + 5, sw);
    sw += S; if (sw == C_NSLOT * S) sw = 0;
    issue(2 * j + 6, sw);
    sw += S; if (sw == C_NSLOT * S) sw = 0;
  }
  wait_vm<2 * NDMA>(); bar();
  issue(19, sw);
  const int tl = min(60 * w4 + lane, C_T - 1), tg = min(t0 + tl, SL - 1);
  const gu64* gp = (const gu64*)(ws + OFF_G2) + tg;
  const float* csp = (const float*)(ws + OFF_CS) + ((lane >> 4) & 1) * FF + 64 * jline + 16 * w4 + (lane & 15);
  const char* tb = ws + OFF_WP2 + (size_t)(4 * (xcd & 1)) * (54 * 5120);
  const int tl0 = ((int)(blockIdx.x >> 3) * 4 + w4) * 68;
  u2_t gv; float csv; unsigned tv0, tv1, tv2;
  asm volatile("global_load_dwordx2 %0, %1, off sc1" : "=v"(gv) : "v"(gp) : "memory");
  asm volatile("global_load_dword %0, %1, off" : "=v"(csv) : "v"(csp) : "memory");
  asm volatile("global_load_dword %0, %1, off" : "=v"(tv0) : "v"(tb + (size_t)min(tl0 + lane, 8639) * 128) : "memory");
  asm volatile("global_load_dword %0, %1, off" : "=v"(tv1) : "v"(tb + (size_t)min(tl0 + 64 + (lane & 3), 8639) * 128) : "memory");
  {
    unsigned long long kd;
    asm volatile("s_getpc_b64 s[20:21]\n\ts_add_u32 s20, s20, kD@rel32@lo+4\n\ts_addc_u32 s21, s21, kD@rel32@hi+12\n\ts_mov_b64 %0, s[20:21]" : "=s"(kd) :: "s20", "s21", "scc");
    const char* tp = (lane == 0) ? (const char*)&g_ptrs[xcd] : (const char*)kd + min((int)(blockIdx.x >> 3) * 4 + w4, 79) * 128;
    asm volatile("global_load_dword %0, %1, off" : "=v"(tv2) : "v"(tp) : "memory");
  }
  wait_vm<NDMA + 5>(); bar();
  asm volatile("s_waitcnt vmcnt(0)" : "+v"(gv), "+v"(csv), "+v"(tv0), "+v"(tv1), "+v"(tv2)::"memory");
  bar();
  {
    bool ok = gv.y == 1u;
    for (int spin = 0; spin < 4096 && !__all(ok); ++spin) {
      const unsigned long long g = __hip_atomic_load(gp, __ATOMIC_RELAXED, __HIP_MEMORY_SCOPE_AGENT);
      gv.x = (unsigned)g; ok = (unsigned)(g >> 32) == 1u;
      __builtin_amdgcn_s_sleep(2);
    }
    if (!__all(ok)) gv.x = ln_row(h8, tg);
    if (lane < 60) *(float*)(smem + C_STAT + (60 * w4 + lane) * 4) = bf_lo(gv.x);
    if (lane < 32) *(float*)(smem + C_CS + (lane >> 4) * 256 + (16 * w4 + (lane & 15)) * 4) = csv;
  }
  asm volatile("s_waitcnt lgkmcnt(0)" ::: "memory");
  bar();
}

struct CFrag { b8_t A[2][2]; b8_t B[8]; };

extern "C" __global__ __launch_bounds__(512, 2) void kC() {
  extern __shared__ __attribute__((aligned(16))) char smem[];
  char* __restrict__ ws = ptrs()->ws;
  const u16* __restrict__ wg = ptrs()->wg;
  const u16* __restrict__ wu = ptrs()->wu;
  const int tid = threadIdx.x, lane = tid & 63;
  const int w = __builtin_amdgcn_readfirstlane(tid >> 6);
  int panel, jline;
  const int xcd = blockIdx.x & 7, rank = blockIdx.x >> 3;
  {
    const int g4 = xcd >> 1, a = xcd & 1, nl = (g4 < 3) ? 7 : 6, total = 9 * nl, n0 = (total + 1) >> 1;
    if (a == 0 ? rank >= n0 : rank >= total - n0) {
      const int sp = (xcd < 6) ? (xcd >> 1) : 3 + 5 * (xcd - 6) + (rank - 27);
      const int wv = sp * 8 + w;
      for (int tile = wv; tile < 128; tile += 104) {
        const int t = 16 * tile + (lane & 15);
        const char* hp = ws + OFF_H8 + (size_t)(lane >> 4) * 32768 + t * 16;
        u4_t v[20];
#pragma unroll
        for (int i = 0; i < 20; ++i) v[i] = *(const u4_t*)(hp + (size_t)i * (4 * 32768));
        float s0 = 0.f, s1 = 0.f, q0 = 0.f, q1 = 0.f;
        unsigned k_both = 0x3f803f80u;
        asm volatile("" : "+s"(k_both));
#pragma unroll
        for (int i = 0; i < 20; ++i) {
          s0 = dot2bf(v[i][0], k_both, s0); q0 = dot2bf(v[i][0], v[i][0], q0);
          s1 = dot2bf(v[i][1], k_both, s1); q1 = dot2bf(v[i][1], v[i][1], q1);
          s0 = dot2bf(v[i][2], k_both, s0); q0 = dot2bf(v[i][2], v[i][2], q0);
          s1 = dot2bf(v[i][3], k_both, s1); q1 = dot2bf(v[i][3], v[i][3], q1);
        }
        float s = s0 + s1, q = q0 + q1;
        s += __shfl_xor(s, 16); q += __shfl_xor(q, 16);
        s += __shfl_xor(s, 32); q += __shfl_xor(q, 32);
        if (lane < 16)
          __hip_atomic_store((gu64*)(ws + OFF_G2) + t, (1ull << 32) | ln_pack(s, q), __ATOMIC_RELAXED, __HIP_MEMORY_SCOPE_AGENT);
      }
      return;
    }
    const int idx = rank + a * n0;
    panel = idx / nl; jline = 7 * g4 + idx % nl;
  }
  const int t0 = panel * C_T;
  if (w >= 4) {
    const int w4 = w - 4;
    const char* wbase = (const char*)((w4 >> 1) ? wu : wg);
    if (w4 < 3) c_loader<6>(smem, ws, wbase, w4, lane, t0, jline, xcd);
    else c_loader<5>(smem, ws, wbase, w4, lane, t0, jline, xcd);
    return;
  }
  const int s = w >> 1, th = w & 1;
  const int fr = lane & 15, kg = lane >> 4, q = (lane & 15) >> 2, p = lane & 3;
  const unsigned hb0 = kg * 3840 + fr * 16 + 256 * 7;
  const unsigned hb1 = kg * 3840 + fr * 16 + 256 * (th ? 8 : 0);
  unsigned wa[2];
#pragma unroll
  for (int v = 0; v < 2; ++v) {
    const int sub = 2 * s + (v ? 1 - th : th);
    const int f = (q >> 1) | (2 * (kg & 1));
    wa[v] = C_GOFF + (8 * kg + q) * 128 + ((sub ^ f) * 32) + 8 * p;
  }
  if (panel == 8 && th) {
    f4_t g0 = {0.f, 0.f, 0.f, 0.f}, u0 = {0.f, 0.f, 0.f, 0.f};
    auto stepl = [&](unsigned so) {
      const char* sp = smem + so;
      const s4_t gl = tr_read(sp + wa[0]), gh = tr_read(sp + wa[0] + 512), ul = tr_read(sp + wa[0] + 4096), uh = tr_read(sp + wa[0] + 4096 + 512);
      const b8_t B = *(const b8_t*)(sp + hb0);
      const u2_t a = __builtin_bit_cast(u2_t, gl), b = __builtin_bit_cast(u2_t, gh), c2 = __builtin_bit_cast(u2_t, ul), d = __builtin_bit_cast(u2_t, uh);
      g0 = __builtin_amdgcn_mfma_f32_16x16x32_bf16(__builtin_bit_cast(b8_t, (u4_t{a.x, a.y, b.x, b.y})), B, g0, 0, 0, 0);
      u0 = __builtin_amdgcn_mfma_f32_16x16x32_bf16(__builtin_bit_cast(b8_t, (u4_t{c2.x, c2.y, d.x, d.y})), B, u0, 0, 0, 0);
      asm volatile("s_waitcnt lgkmcnt(0)" ::: "memory");
    };
    bar();
    stepl(0);
    unsigned sl = C_SLOT;
#pragma unroll 1
    for (int j = 0; j < 9; ++j) {
      bar();
      stepl(sl); sl += C_SLOT; if (sl == C_NSLOT * C_SLOT) sl = 0;
      stepl(sl); sl += C_SLOT; if (sl == C_NSLOT * C_SLOT) sl = 0;
    }
    bar();
    stepl(sl);
    bar();
    const int sub = 2 * s + th;
    const f4_t csg0 = *(const f4_t*)(smem + C_CS + (16 * sub + 4 * kg) * 4), csu0 = *(const f4_t*)(smem + C_CS + 256 + (16 * sub + 4 * kg) * 4);
    const float m0 = *(const float*)(smem + C_STAT + (16 * 7 + fr) * 4);
    const f4_t r = (g0 - m0 * csg0) * (u0 - m0 * csu0);
    *(u2_t*)(ws + OFF_HH + (size_t)(2 * jline + s) * 1024 + lane * 16 + (size_t)(15 * 8 + 7) * (54 * 1024) + 8) = u2_t{pk_bf16(r[0], r[1]), pk_bf16(r[2], r[3])};
    return;
  }
  f4_t ag[8][2], au[8][2];
#pragma unroll
  for (int u = 0; u < 8; ++u)
#pragma unroll
    for (int v = 0; v < 2; ++v) { ag[u][v] = f4_t{0.f, 0.f, 0.f, 0.f}; au[u][v] = f4_t{0.f, 0.f, 0.f, 0.f}; }
  auto load = [&](CFrag& F, unsigned so) {
    const char* sp = smem + so;
#pragma unroll
    for (int v = 0; v < 2; ++v)
#pragma unroll
      for (int m = 0; m < 2; ++m) {
        s4_t lo = tr_read(sp + wa[v] + m * 4096), hi = tr_read(sp + wa[v] + m * 4096 + 512);
        u2_t a = __builtin_bit_cast(u2_t, lo), b = __builtin_bit_cast(u2_t, hi);
        F.A[v][m] = __builtin_bit_cast(b8_t, (u4_t{a.x, a.y, b.x, b.y}));
      }
    F.B[0] = *(const b8_t*)(sp + hb0);
#pragma unroll
    for (int u = 1; u < 8; ++u) F.B[u] = *(const b8_t*)(sp + hb1 + 256 * (u - 1));
  };
  auto mma = [&](const CFrag& F) {
    ag[0][0] = __builtin_amdgcn_mfma_f32_16x16x32_bf16(F.A[0][0], F.B[0], ag[0][0], 0, 0, 0);
    au[0][0] = __builtin_amdgcn_mfma_f32_16x16x32_bf16(F.A[0][1], F.B[0], au[0][0], 0, 0, 0);
#pragma unroll
    for (int u = 1; u < 8; ++u)
#pragma unroll
      for (int v = 0; v < 2; ++v) {
        ag[u][v] = __builtin_amdgcn_mfma_f32_16x16x32_bf16(F.A[v][0], F.B[u], ag[u][v], 0, 0, 0);
        au[u][v] = __builtin_amdgcn_mfma_f32_16x16x32_bf16(F.A[v][1], F.B[u], au[u][v], 0, 0, 0);
      }
  };
  auto interleave = [&]() {
#pragma unroll
    for (int i = 0; i < 16; ++i) {
      __builtin_amdgcn_sched_group_barrier(0x008, 1, 0);
      __builtin_amdgcn_sched_group_barrier(0x100, 1, 0);
    }
    __builtin_amdgcn_sched_group_barrier(0x008, 14, 0);
  };
  CFrag F0, F1;
  bar();
  load(F0, 0);
  unsigned sn = C_SLOT;
  SB;
#pragma unroll 1
  for (int j = 0; j < 9; ++j) {
    asm volatile("s_waitcnt lgkmcnt(0)" ::: "memory");
    bar(); SB;
    load(F1, sn);
    mma(F0);
    interleave();
    SB;
    sn += C_SLOT; if (sn == C_NSLOT * C_SLOT) sn = 0;
    load(F0, sn);
    mma(F1);
    interleave();
    SB;
    sn += C_SLOT; if (sn == C_NSLOT * C_SLOT) sn = 0;
  }
  asm volatile("s_waitcnt lgkmcnt(0)" ::: "memory");
  bar(); SB;
  load(F1, sn);
  mma(F0);
  interleave();
  SB;
  bar(); SB;
  f4_t csg[2], csu[2];
  float mu[8];
#pragma unroll
  for (int v = 0; v < 2; ++v) {
    const int sub = 2 * s + (v ? 1 - th : th);
    csg[v] = *(const f4_t*)(smem + C_CS + (16 * sub + 4 * kg) * 4);
    csu[v] = *(const f4_t*)(smem + C_CS + 256 + (16 * sub + 4 * kg) * 4);
  }
#pragma unroll
  for (int u = 0; u < 8; ++u) {
    const int tile = (u == 0) ? 7 : (th ? 7 + u : u - 1);
    mu[u] = *(const float*)(smem + C_STAT + (16 * tile + fr) * 4);
  }
  mma(F1);
  char* hh = ws + OFF_HH + (size_t)(2 * jline + s) * 1024 + lane * 16;
  char* sink = ws + OFF_DUMMY + lane * 16;
  auto epi = [&](auto thc) {
    constexpr int TH = decltype(thc)::value;
#pragma unroll
    for (int u = 0; u < 8; ++u) {
      const int tile = (u == 0) ? 7 : (TH ? 7 + u : u - 1);
      const int t16 = 15 * panel + tile;
      char* dst = (t16 < 128) ? hh + (size_t)t16 * (54 * 1024) : sink;
      u2_t o[2];
#pragma unroll
      for (int v = 0; v < 2; ++v) {
        if (u == 0 && v == 1) continue;
        const f4_t r = (ag[u][v] - mu[u] * csg[v]) * (au[u][v] - mu[u] * csu[v]);
        o[v] = u2_t{pk_bf16(r[0], r[1]), pk_bf16(r[2], r[3])};
      }
      if (u == 0) *(u2_t*)(dst + 8 * TH) = o[0];
      else *(u4_t*)(dst) = u4_t{o[TH].x, o[TH].y, o[1 - TH].x, o[1 - TH].y};
    }
  };
  if (th) epi(IC(1)); else epi(IC(0));
}

constexpr int D_SLOT = 15360, D_NSLOT = 9;
constexpr int D_LDS = D_NSLOT * D_SLOT;

template <int NDMA>
__device__ __forceinline__ void d_loader(char* smem, const char* wp, int w4, int lane) {
  const unsigned lds0 = (unsigned)(size_t)(LAS char*)smem;
  const unsigned voff = lane * 16;
  auto issue = [&](int step, unsigned so) {
#pragma unroll
    for (int k = 0; k < NDMA; ++k) {
      const int i = w4 + 4 * k;
      glds16(wp + (size_t)step * D_SLOT + i * 1024, voff, lds0 + so + i * 1024);
    }
  };
  constexpr unsigned S = D_SLOT;
  issue(0, 0); issue(1, S); issue(2, 2 * S);
  wait_vm<2 * NDMA>(); bar();
  issue(3, 3 * S); issue(4, 4 * S); issue(5, 5 * S);
  wait_vm<3 * NDMA>(); bar();
  issue(6, 6 * S); issue(7, 7 * S); issue(8, 8 * S);
  unsigned sw = 0;
#pragma unroll 1
  for (int j = 1; j < 5; ++j) {
    wait_vm<4 * NDMA>();
    bar();
    issue(2 * j + 7, sw);
    sw += S; if (sw == D_NSLOT * S) sw = 0;
    issue(2 * j + 8, sw);
    sw += S; if (sw == D_NSLOT * S) sw = 0;
  }
  wait_vm<4 * NDMA>(); bar();
  issue(17, sw);
  wait_vm<3 * NDMA>(); bar();
  wait_vm<1 * NDMA>(); bar();
  wait_vm<0>(); bar();
}

extern "C" __global__ __launch_bounds__(512, 2) void kD() {
  extern __shared__ __attribute__((aligned(16))) char smem[];
  char* __restrict__ ws = ptrs()->ws;
  u16* __restrict__ out = ptrs()->out;
  const int tid = threadIdx.x, lane = tid & 63;
  const int w = __builtin_amdgcn_readfirstlane(tid >> 6);
  const int xcd = blockIdx.x & 7, rank = blockIdx.x >> 3;
  const int it = 8 * (xcd >> 1) + (rank & 7), dg = 4 * (xcd & 1) + (rank >> 3);
  if (w >= 4) {
    const int w4 = w - 4;
    const char* wp = ws + OFF_WP2 + (size_t)dg * (54 * 5120);
    if (w4 < 3) d_loader<4>(smem, wp, w4, lane);
    else d_loader<3>(smem, wp, w4, lane);
    return;
  }
  const int fr = lane & 15, g = lane >> 4;
  const int t = 64 * it + 16 * w + fr;
  const char* hp = ws + OFF_HH + (size_t)(4 * it + w) * (54 * 1024);
  const unsigned voff = lane * 16;
  u4_t R[18];
#pragma unroll
  for (int i = 0; i < 9; ++i)
    asm volatile("global_load_dwordx4 %0, %1, %2" : "=v"(R[i]) : "v"(voff), "s"(hp + (size_t)i * 1024) : "memory");
  u2_t res[5]; unsigned st;
  f4_t acc[5];
#pragma unroll
  for (int dt = 0; dt < 5; ++dt) acc[dt] = f4_t{0.f, 0.f, 0.f, 0.f};
  b8_t A0[5], A1[5];
  auto rdA = [&](b8_t (&A)[5], unsigned off) {
    const char* sp = smem + off + lane * 16;
#pragma unroll
    for (int dt = 0; dt < 5; ++dt) A[dt] = *(const b8_t*)(sp + dt * 1024);
  };
  bar();
  rdA(A0, 0);
  unsigned so = 0;
  auto kstep = [&](auto mode, auto idx, b8_t (&Ac)[5], b8_t (&An)[5], const char* hnext) {
    constexpr int MODE = decltype(mode)::value, i = decltype(idx)::value, kk = i % 3;
    if (i % 6 == 0) bar();
    unsigned nx = so + (kk + 1) * 5120;
    if (kk == 2) { so += D_SLOT; if (so == D_NSLOT * D_SLOT) so = 0; nx = so; }
    SB;
    if (!(MODE == 2 && i == 17)) rdA(An, nx);
    constexpr int NV = (MODE == 0) ? (i < 9 ? 8 + i : 26 - i) : (MODE == 1) ? (i < 9 ? 25 - i : 17) : (i == 0 ? 17 : 23 - i);
    asm volatile("s_waitcnt vmcnt(%1)" : "+v"(R[i]) : "i"(NV) : "memory");
    const b8_t B = __builtin_bit_cast(b8_t, R[i]);
#pragma unroll
    for (int dt = 0; dt < 5; ++dt) acc[dt] = __builtin_amdgcn_mfma_f32_16x16x32_bf16(Ac[dt], B, acc[dt], 0, 0, 0);
    SB;
    if (MODE == 0 && i < 9)
      asm volatile("global_load_dwordx4 %0, %1, %2" : "=v"(R[i < 9 ? 9 + i : 0]) : "v"(voff), "s"(hp + (size_t)(9 + i) * 1024) : "memory");
    if (MODE != 2) asm volatile("global_load_dwordx4 %0, %1, %2" : "+v"(R[i]) : "v"(voff), "s"(hnext + (size_t)i * 1024) : "memory");
    if (MODE == 2 && i == 0) {
#pragma unroll
      for (int dt = 0; dt < 5; ++dt) {
        const int d = 80 * dg + 16 * dt + 4 * g;
        const char* rp = ws + OFF_H8 + ((size_t)(d >> 3) * SL + t) * 16 + (d & 4) * 2;
        asm volatile("global_load_dwordx2 %0, %1, off" : "=v"(res[dt]) : "v"(rp) : "memory");
      }
      asm volatile("global_load_dword %0, %1, off" : "=v"(st) : "v"(ws + OFF_G2 + (size_t)t * 8) : "memory");
    }
    SB;
  };
  auto round = [&](auto mode, const char* hnext) {
    kstep(mode, IC(0), A0, A1, hnext); kstep(mode, IC(1), A1, A0, hnext); kstep(mode, IC(2), A0, A1, hnext);
    kstep(mode, IC(3), A1, A0, hnext); kstep(mode, IC(4), A0, A1, hnext); kstep(mode, IC(5), A1, A0, hnext);
    kstep(mode, IC(6), A0, A1, hnext); kstep(mode, IC(7), A1, A0, hnext); kstep(mode, IC(8), A0, A1, hnext);
    kstep(mode, IC(9), A1, A0, hnext); kstep(mode, IC(10), A0, A1, hnext); kstep(mode, IC(11), A1, A0, hnext);
    kstep(mode, IC(12), A0, A1, hnext); kstep(mode, IC(13), A1, A0, hnext); kstep(mode, IC(14), A0, A1, hnext);
    kstep(mode, IC(15), A1, A0, hnext); kstep(mode, IC(16), A0, A1, hnext); kstep(mode, IC(17), A1, A0, hnext);
  };
  round(IC(0), hp + 18 * 1024);
  round(IC(1), hp + 36 * 1024);
  round(IC(2), hp);
  asm volatile("s_waitcnt vmcnt(0)" : "+v"(res[0]), "+v"(res[1]), "+v"(res[2]), "+v"(res[3]), "+v"(res[4]), "+v"(st)::"memory");
  const float sd = bf_hi(st), r2 = 1.0f / (sd * sd);
#pragma unroll
  for (int dt = 0; dt < 5; ++dt) {
    const int d = 80 * dg + 16 * dt + 4 * g;
    const float h0 = bf_lo(res[dt].x), h1 = bf_hi(res[dt].x), h2 = bf_lo(res[dt].y), h3 = bf_hi(res[dt].y);
    *(u2_t*)(out + (size_t)t * DM + d) = u2_t{pk_bf16(h0 + r2 * acc[dt][0], h1 + r2 * acc[dt][1]), pk_bf16(h2 + r2 * acc[dt][2], h3 + r2 * acc[dt][3])};
  }
}

extern "C" void kernel_launch(void* const* d_in, const int* in_sizes, int n_in, void* d_out, int out_size, void* d_ws,
                              size_t ws_size, hipStream_t stream) {
  char* ws = (char*)d_ws;
  (void)hipFuncSetAttribute((const void*)kB, hipFuncAttributeMaxDynamicSharedMemorySize, B_LDS);
  (void)hipFuncSetAttribute((const void*)kC, hipFuncAttributeMaxDynamicSharedMemorySize, C_LDS);
  (void)hipFuncSetAttribute((const void*)kD, hipFuncAttributeMaxDynamicSharedMemorySize, D_LDS);
  kB<<<256, 512, B_LDS, stream>>>((const u16*)d_in[0], (const u16*)d_in[1], (const u16*)d_in[2], (const u16*)d_in[3], (const u16*)d_in[4],
                                  (const u16*)d_in[5], (const u16*)d_in[6], (const u16*)d_in[7], (const u16*)d_in[8], (const u16*)d_in[10],
                                  (const u16*)d_in[11], (const u16*)d_in[12], (u16*)d_out, ws);
  kC<<<256, 512, C_LDS, stream>>>();
  kD<<<256, 512, D_LDS, stream>>>();
}
```
